```python
import torch

def _softmax(x, axis=-1):
    return torch.softmax(x, dim=axis)
def _gelu(x):
    return torch.nn.functional.gelu(x, approximate="tanh")
def _silu(x):
    return torch.nn.functional.silu(x)
def _sigmoid(x):
    return torch.sigmoid(x)
def _layernorm(x, axis=-1, eps=1e-5):
    m = x.mean(dim=axis, keepdim=True)
    v = x.var(dim=axis, keepdim=True, unbiased=False)
    return (x - m) / torch.sqrt(v + eps)
def _scatter_add(tgt, idx, src, axis=0):
    return tgt.clone().index_add_(axis, idx, src)
def _segment_sum(src, seg, n):
    out = torch.zeros((n,) + src.shape[1:], dtype=src.dtype)
    return out.index_add_(0, seg, src)
def _pad(x, pw):
    flat = tuple(v for pair in reversed(pw) for v in pair)
    return torch.nn.functional.pad(x, flat)

kernel_name = 'recurrent_scan__sliding_block_cached__glu_gated_glu__prompt'

def setup_inputs():

    _w = (lambda a: (lambda t: t.to(torch.bfloat16) if t.is_floating_point() else t)(torch.as_tensor(a)))
    def _A_setup():
        B, Sq, D = 1, 2048, 1024
        Hq, Hkv, Sk, Dh = 32, 4, 2048, 128
        assert Hq % Hkv == 0, (Hq, Hkv)
        return {
            "x": _w((lambda *s: torch.randn(*s, dtype=torch.bfloat16) * 0.05)(B, Sq, D)),
            "k_cache": _w((lambda *s: torch.randn(*s, dtype=torch.bfloat16) * 0.8)(B, Hkv, Sk, Dh)),
            "v_cache": _w((lambda *s: torch.randn(*s, dtype=torch.bfloat16) * 0.2)(B, Hkv, Sk, Dh)),
            "wq": _w((lambda *s: torch.randn(*s, dtype=torch.bfloat16) * 0.8)(D, Hq * Dh) * (D ** -0.5)),
            "wo": _w((lambda *s: torch.randn(*s, dtype=torch.bfloat16) * 0.2)(Hq * Dh, D) * ((Hq * Dh) ** -0.5)),
        }
    _A_in = _A_setup()

    def _B_setup():
        B, S, D, F = 1, 2048, 1024, 3584
        return {
            "x": _w((lambda *s: torch.randn(*s, dtype=torch.bfloat16) * 0.05)(B, S, D)),
            "w_gate": _w((lambda *s: torch.randn(*s, dtype=torch.bfloat16) * 0.2)(D, F)),
            "w_up": _w((lambda *s: torch.randn(*s, dtype=torch.bfloat16) * 0.1)(D, F)),
            "w_down": _w((lambda *s: torch.randn(*s, dtype=torch.bfloat16) * 0.1)(F, D)),
        }
    _B_in = _B_setup()

    return {
        "A_x": _A_in["x"],
        "A_k_cache": _A_in["k_cache"],
        "A_v_cache": _A_in["v_cache"],
        "A_wq": _A_in["wq"],
        "A_wo": _A_in["wo"],
        "B_x": _B_in["x"],
        "B_w_gate": _B_in["w_gate"],
        "B_w_up": _B_in["w_up"],
        "B_w_down": _B_in["w_down"],
    }

def reference(A_x, A_k_cache, A_v_cache, A_wq, A_wo, B_x, B_w_gate, B_w_up, B_w_down):
    def _A_ref(x, k_cache, v_cache, wq, wo):
        B, Sq, D = x.shape
        Hq, Dh = 32, 128
        Hkv, Sk = k_cache.shape[1], k_cache.shape[2]
        W = min(4096, max(256, Sk // 4))
        rep = Hq // Hkv
        q = (lambda x, *d: x.permute(*d))((x @ wq).reshape(B, Sq, Hq, Dh), 0, 2, 1, 3)
        k = (lambda x, n, axis: torch.repeat_interleave(x, n, dim=axis))(k_cache[:, :, Sk - W:, :], rep, axis=1)
        v = (lambda x, n, axis: torch.repeat_interleave(x, n, dim=axis))(v_cache[:, :, Sk - W:, :], rep, axis=1)
        scores = torch.einsum("bhqd,bhkd->bhqk", q, k) * (Dh ** -0.5)
        p = _softmax(scores, axis=-1)
        o = torch.einsum("bhqk,bhkd->bhqd", p, v)
        return (lambda x, *d: x.permute(*d))(o, 0, 2, 1, 3).reshape(B, Sq, Hq * Dh) @ wo

    def _B_ref(x, w_gate, w_up, w_down):
        g = torch.einsum("bsd,df->bsf", x, w_gate)
        u = torch.einsum("bsd,df->bsf", x, w_up)
        h = _sigmoid(g) * u
        return torch.einsum("bsf,fd->bsd", h, w_down)

    x = A_x
    xn = _layernorm(x, axis=-1)
    s = _A_ref(xn, A_k_cache, A_v_cache, A_wq, A_wo)
    g = _silu(_B_ref(xn, B_w_gate, B_w_up, B_w_down))
    return x + s * g
```

```cpp
#include <hip/hip_runtime.h>
#include <hip/hip_bf16.h>
typedef unsigned short u16;
__device__ __forceinline__ float b2f(u16 v){ return __uint_as_float(((unsigned)v)<<16); }
__device__ __forceinline__ u16 f2b(float f){ unsigned u=__float_as_uint(f); u += 0x7FFFu + ((u>>16)&1u); return (u16)(u>>16); }
__device__ __forceinline__ float rb(float f){ return b2f(f2b(f)); }

__global__ void k_ln(const u16* x, u16* xn){
  int row = blockIdx.x; int t = threadIdx.x;
  __shared__ float red[256];
  float v[4]; float s=0;
  for(int i=0;i<4;i++){ v[i]=b2f(x[row*1024+t*4+i]); s+=v[i]; }
  red[t]=s; __syncthreads();
  for(int o=128;o>0;o>>=1){ if(t<o) red[t]+=red[t+o]; __syncthreads(); }
  float m = red[0]/1024.f; __syncthreads();
  float q=0; for(int i=0;i<4;i++){ float d=v[i]-m; q+=d*d; }
  red[t]=q; __syncthreads();
  for(int o=128;o>0;o>>=1){ if(t<o) red[t]+=red[t+o]; __syncthreads(); }
  float var = red[0]/1024.f;
  float r = 1.0f/sqrtf(var+1e-5f);
  for(int i=0;i<4;i++) xn[row*1024+t*4+i]=f2b((v[i]-m)*r);
}
__global__ void k_gemm(const u16* A, const u16* B, u16* C, int M, int N, int K){
  __shared__ float As[32][33], Bs[32][33];
  int tx=threadIdx.x&31, ty=threadIdx.x>>5;
  int bm=blockIdx.y*32, bn=blockIdx.x*32;
  float acc[4]={0,0,0,0};
  for(int k0=0;k0<K;k0+=32){
    for(int i=0;i<4;i++){ int r=ty*4+i; As[r][tx]=b2f(A[(size_t)(bm+r)*K+k0+tx]); Bs[r][tx]=b2f(B[(size_t)(k0+r)*N+bn+tx]); }
    __syncthreads();
    for(int k=0;k<32;k++){ float b=Bs[k][tx]; for(int i=0;i<4;i++) acc[i]+=As[ty*4+i][k]*b; }
    __syncthreads();
  }
  for(int i=0;i<4;i++) C[(size_t)(bm+ty*4+i)*N+bn+tx]=f2b(acc[i]);
}
__global__ void k_attn(const u16* q, const u16* kc, const u16* vc, u16* o){
  int row=blockIdx.x, h=blockIdx.y, t=threadIdx.x;
  int g=h/8;
  __shared__ float qs[128]; __shared__ float p[512]; __shared__ float red[256];
  if(t<128) qs[t]=b2f(q[(size_t)row*4096+h*128+t]);
  __syncthreads();
  float sc[2];
  for(int i=0;i<2;i++){ int key=t+256*i; const u16* kk=kc+((size_t)g*2048+1536+key)*128; float s=0; for(int d=0;d<128;d++) s+=qs[d]*b2f(kk[d]); sc[i]=rb(rb(s)*0.08838834764831845f); }
  float m=fmaxf(sc[0],sc[1]); red[t]=m; __syncthreads();
  for(int o2=128;o2>0;o2>>=1){ if(t<o2) red[t]=fmaxf(red[t],red[t+o2]); __syncthreads(); }
  m=red[0]; __syncthreads();
  float e0=__expf(sc[0]-m), e1=__expf(sc[1]-m); red[t]=e0+e1; __syncthreads();
  for(int o2=128;o2>0;o2>>=1){ if(t<o2) red[t]+=red[t+o2]; __syncthreads(); }
  float l=red[0];
  p[t]=rb(e0/l); p[t+256]=rb(e1/l); __syncthreads();
  if(t<128){ float a=0; for(int key=0;key<512;key++) a+=p[key]*b2f(vc[((size_t)g*2048+1536+key)*128+t]); o[(size_t)row*4096+h*128+t]=f2b(a); }
}
__global__ void k_h(const u16* g, const u16* u, u16* h, int n){ int i=blockIdx.x*256+threadIdx.x; if(i<n){ float gv=b2f(g[i]); float s=rb(1.f/(1.f+__expf(-gv))); h[i]=f2b(s*b2f(u[i])); } }
__global__ void k_fin(const u16* x, const u16* s, const u16* y, u16* out, int n){ int i=blockIdx.x*256+threadIdx.x; if(i<n){ float yv=b2f(y[i]); float g=rb(yv/(1.f+__expf(-yv))); out[i]=f2b(b2f(x[i])+rb(b2f(s[i])*g)); } }

extern "C" void kernel_launch(void* const* d_in, const int* in_sizes, int n_in,
                              void* d_out, int out_size, void* d_ws, size_t ws_size,
                              hipStream_t stream) {
  const u16* x=(const u16*)d_in[0]; const u16* kc=(const u16*)d_in[1]; const u16* vc=(const u16*)d_in[2];
  const u16* wq=(const u16*)d_in[3]; const u16* wo=(const u16*)d_in[4];
  const u16* wg=(const u16*)d_in[6]; const u16* wu=(const u16*)d_in[7]; const u16* wd=(const u16*)d_in[8];
  u16* ws=(u16*)d_ws;
  u16* xn=ws; u16* q=xn+2048*1024; u16* o=q+2048*4096; u16* g=o+2048*4096; u16* u=g+2048*3584; u16* h=u+2048*3584; u16* s=h+2048*3584; u16* y=s+2048*1024;
  k_ln<<<2048,256,0,stream>>>(x,xn);
  k_gemm<<<dim3(4096/32,2048/32),256,0,stream>>>(xn,wq,q,2048,4096,1024);
  k_attn<<<dim3(2048,32),256,0,stream>>>(q,kc,vc,o);
  k_gemm<<<dim3(1024/32,2048/32),256,0,stream>>>(o,wo,s,2048,1024,4096);
  k_gemm<<<dim3(3584/32,2048/32),256,0,stream>>>(xn,wg,g,2048,3584,1024);
  k_gemm<<<dim3(3584/32,2048/32),256,0,stream>>>(xn,wu,u,2048,3584,1024);
  k_h<<<2048*3584/256,256,0,stream>>>(g,u,h,2048*3584);
  k_gemm<<<dim3(1024/32,2048/32),256,0,stream>>>(h,wd,y,2048,1024,3584);
  k_fin<<<2048*1024/256,256,0,stream>>>(x,s,y,(u16*)d_out,2048*1024);
}
```

```cpp
#include <hip/hip_runtime.h>
struct KArgs { const void* x; const void* kc; const void* vc; const void* wq; const void* wo; const void* wg; const void* wu; const void* wd; void* out; void* ws; };
extern "C" __global__ __launch_bounds__(256, 1) void fused_block(KArgs a) {
  __shared__ __attribute__((aligned(16))) char lds[163840];
  asm volatile("" :: "v"((unsigned)(size_t)lds));
  asm volatile(R"ASM(
s_mov_b64 s[96:97], %0
s_mov_b32 s98, %1
s_getpc_b64 s[36:37]
KTEXT0:
s_load_dwordx16 s[4:19], s[96:97], 0
s_load_dwordx4 s[20:23], s[96:97], 64
v_lshrrev_b32 v26, 6, v0
v_and_b32 v1, 63, v0
v_readfirstlane_b32 s43, v26
v_lshlrev_b32 v2, 4, v1
s_and_b32 s37, s37, 0xffff
s_mov_b32 s49, s36
s_mov_b32 s50, s37
s_mov_b32 s38, KTEXT1-KTEXT0
s_mov_b32 s39, 0x20000
s_lshr_b32 s52, s98, 3
s_lshl_b32 s51, s52, 2
s_add_u32 s51, s51, s43
s_lshl_b32 s51, s51, 10
buffer_load_dwordx4 v[100:103], v2, s[36:39], s51 offen
s_and_b32 s51, s98, 7
s_lshr_b32 s53, s51, 2
s_lshl_b32 s53, s53, 2
s_lshr_b32 s54, s52, 3
s_add_u32 s41, s53, s54
s_and_b32 s59, s51, 3
s_and_b32 s60, s52, 7
s_lshl_b32 s42, s59, 3
s_add_u32 s42, s42, s60
s_lshl_b32 s40, s41, 5
s_add_u32 s40, s40, s42
s_lshl_b32 s51, s40, 3
s_lshl_b32 s52, s43, 1
s_add_u32 s51, s51, s52
s_lshl_b32 s51, s51, 11
v_lshrrev_b32 v26, 5, v1
v_and_b32 v27, 31, v1
v_lshlrev_b32 v26, 11, v26
v_lshl_add_u32 v26, v27, 4, v26
s_waitcnt lgkmcnt(0)
s_mov_b32 s24, s4
s_mov_b32 s25, s5
s_and_b32 s25, s25, 0xffff
s_mov_b32 s26, 4194304
s_mov_b32 s27, 0x20000
buffer_load_dwordx4 v[32:35], v26, s[24:27], s51 offen offset:0
buffer_load_dwordx4 v[36:39], v26, s[24:27], s51 offen offset:512
buffer_load_dwordx4 v[40:43], v26, s[24:27], s51 offen offset:1024
buffer_load_dwordx4 v[44:47], v26, s[24:27], s51 offen offset:1536
v_mov_b32 v106, 0
global_load_dwordx2 v[108:109], v106, s[22:23] sc1
s_lshr_b32 s51, s40, 6
s_and_b32 s52, s40, 63
s_lshl_b32 s52, s52, 2
s_add_u32 s52, s52, s43
v_and_b32 v27, 15, v1
v_lshrrev_b32 v28, 4, v1
s_nop 7
s_nop 7
s_lshl_b32 s56, s51, 11
s_add_u32 s56, s56, 1536
s_mov_b32 s34, 2097152
s_mov_b32 s35, 0x20000
s_cmp_lt_u32 s52, 128
s_cbranch_scc0 KVV_1
s_lshr_b32 s53, s52, 4
s_lshr_b32 s54, s52, 2
s_and_b32 s54, s54, 3
s_and_b32 s55, s52, 3
s_lshl_b32 s46, s53, 6
s_add_u32 s56, s56, s46
s_lshl_b32 s46, s54, 4
s_add_u32 s56, s56, s46
s_lshl_b32 s56, s56, 8
s_lshl_b32 s46, s55, 6
s_add_u32 s56, s56, s46
v_lshlrev_b32 v29, 8, v27
v_lshl_add_u32 v29, v28, 3, v29
s_mov_b32 s32, s6
s_and_b32 s33, s7, 0xffff
s_nop 0
buffer_load_dwordx2 v[80:81], v29, s[32:35], s56 offen
buffer_load_dwordx2 v[82:83], v29, s[32:35], s56 offen offset:32
s_lshl_b32 s56, s51, 3
s_add_u32 s56, s56, s53
s_lshl_b32 s56, s56, 5
s_lshl_b32 s46, s54, 2
s_add_u32 s56, s56, s46
s_add_u32 s56, s56, s55
s_lshl_b32 s58, s56, 10
s_branch KVE_2
KVV_1:
s_sub_u32 s52, s52, 128
s_lshr_b32 s53, s52, 4
s_lshr_b32 s54, s52, 1
s_and_b32 s54, s54, 7
s_and_b32 s55, s52, 1
s_lshl_b32 s46, s53, 6
s_add_u32 s56, s56, s46
s_lshl_b32 s46, s55, 5
s_add_u32 s56, s56, s46
s_lshl_b32 s56, s56, 8
s_lshl_b32 s46, s54, 5
s_add_u32 s56, s56, s46
v_lshlrev_b32 v29, 10, v28
v_lshl_add_u32 v29, v27, 1, v29
v_add_u32 v30, 4096, v29
s_mov_b32 s32, s8
s_and_b32 s33, s9, 0xffff
s_nop 0
buffer_load_ushort v90, v29, s[32:35], s56 offen offset:0
buffer_load_ushort v91, v29, s[32:35], s56 offen offset:256
buffer_load_ushort v92, v29, s[32:35], s56 offen offset:512
buffer_load_ushort v93, v29, s[32:35], s56 offen offset:768
buffer_load_ushort v94, v30, s[32:35], s56 offen offset:0
buffer_load_ushort v95, v30, s[32:35], s56 offen offset:256
buffer_load_ushort v96, v30, s[32:35], s56 offen offset:512
buffer_load_ushort v97, v30, s[32:35], s56 offen offset:768
s_lshl_b32 s56, s51, 3
s_add_u32 s56, s56, s53
s_lshl_b32 s56, s56, 5
s_add_u32 s56, s56, 16
s_lshl_b32 s46, s54, 1
s_add_u32 s56, s56, s46
s_add_u32 s56, s56, s55
s_lshl_b32 s58, s56, 10
KVE_2:
s_nop 0
v_lshrrev_b32 v98, 5, v1
s_lshl_b32 s51, s43, 1
v_add_u32 v98, s51, v98
v_mul_u32_u24 v98, 2064, v98
v_and_b32 v27, 31, v1
v_lshl_add_u32 v98, v27, 4, v98
v_add_u32 v98, 131072, v98
v_and_b32 v26, 7, v0
v_bfe_u32 v27, v0, 3, 2
v_lshrrev_b32 v28, 5, v0
v_mul_u32_u24 v99, 2064, v26
v_lshl_add_u32 v99, v27, 3, v99
v_lshl_add_u32 v99, v28, 6, v99
v_add_u32 v99, 131072, v99
v_lshlrev_b32 v104, 4, v26
v_lshl_add_u32 v104, v27, 8, v104
v_lshl_add_u32 v104, v28, 12, v104
s_lshr_b32 s51, s40, 3
s_lshl_b32 s51, s51, 17
s_lshr_b32 s52, s40, 1
s_and_b32 s52, s52, 3
s_lshl_b32 s52, s52, 10
s_add_u32 s51, s51, s52
s_and_b32 s52, s40, 1
s_lshl_b32 s52, s52, 7
s_add_u32 s51, s51, s52
s_add_u32 s36, s22, 1048576
s_addc_u32 s37, s23, 0
s_and_b32 s37, s37, 0xffff
s_mov_b32 s38, 4194304
s_mov_b32 s39, 0x20000
v_lshrrev_b32 v29, 2, v1
v_mul_u32_u24 v3, 224, v29
v_and_b32 v29, 3, v1
v_lshl_add_u32 v3, v29, 3, v3
s_and_b32 s53, s42, 1
s_lshl_b32 s53, s53, 1
v_add_u32 v29, 0, v1
v_mul_u32_u24 v30, 4682, v29
v_lshrrev_b32 v30, 16, v30
v_mul_u32_u24 v31, 14, v30
v_sub_u32 v29, v29, v31
v_add_u32 v29, s53, v29
v_subrev_u32 v31, 14, v29
v_cmp_le_u32 vcc, 14, v29
s_nop 1
v_cndmask_b32 v29, v29, v31, vcc
v_mul_u32_u24 v6, 7168, v30
v_lshl_add_u32 v6, v29, 4, v6
v_add_u32 v29, 64, v1
v_mul_u32_u24 v30, 4682, v29
v_lshrrev_b32 v30, 16, v30
v_mul_u32_u24 v31, 14, v30
v_sub_u32 v29, v29, v31
v_add_u32 v29, s53, v29
v_subrev_u32 v31, 14, v29
v_cmp_le_u32 vcc, 14, v29
s_nop 1
v_cndmask_b32 v29, v29, v31, vcc
v_mul_u32_u24 v7, 7168, v30
v_lshl_add_u32 v7, v29, 4, v7
v_add_u32 v29, 128, v1
v_mul_u32_u24 v30, 4682, v29
v_lshrrev_b32 v30, 16, v30
v_mul_u32_u24 v31, 14, v30
v_sub_u32 v29, v29, v31
v_add_u32 v29, s53, v29
v_subrev_u32 v31, 14, v29
v_cmp_le_u32 vcc, 14, v29
s_nop 1
v_cndmask_b32 v29, v29, v31, vcc
v_mul_u32_u24 v8, 7168, v30
v_lshl_add_u32 v8, v29, 4, v8
v_add_u32 v29, 192, v1
v_mul_u32_u24 v30, 4682, v29
v_lshrrev_b32 v30, 16, v30
v_mul_u32_u24 v31, 14, v30
v_sub_u32 v29, v29, v31
v_add_u32 v29, s53, v29
v_subrev_u32 v31, 14, v29
v_cmp_le_u32 vcc, 14, v29
s_nop 1
v_cndmask_b32 v29, v29, v31, vcc
v_mul_u32_u24 v9, 7168, v30
v_lshl_add_u32 v9, v29, 4, v9
v_add_u32 v29, 256, v1
v_mul_u32_u24 v30, 4682, v29
v_lshrrev_b32 v30, 16, v30
v_mul_u32_u24 v31, 14, v30
v_sub_u32 v29, v29, v31
v_add_u32 v29, s53, v29
v_subrev_u32 v31, 14, v29
v_cmp_le_u32 vcc, 14, v29
s_nop 1
v_cndmask_b32 v29, v29, v31, vcc
v_mul_u32_u24 v10, 7168, v30
v_lshl_add_u32 v10, v29, 4, v10
v_add_u32 v29, 320, v1
v_mul_u32_u24 v30, 4682, v29
v_lshrrev_b32 v30, 16, v30
v_mul_u32_u24 v31, 14, v30
v_sub_u32 v29, v29, v31
v_add_u32 v29, s53, v29
v_subrev_u32 v31, 14, v29
v_cmp_le_u32 vcc, 14, v29
s_nop 1
v_cndmask_b32 v29, v29, v31, vcc
v_mul_u32_u24 v11, 7168, v30
v_lshl_add_u32 v11, v29, 4, v11
v_add_u32 v29, 384, v1
v_mul_u32_u24 v30, 4682, v29
v_lshrrev_b32 v30, 16, v30
v_mul_u32_u24 v31, 14, v30
v_sub_u32 v29, v29, v31
v_add_u32 v29, s53, v29
v_subrev_u32 v31, 14, v29
v_cmp_le_u32 vcc, 14, v29
s_nop 1
v_cndmask_b32 v29, v29, v31, vcc
v_mul_u32_u24 v12, 7168, v30
v_lshl_add_u32 v12, v29, 4, v12
s_cmp_lt_u32 s43, 2
s_cselect_b32 s28, s14, s16
s_cselect_b32 s29, s15, s17
s_and_b32 s29, s29, 0xffff
s_mov_b32 s30, 7340032
s_mov_b32 s31, 0x20000
s_and_b32 s53, s43, 1
s_mul_i32 s48, s53, 229376
s_mul_i32 s54, s42, 224
s_add_u32 s48, s48, s54
s_mul_i32 s57, s43, 7168
s_and_b32 s0, s41, 3
s_lshl_b32 s0, s0, 1
s_add_u32 s0, s0, s60
s_and_b32 s0, s0, 7
s_and_b32 s101, s41, 3
s_add_u32 s101, s101, s60
s_and_b32 s101, s101, 3
s_mov_b32 s1, 0
s_lshr_b32 s99, s98, 3
s_and_b32 s99, s99, 7
s_cmp_eq_u32 s43, 0
s_cselect_b32 s99, s99, 99
s_add_u32 s54, s101, 0
s_and_b32 s54, s54, 3
s_mul_i32 s54, s54, 458752
s_add_u32 s54, s54, s48
s_add_u32 m0, s57, 0
s_nop 0
buffer_load_dwordx4 v6, s[28:31], s54 offen lds
s_add_u32 m0, s57, 1024
s_nop 0
buffer_load_dwordx4 v7, s[28:31], s54 offen lds
s_add_u32 m0, s57, 2048
s_nop 0
buffer_load_dwordx4 v8, s[28:31], s54 offen lds
s_add_u32 m0, s57, 3072
s_nop 0
buffer_load_dwordx4 v9, s[28:31], s54 offen lds
s_add_u32 m0, s57, 4096
s_nop 0
buffer_load_dwordx4 v10, s[28:31], s54 offen lds
s_add_u32 m0, s57, 5120
s_nop 0
buffer_load_dwordx4 v11, s[28:31], s54 offen lds
s_add_u32 m0, s57, 6144
s_nop 0
buffer_load_dwordx4 v12, s[28:31], s54 offen lds
s_waitcnt vmcnt(11)
v_lshlrev_b32 v48, 16, v32
v_and_b32 v49, 0xffff0000, v32
v_lshlrev_b32 v50, 16, v33
v_and_b32 v51, 0xffff0000, v33
v_lshlrev_b32 v52, 16, v34
v_and_b32 v53, 0xffff0000, v34
v_lshlrev_b32 v54, 16, v35
v_and_b32 v55, 0xffff0000, v35
s_waitcnt vmcnt(10)
v_lshlrev_b32 v56, 16, v36
v_and_b32 v57, 0xffff0000, v36
v_lshlrev_b32 v58, 16, v37
v_and_b32 v59, 0xffff0000, v37
v_lshlrev_b32 v60, 16, v38
v_and_b32 v61, 0xffff0000, v38
v_lshlrev_b32 v62, 16, v39
v_and_b32 v63, 0xffff0000, v39
s_waitcnt vmcnt(9)
v_lshlrev_b32 v64, 16, v40
v_and_b32 v65, 0xffff0000, v40
v_lshlrev_b32 v66, 16, v41
v_and_b32 v67, 0xffff0000, v41
v_lshlrev_b32 v68, 16, v42
v_and_b32 v69, 0xffff0000, v42
v_lshlrev_b32 v70, 16, v43
v_and_b32 v71, 0xffff0000, v43
s_waitcnt vmcnt(8)
v_lshlrev_b32 v72, 16, v44
v_and_b32 v73, 0xffff0000, v44
v_lshlrev_b32 v74, 16, v45
v_and_b32 v75, 0xffff0000, v45
v_lshlrev_b32 v76, 16, v46
v_and_b32 v77, 0xffff0000, v46
v_lshlrev_b32 v78, 16, v47
v_and_b32 v79, 0xffff0000, v47
v_pk_add_f32 v[84:85], v[48:49], v[50:51]
v_pk_add_f32 v[86:87], v[52:53], v[54:55]
v_pk_add_f32 v[84:85], v[84:85], v[56:57]
v_pk_add_f32 v[84:85], v[84:85], v[58:59]
v_pk_add_f32 v[86:87], v[86:87], v[60:61]
v_pk_add_f32 v[86:87], v[86:87], v[62:63]
v_pk_add_f32 v[84:85], v[84:85], v[64:65]
v_pk_add_f32 v[84:85], v[84:85], v[66:67]
v_pk_add_f32 v[86:87], v[86:87], v[68:69]
v_pk_add_f32 v[86:87], v[86:87], v[70:71]
v_pk_add_f32 v[84:85], v[84:85], v[72:73]
v_pk_add_f32 v[84:85], v[84:85], v[74:75]
v_pk_add_f32 v[86:87], v[86:87], v[76:77]
v_pk_add_f32 v[86:87], v[86:87], v[78:79]
v_pk_add_f32 v[84:85], v[84:85], v[86:87]
v_add_f32 v84, v84, v85
s_nop 1
v_add_f32_dpp v84, v84, v84 quad_perm:[1,0,3,2] row_mask:0xf bank_mask:0xf
s_nop 1
v_add_f32_dpp v84, v84, v84 quad_perm:[2,3,0,1] row_mask:0xf bank_mask:0xf
s_nop 1
v_add_f32_dpp v84, v84, v84 row_half_mirror row_mask:0xf bank_mask:0xf
s_nop 1
v_add_f32_dpp v84, v84, v84 row_mirror row_mask:0xf bank_mask:0xf
v_mov_b32 v85, v84
s_nop 1
v_permlane16_swap_b32 v84, v85
v_add_f32 v84, v84, v85
v_mul_f32 v84, 0xba800000, v84
v_mov_b32 v85, v84
v_pk_add_f32 v[48:49], v[48:49], v[84:85]
v_pk_add_f32 v[50:51], v[50:51], v[84:85]
v_pk_add_f32 v[52:53], v[52:53], v[84:85]
v_pk_add_f32 v[54:55], v[54:55], v[84:85]
v_pk_add_f32 v[56:57], v[56:57], v[84:85]
v_pk_add_f32 v[58:59], v[58:59], v[84:85]
v_pk_add_f32 v[60:61], v[60:61], v[84:85]
v_pk_add_f32 v[62:63], v[62:63], v[84:85]
v_pk_add_f32 v[64:65], v[64:65], v[84:85]
v_pk_add_f32 v[66:67], v[66:67], v[84:85]
v_pk_add_f32 v[68:69], v[68:69], v[84:85]
v_pk_add_f32 v[70:71], v[70:71], v[84:85]
v_pk_add_f32 v[72:73], v[72:73], v[84:85]
v_pk_add_f32 v[74:75], v[74:75], v[84:85]
v_pk_add_f32 v[76:77], v[76:77], v[84:85]
v_pk_add_f32 v[78:79], v[78:79], v[84:85]
v_pk_mul_f32 v[86:87], v[48:49], v[48:49]
v_pk_mul_f32 v[88:89], v[50:51], v[50:51]
v_pk_fma_f32 v[86:87], v[52:53], v[52:53], v[86:87]
v_pk_fma_f32 v[88:89], v[54:55], v[54:55], v[88:89]
v_pk_fma_f32 v[86:87], v[56:57], v[56:57], v[86:87]
v_pk_fma_f32 v[88:89], v[58:59], v[58:59], v[88:89]
v_pk_fma_f32 v[86:87], v[60:61], v[60:61], v[86:87]
v_pk_fma_f32 v[88:89], v[62:63], v[62:63], v[88:89]
v_pk_fma_f32 v[86:87], v[64:65], v[64:65], v[86:87]
v_pk_fma_f32 v[88:89], v[66:67], v[66:67], v[88:89]
v_pk_fma_f32 v[86:87], v[68:69], v[68:69], v[86:87]
v_pk_fma_f32 v[88:89], v[70:71], v[70:71], v[88:89]
v_pk_fma_f32 v[86:87], v[72:73], v[72:73], v[86:87]
v_pk_fma_f32 v[88:89], v[74:75], v[74:75], v[88:89]
v_pk_fma_f32 v[86:87], v[76:77], v[76:77], v[86:87]
v_pk_fma_f32 v[88:89], v[78:79], v[78:79], v[88:89]
v_pk_add_f32 v[86:87], v[86:87], v[88:89]
v_add_f32 v86, v86, v87
s_nop 1
v_add_f32_dpp v86, v86, v86 quad_perm:[1,0,3,2] row_mask:0xf bank_mask:0xf
s_nop 1
v_add_f32_dpp v86, v86, v86 quad_perm:[2,3,0,1] row_mask:0xf bank_mask:0xf
s_nop 1
v_add_f32_dpp v86, v86, v86 row_half_mirror row_mask:0xf bank_mask:0xf
s_nop 1
v_add_f32_dpp v86, v86, v86 row_mirror row_mask:0xf bank_mask:0xf
v_mov_b32 v87, v86
s_nop 1
v_permlane16_swap_b32 v86, v87
v_add_f32 v86, v86, v87
v_mov_b32 v87, 0x3727c5ac
v_fmac_f32 v87, 0x3a800000, v86
v_rsq_f32 v86, v87
s_nop 0
v_mov_b32 v87, v86
v_pk_mul_f32 v[48:49], v[48:49], v[86:87]
v_pk_mul_f32 v[50:51], v[50:51], v[86:87]
v_pk_mul_f32 v[52:53], v[52:53], v[86:87]
v_pk_mul_f32 v[54:55], v[54:55], v[86:87]
v_pk_mul_f32 v[56:57], v[56:57], v[86:87]
v_pk_mul_f32 v[58:59], v[58:59], v[86:87]
v_pk_mul_f32 v[60:61], v[60:61], v[86:87]
v_pk_mul_f32 v[62:63], v[62:63], v[86:87]
v_pk_mul_f32 v[64:65], v[64:65], v[86:87]
v_pk_mul_f32 v[66:67], v[66:67], v[86:87]
v_pk_mul_f32 v[68:69], v[68:69], v[86:87]
v_pk_mul_f32 v[70:71], v[70:71], v[86:87]
v_pk_mul_f32 v[72:73], v[72:73], v[86:87]
v_pk_mul_f32 v[74:75], v[74:75], v[86:87]
v_pk_mul_f32 v[76:77], v[76:77], v[86:87]
v_pk_mul_f32 v[78:79], v[78:79], v[86:87]
v_cvt_pk_bf16_f32 v32, v48, v49
v_cvt_pk_bf16_f32 v33, v50, v51
v_cvt_pk_bf16_f32 v34, v52, v53
v_cvt_pk_bf16_f32 v35, v54, v55
v_cvt_pk_bf16_f32 v36, v56, v57
v_cvt_pk_bf16_f32 v37, v58, v59
v_cvt_pk_bf16_f32 v38, v60, v61
v_cvt_pk_bf16_f32 v39, v62, v63
v_cvt_pk_bf16_f32 v40, v64, v65
v_cvt_pk_bf16_f32 v41, v66, v67
v_cvt_pk_bf16_f32 v42, v68, v69
v_cvt_pk_bf16_f32 v43, v70, v71
v_cvt_pk_bf16_f32 v44, v72, v73
v_cvt_pk_bf16_f32 v45, v74, v75
v_cvt_pk_bf16_f32 v46, v76, v77
v_cvt_pk_bf16_f32 v47, v78, v79
ds_write_b128 v98, v[32:35] offset:0
ds_write_b128 v98, v[36:39] offset:512
ds_write_b128 v98, v[40:43] offset:1024
ds_write_b128 v98, v[44:47] offset:1536
s_waitcnt lgkmcnt(0)
s_barrier
ds_read_b64 v[32:33], v99 offset:0
ds_read_b64 v[34:35], v99 offset:32
ds_read_b64 v[36:37], v99 offset:512
ds_read_b64 v[38:39], v99 offset:544
ds_read_b64 v[40:41], v99 offset:1024
ds_read_b64 v[42:43], v99 offset:1056
ds_read_b64 v[44:45], v99 offset:1536
ds_read_b64 v[46:47], v99 offset:1568
s_waitcnt lgkmcnt(6)
buffer_store_dwordx4 v[32:35], v104, s[36:39], s51 offen sc1
s_add_u32 s51, s51, 32768
s_waitcnt lgkmcnt(4)
buffer_store_dwordx4 v[36:39], v104, s[36:39], s51 offen sc1
s_add_u32 s51, s51, 32768
s_waitcnt lgkmcnt(2)
buffer_store_dwordx4 v[40:43], v104, s[36:39], s51 offen sc1
s_add_u32 s51, s51, 32768
s_waitcnt lgkmcnt(0)
buffer_store_dwordx4 v[44:47], v104, s[36:39], s51 offen sc1
s_add_u32 s32, s22, 5242880
s_addc_u32 s33, s23, 0
s_and_b32 s33, s33, 0xffff
s_mov_b32 s34, 1048576
s_mov_b32 s35, 0x20000
s_nop 7
s_nop 7
s_waitcnt vmcnt(11)
s_bitcmp1_b32 s40, 5
s_cbranch_scc0 KVS_4
v_lshl_or_b32 v80, v91, 16, v90
v_lshl_or_b32 v81, v93, 16, v92
v_lshl_or_b32 v82, v95, 16, v94
v_lshl_or_b32 v83, v97, 16, v96
s_branch KVP_3
KVS_4:
KVP_3:
s_nop 1
buffer_store_dwordx4 v[80:83], v2, s[32:35], s58 offen sc1
s_waitcnt vmcnt(12)
v_readfirstlane_b32 s44, v108
v_readfirstlane_b32 s45, v109
s_nop 1
s_add_u32 s44, s44, 1
s_addc_u32 s45, s45, 0
v_lshrrev_b32 v29, 5, v1
v_lshlrev_b32 v29, 11, v29
v_bfe_u32 v30, v1, 2, 3
v_lshl_add_u32 v29, v30, 7, v29
v_and_b32 v31, 3, v1
v_lshl_add_u32 v29, v31, 3, v29
v_lshrrev_b32 v30, 1, v30
v_xor_b32 v31, 0, v30
v_lshl_add_u32 v14, v31, 5, v29
v_xor_b32 v31, 1, v30
v_lshl_add_u32 v15, v31, 5, v29
v_xor_b32 v31, 2, v30
v_lshl_add_u32 v16, v31, 5, v29
v_xor_b32 v31, 3, v30
v_lshl_add_u32 v17, v31, 5, v29
s_mov_b32 s88, s10
s_mov_b32 s89, s11
s_and_b32 s89, s89, 0xffff
s_mov_b32 s90, 8388608
s_mov_b32 s91, 0x20000
v_bfe_u32 v29, v1, 1, 2
v_bfe_u32 v30, v1, 4, 2
v_xor_b32 v29, v29, v30
v_and_b32 v30, 1, v1
v_lshl_add_u32 v29, v29, 1, v30
v_lshrrev_b32 v30, 3, v1
v_mul_u32_u24 v13, 8192, v30
v_lshl_add_u32 v13, v29, 4, v13
s_lshl_b32 s85, s43, 17
s_lshl_b32 s51, s42, 8
s_add_u32 s85, s85, s51
s_lshl_b32 s70, s43, 12
s_add_u32 s72, s22, 5242880
s_addc_u32 s73, s23, 0
s_and_b32 s73, s73, 0xffff
s_mov_b32 s74, 1048576
s_mov_b32 s75, 0x20000
s_lshr_b32 s71, s42, 3
s_lshl_b32 s71, s71, 18
s_lshl_b32 s51, s43, 12
s_add_u32 s71, s71, s51
s_lshl_b32 s51, s41, 2
s_add_u32 s51, s51, s43
s_mul_i32 s51, s51, 458752
s_mul_i32 s52, s42, 7
s_and_b32 s53, s42, 1
s_add_u32 s52, s52, s53
s_lshr_b32 s52, s52, 1
s_lshl_b32 s52, s52, 12
s_add_u32 s86, s51, s52
s_add_u32 s54, s86, 12288
s_sub_u32 s55, s86, 4088
s_cmp_eq_u32 s53, 0
s_cselect_b32 s87, s54, s55
s_waitcnt vmcnt(0)
s_barrier
s_nop 7
s_nop 7
s_cmp_lg_u32 s43, 0
s_cbranch_scc1 PUB_5
s_lshl_b32 s56, s40, 3
s_add_u32 s56, s56, 4096
v_mov_b32 v29, s56
v_mov_b32 v30, s44
v_mov_b32 v31, s45
s_mov_b64 exec, 1
s_nop 1
global_store_dwordx2 v29, v[30:31], s[22:23] sc1
s_mov_b64 exec, -1
PUB_5:
s_nop 1
s_add_u32 s54, s101, 1
s_and_b32 s54, s54, 3
s_mul_i32 s54, s54, 458752
s_add_u32 s54, s54, s48
s_add_u32 m0, s57, 28672
s_nop 0
buffer_load_dwordx4 v6, s[28:31], s54 offen lds
s_add_u32 m0, s57, 29696
s_nop 0
buffer_load_dwordx4 v7, s[28:31], s54 offen lds
s_add_u32 m0, s57, 30720
s_nop 0
buffer_load_dwordx4 v8, s[28:31], s54 offen lds
s_add_u32 m0, s57, 31744
s_nop 0
buffer_load_dwordx4 v9, s[28:31], s54 offen lds
s_add_u32 m0, s57, 32768
s_nop 0
buffer_load_dwordx4 v10, s[28:31], s54 offen lds
s_add_u32 m0, s57, 33792
s_nop 0
buffer_load_dwordx4 v11, s[28:31], s54 offen lds
s_add_u32 m0, s57, 34816
s_nop 0
buffer_load_dwordx4 v12, s[28:31], s54 offen lds
s_add_u32 s54, s101, 2
s_and_b32 s54, s54, 3
s_mul_i32 s54, s54, 458752
s_add_u32 s54, s54, s48
s_add_u32 m0, s57, 57344
s_nop 0
buffer_load_dwordx4 v6, s[28:31], s54 offen lds
s_add_u32 m0, s57, 58368
s_nop 0
buffer_load_dwordx4 v7, s[28:31], s54 offen lds
s_add_u32 m0, s57, 59392
s_nop 0
buffer_load_dwordx4 v8, s[28:31], s54 offen lds
s_add_u32 m0, s57, 60416
s_nop 0
buffer_load_dwordx4 v9, s[28:31], s54 offen lds
s_add_u32 m0, s57, 61440
s_nop 0
buffer_load_dwordx4 v10, s[28:31], s54 offen lds
s_add_u32 m0, s57, 62464
s_nop 0
buffer_load_dwordx4 v11, s[28:31], s54 offen lds
s_add_u32 m0, s57, 63488
s_nop 0
buffer_load_dwordx4 v12, s[28:31], s54 offen lds
s_lshl_b32 s51, s41, 8
s_add_u32 s51, s51, 4096
s_lshr_b32 s52, s42, 3
s_lshl_b32 s52, s52, 9
s_add_u32 s52, s52, 4096
s_nop 7
s_nop 7
s_cmp_lg_u32 s43, 0
s_cbranch_scc1 POLL_6_x
v_and_b32 v26, 31, v1
v_lshlrev_b32 v26, 3, v26
v_add_u32 v26, s51, v26
v_and_b32 v27, 63, v1
v_lshlrev_b32 v27, 3, v27
v_add_u32 v27, s52, v27
s_mov_b32 s61, 0
POLL_6:
global_load_dwordx2 v[28:29], v26, s[22:23] sc1
global_load_dwordx2 v[30:31], v27, s[22:23] sc1
s_waitcnt vmcnt(0)
v_cmp_eq_u64 vcc, s[44:45], v[28:29]
v_cmp_eq_u64 s[62:63], s[44:45], v[30:31]
s_nop 4
s_and_b64 vcc, vcc, s[62:63]
s_nop 4
s_cmp_eq_u64 vcc, -1
s_cbranch_scc1 POLL_6_o
s_sleep 1
s_add_u32 s61, s61, 1
s_cmp_lt_u32 s61, 0x20000
s_cbranch_scc1 POLL_6
POLL_6_o:
POLL_6_x:
s_nop 7
s_barrier
s_add_u32 s24, s22, 1048576
s_addc_u32 s25, s23, 0
s_and_b32 s25, s25, 0xffff
s_mov_b32 s26, 4194304
s_mov_b32 s27, 0x20000
s_lshl_b32 s58, s41, 2
s_add_u32 s58, s58, s43
s_lshl_b32 s58, s58, 17
s_add_u32 s47, s101, 0
s_and_b32 s47, s47, 3
s_mul_i32 s47, s47, 8192
s_add_u32 s47, s47, s58
buffer_load_dwordx4 a[0:3], v2, s[24:27], s47 offen offset:0 sc1
buffer_load_dwordx4 a[4:7], v2, s[24:27], s47 offen offset:1024 sc1
buffer_load_dwordx4 a[8:11], v2, s[24:27], s47 offen offset:2048 sc1
buffer_load_dwordx4 a[12:15], v2, s[24:27], s47 offen offset:3072 sc1
s_add_u32 s47, s47, 4096
buffer_load_dwordx4 a[16:19], v2, s[24:27], s47 offen offset:0 sc1
buffer_load_dwordx4 a[20:23], v2, s[24:27], s47 offen offset:1024 sc1
buffer_load_dwordx4 a[24:27], v2, s[24:27], s47 offen offset:2048 sc1
buffer_load_dwordx4 a[28:31], v2, s[24:27], s47 offen offset:3072 sc1
s_add_u32 s47, s101, 1
s_and_b32 s47, s47, 3
s_mul_i32 s47, s47, 8192
s_add_u32 s47, s47, s58
buffer_load_dwordx4 a[32:35], v2, s[24:27], s47 offen offset:0 sc1
buffer_load_dwordx4 a[36:39], v2, s[24:27], s47 offen offset:1024 sc1
buffer_load_dwordx4 a[40:43], v2, s[24:27], s47 offen offset:2048 sc1
buffer_load_dwordx4 a[44:47], v2, s[24:27], s47 offen offset:3072 sc1
s_add_u32 s47, s47, 4096
s_barrier
v_mov_b32 v4, v3
ds_read_b64_tr_b16 a[240:241], v4 offset:0
ds_read_b64_tr_b16 a[242:243], v4 offset:3584
ds_read_b64_tr_b16 a[244:245], v4 offset:32
ds_read_b64_tr_b16 a[246:247], v4 offset:3616
ds_read_b64_tr_b16 a[248:249], v4 offset:64
ds_read_b64_tr_b16 a[250:251], v4 offset:3648
s_waitcnt vmcnt(11) lgkmcnt(4)
v_mfma_f32_16x16x32_bf16 v[32:35], a[240:243], a[0:3], 0
ds_read_b64_tr_b16 a[252:253], v4 offset:96
s_waitcnt vmcnt(10)
v_mfma_f32_16x16x32_bf16 v[36:39], a[240:243], a[4:7], 0
ds_read_b64_tr_b16 a[254:255], v4 offset:3680
s_waitcnt vmcnt(9)
v_mfma_f32_16x16x32_bf16 v[40:43], a[240:243], a[8:11], 0
buffer_load_dwordx4 a[48:51], v2, s[24:27], s47 offen offset:0 sc1
s_waitcnt vmcnt(9)
v_mfma_f32_16x16x32_bf16 v[44:47], a[240:243], a[12:15], 0
s_waitcnt lgkmcnt(4)
v_mfma_f32_16x16x32_bf16 v[60:63], a[244:247], a[12:15], 0
ds_read_b64_tr_b16 a[240:241], v4 offset:128
v_mfma_f32_16x16x32_bf16 v[56:59], a[244:247], a[8:11], 0
ds_read_b64_tr_b16 a[242:243], v4 offset:3712
v_mfma_f32_16x16x32_bf16 v[52:55], a[244:247], a[4:7], 0
v_mfma_f32_16x16x32_bf16 v[48:51], a[244:247], a[0:3], 0
s_waitcnt lgkmcnt(4)
v_mfma_f32_16x16x32_bf16 v[64:67], a[248:251], a[0:3], 0
ds_read_b64_tr_b16 a[244:245], v4 offset:160
v_add_u32 v5, 28672, v3
v_mfma_f32_16x16x32_bf16 v[68:71], a[248:251], a[4:7], 0
ds_read_b64_tr_b16 a[246:247], v4 offset:3744
v_mfma_f32_16x16x32_bf16 v[72:75], a[248:251], a[8:11], 0
buffer_load_dwordx4 a[52:55], v2, s[24:27], s47 offen offset:1024 sc1
v_mfma_f32_16x16x32_bf16 v[76:79], a[248:251], a[12:15], 0
s_waitcnt lgkmcnt(4)
v_mfma_f32_16x16x32_bf16 v[92:95], a[252:255], a[12:15], 0
ds_read_b64_tr_b16 a[248:249], v4 offset:192
v_mfma_f32_16x16x32_bf16 v[88:91], a[252:255], a[8:11], 0
ds_read_b64_tr_b16 a[250:251], v4 offset:3776
v_mfma_f32_16x16x32_bf16 v[84:87], a[252:255], a[4:7], 0
v_mfma_f32_16x16x32_bf16 v[80:83], a[252:255], a[0:3], 0
s_waitcnt lgkmcnt(4)
v_mfma_f32_16x16x32_bf16 v[96:99], a[240:243], a[0:3], 0
ds_read_b64_tr_b16 a[252:253], v4 offset:14336
v_mfma_f32_16x16x32_bf16 v[100:103], a[240:243], a[4:7], 0
ds_read_b64_tr_b16 a[254:255], v4 offset:17920
v_mfma_f32_16x16x32_bf16 v[104:107], a[240:243], a[8:11], 0
buffer_load_dwordx4 a[56:59], v2, s[24:27], s47 offen offset:2048 sc1
v_mfma_f32_16x16x32_bf16 v[108:111], a[240:243], a[12:15], 0
s_waitcnt lgkmcnt(4)
v_mfma_f32_16x16x32_bf16 v[124:127], a[244:247], a[12:15], 0
ds_read_b64_tr_b16 a[240:241], v4 offset:14368
v_mfma_f32_16x16x32_bf16 v[120:123], a[244:247], a[8:11], 0
ds_read_b64_tr_b16 a[242:243], v4 offset:17952
v_mfma_f32_16x16x32_bf16 v[116:119], a[244:247], a[4:7], 0
v_mfma_f32_16x16x32_bf16 v[112:115], a[244:247], a[0:3], 0
s_waitcnt lgkmcnt(4)
v_mfma_f32_16x16x32_bf16 v[128:131], a[248:251], a[0:3], 0
ds_read_b64_tr_b16 a[244:245], v4 offset:14400
v_mfma_f32_16x16x32_bf16 v[132:135], a[248:251], a[4:7], 0
ds_read_b64_tr_b16 a[246:247], v4 offset:17984
v_mfma_f32_16x16x32_bf16 v[136:139], a[248:251], a[8:11], 0
buffer_load_dwordx4 a[60:63], v2, s[24:27], s47 offen offset:3072 sc1
v_mfma_f32_16x16x32_bf16 v[140:143], a[248:251], a[12:15], 0
s_waitcnt lgkmcnt(4)
s_add_u32 s47, s101, 2
s_and_b32 s47, s47, 3
v_mfma_f32_16x16x32_bf16 v[156:159], a[252:255], a[12:15], 0
s_mul_i32 s47, s47, 8192
s_add_u32 s47, s47, s58
v_mfma_f32_16x16x32_bf16 v[152:155], a[252:255], a[8:11], 0
ds_read_b64_tr_b16 a[248:249], v4 offset:14432
v_mfma_f32_16x16x32_bf16 v[148:151], a[252:255], a[4:7], 0
buffer_load_dwordx4 a[64:67], v2, s[24:27], s47 offen offset:0 sc1
ds_read_b64_tr_b16 a[250:251], v4 offset:18016
v_mfma_f32_16x16x32_bf16 v[144:147], a[252:255], a[0:3], 0
s_waitcnt lgkmcnt(4)
v_mfma_f32_16x16x32_bf16 v[160:163], a[240:243], a[0:3], 0
ds_read_b64_tr_b16 a[252:253], v4 offset:14464
v_mfma_f32_16x16x32_bf16 v[164:167], a[240:243], a[4:7], 0
ds_read_b64_tr_b16 a[254:255], v4 offset:18048
v_mfma_f32_16x16x32_bf16 v[168:171], a[240:243], a[8:11], 0
v_mfma_f32_16x16x32_bf16 v[172:175], a[240:243], a[12:15], 0
s_waitcnt lgkmcnt(4)
v_mfma_f32_16x16x32_bf16 v[188:191], a[244:247], a[12:15], 0
ds_read_b64_tr_b16 a[240:241], v4 offset:14496
v_mfma_f32_16x16x32_bf16 v[184:187], a[244:247], a[8:11], 0
ds_read_b64_tr_b16 a[242:243], v4 offset:18080
v_mfma_f32_16x16x32_bf16 v[180:183], a[244:247], a[4:7], 0
buffer_load_dwordx4 a[68:71], v2, s[24:27], s47 offen offset:1024 sc1
v_mfma_f32_16x16x32_bf16 v[176:179], a[244:247], a[0:3], 0
s_waitcnt lgkmcnt(4)
s_add_u32 s54, s101, 3
s_and_b32 s54, s54, 3
v_mfma_f32_16x16x32_bf16 v[192:195], a[248:251], a[0:3], 0
ds_read_b64_tr_b16 a[244:245], v4 offset:14528
s_mul_i32 s54, s54, 458752
v_mfma_f32_16x16x32_bf16 v[196:199], a[248:251], a[4:7], 0
ds_read_b64_tr_b16 a[246:247], v4 offset:18112
s_add_u32 s54, s54, s48
v_mfma_f32_16x16x32_bf16 v[200:203], a[248:251], a[8:11], 0
s_add_u32 m0, s57, 86016
v_mfma_f32_16x16x32_bf16 v[204:207], a[248:251], a[12:15], 0
s_waitcnt lgkmcnt(4)
v_mfma_f32_16x16x32_bf16 v[220:223], a[252:255], a[12:15], 0
ds_read_b64_tr_b16 a[248:249], v4 offset:7168
v_mfma_f32_16x16x32_bf16 v[216:219], a[252:255], a[8:11], 0
ds_read_b64_tr_b16 a[250:251], v4 offset:10752
v_mfma_f32_16x16x32_bf16 v[212:215], a[252:255], a[4:7], 0
buffer_load_dwordx4 a[72:75], v2, s[24:27], s47 offen offset:2048 sc1
v_mfma_f32_16x16x32_bf16 v[208:211], a[252:255], a[0:3], 0
s_waitcnt lgkmcnt(4)
v_mfma_f32_16x16x32_bf16 v[224:227], a[240:243], a[0:3], 0
ds_read_b64_tr_b16 a[252:253], v4 offset:7200
v_mfma_f32_16x16x32_bf16 v[228:231], a[240:243], a[4:7], 0
ds_read_b64_tr_b16 a[254:255], v4 offset:10784
v_mfma_f32_16x16x32_bf16 v[232:235], a[240:243], a[8:11], 0
v_mfma_f32_16x16x32_bf16 v[236:239], a[240:243], a[12:15], 0
s_waitcnt lgkmcnt(4)
v_mfma_f32_16x16x32_bf16 v[252:255], a[244:247], a[12:15], 0
ds_read_b64_tr_b16 a[240:241], v4 offset:7232
v_mfma_f32_16x16x32_bf16 v[248:251], a[244:247], a[8:11], 0
ds_read_b64_tr_b16 a[242:243], v4 offset:10816
v_mfma_f32_16x16x32_bf16 v[244:247], a[244:247], a[4:7], 0
buffer_load_dwordx4 a[76:79], v2, s[24:27], s47 offen offset:3072 sc1
v_mfma_f32_16x16x32_bf16 v[240:243], a[244:247], a[0:3], 0
s_waitcnt lgkmcnt(4)
s_waitcnt vmcnt(12)
s_add_u32 s47, s47, 4096
v_mfma_f32_16x16x32_bf16 v[32:35], a[248:251], a[16:19], v[32:35]
ds_read_b64_tr_b16 a[244:245], v4 offset:7264
v_mfma_f32_16x16x32_bf16 v[36:39], a[248:251], a[20:23], v[36:39]
buffer_load_dwordx4 a[80:83], v2, s[24:27], s47 offen offset:0 sc1
v_mfma_f32_16x16x32_bf16 v[40:43], a[248:251], a[24:27], v[40:43]
ds_read_b64_tr_b16 a[246:247], v4 offset:10848
v_mfma_f32_16x16x32_bf16 v[44:47], a[248:251], a[28:31], v[44:47]
s_waitcnt lgkmcnt(4)
v_mfma_f32_16x16x32_bf16 v[60:63], a[252:255], a[28:31], v[60:63]
ds_read_b64_tr_b16 a[248:249], v4 offset:7296
v_mfma_f32_16x16x32_bf16 v[56:59], a[252:255], a[24:27], v[56:59]
buffer_load_dwordx4 a[84:87], v2, s[24:27], s47 offen offset:1024 sc1
v_mfma_f32_16x16x32_bf16 v[52:55], a[252:255], a[20:23], v[52:55]
buffer_load_dwordx4 a[88:91], v2, s[24:27], s47 offen offset:2048 sc1
ds_read_b64_tr_b16 a[250:251], v4 offset:10880
v_mfma_f32_16x16x32_bf16 v[48:51], a[252:255], a[16:19], v[48:51]
s_waitcnt lgkmcnt(4)
v_mfma_f32_16x16x32_bf16 v[64:67], a[240:243], a[16:19], v[64:67]
ds_read_b64_tr_b16 a[252:253], v4 offset:7328
v_mfma_f32_16x16x32_bf16 v[68:71], a[240:243], a[20:23], v[68:71]
buffer_load_dwordx4 a[92:95], v2, s[24:27], s47 offen offset:3072 sc1
v_mfma_f32_16x16x32_bf16 v[72:75], a[240:243], a[24:27], v[72:75]
ds_read_b64_tr_b16 a[254:255], v4 offset:10912
s_add_u32 s47, s101, 3
v_mfma_f32_16x16x32_bf16 v[76:79], a[240:243], a[28:31], v[76:79]
s_barrier
s_waitcnt lgkmcnt(4)
v_mfma_f32_16x16x32_bf16 v[92:95], a[244:247], a[28:31], v[92:95]
s_and_b32 s47, s47, 3
s_mul_i32 s47, s47, 8192
s_add_u32 s47, s47, s58
v_mfma_f32_16x16x32_bf16 v[88:91], a[244:247], a[24:27], v[88:91]
buffer_load_dwordx4 a[96:99], v2, s[24:27], s47 offen offset:0 sc1
v_mfma_f32_16x16x32_bf16 v[84:87], a[244:247], a[20:23], v[84:87]
buffer_load_dwordx4 a[100:103], v2, s[24:27], s47 offen offset:1024 sc1
ds_read_b64_tr_b16 a[240:241], v4 offset:7360
ds_read_b64_tr_b16 a[242:243], v4 offset:10944
v_mfma_f32_16x16x32_bf16 v[80:83], a[244:247], a[16:19], v[80:83]
s_waitcnt lgkmcnt(4)
v_mfma_f32_16x16x32_bf16 v[96:99], a[248:251], a[16:19], v[96:99]
ds_read_b64_tr_b16 a[244:245], v4 offset:21504
v_mfma_f32_16x16x32_bf16 v[100:103], a[248:251], a[20:23], v[100:103]
buffer_load_dwordx4 a[104:107], v2, s[24:27], s47 offen offset:2048 sc1
v_mfma_f32_16x16x32_bf16 v[104:107], a[248:251], a[24:27], v[104:107]
ds_read_b64_tr_b16 a[246:247], v4 offset:25088
v_mfma_f32_16x16x32_bf16 v[108:111], a[248:251], a[28:31], v[108:111]
s_waitcnt lgkmcnt(4)
v_mfma_f32_16x16x32_bf16 v[124:127], a[252:255], a[28:31], v[124:127]
ds_read_b64_tr_b16 a[248:249], v4 offset:21536
v_mfma_f32_16x16x32_bf16 v[120:123], a[252:255], a[24:27], v[120:123]
buffer_load_dwordx4 a[108:111], v2, s[24:27], s47 offen offset:3072 sc1
v_mfma_f32_16x16x32_bf16 v[116:119], a[252:255], a[20:23], v[116:119]
buffer_load_dwordx4 v6, s[28:31], s54 offen lds
ds_read_b64_tr_b16 a[250:251], v4 offset:25120
v_mfma_f32_16x16x32_bf16 v[112:115], a[252:255], a[16:19], v[112:115]
s_waitcnt lgkmcnt(4)
s_add_u32 m0, s57, 87040
s_add_u32 s47, s47, 4096
v_mfma_f32_16x16x32_bf16 v[128:131], a[240:243], a[16:19], v[128:131]
ds_read_b64_tr_b16 a[252:253], v4 offset:21568
v_mfma_f32_16x16x32_bf16 v[132:135], a[240:243], a[20:23], v[132:135]
ds_read_b64_tr_b16 a[254:255], v4 offset:25152
v_mfma_f32_16x16x32_bf16 v[136:139], a[240:243], a[24:27], v[136:139]
buffer_load_dwordx4 v7, s[28:31], s54 offen lds
v_mfma_f32_16x16x32_bf16 v[140:143], a[240:243], a[28:31], v[140:143]
s_waitcnt lgkmcnt(4)
s_add_u32 m0, s57, 88064
v_mfma_f32_16x16x32_bf16 v[156:159], a[244:247], a[28:31], v[156:159]
ds_read_b64_tr_b16 a[240:241], v4 offset:21600
v_mfma_f32_16x16x32_bf16 v[152:155], a[244:247], a[24:27], v[152:155]
ds_read_b64_tr_b16 a[242:243], v4 offset:25184
v_mfma_f32_16x16x32_bf16 v[148:151], a[244:247], a[20:23], v[148:151]
buffer_load_dwordx4 v8, s[28:31], s54 offen lds
v_mfma_f32_16x16x32_bf16 v[144:147], a[244:247], a[16:19], v[144:147]
s_waitcnt lgkmcnt(4)
s_add_u32 m0, s57, 89088
v_mfma_f32_16x16x32_bf16 v[160:163], a[248:251], a[16:19], v[160:163]
ds_read_b64_tr_b16 a[244:245], v4 offset:21632
v_mfma_f32_16x16x32_bf16 v[164:167], a[248:251], a[20:23], v[164:167]
ds_read_b64_tr_b16 a[246:247], v4 offset:25216
v_mfma_f32_16x16x32_bf16 v[168:171], a[248:251], a[24:27], v[168:171]
v_mfma_f32_16x16x32_bf16 v[172:175], a[248:251], a[28:31], v[172:175]
s_waitcnt lgkmcnt(4)
v_mfma_f32_16x16x32_bf16 v[188:191], a[252:255], a[28:31], v[188:191]
ds_read_b64_tr_b16 a[248:249], v4 offset:21664
v_mfma_f32_16x16x32_bf16 v[184:187], a[252:255], a[24:27], v[184:187]
ds_read_b64_tr_b16 a[250:251], v4 offset:25248
v_mfma_f32_16x16x32_bf16 v[180:183], a[252:255], a[20:23], v[180:183]
buffer_load_dwordx4 v9, s[28:31], s54 offen lds
v_mfma_f32_16x16x32_bf16 v[176:179], a[252:255], a[16:19], v[176:179]
s_waitcnt lgkmcnt(4)
s_add_u32 m0, s57, 90112
v_mfma_f32_16x16x32_bf16 v[192:195], a[240:243], a[16:19], v[192:195]
ds_read_b64_tr_b16 a[252:253], v4 offset:21696
v_mfma_f32_16x16x32_bf16 v[196:199], a[240:243], a[20:23], v[196:199]
ds_read_b64_tr_b16 a[254:255], v4 offset:25280
v_mfma_f32_16x16x32_bf16 v[200:203], a[240:243], a[24:27], v[200:203]
buffer_load_dwordx4 v10, s[28:31], s54 offen lds
v_mfma_f32_16x16x32_bf16 v[204:207], a[240:243], a[28:31], v[204:207]
s_waitcnt lgkmcnt(4)
s_add_u32 m0, s57, 91136
v_mfma_f32_16x16x32_bf16 v[220:223], a[244:247], a[28:31], v[220:223]
ds_read_b64_tr_b16 a[240:241], v5 offset:0
v_mfma_f32_16x16x32_bf16 v[216:219], a[244:247], a[24:27], v[216:219]
ds_read_b64_tr_b16 a[242:243], v5 offset:3584
v_mfma_f32_16x16x32_bf16 v[212:215], a[244:247], a[20:23], v[212:215]
buffer_load_dwordx4 v11, s[28:31], s54 offen lds
v_mfma_f32_16x16x32_bf16 v[208:211], a[244:247], a[16:19], v[208:211]
s_waitcnt lgkmcnt(4)
s_add_u32 m0, s57, 92160
v_mfma_f32_16x16x32_bf16 v[224:227], a[248:251], a[16:19], v[224:227]
ds_read_b64_tr_b16 a[244:245], v5 offset:32
v_mfma_f32_16x16x32_bf16 v[228:231], a[248:251], a[20:23], v[228:231]
ds_read_b64_tr_b16 a[246:247], v5 offset:3616
v_mfma_f32_16x16x32_bf16 v[232:235], a[248:251], a[24:27], v[232:235]
buffer_load_dwordx4 v12, s[28:31], s54 offen lds
v_mfma_f32_16x16x32_bf16 v[236:239], a[248:251], a[28:31], v[236:239]
s_waitcnt lgkmcnt(4)
v_mfma_f32_16x16x32_bf16 v[252:255], a[252:255], a[28:31], v[252:255]
ds_read_b64_tr_b16 a[248:249], v5 offset:64
v_mfma_f32_16x16x32_bf16 v[248:251], a[252:255], a[24:27], v[248:251]
ds_read_b64_tr_b16 a[250:251], v5 offset:3648
v_mfma_f32_16x16x32_bf16 v[244:247], a[252:255], a[20:23], v[244:247]
v_mfma_f32_16x16x32_bf16 v[240:243], a[252:255], a[16:19], v[240:243]
s_waitcnt lgkmcnt(4)
s_waitcnt vmcnt(23)
v_mfma_f32_16x16x32_bf16 v[32:35], a[240:243], a[32:35], v[32:35]
ds_read_b64_tr_b16 a[252:253], v5 offset:96
v_mfma_f32_16x16x32_bf16 v[36:39], a[240:243], a[36:39], v[36:39]
ds_read_b64_tr_b16 a[254:255], v5 offset:3680
v_mfma_f32_16x16x32_bf16 v[40:43], a[240:243], a[40:43], v[40:43]
buffer_load_dwordx4 a[112:115], v2, s[24:27], s47 offen offset:0 sc1
v_mfma_f32_16x16x32_bf16 v[44:47], a[240:243], a[44:47], v[44:47]
s_waitcnt lgkmcnt(4)
v_mfma_f32_16x16x32_bf16 v[60:63], a[244:247], a[44:47], v[60:63]
ds_read_b64_tr_b16 a[240:241], v5 offset:128
v_mfma_f32_16x16x32_bf16 v[56:59], a[244:247], a[40:43], v[56:59]
ds_read_b64_tr_b16 a[242:243], v5 offset:3712
v_mfma_f32_16x16x32_bf16 v[52:55], a[244:247], a[36:39], v[52:55]
v_mfma_f32_16x16x32_bf16 v[48:51], a[244:247], a[32:35], v[48:51]
s_waitcnt lgkmcnt(4)
v_mfma_f32_16x16x32_bf16 v[64:67], a[248:251], a[32:35], v[64:67]
ds_read_b64_tr_b16 a[244:245], v5 offset:160
v_add_u32 v4, 57344, v3
v_mfma_f32_16x16x32_bf16 v[68:71], a[248:251], a[36:39], v[68:71]
ds_read_b64_tr_b16 a[246:247], v5 offset:3744
v_mfma_f32_16x16x32_bf16 v[72:75], a[248:251], a[40:43], v[72:75]
buffer_load_dwordx4 a[116:119], v2, s[24:27], s47 offen offset:1024 sc1
v_mfma_f32_16x16x32_bf16 v[76:79], a[248:251], a[44:47], v[76:79]
s_waitcnt lgkmcnt(4)
v_mfma_f32_16x16x32_bf16 v[92:95], a[252:255], a[44:47], v[92:95]
ds_read_b64_tr_b16 a[248:249], v5 offset:192
v_mfma_f32_16x16x32_bf16 v[88:91], a[252:255], a[40:43], v[88:91]
ds_read_b64_tr_b16 a[250:251], v5 offset:3776
v_mfma_f32_16x16x32_bf16 v[84:87], a[252:255], a[36:39], v[84:87]
v_mfma_f32_16x16x32_bf16 v[80:83], a[252:255], a[32:35], v[80:83]
s_waitcnt lgkmcnt(4)
v_mfma_f32_16x16x32_bf16 v[96:99], a[240:243], a[32:35], v[96:99]
ds_read_b64_tr_b16 a[252:253], v5 offset:14336
v_mfma_f32_16x16x32_bf16 v[100:103], a[240:243], a[36:39], v[100:103]
ds_read_b64_tr_b16 a[254:255], v5 offset:17920
v_mfma_f32_16x16x32_bf16 v[104:107], a[240:243], a[40:43], v[104:107]
buffer_load_dwordx4 a[120:123], v2, s[24:27], s47 offen offset:2048 sc1
v_mfma_f32_16x16x32_bf16 v[108:111], a[240:243], a[44:47], v[108:111]
s_waitcnt lgkmcnt(4)
v_mfma_f32_16x16x32_bf16 v[124:127], a[244:247], a[44:47], v[124:127]
ds_read_b64_tr_b16 a[240:241], v5 offset:14368
v_mfma_f32_16x16x32_bf16 v[120:123], a[244:247], a[40:43], v[120:123]
ds_read_b64_tr_b16 a[242:243], v5 offset:17952
v_mfma_f32_16x16x32_bf16 v[116:119], a[244:247], a[36:39], v[116:119]
v_mfma_f32_16x16x32_bf16 v[112:115], a[244:247], a[32:35], v[112:115]
s_waitcnt lgkmcnt(4)
v_mfma_f32_16x16x32_bf16 v[128:131], a[248:251], a[32:35], v[128:131]
ds_read_b64_tr_b16 a[244:245], v5 offset:14400
v_mfma_f32_16x16x32_bf16 v[132:135], a[248:251], a[36:39], v[132:135]
ds_read_b64_tr_b16 a[246:247], v5 offset:17984
v_mfma_f32_16x16x32_bf16 v[136:139], a[248:251], a[40:43], v[136:139]
buffer_load_dwordx4 a[124:127], v2, s[24:27], s47 offen offset:3072 sc1
v_mfma_f32_16x16x32_bf16 v[140:143], a[248:251], a[44:47], v[140:143]
s_waitcnt lgkmcnt(4)
s_add_u32 s47, s101, 0
s_and_b32 s47, s47, 3
v_mfma_f32_16x16x32_bf16 v[156:159], a[252:255], a[44:47], v[156:159]
s_mul_i32 s47, s47, 8192
s_add_u32 s47, s47, s58
s_add_u32 s47, s47, 32768
v_mfma_f32_16x16x32_bf16 v[152:155], a[252:255], a[40:43], v[152:155]
ds_read_b64_tr_b16 a[248:249], v5 offset:14432
v_mfma_f32_16x16x32_bf16 v[148:151], a[252:255], a[36:39], v[148:151]
buffer_load_dwordx4 a[0:3], v2, s[24:27], s47 offen offset:0 sc1
ds_read_b64_tr_b16 a[250:251], v5 offset:18016
v_mfma_f32_16x16x32_bf16 v[144:147], a[252:255], a[32:35], v[144:147]
s_waitcnt lgkmcnt(4)
v_mfma_f32_16x16x32_bf16 v[160:163], a[240:243], a[32:35], v[160:163]
ds_read_b64_tr_b16 a[252:253], v5 offset:14464
v_mfma_f32_16x16x32_bf16 v[164:167], a[240:243], a[36:39], v[164:167]
ds_read_b64_tr_b16 a[254:255], v5 offset:18048
v_mfma_f32_16x16x32_bf16 v[168:171], a[240:243], a[40:43], v[168:171]
v_mfma_f32_16x16x32_bf16 v[172:175], a[240:243], a[44:47], v[172:175]
s_waitcnt lgkmcnt(4)
v_mfma_f32_16x16x32_bf16 v[188:191], a[244:247], a[44:47], v[188:191]
ds_read_b64_tr_b16 a[240:241], v5 offset:14496
v_mfma_f32_16x16x32_bf16 v[184:187], a[244:247], a[40:43], v[184:187]
ds_read_b64_tr_b16 a[242:243], v5 offset:18080
v_mfma_f32_16x16x32_bf16 v[180:183], a[244:247], a[36:39], v[180:183]
buffer_load_dwordx4 a[4:7], v2, s[24:27], s47 offen offset:1024 sc1
v_mfma_f32_16x16x32_bf16 v[176:179], a[244:247], a[32:35], v[176:179]
s_waitcnt lgkmcnt(4)
s_add_u32 s54, s101, 0
s_and_b32 s54, s54, 3
v_mfma_f32_16x16x32_bf16 v[192:195], a[248:251], a[32:35], v[192:195]
ds_read_b64_tr_b16 a[244:245], v5 offset:14528
s_mul_i32 s54, s54, 458752
v_mfma_f32_16x16x32_bf16 v[196:199], a[248:251], a[36:39], v[196:199]
ds_read_b64_tr_b16 a[246:247], v5 offset:18112
s_add_u32 s54, s54, s48
v_mfma_f32_16x16x32_bf16 v[200:203], a[248:251], a[40:43], v[200:203]
s_add_u32 s54, s54, 1835008
s_add_u32 m0, s57, 0
v_mfma_f32_16x16x32_bf16 v[204:207], a[248:251], a[44:47], v[204:207]
s_waitcnt lgkmcnt(4)
v_mfma_f32_16x16x32_bf16 v[220:223], a[252:255], a[44:47], v[220:223]
ds_read_b64_tr_b16 a[248:249], v5 offset:7168
v_mfma_f32_16x16x32_bf16 v[216:219], a[252:255], a[40:43], v[216:219]
ds_read_b64_tr_b16 a[250:251], v5 offset:10752
v_mfma_f32_16x16x32_bf16 v[212:215], a[252:255], a[36:39], v[212:215]
buffer_load_dwordx4 a[8:11], v2, s[24:27], s47 offen offset:2048 sc1
v_mfma_f32_16x16x32_bf16 v[208:211], a[252:255], a[32:35], v[208:211]
s_waitcnt lgkmcnt(4)
v_mfma_f32_16x16x32_bf16 v[224:227], a[240:243], a[32:35], v[224:227]
ds_read_b64_tr_b16 a[252:253], v5 offset:7200
v_mfma_f32_16x16x32_bf16 v[228:231], a[240:243], a[36:39], v[228:231]
ds_read_b64_tr_b16 a[254:255], v5 offset:10784
v_mfma_f32_16x16x32_bf16 v[232:235], a[240:243], a[40:43], v[232:235]
v_mfma_f32_16x16x32_bf16 v[236:239], a[240:243], a[44:47], v[236:239]
s_waitcnt lgkmcnt(4)
v_mfma_f32_16x16x32_bf16 v[252:255], a[244:247], a[44:47], v[252:255]
ds_read_b64_tr_b16 a[240:241], v5 offset:7232
v_mfma_f32_16x16x32_bf16 v[248:251], a[244:247], a[40:43], v[248:251]
ds_read_b64_tr_b16 a[242:243], v5 offset:10816
v_mfma_f32_16x16x32_bf16 v[244:247], a[244:247], a[36:39], v[244:247]
buffer_load_dwordx4 a[12:15], v2, s[24:27], s47 offen offset:3072 sc1
v_mfma_f32_16x16x32_bf16 v[240:243], a[244:247], a[32:35], v[240:243]
s_waitcnt lgkmcnt(4)
s_waitcnt vmcnt(27)
s_add_u32 s47, s47, 4096
v_mfma_f32_16x16x32_bf16 v[32:35], a[248:251], a[48:51], v[32:35]
ds_read_b64_tr_b16 a[244:245], v5 offset:7264
v_mfma_f32_16x16x32_bf16 v[36:39], a[248:251], a[52:55], v[36:39]
buffer_load_dwordx4 a[16:19], v2, s[24:27], s47 offen offset:0 sc1
v_mfma_f32_16x16x32_bf16 v[40:43], a[248:251], a[56:59], v[40:43]
ds_read_b64_tr_b16 a[246:247], v5 offset:10848
v_mfma_f32_16x16x32_bf16 v[44:47], a[248:251], a[60:63], v[44:47]
s_waitcnt lgkmcnt(4)
v_mfma_f32_16x16x32_bf16 v[60:63], a[252:255], a[60:63], v[60:63]
ds_read_b64_tr_b16 a[248:249], v5 offset:7296
v_mfma_f32_16x16x32_bf16 v[56:59], a[252:255], a[56:59], v[56:59]
buffer_load_dwordx4 a[20:23], v2, s[24:27], s47 offen offset:1024 sc1
v_mfma_f32_16x16x32_bf16 v[52:55], a[252:255], a[52:55], v[52:55]
buffer_load_dwordx4 a[24:27], v2, s[24:27], s47 offen offset:2048 sc1
ds_read_b64_tr_b16 a[250:251], v5 offset:10880
v_mfma_f32_16x16x32_bf16 v[48:51], a[252:255], a[48:51], v[48:51]
s_waitcnt lgkmcnt(4)
v_mfma_f32_16x16x32_bf16 v[64:67], a[240:243], a[48:51], v[64:67]
ds_read_b64_tr_b16 a[252:253], v5 offset:7328
v_mfma_f32_16x16x32_bf16 v[68:71], a[240:243], a[52:55], v[68:71]
buffer_load_dwordx4 a[28:31], v2, s[24:27], s47 offen offset:3072 sc1
v_mfma_f32_16x16x32_bf16 v[72:75], a[240:243], a[56:59], v[72:75]
ds_read_b64_tr_b16 a[254:255], v5 offset:10912
s_add_u32 s47, s101, 1
v_mfma_f32_16x16x32_bf16 v[76:79], a[240:243], a[60:63], v[76:79]
s_barrier
s_waitcnt lgkmcnt(4)
v_mfma_f32_16x16x32_bf16 v[92:95], a[244:247], a[60:63], v[92:95]
s_and_b32 s47, s47, 3
s_mul_i32 s47, s47, 8192
s_add_u32 s47, s47, s58
s_add_u32 s47, s47, 32768
v_mfma_f32_16x16x32_bf16 v[88:91], a[244:247], a[56:59], v[88:91]
buffer_load_dwordx4 a[32:35], v2, s[24:27], s47 offen offset:0 sc1
v_mfma_f32_16x16x32_bf16 v[84:87], a[244:247], a[52:55], v[84:87]
buffer_load_dwordx4 a[36:39], v2, s[24:27], s47 offen offset:1024 sc1
ds_read_b64_tr_b16 a[240:241], v5 offset:7360
ds_read_b64_tr_b16 a[242:243], v5 offset:10944
v_mfma_f32_16x16x32_bf16 v[80:83], a[244:247], a[48:51], v[80:83]
s_waitcnt lgkmcnt(4)
v_mfma_f32_16x16x32_bf16 v[96:99], a[248:251], a[48:51], v[96:99]
ds_read_b64_tr_b16 a[244:245], v5 offset:21504
v_mfma_f32_16x16x32_bf16 v[100:103], a[248:251], a[52:55], v[100:103]
buffer_load_dwordx4 a[40:43], v2, s[24:27], s47 offen offset:2048 sc1
v_mfma_f32_16x16x32_bf16 v[104:107], a[248:251], a[56:59], v[104:107]
ds_read_b64_tr_b16 a[246:247], v5 offset:25088
v_mfma_f32_16x16x32_bf16 v[108:111], a[248:251], a[60:63], v[108:111]
s_waitcnt lgkmcnt(4)
v_mfma_f32_16x16x32_bf16 v[124:127], a[252:255], a[60:63], v[124:127]
ds_read_b64_tr_b16 a[248:249], v5 offset:21536
v_mfma_f32_16x16x32_bf16 v[120:123], a[252:255], a[56:59], v[120:123]
buffer_load_dwordx4 a[44:47], v2, s[24:27], s47 offen offset:3072 sc1
v_mfma_f32_16x16x32_bf16 v[116:119], a[252:255], a[52:55], v[116:119]
buffer_load_dwordx4 v6, s[28:31], s54 offen lds
ds_read_b64_tr_b16 a[250:251], v5 offset:25120
v_mfma_f32_16x16x32_bf16 v[112:115], a[252:255], a[48:51], v[112:115]
s_waitcnt lgkmcnt(4)
s_add_u32 m0, s57, 1024
s_add_u32 s47, s47, 4096
v_mfma_f32_16x16x32_bf16 v[128:131], a[240:243], a[48:51], v[128:131]
ds_read_b64_tr_b16 a[252:253], v5 offset:21568
v_mfma_f32_16x16x32_bf16 v[132:135], a[240:243], a[52:55], v[132:135]
ds_read_b64_tr_b16 a[254:255], v5 offset:25152
v_mfma_f32_16x16x32_bf16 v[136:139], a[240:243], a[56:59], v[136:139]
buffer_load_dwordx4 v7, s[28:31], s54 offen lds
v_mfma_f32_16x16x32_bf16 v[140:143], a[240:243], a[60:63], v[140:143]
s_waitcnt lgkmcnt(4)
s_add_u32 m0, s57, 2048
v_mfma_f32_16x16x32_bf16 v[156:159], a[244:247], a[60:63], v[156:159]
ds_read_b64_tr_b16 a[240:241], v5 offset:21600
v_mfma_f32_16x16x32_bf16 v[152:155], a[244:247], a[56:59], v[152:155]
ds_read_b64_tr_b16 a[242:243], v5 offset:25184
v_mfma_f32_16x16x32_bf16 v[148:151], a[244:247], a[52:55], v[148:151]
buffer_load_dwordx4 v8, s[28:31], s54 offen lds
v_mfma_f32_16x16x32_bf16 v[144:147], a[244:247], a[48:51], v[144:147]
s_waitcnt lgkmcnt(4)
s_add_u32 m0, s57, 3072
v_mfma_f32_16x16x32_bf16 v[160:163], a[248:251], a[48:51], v[160:163]
ds_read_b64_tr_b16 a[244:245], v5 offset:21632
v_mfma_f32_16x16x32_bf16 v[164:167], a[248:251], a[52:55], v[164:167]
ds_read_b64_tr_b16 a[246:247], v5 offset:25216
v_mfma_f32_16x16x32_bf16 v[168:171], a[248:251], a[56:59], v[168:171]
v_mfma_f32_16x16x32_bf16 v[172:175], a[248:251], a[60:63], v[172:175]
s_waitcnt lgkmcnt(4)
v_mfma_f32_16x16x32_bf16 v[188:191], a[252:255], a[60:63], v[188:191]
ds_read_b64_tr_b16 a[248:249], v5 offset:21664
v_mfma_f32_16x16x32_bf16 v[184:187], a[252:255], a[56:59], v[184:187]
ds_read_b64_tr_b16 a[250:251], v5 offset:25248
v_mfma_f32_16x16x32_bf16 v[180:183], a[252:255], a[52:55], v[180:183]
buffer_load_dwordx4 v9, s[28:31], s54 offen lds
v_mfma_f32_16x16x32_bf16 v[176:179], a[252:255], a[48:51], v[176:179]
s_waitcnt lgkmcnt(4)
s_add_u32 m0, s57, 4096
v_mfma_f32_16x16x32_bf16 v[192:195], a[240:243], a[48:51], v[192:195]
ds_read_b64_tr_b16 a[252:253], v5 offset:21696
v_mfma_f32_16x16x32_bf16 v[196:199], a[240:243], a[52:55], v[196:199]
ds_read_b64_tr_b16 a[254:255], v5 offset:25280
v_mfma_f32_16x16x32_bf16 v[200:203], a[240:243], a[56:59], v[200:203]
buffer_load_dwordx4 v10, s[28:31], s54 offen lds
v_mfma_f32_16x16x32_bf16 v[204:207], a[240:243], a[60:63], v[204:207]
s_waitcnt lgkmcnt(4)
s_add_u32 m0, s57, 5120
v_mfma_f32_16x16x32_bf16 v[220:223], a[244:247], a[60:63], v[220:223]
ds_read_b64_tr_b16 a[240:241], v4 offset:0
v_mfma_f32_16x16x32_bf16 v[216:219], a[244:247], a[56:59], v[216:219]
ds_read_b64_tr_b16 a[242:243], v4 offset:3584
v_mfma_f32_16x16x32_bf16 v[212:215], a[244:247], a[52:55], v[212:215]
buffer_load_dwordx4 v11, s[28:31], s54 offen lds
v_mfma_f32_16x16x32_bf16 v[208:211], a[244:247], a[48:51], v[208:211]
s_waitcnt lgkmcnt(4)
s_add_u32 m0, s57, 6144
v_mfma_f32_16x16x32_bf16 v[224:227], a[248:251], a[48:51], v[224:227]
ds_read_b64_tr_b16 a[244:245], v4 offset:32
v_mfma_f32_16x16x32_bf16 v[228:231], a[248:251], a[52:55], v[228:231]
ds_read_b64_tr_b16 a[246:247], v4 offset:3616
v_mfma_f32_16x16x32_bf16 v[232:235], a[248:251], a[56:59], v[232:235]
buffer_load_dwordx4 v12, s[28:31], s54 offen lds
v_mfma_f32_16x16x32_bf16 v[236:239], a[248:251], a[60:63], v[236:239]
s_waitcnt lgkmcnt(4)
v_mfma_f32_16x16x32_bf16 v[252:255], a[252:255], a[60:63], v[252:255]
ds_read_b64_tr_b16 a[248:249], v4 offset:64
v_mfma_f32_16x16x32_bf16 v[248:251], a[252:255], a[56:59], v[248:251]
ds_read_b64_tr_b16 a[250:251], v4 offset:3648
v_mfma_f32_16x16x32_bf16 v[244:247], a[252:255], a[52:55], v[244:247]
v_mfma_f32_16x16x32_bf16 v[240:243], a[252:255], a[48:51], v[240:243]
s_waitcnt lgkmcnt(4)
s_waitcnt vmcnt(38)
v_mfma_f32_16x16x32_bf16 v[32:35], a[240:243], a[64:67], v[32:35]
ds_read_b64_tr_b16 a[252:253], v4 offset:96
v_mfma_f32_16x16x32_bf16 v[36:39], a[240:243], a[68:71], v[36:39]
ds_read_b64_tr_b16 a[254:255], v4 offset:3680
v_mfma_f32_16x16x32_bf16 v[40:43], a[240:243], a[72:75], v[40:43]
v_mfma_f32_16x16x32_bf16 v[44:47], a[240:243], a[76:79], v[44:47]
s_waitcnt lgkmcnt(4)
v_mfma_f32_16x16x32_bf16 v[60:63], a[244:247], a[76:79], v[60:63]
ds_read_b64_tr_b16 a[240:241], v4 offset:128
v_mfma_f32_16x16x32_bf16 v[56:59], a[244:247], a[72:75], v[56:59]
ds_read_b64_tr_b16 a[242:243], v4 offset:3712
v_mfma_f32_16x16x32_bf16 v[52:55], a[244:247], a[68:71], v[52:55]
buffer_load_dwordx4 a[48:51], v2, s[24:27], s47 offen offset:0 sc1
v_mfma_f32_16x16x32_bf16 v[48:51], a[244:247], a[64:67], v[48:51]
s_waitcnt lgkmcnt(4)
v_mfma_f32_16x16x32_bf16 v[64:67], a[248:251], a[64:67], v[64:67]
ds_read_b64_tr_b16 a[244:245], v4 offset:160
v_add_u32 v5, 86016, v3
v_mfma_f32_16x16x32_bf16 v[68:71], a[248:251], a[68:71], v[68:71]
ds_read_b64_tr_b16 a[246:247], v4 offset:3744
v_mfma_f32_16x16x32_bf16 v[72:75], a[248:251], a[72:75], v[72:75]
v_mfma_f32_16x16x32_bf16 v[76:79], a[248:251], a[76:79], v[76:79]
s_waitcnt lgkmcnt(4)
v_mfma_f32_16x16x32_bf16 v[92:95], a[252:255], a[76:79], v[92:95]
ds_read_b64_tr_b16 a[248:249], v4 offset:192
v_mfma_f32_16x16x32_bf16 v[88:91], a[252:255], a[72:75], v[88:91]
ds_read_b64_tr_b16 a[250:251], v4 offset:3776
v_mfma_f32_16x16x32_bf16 v[84:87], a[252:255], a[68:71], v[84:87]
v_mfma_f32_16x16x32_bf16 v[80:83], a[252:255], a[64:67], v[80:83]
s_waitcnt lgkmcnt(4)
v_mfma_f32_16x16x32_bf16 v[96:99], a[240:243], a[64:67], v[96:99]
ds_read_b64_tr_b16 a[252:253], v4 offset:14336
v_mfma_f32_16x16x32_bf16 v[100:103], a[240:243], a[68:71], v[100:103]
ds_read_b64_tr_b16 a[254:255], v4 offset:17920
v_mfma_f32_16x16x32_bf16 v[104:107], a[240:243], a[72:75], v[104:107]
v_mfma_f32_16x16x32_bf16 v[108:111], a[240:243], a[76:79], v[108:111]
s_waitcnt lgkmcnt(4)
v_mfma_f32_16x16x32_bf16 v[124:127], a[244:247], a[76:79], v[124:127]
ds_read_b64_tr_b16 a[240:241], v4 offset:14368
v_mfma_f32_16x16x32_bf16 v[120:123], a[244:247], a[72:75], v[120:123]
ds_read_b64_tr_b16 a[242:243], v4 offset:17952
v_mfma_f32_16x16x32_bf16 v[116:119], a[244:247], a[68:71], v[116:119]
buffer_load_dwordx4 a[52:55], v2, s[24:27], s47 offen offset:1024 sc1
v_mfma_f32_16x16x32_bf16 v[112:115], a[244:247], a[64:67], v[112:115]
s_waitcnt lgkmcnt(4)
v_mfma_f32_16x16x32_bf16 v[128:131], a[248:251], a[64:67], v[128:131]
ds_read_b64_tr_b16 a[244:245], v4 offset:14400
v_mfma_f32_16x16x32_bf16 v[132:135], a[248:251], a[68:71], v[132:135]
ds_read_b64_tr_b16 a[246:247], v4 offset:17984
v_mfma_f32_16x16x32_bf16 v[136:139], a[248:251], a[72:75], v[136:139]
v_mfma_f32_16x16x32_bf16 v[140:143], a[248:251], a[76:79], v[140:143]
s_waitcnt lgkmcnt(4)
v_mfma_f32_16x16x32_bf16 v[156:159], a[252:255], a[76:79], v[156:159]
ds_read_b64_tr_b16 a[248:249], v4 offset:14432
v_mfma_f32_16x16x32_bf16 v[152:155], a[252:255], a[72:75], v[152:155]
ds_read_b64_tr_b16 a[250:251], v4 offset:18016
v_mfma_f32_16x16x32_bf16 v[148:151], a[252:255], a[68:71], v[148:151]
v_mfma_f32_16x16x32_bf16 v[144:147], a[252:255], a[64:67], v[144:147]
s_waitcnt lgkmcnt(4)
v_mfma_f32_16x16x32_bf16 v[160:163], a[240:243], a[64:67], v[160:163]
ds_read_b64_tr_b16 a[252:253], v4 offset:14464
v_mfma_f32_16x16x32_bf16 v[164:167], a[240:243], a[68:71], v[164:167]
ds_read_b64_tr_b16 a[254:255], v4 offset:18048
v_mfma_f32_16x16x32_bf16 v[168:171], a[240:243], a[72:75], v[168:171]
buffer_load_dwordx4 a[56:59], v2, s[24:27], s47 offen offset:2048 sc1
v_mfma_f32_16x16x32_bf16 v[172:175], a[240:243], a[76:79], v[172:175]
s_waitcnt lgkmcnt(4)
v_mfma_f32_16x16x32_bf16 v[188:191], a[244:247], a[76:79], v[188:191]
ds_read_b64_tr_b16 a[240:241], v4 offset:14496
v_mfma_f32_16x16x32_bf16 v[184:187], a[244:247], a[72:75], v[184:187]
ds_read_b64_tr_b16 a[242:243], v4 offset:18080
v_mfma_f32_16x16x32_bf16 v[180:183], a[244:247], a[68:71], v[180:183]
s_add_u32 s54, s101, 1
s_and_b32 s54, s54, 3
s_mul_i32 s54, s54, 458752
v_mfma_f32_16x16x32_bf16 v[176:179], a[244:247], a[64:67], v[176:179]
s_waitcnt lgkmcnt(4)
s_add_u32 s54, s54, s48
s_add_u32 s54, s54, 1835008
v_mfma_f32_16x16x32_bf16 v[192:195], a[248:251], a[64:67], v[192:195]
ds_read_b64_tr_b16 a[244:245], v4 offset:14528
s_add_u32 m0, s57, 28672
v_mfma_f32_16x16x32_bf16 v[196:199], a[248:251], a[68:71], v[196:199]
ds_read_b64_tr_b16 a[246:247], v4 offset:18112
v_mfma_f32_16x16x32_bf16 v[200:203], a[248:251], a[72:75], v[200:203]
v_mfma_f32_16x16x32_bf16 v[204:207], a[248:251], a[76:79], v[204:207]
s_waitcnt lgkmcnt(4)
v_mfma_f32_16x16x32_bf16 v[220:223], a[252:255], a[76:79], v[220:223]
ds_read_b64_tr_b16 a[248:249], v4 offset:7168
v_mfma_f32_16x16x32_bf16 v[216:219], a[252:255], a[72:75], v[216:219]
ds_read_b64_tr_b16 a[250:251], v4 offset:10752
v_mfma_f32_16x16x32_bf16 v[212:215], a[252:255], a[68:71], v[212:215]
v_mfma_f32_16x16x32_bf16 v[208:211], a[252:255], a[64:67], v[208:211]
s_waitcnt lgkmcnt(4)
v_mfma_f32_16x16x32_bf16 v[224:227], a[240:243], a[64:67], v[224:227]
ds_read_b64_tr_b16 a[252:253], v4 offset:7200
v_mfma_f32_16x16x32_bf16 v[228:231], a[240:243], a[68:71], v[228:231]
ds_read_b64_tr_b16 a[254:255], v4 offset:10784
v_mfma_f32_16x16x32_bf16 v[232:235], a[240:243], a[72:75], v[232:235]
buffer_load_dwordx4 a[60:63], v2, s[24:27], s47 offen offset:3072 sc1
v_mfma_f32_16x16x32_bf16 v[236:239], a[240:243], a[76:79], v[236:239]
s_waitcnt lgkmcnt(4)
s_add_u32 s47, s101, 2
s_and_b32 s47, s47, 3
v_mfma_f32_16x16x32_bf16 v[252:255], a[244:247], a[76:79], v[252:255]
ds_read_b64_tr_b16 a[240:241], v4 offset:7232
s_mul_i32 s47, s47, 8192
v_mfma_f32_16x16x32_bf16 v[248:251], a[244:247], a[72:75], v[248:251]
ds_read_b64_tr_b16 a[242:243], v4 offset:10816
s_add_u32 s47, s47, s58
v_mfma_f32_16x16x32_bf16 v[244:247], a[244:247], a[68:71], v[244:247]
s_add_u32 s47, s47, 32768
v_mfma_f32_16x16x32_bf16 v[240:243], a[244:247], a[64:67], v[240:243]
s_waitcnt lgkmcnt(4)
s_waitcnt vmcnt(38)
v_mfma_f32_16x16x32_bf16 v[32:35], a[248:251], a[80:83], v[32:35]
ds_read_b64_tr_b16 a[244:245], v4 offset:7264
v_mfma_f32_16x16x32_bf16 v[36:39], a[248:251], a[84:87], v[36:39]
ds_read_b64_tr_b16 a[246:247], v4 offset:10848
v_mfma_f32_16x16x32_bf16 v[40:43], a[248:251], a[88:91], v[40:43]
buffer_load_dwordx4 a[64:67], v2, s[24:27], s47 offen offset:0 sc1
v_mfma_f32_16x16x32_bf16 v[44:47], a[248:251], a[92:95], v[44:47]
s_waitcnt lgkmcnt(4)
v_mfma_f32_16x16x32_bf16 v[60:63], a[252:255], a[92:95], v[60:63]
ds_read_b64_tr_b16 a[248:249], v4 offset:7296
v_mfma_f32_16x16x32_bf16 v[56:59], a[252:255], a[88:91], v[56:59]
ds_read_b64_tr_b16 a[250:251], v4 offset:10880
v_mfma_f32_16x16x32_bf16 v[52:55], a[252:255], a[84:87], v[52:55]
buffer_load_dwordx4 a[68:71], v2, s[24:27], s47 offen offset:1024 sc1
v_mfma_f32_16x16x32_bf16 v[48:51], a[252:255], a[80:83], v[48:51]
s_waitcnt lgkmcnt(4)
v_mfma_f32_16x16x32_bf16 v[64:67], a[240:243], a[80:83], v[64:67]
ds_read_b64_tr_b16 a[252:253], v4 offset:7328
v_mfma_f32_16x16x32_bf16 v[68:71], a[240:243], a[84:87], v[68:71]
ds_read_b64_tr_b16 a[254:255], v4 offset:10912
v_mfma_f32_16x16x32_bf16 v[72:75], a[240:243], a[88:91], v[72:75]
v_mfma_f32_16x16x32_bf16 v[76:79], a[240:243], a[92:95], v[76:79]
s_waitcnt vmcnt(29)
s_barrier
s_waitcnt lgkmcnt(4)
v_mfma_f32_16x16x32_bf16 v[92:95], a[244:247], a[92:95], v[92:95]
ds_read_b64_tr_b16 a[240:241], v4 offset:7360
v_mfma_f32_16x16x32_bf16 v[88:91], a[244:247], a[88:91], v[88:91]
ds_read_b64_tr_b16 a[242:243], v4 offset:10944
v_mfma_f32_16x16x32_bf16 v[84:87], a[244:247], a[84:87], v[84:87]
buffer_load_dwordx4 a[72:75], v2, s[24:27], s47 offen offset:2048 sc1
v_mfma_f32_16x16x32_bf16 v[80:83], a[244:247], a[80:83], v[80:83]
s_waitcnt lgkmcnt(4)
v_mfma_f32_16x16x32_bf16 v[96:99], a[248:251], a[80:83], v[96:99]
ds_read_b64_tr_b16 a[244:245], v4 offset:21504
v_mfma_f32_16x16x32_bf16 v[100:103], a[248:251], a[84:87], v[100:103]
ds_read_b64_tr_b16 a[246:247], v4 offset:25088
v_mfma_f32_16x16x32_bf16 v[104:107], a[248:251], a[88:91], v[104:107]
buffer_load_dwordx4 a[76:79], v2, s[24:27], s47 offen offset:3072 sc1
v_mfma_f32_16x16x32_bf16 v[108:111], a[248:251], a[92:95], v[108:111]
s_waitcnt lgkmcnt(4)
s_add_u32 s47, s47, 4096
v_mfma_f32_16x16x32_bf16 v[124:127], a[252:255], a[92:95], v[124:127]
ds_read_b64_tr_b16 a[248:249], v4 offset:21536
v_mfma_f32_16x16x32_bf16 v[120:123], a[252:255], a[88:91], v[120:123]
ds_read_b64_tr_b16 a[250:251], v4 offset:25120
v_mfma_f32_16x16x32_bf16 v[116:119], a[252:255], a[84:87], v[116:119]
buffer_load_dwordx4 v6, s[28:31], s54 offen lds
v_mfma_f32_16x16x32_bf16 v[112:115], a[252:255], a[80:83], v[112:115]
s_waitcnt lgkmcnt(4)
s_add_u32 m0, s57, 29696
v_mfma_f32_16x16x32_bf16 v[128:131], a[240:243], a[80:83], v[128:131]
ds_read_b64_tr_b16 a[252:253], v4 offset:21568
v_mfma_f32_16x16x32_bf16 v[132:135], a[240:243], a[84:87], v[132:135]
ds_read_b64_tr_b16 a[254:255], v4 offset:25152
v_mfma_f32_16x16x32_bf16 v[136:139], a[240:243], a[88:91], v[136:139]
buffer_load_dwordx4 v7, s[28:31], s54 offen lds
v_mfma_f32_16x16x32_bf16 v[140:143], a[240:243], a[92:95], v[140:143]
s_waitcnt lgkmcnt(4)
s_add_u32 m0, s57, 30720
v_mfma_f32_16x16x32_bf16 v[156:159], a[244:247], a[92:95], v[156:159]
ds_read_b64_tr_b16 a[240:241], v4 offset:21600
v_mfma_f32_16x16x32_bf16 v[152:155], a[244:247], a[88:91], v[152:155]
ds_read_b64_tr_b16 a[242:243], v4 offset:25184
v_mfma_f32_16x16x32_bf16 v[148:151], a[244:247], a[84:87], v[148:151]
buffer_load_dwordx4 v8, s[28:31], s54 offen lds
v_mfma_f32_16x16x32_bf16 v[144:147], a[244:247], a[80:83], v[144:147]
s_waitcnt lgkmcnt(4)
s_add_u32 m0, s57, 31744
v_mfma_f32_16x16x32_bf16 v[160:163], a[248:251], a[80:83], v[160:163]
ds_read_b64_tr_b16 a[244:245], v4 offset:21632
v_mfma_f32_16x16x32_bf16 v[164:167], a[248:251], a[84:87], v[164:167]
ds_read_b64_tr_b16 a[246:247], v4 offset:25216
v_mfma_f32_16x16x32_bf16 v[168:171], a[248:251], a[88:91], v[168:171]
v_mfma_f32_16x16x32_bf16 v[172:175], a[248:251], a[92:95], v[172:175]
s_waitcnt lgkmcnt(4)
v_mfma_f32_16x16x32_bf16 v[188:191], a[252:255], a[92:95], v[188:191]
ds_read_b64_tr_b16 a[248:249], v4 offset:21664
v_mfma_f32_16x16x32_bf16 v[184:187], a[252:255], a[88:91], v[184:187]
ds_read_b64_tr_b16 a[250:251], v4 offset:25248
v_mfma_f32_16x16x32_bf16 v[180:183], a[252:255], a[84:87], v[180:183]
buffer_load_dwordx4 v9, s[28:31], s54 offen lds
v_mfma_f32_16x16x32_bf16 v[176:179], a[252:255], a[80:83], v[176:179]
s_waitcnt lgkmcnt(4)
s_add_u32 m0, s57, 32768
v_mfma_f32_16x16x32_bf16 v[192:195], a[240:243], a[80:83], v[192:195]
ds_read_b64_tr_b16 a[252:253], v4 offset:21696
v_mfma_f32_16x16x32_bf16 v[196:199], a[240:243], a[84:87], v[196:199]
ds_read_b64_tr_b16 a[254:255], v4 offset:25280
v_mfma_f32_16x16x32_bf16 v[200:203], a[240:243], a[88:91], v[200:203]
buffer_load_dwordx4 v10, s[28:31], s54 offen lds
v_mfma_f32_16x16x32_bf16 v[204:207], a[240:243], a[92:95], v[204:207]
s_waitcnt lgkmcnt(4)
s_add_u32 m0, s57, 33792
v_mfma_f32_16x16x32_bf16 v[220:223], a[244:247], a[92:95], v[220:223]
ds_read_b64_tr_b16 a[240:241], v5 offset:0
v_mfma_f32_16x16x32_bf16 v[216:219], a[244:247], a[88:91], v[216:219]
ds_read_b64_tr_b16 a[242:243], v5 offset:3584
v_mfma_f32_16x16x32_bf16 v[212:215], a[244:247], a[84:87], v[212:215]
buffer_load_dwordx4 v11, s[28:31], s54 offen lds
v_mfma_f32_16x16x32_bf16 v[208:211], a[244:247], a[80:83], v[208:211]
s_waitcnt lgkmcnt(4)
s_add_u32 m0, s57, 34816
v_mfma_f32_16x16x32_bf16 v[224:227], a[248:251], a[80:83], v[224:227]
ds_read_b64_tr_b16 a[244:245], v5 offset:32
v_mfma_f32_16x16x32_bf16 v[228:231], a[248:251], a[84:87], v[228:231]
ds_read_b64_tr_b16 a[246:247], v5 offset:3616
v_mfma_f32_16x16x32_bf16 v[232:235], a[248:251], a[88:91], v[232:235]
buffer_load_dwordx4 v12, s[28:31], s54 offen lds
v_mfma_f32_16x16x32_bf16 v[236:239], a[248:251], a[92:95], v[236:239]
s_waitcnt lgkmcnt(4)
v_mfma_f32_16x16x32_bf16 v[252:255], a[252:255], a[92:95], v[252:255]
ds_read_b64_tr_b16 a[248:249], v5 offset:64
v_mfma_f32_16x16x32_bf16 v[248:251], a[252:255], a[88:91], v[248:251]
ds_read_b64_tr_b16 a[250:251], v5 offset:3648
v_mfma_f32_16x16x32_bf16 v[244:247], a[252:255], a[84:87], v[244:247]
v_mfma_f32_16x16x32_bf16 v[240:243], a[252:255], a[80:83], v[240:243]
s_waitcnt lgkmcnt(4)
v_mfma_f32_16x16x32_bf16 v[32:35], a[240:243], a[96:99], v[32:35]
ds_read_b64_tr_b16 a[252:253], v5 offset:96
v_mfma_f32_16x16x32_bf16 v[36:39], a[240:243], a[100:103], v[36:39]
ds_read_b64_tr_b16 a[254:255], v5 offset:3680
v_mfma_f32_16x16x32_bf16 v[40:43], a[240:243], a[104:107], v[40:43]
v_mfma_f32_16x16x32_bf16 v[44:47], a[240:243], a[108:111], v[44:47]
s_waitcnt lgkmcnt(4)
v_mfma_f32_16x16x32_bf16 v[60:63], a[244:247], a[108:111], v[60:63]
ds_read_b64_tr_b16 a[240:241], v5 offset:128
v_mfma_f32_16x16x32_bf16 v[56:59], a[244:247], a[104:107], v[56:59]
ds_read_b64_tr_b16 a[242:243], v5 offset:3712
v_mfma_f32_16x16x32_bf16 v[52:55], a[244:247], a[100:103], v[52:55]
buffer_load_dwordx4 a[80:83], v2, s[24:27], s47 offen offset:0 sc1
v_mfma_f32_16x16x32_bf16 v[48:51], a[244:247], a[96:99], v[48:51]
s_waitcnt lgkmcnt(4)
v_mfma_f32_16x16x32_bf16 v[64:67], a[248:251], a[96:99], v[64:67]
ds_read_b64_tr_b16 a[244:245], v5 offset:160
v_add_u32 v4, 0, v3
v_mfma_f32_16x16x32_bf16 v[68:71], a[248:251], a[100:103], v[68:71]
ds_read_b64_tr_b16 a[246:247], v5 offset:3744
v_mfma_f32_16x16x32_bf16 v[72:75], a[248:251], a[104:107], v[72:75]
v_mfma_f32_16x16x32_bf16 v[76:79], a[248:251], a[108:111], v[76:79]
s_waitcnt lgkmcnt(4)
v_mfma_f32_16x16x32_bf16 v[92:95], a[252:255], a[108:111], v[92:95]
ds_read_b64_tr_b16 a[248:249], v5 offset:192
v_mfma_f32_16x16x32_bf16 v[88:91], a[252:255], a[104:107], v[88:91]
ds_read_b64_tr_b16 a[250:251], v5 offset:3776
v_mfma_f32_16x16x32_bf16 v[84:87], a[252:255], a[100:103], v[84:87]
v_mfma_f32_16x16x32_bf16 v[80:83], a[252:255], a[96:99], v[80:83]
s_waitcnt lgkmcnt(4)
v_mfma_f32_16x16x32_bf16 v[96:99], a[240:243], a[96:99], v[96:99]
ds_read_b64_tr_b16 a[252:253], v5 offset:14336
v_mfma_f32_16x16x32_bf16 v[100:103], a[240:243], a[100:103], v[100:103]
ds_read_b64_tr_b16 a[254:255], v5 offset:17920
v_mfma_f32_16x16x32_bf16 v[104:107], a[240:243], a[104:107], v[104:107]
v_mfma_f32_16x16x32_bf16 v[108:111], a[240:243], a[108:111], v[108:111]
s_waitcnt lgkmcnt(4)
v_mfma_f32_16x16x32_bf16 v[124:127], a[244:247], a[108:111], v[124:127]
ds_read_b64_tr_b16 a[240:241], v5 offset:14368
v_mfma_f32_16x16x32_bf16 v[120:123], a[244:247], a[104:107], v[120:123]
ds_read_b64_tr_b16 a[242:243], v5 offset:17952
v_mfma_f32_16x16x32_bf16 v[116:119], a[244:247], a[100:103], v[116:119]
buffer_load_dwordx4 a[84:87], v2, s[24:27], s47 offen offset:1024 sc1
v_mfma_f32_16x16x32_bf16 v[112:115], a[244:247], a[96:99], v[112:115]
s_waitcnt lgkmcnt(4)
v_mfma_f32_16x16x32_bf16 v[128:131], a[248:251], a[96:99], v[128:131]
ds_read_b64_tr_b16 a[244:245], v5 offset:14400
v_mfma_f32_16x16x32_bf16 v[132:135], a[248:251], a[100:103], v[132:135]
ds_read_b64_tr_b16 a[246:247], v5 offset:17984
v_mfma_f32_16x16x32_bf16 v[136:139], a[248:251], a[104:107], v[136:139]
v_mfma_f32_16x16x32_bf16 v[140:143], a[248:251], a[108:111], v[140:143]
s_waitcnt lgkmcnt(4)
v_mfma_f32_16x16x32_bf16 v[156:159], a[252:255], a[108:111], v[156:159]
ds_read_b64_tr_b16 a[248:249], v5 offset:14432
v_mfma_f32_16x16x32_bf16 v[152:155], a[252:255], a[104:107], v[152:155]
ds_read_b64_tr_b16 a[250:251], v5 offset:18016
v_mfma_f32_16x16x32_bf16 v[148:151], a[252:255], a[100:103], v[148:151]
v_mfma_f32_16x16x32_bf16 v[144:147], a[252:255], a[96:99], v[144:147]
s_waitcnt lgkmcnt(4)
v_mfma_f32_16x16x32_bf16 v[160:163], a[240:243], a[96:99], v[160:163]
ds_read_b64_tr_b16 a[252:253], v5 offset:14464
v_mfma_f32_16x16x32_bf16 v[164:167], a[240:243], a[100:103], v[164:167]
ds_read_b64_tr_b16 a[254:255], v5 offset:18048
v_mfma_f32_16x16x32_bf16 v[168:171], a[240:243], a[104:107], v[168:171]
buffer_load_dwordx4 a[88:91], v2, s[24:27], s47 offen offset:2048 sc1
v_mfma_f32_16x16x32_bf16 v[172:175], a[240:243], a[108:111], v[172:175]
s_waitcnt lgkmcnt(4)
v_mfma_f32_16x16x32_bf16 v[188:191], a[244:247], a[108:111], v[188:191]
ds_read_b64_tr_b16 a[240:241], v5 offset:14496
v_mfma_f32_16x16x32_bf16 v[184:187], a[244:247], a[104:107], v[184:187]
ds_read_b64_tr_b16 a[242:243], v5 offset:18080
v_mfma_f32_16x16x32_bf16 v[180:183], a[244:247], a[100:103], v[180:183]
s_add_u32 s54, s101, 2
s_and_b32 s54, s54, 3
s_mul_i32 s54, s54, 458752
v_mfma_f32_16x16x32_bf16 v[176:179], a[244:247], a[96:99], v[176:179]
s_waitcnt lgkmcnt(4)
s_add_u32 s54, s54, s48
s_add_u32 s54, s54, 1835008
v_mfma_f32_16x16x32_bf16 v[192:195], a[248:251], a[96:99], v[192:195]
ds_read_b64_tr_b16 a[244:245], v5 offset:14528
s_add_u32 m0, s57, 57344
v_mfma_f32_16x16x32_bf16 v[196:199], a[248:251], a[100:103], v[196:199]
ds_read_b64_tr_b16 a[246:247], v5 offset:18112
v_mfma_f32_16x16x32_bf16 v[200:203], a[248:251], a[104:107], v[200:203]
v_mfma_f32_16x16x32_bf16 v[204:207], a[248:251], a[108:111], v[204:207]
s_waitcnt lgkmcnt(4)
v_mfma_f32_16x16x32_bf16 v[220:223], a[252:255], a[108:111], v[220:223]
ds_read_b64_tr_b16 a[248:249], v5 offset:7168
v_mfma_f32_16x16x32_bf16 v[216:219], a[252:255], a[104:107], v[216:219]
ds_read_b64_tr_b16 a[250:251], v5 offset:10752
v_mfma_f32_16x16x32_bf16 v[212:215], a[252:255], a[100:103], v[212:215]
v_mfma_f32_16x16x32_bf16 v[208:211], a[252:255], a[96:99], v[208:211]
s_waitcnt lgkmcnt(4)
v_mfma_f32_16x16x32_bf16 v[224:227], a[240:243], a[96:99], v[224:227]
ds_read_b64_tr_b16 a[252:253], v5 offset:7200
v_mfma_f32_16x16x32_bf16 v[228:231], a[240:243], a[100:103], v[228:231]
ds_read_b64_tr_b16 a[254:255], v5 offset:10784
v_mfma_f32_16x16x32_bf16 v[232:235], a[240:243], a[104:107], v[232:235]
buffer_load_dwordx4 a[92:95], v2, s[24:27], s47 offen offset:3072 sc1
v_mfma_f32_16x16x32_bf16 v[236:239], a[240:243], a[108:111], v[236:239]
s_waitcnt lgkmcnt(4)
s_add_u32 s47, s101, 3
s_and_b32 s47, s47, 3
v_mfma_f32_16x16x32_bf16 v[252:255], a[244:247], a[108:111], v[252:255]
ds_read_b64_tr_b16 a[240:241], v5 offset:7232
s_mul_i32 s47, s47, 8192
v_mfma_f32_16x16x32_bf16 v[248:251], a[244:247], a[104:107], v[248:251]
ds_read_b64_tr_b16 a[242:243], v5 offset:10816
s_add_u32 s47, s47, s58
v_mfma_f32_16x16x32_bf16 v[244:247], a[244:247], a[100:103], v[244:247]
s_add_u32 s47, s47, 32768
v_mfma_f32_16x16x32_bf16 v[240:243], a[244:247], a[96:99], v[240:243]
s_waitcnt lgkmcnt(4)
s_waitcnt vmcnt(38)
v_mfma_f32_16x16x32_bf16 v[32:35], a[248:251], a[112:115], v[32:35]
ds_read_b64_tr_b16 a[244:245], v5 offset:7264
v_mfma_f32_16x16x32_bf16 v[36:39], a[248:251], a[116:119], v[36:39]
ds_read_b64_tr_b16 a[246:247], v5 offset:10848
v_mfma_f32_16x16x32_bf16 v[40:43], a[248:251], a[120:123], v[40:43]
buffer_load_dwordx4 a[96:99], v2, s[24:27], s47 offen offset:0 sc1
v_mfma_f32_16x16x32_bf16 v[44:47], a[248:251], a[124:127], v[44:47]
s_waitcnt lgkmcnt(4)
v_mfma_f32_16x16x32_bf16 v[60:63], a[252:255], a[124:127], v[60:63]
ds_read_b64_tr_b16 a[248:249], v5 offset:7296
v_mfma_f32_16x16x32_bf16 v[56:59], a[252:255], a[120:123], v[56:59]
ds_read_b64_tr_b16 a[250:251], v5 offset:10880
v_mfma_f32_16x16x32_bf16 v[52:55], a[252:255], a[116:119], v[52:55]
buffer_load_dwordx4 a[100:103], v2, s[24:27], s47 offen offset:1024 sc1
v_mfma_f32_16x16x32_bf16 v[48:51], a[252:255], a[112:115], v[48:51]
s_waitcnt lgkmcnt(4)
v_mfma_f32_16x16x32_bf16 v[64:67], a[240:243], a[112:115], v[64:67]
ds_read_b64_tr_b16 a[252:253], v5 offset:7328
v_mfma_f32_16x16x32_bf16 v[68:71], a[240:243], a[116:119], v[68:71]
ds_read_b64_tr_b16 a[254:255], v5 offset:10912
v_mfma_f32_16x16x32_bf16 v[72:75], a[240:243], a[120:123], v[72:75]
v_mfma_f32_16x16x32_bf16 v[76:79], a[240:243], a[124:127], v[76:79]
s_waitcnt vmcnt(21)
s_barrier
s_waitcnt lgkmcnt(4)
v_mfma_f32_16x16x32_bf16 v[92:95], a[244:247], a[124:127], v[92:95]
ds_read_b64_tr_b16 a[240:241], v5 offset:7360
v_mfma_f32_16x16x32_bf16 v[88:91], a[244:247], a[120:123], v[88:91]
ds_read_b64_tr_b16 a[242:243], v5 offset:10944
v_mfma_f32_16x16x32_bf16 v[84:87], a[244:247], a[116:119], v[84:87]
buffer_load_dwordx4 a[104:107], v2, s[24:27], s47 offen offset:2048 sc1
v_mfma_f32_16x16x32_bf16 v[80:83], a[244:247], a[112:115], v[80:83]
s_waitcnt lgkmcnt(4)
v_mfma_f32_16x16x32_bf16 v[96:99], a[248:251], a[112:115], v[96:99]
ds_read_b64_tr_b16 a[244:245], v5 offset:21504
v_mfma_f32_16x16x32_bf16 v[100:103], a[248:251], a[116:119], v[100:103]
ds_read_b64_tr_b16 a[246:247], v5 offset:25088
v_mfma_f32_16x16x32_bf16 v[104:107], a[248:251], a[120:123], v[104:107]
buffer_load_dwordx4 a[108:111], v2, s[24:27], s47 offen offset:3072 sc1
v_mfma_f32_16x16x32_bf16 v[108:111], a[248:251], a[124:127], v[108:111]
s_waitcnt lgkmcnt(4)
s_add_u32 s47, s47, 4096
v_mfma_f32_16x16x32_bf16 v[124:127], a[252:255], a[124:127], v[124:127]
ds_read_b64_tr_b16 a[248:249], v5 offset:21536
v_mfma_f32_16x16x32_bf16 v[120:123], a[252:255], a[120:123], v[120:123]
ds_read_b64_tr_b16 a[250:251], v5 offset:25120
v_mfma_f32_16x16x32_bf16 v[116:119], a[252:255], a[116:119], v[116:119]
buffer_load_dwordx4 v6, s[28:31], s54 offen lds
v_mfma_f32_16x16x32_bf16 v[112:115], a[252:255], a[112:115], v[112:115]
s_waitcnt lgkmcnt(4)
s_add_u32 m0, s57, 58368
v_mfma_f32_16x16x32_bf16 v[128:131], a[240:243], a[112:115], v[128:131]
ds_read_b64_tr_b16 a[252:253], v5 offset:21568
v_mfma_f32_16x16x32_bf16 v[132:135], a[240:243], a[116:119], v[132:135]
ds_read_b64_tr_b16 a[254:255], v5 offset:25152
v_mfma_f32_16x16x32_bf16 v[136:139], a[240:243], a[120:123], v[136:139]
buffer_load_dwordx4 v7, s[28:31], s54 offen lds
v_mfma_f32_16x16x32_bf16 v[140:143], a[240:243], a[124:127], v[140:143]
s_waitcnt lgkmcnt(4)
s_add_u32 m0, s57, 59392
v_mfma_f32_16x16x32_bf16 v[156:159], a[244:247], a[124:127], v[156:159]
ds_read_b64_tr_b16 a[240:241], v5 offset:21600
v_mfma_f32_16x16x32_bf16 v[152:155], a[244:247], a[120:123], v[152:155]
ds_read_b64_tr_b16 a[242:243], v5 offset:25184
v_mfma_f32_16x16x32_bf16 v[148:151], a[244:247], a[116:119], v[148:151]
buffer_load_dwordx4 v8, s[28:31], s54 offen lds
v_mfma_f32_16x16x32_bf16 v[144:147], a[244:247], a[112:115], v[144:147]
s_waitcnt lgkmcnt(4)
s_add_u32 m0, s57, 60416
v_mfma_f32_16x16x32_bf16 v[160:163], a[248:251], a[112:115], v[160:163]
ds_read_b64_tr_b16 a[244:245], v5 offset:21632
v_mfma_f32_16x16x32_bf16 v[164:167], a[248:251], a[116:119], v[164:167]
ds_read_b64_tr_b16 a[246:247], v5 offset:25216
v_mfma_f32_16x16x32_bf16 v[168:171], a[248:251], a[120:123], v[168:171]
v_mfma_f32_16x16x32_bf16 v[172:175], a[248:251], a[124:127], v[172:175]
s_waitcnt lgkmcnt(4)
v_mfma_f32_16x16x32_bf16 v[188:191], a[252:255], a[124:127], v[188:191]
ds_read_b64_tr_b16 a[248:249], v5 offset:21664
v_mfma_f32_16x16x32_bf16 v[184:187], a[252:255], a[120:123], v[184:187]
ds_read_b64_tr_b16 a[250:251], v5 offset:25248
v_mfma_f32_16x16x32_bf16 v[180:183], a[252:255], a[116:119], v[180:183]
buffer_load_dwordx4 v9, s[28:31], s54 offen lds
v_mfma_f32_16x16x32_bf16 v[176:179], a[252:255], a[112:115], v[176:179]
s_waitcnt lgkmcnt(4)
s_add_u32 m0, s57, 61440
v_mfma_f32_16x16x32_bf16 v[192:195], a[240:243], a[112:115], v[192:195]
ds_read_b64_tr_b16 a[252:253], v5 offset:21696
v_mfma_f32_16x16x32_bf16 v[196:199], a[240:243], a[116:119], v[196:199]
ds_read_b64_tr_b16 a[254:255], v5 offset:25280
v_mfma_f32_16x16x32_bf16 v[200:203], a[240:243], a[120:123], v[200:203]
buffer_load_dwordx4 v10, s[28:31], s54 offen lds
v_mfma_f32_16x16x32_bf16 v[204:207], a[240:243], a[124:127], v[204:207]
s_waitcnt lgkmcnt(4)
s_add_u32 m0, s57, 62464
v_mfma_f32_16x16x32_bf16 v[220:223], a[244:247], a[124:127], v[220:223]
ds_read_b64_tr_b16 a[240:241], v4 offset:0
v_mfma_f32_16x16x32_bf16 v[216:219], a[244:247], a[120:123], v[216:219]
ds_read_b64_tr_b16 a[242:243], v4 offset:3584
v_mfma_f32_16x16x32_bf16 v[212:215], a[244:247], a[116:119], v[212:215]
buffer_load_dwordx4 v11, s[28:31], s54 offen lds
v_mfma_f32_16x16x32_bf16 v[208:211], a[244:247], a[112:115], v[208:211]
s_waitcnt lgkmcnt(4)
s_add_u32 m0, s57, 63488
v_mfma_f32_16x16x32_bf16 v[224:227], a[248:251], a[112:115], v[224:227]
ds_read_b64_tr_b16 a[244:245], v4 offset:32
v_mfma_f32_16x16x32_bf16 v[228:231], a[248:251], a[116:119], v[228:231]
ds_read_b64_tr_b16 a[246:247], v4 offset:3616
v_mfma_f32_16x16x32_bf16 v[232:235], a[248:251], a[120:123], v[232:235]
buffer_load_dwordx4 v12, s[28:31], s54 offen lds
v_mfma_f32_16x16x32_bf16 v[236:239], a[248:251], a[124:127], v[236:239]
s_waitcnt lgkmcnt(4)
v_mfma_f32_16x16x32_bf16 v[252:255], a[252:255], a[124:127], v[252:255]
ds_read_b64_tr_b16 a[248:249], v4 offset:64
v_mfma_f32_16x16x32_bf16 v[248:251], a[252:255], a[120:123], v[248:251]
ds_read_b64_tr_b16 a[250:251], v4 offset:3648
v_mfma_f32_16x16x32_bf16 v[244:247], a[252:255], a[116:119], v[244:247]
v_mfma_f32_16x16x32_bf16 v[240:243], a[252:255], a[112:115], v[240:243]
s_waitcnt lgkmcnt(4)
s_mov_b32 s46, 2
s_mov_b32 s2, 8
s_mov_b32 s1, 7
GUL_7:
v_mfma_f32_16x16x32_bf16 v[32:35], a[240:243], a[0:3], v[32:35]
ds_read_b64_tr_b16 a[252:253], v4 offset:96
v_mfma_f32_16x16x32_bf16 v[36:39], a[240:243], a[4:7], v[36:39]
ds_read_b64_tr_b16 a[254:255], v4 offset:3680
v_mfma_f32_16x16x32_bf16 v[40:43], a[240:243], a[8:11], v[40:43]
v_mfma_f32_16x16x32_bf16 v[44:47], a[240:243], a[12:15], v[44:47]
s_waitcnt lgkmcnt(4)
v_mfma_f32_16x16x32_bf16 v[60:63], a[244:247], a[12:15], v[60:63]
ds_read_b64_tr_b16 a[240:241], v4 offset:128
v_mfma_f32_16x16x32_bf16 v[56:59], a[244:247], a[8:11], v[56:59]
ds_read_b64_tr_b16 a[242:243], v4 offset:3712
v_mfma_f32_16x16x32_bf16 v[52:55], a[244:247], a[4:7], v[52:55]
buffer_load_dwordx4 a[112:115], v2, s[24:27], s47 offen offset:0 sc1
v_mfma_f32_16x16x32_bf16 v[48:51], a[244:247], a[0:3], v[48:51]
s_waitcnt lgkmcnt(4)
v_mfma_f32_16x16x32_bf16 v[64:67], a[248:251], a[0:3], v[64:67]
ds_read_b64_tr_b16 a[244:245], v4 offset:160
v_add_u32 v5, 28672, v3
v_mfma_f32_16x16x32_bf16 v[68:71], a[248:251], a[4:7], v[68:71]
ds_read_b64_tr_b16 a[246:247], v4 offset:3744
v_mfma_f32_16x16x32_bf16 v[72:75], a[248:251], a[8:11], v[72:75]
v_mfma_f32_16x16x32_bf16 v[76:79], a[248:251], a[12:15], v[76:79]
s_waitcnt lgkmcnt(4)
v_mfma_f32_16x16x32_bf16 v[92:95], a[252:255], a[12:15], v[92:95]
ds_read_b64_tr_b16 a[248:249], v4 offset:192
v_mfma_f32_16x16x32_bf16 v[88:91], a[252:255], a[8:11], v[88:91]
ds_read_b64_tr_b16 a[250:251], v4 offset:3776
v_mfma_f32_16x16x32_bf16 v[84:87], a[252:255], a[4:7], v[84:87]
v_mfma_f32_16x16x32_bf16 v[80:83], a[252:255], a[0:3], v[80:83]
s_waitcnt lgkmcnt(4)
v_mfma_f32_16x16x32_bf16 v[96:99], a[240:243], a[0:3], v[96:99]
ds_read_b64_tr_b16 a[252:253], v4 offset:14336
v_mfma_f32_16x16x32_bf16 v[100:103], a[240:243], a[4:7], v[100:103]
ds_read_b64_tr_b16 a[254:255], v4 offset:17920
v_mfma_f32_16x16x32_bf16 v[104:107], a[240:243], a[8:11], v[104:107]
v_mfma_f32_16x16x32_bf16 v[108:111], a[240:243], a[12:15], v[108:111]
s_waitcnt lgkmcnt(4)
v_mfma_f32_16x16x32_bf16 v[124:127], a[244:247], a[12:15], v[124:127]
ds_read_b64_tr_b16 a[240:241], v4 offset:14368
v_mfma_f32_16x16x32_bf16 v[120:123], a[244:247], a[8:11], v[120:123]
ds_read_b64_tr_b16 a[242:243], v4 offset:17952
v_mfma_f32_16x16x32_bf16 v[116:119], a[244:247], a[4:7], v[116:119]
buffer_load_dwordx4 a[116:119], v2, s[24:27], s47 offen offset:1024 sc1
v_mfma_f32_16x16x32_bf16 v[112:115], a[244:247], a[0:3], v[112:115]
s_waitcnt lgkmcnt(4)
v_mfma_f32_16x16x32_bf16 v[128:131], a[248:251], a[0:3], v[128:131]
ds_read_b64_tr_b16 a[244:245], v4 offset:14400
v_mfma_f32_16x16x32_bf16 v[132:135], a[248:251], a[4:7], v[132:135]
ds_read_b64_tr_b16 a[246:247], v4 offset:17984
v_mfma_f32_16x16x32_bf16 v[136:139], a[248:251], a[8:11], v[136:139]
v_mfma_f32_16x16x32_bf16 v[140:143], a[248:251], a[12:15], v[140:143]
s_waitcnt lgkmcnt(4)
v_mfma_f32_16x16x32_bf16 v[156:159], a[252:255], a[12:15], v[156:159]
ds_read_b64_tr_b16 a[248:249], v4 offset:14432
v_mfma_f32_16x16x32_bf16 v[152:155], a[252:255], a[8:11], v[152:155]
ds_read_b64_tr_b16 a[250:251], v4 offset:18016
v_mfma_f32_16x16x32_bf16 v[148:151], a[252:255], a[4:7], v[148:151]
v_mfma_f32_16x16x32_bf16 v[144:147], a[252:255], a[0:3], v[144:147]
s_waitcnt lgkmcnt(4)
v_mfma_f32_16x16x32_bf16 v[160:163], a[240:243], a[0:3], v[160:163]
ds_read_b64_tr_b16 a[252:253], v4 offset:14464
v_mfma_f32_16x16x32_bf16 v[164:167], a[240:243], a[4:7], v[164:167]
ds_read_b64_tr_b16 a[254:255], v4 offset:18048
v_mfma_f32_16x16x32_bf16 v[168:171], a[240:243], a[8:11], v[168:171]
buffer_load_dwordx4 a[120:123], v2, s[24:27], s47 offen offset:2048 sc1
v_mfma_f32_16x16x32_bf16 v[172:175], a[240:243], a[12:15], v[172:175]
s_waitcnt lgkmcnt(4)
v_mfma_f32_16x16x32_bf16 v[188:191], a[244:247], a[12:15], v[188:191]
ds_read_b64_tr_b16 a[240:241], v4 offset:14496
v_mfma_f32_16x16x32_bf16 v[184:187], a[244:247], a[8:11], v[184:187]
ds_read_b64_tr_b16 a[242:243], v4 offset:18080
v_mfma_f32_16x16x32_bf16 v[180:183], a[244:247], a[4:7], v[180:183]
s_add_u32 s54, s1, s101
s_and_b32 s54, s54, 3
s_and_b32 s55, s1, 12
v_mfma_f32_16x16x32_bf16 v[176:179], a[244:247], a[0:3], v[176:179]
s_waitcnt lgkmcnt(4)
s_or_b32 s54, s54, s55
s_mul_i32 s54, s54, 458752
v_mfma_f32_16x16x32_bf16 v[192:195], a[248:251], a[0:3], v[192:195]
ds_read_b64_tr_b16 a[244:245], v4 offset:14528
s_add_u32 s54, s54, s48
v_mfma_f32_16x16x32_bf16 v[196:199], a[248:251], a[4:7], v[196:199]
ds_read_b64_tr_b16 a[246:247], v4 offset:18112
s_add_u32 s1, s1, 1
v_mfma_f32_16x16x32_bf16 v[200:203], a[248:251], a[8:11], v[200:203]
s_add_u32 m0, s57, 86016
v_mfma_f32_16x16x32_bf16 v[204:207], a[248:251], a[12:15], v[204:207]
s_waitcnt lgkmcnt(4)
v_mfma_f32_16x16x32_bf16 v[220:223], a[252:255], a[12:15], v[220:223]
ds_read_b64_tr_b16 a[248:249], v4 offset:7168
v_mfma_f32_16x16x32_bf16 v[216:219], a[252:255], a[8:11], v[216:219]
ds_read_b64_tr_b16 a[250:251], v4 offset:10752
v_mfma_f32_16x16x32_bf16 v[212:215], a[252:255], a[4:7], v[212:215]
v_mfma_f32_16x16x32_bf16 v[208:211], a[252:255], a[0:3], v[208:211]
s_waitcnt lgkmcnt(4)
v_mfma_f32_16x16x32_bf16 v[224:227], a[240:243], a[0:3], v[224:227]
ds_read_b64_tr_b16 a[252:253], v4 offset:7200
v_mfma_f32_16x16x32_bf16 v[228:231], a[240:243], a[4:7], v[228:231]
ds_read_b64_tr_b16 a[254:255], v4 offset:10784
v_mfma_f32_16x16x32_bf16 v[232:235], a[240:243], a[8:11], v[232:235]
buffer_load_dwordx4 a[124:127], v2, s[24:27], s47 offen offset:3072 sc1
v_mfma_f32_16x16x32_bf16 v[236:239], a[240:243], a[12:15], v[236:239]
s_waitcnt lgkmcnt(4)
s_add_u32 s47, s2, s101
s_and_b32 s47, s47, 3
v_mfma_f32_16x16x32_bf16 v[252:255], a[244:247], a[12:15], v[252:255]
ds_read_b64_tr_b16 a[240:241], v4 offset:7232
s_and_b32 s56, s2, 12
v_mfma_f32_16x16x32_bf16 v[248:251], a[244:247], a[8:11], v[248:251]
ds_read_b64_tr_b16 a[242:243], v4 offset:10816
s_or_b32 s47, s47, s56
v_mfma_f32_16x16x32_bf16 v[244:247], a[244:247], a[4:7], v[244:247]
s_lshl_b32 s47, s47, 13
s_add_u32 s47, s47, s58
s_add_u32 s2, s2, 1
v_mfma_f32_16x16x32_bf16 v[240:243], a[244:247], a[0:3], v[240:243]
s_waitcnt lgkmcnt(4)
v_mfma_f32_16x16x32_bf16 v[32:35], a[248:251], a[16:19], v[32:35]
ds_read_b64_tr_b16 a[244:245], v4 offset:7264
v_mfma_f32_16x16x32_bf16 v[36:39], a[248:251], a[20:23], v[36:39]
ds_read_b64_tr_b16 a[246:247], v4 offset:10848
v_mfma_f32_16x16x32_bf16 v[40:43], a[248:251], a[24:27], v[40:43]
buffer_load_dwordx4 a[0:3], v2, s[24:27], s47 offen offset:0 sc1
v_mfma_f32_16x16x32_bf16 v[44:47], a[248:251], a[28:31], v[44:47]
s_waitcnt lgkmcnt(4)
v_mfma_f32_16x16x32_bf16 v[60:63], a[252:255], a[28:31], v[60:63]
ds_read_b64_tr_b16 a[248:249], v4 offset:7296
v_mfma_f32_16x16x32_bf16 v[56:59], a[252:255], a[24:27], v[56:59]
ds_read_b64_tr_b16 a[250:251], v4 offset:10880
v_mfma_f32_16x16x32_bf16 v[52:55], a[252:255], a[20:23], v[52:55]
buffer_load_dwordx4 a[4:7], v2, s[24:27], s47 offen offset:1024 sc1
v_mfma_f32_16x16x32_bf16 v[48:51], a[252:255], a[16:19], v[48:51]
s_waitcnt lgkmcnt(4)
v_mfma_f32_16x16x32_bf16 v[64:67], a[240:243], a[16:19], v[64:67]
ds_read_b64_tr_b16 a[252:253], v4 offset:7328
v_mfma_f32_16x16x32_bf16 v[68:71], a[240:243], a[20:23], v[68:71]
ds_read_b64_tr_b16 a[254:255], v4 offset:10912
v_mfma_f32_16x16x32_bf16 v[72:75], a[240:243], a[24:27], v[72:75]
v_mfma_f32_16x16x32_bf16 v[76:79], a[240:243], a[28:31], v[76:79]
s_waitcnt vmcnt(21)
s_barrier
s_waitcnt lgkmcnt(4)
v_mfma_f32_16x16x32_bf16 v[92:95], a[244:247], a[28:31], v[92:95]
ds_read_b64_tr_b16 a[240:241], v4 offset:7360
v_mfma_f32_16x16x32_bf16 v[88:91], a[244:247], a[24:27], v[88:91]
ds_read_b64_tr_b16 a[242:243], v4 offset:10944
v_mfma_f32_16x16x32_bf16 v[84:87], a[244:247], a[20:23], v[84:87]
buffer_load_dwordx4 a[8:11], v2, s[24:27], s47 offen offset:2048 sc1
v_mfma_f32_16x16x32_bf16 v[80:83], a[244:247], a[16:19], v[80:83]
s_waitcnt lgkmcnt(4)
v_mfma_f32_16x16x32_bf16 v[96:99], a[248:251], a[16:19], v[96:99]
ds_read_b64_tr_b16 a[244:245], v4 offset:21504
v_mfma_f32_16x16x32_bf16 v[100:103], a[248:251], a[20:23], v[100:103]
ds_read_b64_tr_b16 a[246:247], v4 offset:25088
v_mfma_f32_16x16x32_bf16 v[104:107], a[248:251], a[24:27], v[104:107]
buffer_load_dwordx4 a[12:15], v2, s[24:27], s47 offen offset:3072 sc1
v_mfma_f32_16x16x32_bf16 v[108:111], a[248:251], a[28:31], v[108:111]
s_waitcnt lgkmcnt(4)
s_add_u32 s47, s47, 4096
v_mfma_f32_16x16x32_bf16 v[124:127], a[252:255], a[28:31], v[124:127]
ds_read_b64_tr_b16 a[248:249], v4 offset:21536
v_mfma_f32_16x16x32_bf16 v[120:123], a[252:255], a[24:27], v[120:123]
ds_read_b64_tr_b16 a[250:251], v4 offset:25120
v_mfma_f32_16x16x32_bf16 v[116:119], a[252:255], a[20:23], v[116:119]
buffer_load_dwordx4 v6, s[28:31], s54 offen lds
v_mfma_f32_16x16x32_bf16 v[112:115], a[252:255], a[16:19], v[112:115]
s_waitcnt lgkmcnt(4)
s_add_u32 m0, s57, 87040
v_mfma_f32_16x16x32_bf16 v[128:131], a[240:243], a[16:19], v[128:131]
ds_read_b64_tr_b16 a[252:253], v4 offset:21568
v_mfma_f32_16x16x32_bf16 v[132:135], a[240:243], a[20:23], v[132:135]
ds_read_b64_tr_b16 a[254:255], v4 offset:25152
v_mfma_f32_16x16x32_bf16 v[136:139], a[240:243], a[24:27], v[136:139]
buffer_load_dwordx4 v7, s[28:31], s54 offen lds
v_mfma_f32_16x16x32_bf16 v[140:143], a[240:243], a[28:31], v[140:143]
s_waitcnt lgkmcnt(4)
s_add_u32 m0, s57, 88064
v_mfma_f32_16x16x32_bf16 v[156:159], a[244:247], a[28:31], v[156:159]
ds_read_b64_tr_b16 a[240:241], v4 offset:21600
v_mfma_f32_16x16x32_bf16 v[152:155], a[244:247], a[24:27], v[152:155]
ds_read_b64_tr_b16 a[242:243], v4 offset:25184
v_mfma_f32_16x16x32_bf16 v[148:151], a[244:247], a[20:23], v[148:151]
buffer_load_dwordx4 v8, s[28:31], s54 offen lds
v_mfma_f32_16x16x32_bf16 v[144:147], a[244:247], a[16:19], v[144:147]
s_waitcnt lgkmcnt(4)
s_add_u32 m0, s57, 89088
v_mfma_f32_16x16x32_bf16 v[160:163], a[248:251], a[16:19], v[160:163]
ds_read_b64_tr_b16 a[244:245], v4 offset:21632
v_mfma_f32_16x16x32_bf16 v[164:167], a[248:251], a[20:23], v[164:167]
ds_read_b64_tr_b16 a[246:247], v4 offset:25216
v_mfma_f32_16x16x32_bf16 v[168:171], a[248:251], a[24:27], v[168:171]
v_mfma_f32_16x16x32_bf16 v[172:175], a[248:251], a[28:31], v[172:175]
s_waitcnt lgkmcnt(4)
v_mfma_f32_16x16x32_bf16 v[188:191], a[252:255], a[28:31], v[188:191]
ds_read_b64_tr_b16 a[248:249], v4 offset:21664
v_mfma_f32_16x16x32_bf16 v[184:187], a[252:255], a[24:27], v[184:187]
ds_read_b64_tr_b16 a[250:251], v4 offset:25248
v_mfma_f32_16x16x32_bf16 v[180:183], a[252:255], a[20:23], v[180:183]
buffer_load_dwordx4 v9, s[28:31], s54 offen lds
v_mfma_f32_16x16x32_bf16 v[176:179], a[252:255], a[16:19], v[176:179]
s_waitcnt lgkmcnt(4)
s_add_u32 m0, s57, 90112
v_mfma_f32_16x16x32_bf16 v[192:195], a[240:243], a[16:19], v[192:195]
ds_read_b64_tr_b16 a[252:253], v4 offset:21696
v_mfma_f32_16x16x32_bf16 v[196:199], a[240:243], a[20:23], v[196:199]
ds_read_b64_tr_b16 a[254:255], v4 offset:25280
v_mfma_f32_16x16x32_bf16 v[200:203], a[240:243], a[24:27], v[200:203]
buffer_load_dwordx4 v10, s[28:31], s54 offen lds
v_mfma_f32_16x16x32_bf16 v[204:207], a[240:243], a[28:31], v[204:207]
s_waitcnt lgkmcnt(4)
s_add_u32 m0, s57, 91136
v_mfma_f32_16x16x32_bf16 v[220:223], a[244:247], a[28:31], v[220:223]
ds_read_b64_tr_b16 a[240:241], v5 offset:0
v_mfma_f32_16x16x32_bf16 v[216:219], a[244:247], a[24:27], v[216:219]
ds_read_b64_tr_b16 a[242:243], v5 offset:3584
v_mfma_f32_16x16x32_bf16 v[212:215], a[244:247], a[20:23], v[212:215]
buffer_load_dwordx4 v11, s[28:31], s54 offen lds
v_mfma_f32_16x16x32_bf16 v[208:211], a[244:247], a[16:19], v[208:211]
s_waitcnt lgkmcnt(4)
s_add_u32 m0, s57, 92160
v_mfma_f32_16x16x32_bf16 v[224:227], a[248:251], a[16:19], v[224:227]
ds_read_b64_tr_b16 a[244:245], v5 offset:32
v_mfma_f32_16x16x32_bf16 v[228:231], a[248:251], a[20:23], v[228:231]
ds_read_b64_tr_b16 a[246:247], v5 offset:3616
v_mfma_f32_16x16x32_bf16 v[232:235], a[248:251], a[24:27], v[232:235]
buffer_load_dwordx4 v12, s[28:31], s54 offen lds
v_mfma_f32_16x16x32_bf16 v[236:239], a[248:251], a[28:31], v[236:239]
s_waitcnt lgkmcnt(4)
v_mfma_f32_16x16x32_bf16 v[252:255], a[252:255], a[28:31], v[252:255]
ds_read_b64_tr_b16 a[248:249], v5 offset:64
v_mfma_f32_16x16x32_bf16 v[248:251], a[252:255], a[24:27], v[248:251]
ds_read_b64_tr_b16 a[250:251], v5 offset:3648
v_mfma_f32_16x16x32_bf16 v[244:247], a[252:255], a[20:23], v[244:247]
v_mfma_f32_16x16x32_bf16 v[240:243], a[252:255], a[16:19], v[240:243]
s_waitcnt lgkmcnt(4)
v_mfma_f32_16x16x32_bf16 v[32:35], a[240:243], a[32:35], v[32:35]
ds_read_b64_tr_b16 a[252:253], v5 offset:96
v_mfma_f32_16x16x32_bf16 v[36:39], a[240:243], a[36:39], v[36:39]
ds_read_b64_tr_b16 a[254:255], v5 offset:3680
v_mfma_f32_16x16x32_bf16 v[40:43], a[240:243], a[40:43], v[40:43]
v_mfma_f32_16x16x32_bf16 v[44:47], a[240:243], a[44:47], v[44:47]
s_waitcnt lgkmcnt(4)
v_mfma_f32_16x16x32_bf16 v[60:63], a[244:247], a[44:47], v[60:63]
ds_read_b64_tr_b16 a[240:241], v5 offset:128
v_mfma_f32_16x16x32_bf16 v[56:59], a[244:247], a[40:43], v[56:59]
ds_read_b64_tr_b16 a[242:243], v5 offset:3712
v_mfma_f32_16x16x32_bf16 v[52:55], a[244:247], a[36:39], v[52:55]
buffer_load_dwordx4 a[16:19], v2, s[24:27], s47 offen offset:0 sc1
v_mfma_f32_16x16x32_bf16 v[48:51], a[244:247], a[32:35], v[48:51]
s_waitcnt lgkmcnt(4)
v_mfma_f32_16x16x32_bf16 v[64:67], a[248:251], a[32:35], v[64:67]
ds_read_b64_tr_b16 a[244:245], v5 offset:160
v_add_u32 v4, 57344, v3
v_mfma_f32_16x16x32_bf16 v[68:71], a[248:251], a[36:39], v[68:71]
ds_read_b64_tr_b16 a[246:247], v5 offset:3744
v_mfma_f32_16x16x32_bf16 v[72:75], a[248:251], a[40:43], v[72:75]
v_mfma_f32_16x16x32_bf16 v[76:79], a[248:251], a[44:47], v[76:79]
s_waitcnt lgkmcnt(4)
v_mfma_f32_16x16x32_bf16 v[92:95], a[252:255], a[44:47], v[92:95]
ds_read_b64_tr_b16 a[248:249], v5 offset:192
v_mfma_f32_16x16x32_bf16 v[88:91], a[252:255], a[40:43], v[88:91]
ds_read_b64_tr_b16 a[250:251], v5 offset:3776
v_mfma_f32_16x16x32_bf16 v[84:87], a[252:255], a[36:39], v[84:87]
v_mfma_f32_16x16x32_bf16 v[80:83], a[252:255], a[32:35], v[80:83]
s_waitcnt lgkmcnt(4)
v_mfma_f32_16x16x32_bf16 v[96:99], a[240:243], a[32:35], v[96:99]
ds_read_b64_tr_b16 a[252:253], v5 offset:14336
v_mfma_f32_16x16x32_bf16 v[100:103], a[240:243], a[36:39], v[100:103]
ds_read_b64_tr_b16 a[254:255], v5 offset:17920
v_mfma_f32_16x16x32_bf16 v[104:107], a[240:243], a[40:43], v[104:107]
v_mfma_f32_16x16x32_bf16 v[108:111], a[240:243], a[44:47], v[108:111]
s_waitcnt lgkmcnt(4)
v_mfma_f32_16x16x32_bf16 v[124:127], a[244:247], a[44:47], v[124:127]
ds_read_b64_tr_b16 a[240:241], v5 offset:14368
v_mfma_f32_16x16x32_bf16 v[120:123], a[244:247], a[40:43], v[120:123]
ds_read_b64_tr_b16 a[242:243], v5 offset:17952
v_mfma_f32_16x16x32_bf16 v[116:119], a[244:247], a[36:39], v[116:119]
buffer_load_dwordx4 a[20:23], v2, s[24:27], s47 offen offset:1024 sc1
v_mfma_f32_16x16x32_bf16 v[112:115], a[244:247], a[32:35], v[112:115]
s_waitcnt lgkmcnt(4)
v_mfma_f32_16x16x32_bf16 v[128:131], a[248:251], a[32:35], v[128:131]
ds_read_b64_tr_b16 a[244:245], v5 offset:14400
v_mfma_f32_16x16x32_bf16 v[132:135], a[248:251], a[36:39], v[132:135]
ds_read_b64_tr_b16 a[246:247], v5 offset:17984
v_mfma_f32_16x16x32_bf16 v[136:139], a[248:251], a[40:43], v[136:139]
v_mfma_f32_16x16x32_bf16 v[140:143], a[248:251], a[44:47], v[140:143]
s_waitcnt lgkmcnt(4)
v_mfma_f32_16x16x32_bf16 v[156:159], a[252:255], a[44:47], v[156:159]
ds_read_b64_tr_b16 a[248:249], v5 offset:14432
v_mfma_f32_16x16x32_bf16 v[152:155], a[252:255], a[40:43], v[152:155]
ds_read_b64_tr_b16 a[250:251], v5 offset:18016
v_mfma_f32_16x16x32_bf16 v[148:151], a[252:255], a[36:39], v[148:151]
v_mfma_f32_16x16x32_bf16 v[144:147], a[252:255], a[32:35], v[144:147]
s_waitcnt lgkmcnt(4)
v_mfma_f32_16x16x32_bf16 v[160:163], a[240:243], a[32:35], v[160:163]
ds_read_b64_tr_b16 a[252:253], v5 offset:14464
v_mfma_f32_16x16x32_bf16 v[164:167], a[240:243], a[36:39], v[164:167]
ds_read_b64_tr_b16 a[254:255], v5 offset:18048
v_mfma_f32_16x16x32_bf16 v[168:171], a[240:243], a[40:43], v[168:171]
buffer_load_dwordx4 a[24:27], v2, s[24:27], s47 offen offset:2048 sc1
v_mfma_f32_16x16x32_bf16 v[172:175], a[240:243], a[44:47], v[172:175]
s_waitcnt lgkmcnt(4)
v_mfma_f32_16x16x32_bf16 v[188:191], a[244:247], a[44:47], v[188:191]
ds_read_b64_tr_b16 a[240:241], v5 offset:14496
v_mfma_f32_16x16x32_bf16 v[184:187], a[244:247], a[40:43], v[184:187]
ds_read_b64_tr_b16 a[242:243], v5 offset:18080
v_mfma_f32_16x16x32_bf16 v[180:183], a[244:247], a[36:39], v[180:183]
s_add_u32 s54, s1, s101
s_and_b32 s54, s54, 3
s_and_b32 s55, s1, 12
v_mfma_f32_16x16x32_bf16 v[176:179], a[244:247], a[32:35], v[176:179]
s_waitcnt lgkmcnt(4)
s_or_b32 s54, s54, s55
s_mul_i32 s54, s54, 458752
v_mfma_f32_16x16x32_bf16 v[192:195], a[248:251], a[32:35], v[192:195]
ds_read_b64_tr_b16 a[244:245], v5 offset:14528
s_add_u32 s54, s54, s48
v_mfma_f32_16x16x32_bf16 v[196:199], a[248:251], a[36:39], v[196:199]
ds_read_b64_tr_b16 a[246:247], v5 offset:18112
s_add_u32 s1, s1, 1
v_mfma_f32_16x16x32_bf16 v[200:203], a[248:251], a[40:43], v[200:203]
s_add_u32 m0, s57, 0
v_mfma_f32_16x16x32_bf16 v[204:207], a[248:251], a[44:47], v[204:207]
s_waitcnt lgkmcnt(4)
v_mfma_f32_16x16x32_bf16 v[220:223], a[252:255], a[44:47], v[220:223]
ds_read_b64_tr_b16 a[248:249], v5 offset:7168
v_mfma_f32_16x16x32_bf16 v[216:219], a[252:255], a[40:43], v[216:219]
ds_read_b64_tr_b16 a[250:251], v5 offset:10752
v_mfma_f32_16x16x32_bf16 v[212:215], a[252:255], a[36:39], v[212:215]
v_mfma_f32_16x16x32_bf16 v[208:211], a[252:255], a[32:35], v[208:211]
s_waitcnt lgkmcnt(4)
v_mfma_f32_16x16x32_bf16 v[224:227], a[240:243], a[32:35], v[224:227]
ds_read_b64_tr_b16 a[252:253], v5 offset:7200
v_mfma_f32_16x16x32_bf16 v[228:231], a[240:243], a[36:39], v[228:231]
ds_read_b64_tr_b16 a[254:255], v5 offset:10784
v_mfma_f32_16x16x32_bf16 v[232:235], a[240:243], a[40:43], v[232:235]
buffer_load_dwordx4 a[28:31], v2, s[24:27], s47 offen offset:3072 sc1
v_mfma_f32_16x16x32_bf16 v[236:239], a[240:243], a[44:47], v[236:239]
s_waitcnt lgkmcnt(4)
s_add_u32 s47, s2, s101
s_and_b32 s47, s47, 3
v_mfma_f32_16x16x32_bf16 v[252:255], a[244:247], a[44:47], v[252:255]
ds_read_b64_tr_b16 a[240:241], v5 offset:7232
s_and_b32 s56, s2, 12
v_mfma_f32_16x16x32_bf16 v[248:251], a[244:247], a[40:43], v[248:251]
ds_read_b64_tr_b16 a[242:243], v5 offset:10816
s_or_b32 s47, s47, s56
v_mfma_f32_16x16x32_bf16 v[244:247], a[244:247], a[36:39], v[244:247]
s_lshl_b32 s47, s47, 13
s_add_u32 s47, s47, s58
s_add_u32 s2, s2, 1
v_mfma_f32_16x16x32_bf16 v[240:243], a[244:247], a[32:35], v[240:243]
s_waitcnt lgkmcnt(4)
v_mfma_f32_16x16x32_bf16 v[32:35], a[248:251], a[48:51], v[32:35]
ds_read_b64_tr_b16 a[244:245], v5 offset:7264
v_mfma_f32_16x16x32_bf16 v[36:39], a[248:251], a[52:55], v[36:39]
ds_read_b64_tr_b16 a[246:247], v5 offset:10848
v_mfma_f32_16x16x32_bf16 v[40:43], a[248:251], a[56:59], v[40:43]
buffer_load_dwordx4 a[32:35], v2, s[24:27], s47 offen offset:0 sc1
v_mfma_f32_16x16x32_bf16 v[44:47], a[248:251], a[60:63], v[44:47]
s_waitcnt lgkmcnt(4)
v_mfma_f32_16x16x32_bf16 v[60:63], a[252:255], a[60:63], v[60:63]
ds_read_b64_tr_b16 a[248:249], v5 offset:7296
v_mfma_f32_16x16x32_bf16 v[56:59], a[252:255], a[56:59], v[56:59]
ds_read_b64_tr_b16 a[250:251], v5 offset:10880
v_mfma_f32_16x16x32_bf16 v[52:55], a[252:255], a[52:55], v[52:55]
buffer_load_dwordx4 a[36:39], v2, s[24:27], s47 offen offset:1024 sc1
v_mfma_f32_16x16x32_bf16 v[48:51], a[252:255], a[48:51], v[48:51]
s_waitcnt lgkmcnt(4)
v_mfma_f32_16x16x32_bf16 v[64:67], a[240:243], a[48:51], v[64:67]
ds_read_b64_tr_b16 a[252:253], v5 offset:7328
v_mfma_f32_16x16x32_bf16 v[68:71], a[240:243], a[52:55], v[68:71]
ds_read_b64_tr_b16 a[254:255], v5 offset:10912
v_mfma_f32_16x16x32_bf16 v[72:75], a[240:243], a[56:59], v[72:75]
v_mfma_f32_16x16x32_bf16 v[76:79], a[240:243], a[60:63], v[76:79]
s_waitcnt vmcnt(21)
s_barrier
s_waitcnt lgkmcnt(4)
v_mfma_f32_16x16x32_bf16 v[92:95], a[244:247], a[60:63], v[92:95]
ds_read_b64_tr_b16 a[240:241], v5 offset:7360
v_mfma_f32_16x16x32_bf16 v[88:91], a[244:247], a[56:59], v[88:91]
ds_read_b64_tr_b16 a[242:243], v5 offset:10944
v_mfma_f32_16x16x32_bf16 v[84:87], a[244:247], a[52:55], v[84:87]
buffer_load_dwordx4 a[40:43], v2, s[24:27], s47 offen offset:2048 sc1
v_mfma_f32_16x16x32_bf16 v[80:83], a[244:247], a[48:51], v[80:83]
s_waitcnt lgkmcnt(4)
v_mfma_f32_16x16x32_bf16 v[96:99], a[248:251], a[48:51], v[96:99]
ds_read_b64_tr_b16 a[244:245], v5 offset:21504
v_mfma_f32_16x16x32_bf16 v[100:103], a[248:251], a[52:55], v[100:103]
ds_read_b64_tr_b16 a[246:247], v5 offset:25088
v_mfma_f32_16x16x32_bf16 v[104:107], a[248:251], a[56:59], v[104:107]
buffer_load_dwordx4 a[44:47], v2, s[24:27], s47 offen offset:3072 sc1
v_mfma_f32_16x16x32_bf16 v[108:111], a[248:251], a[60:63], v[108:111]
s_waitcnt lgkmcnt(4)
s_add_u32 s47, s47, 4096
v_mfma_f32_16x16x32_bf16 v[124:127], a[252:255], a[60:63], v[124:127]
ds_read_b64_tr_b16 a[248:249], v5 offset:21536
v_mfma_f32_16x16x32_bf16 v[120:123], a[252:255], a[56:59], v[120:123]
ds_read_b64_tr_b16 a[250:251], v5 offset:25120
v_mfma_f32_16x16x32_bf16 v[116:119], a[252:255], a[52:55], v[116:119]
buffer_load_dwordx4 v6, s[28:31], s54 offen lds
v_mfma_f32_16x16x32_bf16 v[112:115], a[252:255], a[48:51], v[112:115]
s_waitcnt lgkmcnt(4)
s_add_u32 m0, s57, 1024
v_mfma_f32_16x16x32_bf16 v[128:131], a[240:243], a[48:51], v[128:131]
ds_read_b64_tr_b16 a[252:253], v5 offset:21568
v_mfma_f32_16x16x32_bf16 v[132:135], a[240:243], a[52:55], v[132:135]
ds_read_b64_tr_b16 a[254:255], v5 offset:25152
v_mfma_f32_16x16x32_bf16 v[136:139], a[240:243], a[56:59], v[136:139]
buffer_load_dwordx4 v7, s[28:31], s54 offen lds
v_mfma_f32_16x16x32_bf16 v[140:143], a[240:243], a[60:63], v[140:143]
s_waitcnt lgkmcnt(4)
s_add_u32 m0, s57, 2048
v_mfma_f32_16x16x32_bf16 v[156:159], a[244:247], a[60:63], v[156:159]
ds_read_b64_tr_b16 a[240:241], v5 offset:21600
v_mfma_f32_16x16x32_bf16 v[152:155], a[244:247], a[56:59], v[152:155]
ds_read_b64_tr_b16 a[242:243], v5 offset:25184
v_mfma_f32_16x16x32_bf16 v[148:151], a[244:247], a[52:55], v[148:151]
buffer_load_dwordx4 v8, s[28:31], s54 offen lds
v_mfma_f32_16x16x32_bf16 v[144:147], a[244:247], a[48:51], v[144:147]
s_waitcnt lgkmcnt(4)
s_add_u32 m0, s57, 3072
v_mfma_f32_16x16x32_bf16 v[160:163], a[248:251], a[48:51], v[160:163]
ds_read_b64_tr_b16 a[244:245], v5 offset:21632
v_mfma_f32_16x16x32_bf16 v[164:167], a[248:251], a[52:55], v[164:167]
ds_read_b64_tr_b16 a[246:247], v5 offset:25216
v_mfma_f32_16x16x32_bf16 v[168:171], a[248:251], a[56:59], v[168:171]
v_mfma_f32_16x16x32_bf16 v[172:175], a[248:251], a[60:63], v[172:175]
s_waitcnt lgkmcnt(4)
v_mfma_f32_16x16x32_bf16 v[188:191], a[252:255], a[60:63], v[188:191]
ds_read_b64_tr_b16 a[248:249], v5 offset:21664
v_mfma_f32_16x16x32_bf16 v[184:187], a[252:255], a[56:59], v[184:187]
ds_read_b64_tr_b16 a[250:251], v5 offset:25248
v_mfma_f32_16x16x32_bf16 v[180:183], a[252:255], a[52:55], v[180:183]
buffer_load_dwordx4 v9, s[28:31], s54 offen lds
v_mfma_f32_16x16x32_bf16 v[176:179], a[252:255], a[48:51], v[176:179]
s_waitcnt lgkmcnt(4)
s_add_u32 m0, s57, 4096
v_mfma_f32_16x16x32_bf16 v[192:195], a[240:243], a[48:51], v[192:195]
ds_read_b64_tr_b16 a[252:253], v5 offset:21696
v_mfma_f32_16x16x32_bf16 v[196:199], a[240:243], a[52:55], v[196:199]
ds_read_b64_tr_b16 a[254:255], v5 offset:25280
v_mfma_f32_16x16x32_bf16 v[200:203], a[240:243], a[56:59], v[200:203]
buffer_load_dwordx4 v10, s[28:31], s54 offen lds
v_mfma_f32_16x16x32_bf16 v[204:207], a[240:243], a[60:63], v[204:207]
s_waitcnt lgkmcnt(4)
s_add_u32 m0, s57, 5120
v_mfma_f32_16x16x32_bf16 v[220:223], a[244:247], a[60:63], v[220:223]
ds_read_b64_tr_b16 a[240:241], v4 offset:0
v_mfma_f32_16x16x32_bf16 v[216:219], a[244:247], a[56:59], v[216:219]
ds_read_b64_tr_b16 a[242:243], v4 offset:3584
v_mfma_f32_16x16x32_bf16 v[212:215], a[244:247], a[52:55], v[212:215]
buffer_load_dwordx4 v11, s[28:31], s54 offen lds
v_mfma_f32_16x16x32_bf16 v[208:211], a[244:247], a[48:51], v[208:211]
s_waitcnt lgkmcnt(4)
s_add_u32 m0, s57, 6144
v_mfma_f32_16x16x32_bf16 v[224:227], a[248:251], a[48:51], v[224:227]
ds_read_b64_tr_b16 a[244:245], v4 offset:32
v_mfma_f32_16x16x32_bf16 v[228:231], a[248:251], a[52:55], v[228:231]
ds_read_b64_tr_b16 a[246:247], v4 offset:3616
v_mfma_f32_16x16x32_bf16 v[232:235], a[248:251], a[56:59], v[232:235]
buffer_load_dwordx4 v12, s[28:31], s54 offen lds
v_mfma_f32_16x16x32_bf16 v[236:239], a[248:251], a[60:63], v[236:239]
s_waitcnt lgkmcnt(4)
v_mfma_f32_16x16x32_bf16 v[252:255], a[252:255], a[60:63], v[252:255]
ds_read_b64_tr_b16 a[248:249], v4 offset:64
v_mfma_f32_16x16x32_bf16 v[248:251], a[252:255], a[56:59], v[248:251]
ds_read_b64_tr_b16 a[250:251], v4 offset:3648
v_mfma_f32_16x16x32_bf16 v[244:247], a[252:255], a[52:55], v[244:247]
v_mfma_f32_16x16x32_bf16 v[240:243], a[252:255], a[48:51], v[240:243]
s_waitcnt lgkmcnt(4)
v_mfma_f32_16x16x32_bf16 v[32:35], a[240:243], a[64:67], v[32:35]
ds_read_b64_tr_b16 a[252:253], v4 offset:96
v_mfma_f32_16x16x32_bf16 v[36:39], a[240:243], a[68:71], v[36:39]
ds_read_b64_tr_b16 a[254:255], v4 offset:3680
v_mfma_f32_16x16x32_bf16 v[40:43], a[240:243], a[72:75], v[40:43]
v_mfma_f32_16x16x32_bf16 v[44:47], a[240:243], a[76:79], v[44:47]
s_waitcnt lgkmcnt(4)
v_mfma_f32_16x16x32_bf16 v[60:63], a[244:247], a[76:79], v[60:63]
ds_read_b64_tr_b16 a[240:241], v4 offset:128
v_mfma_f32_16x16x32_bf16 v[56:59], a[244:247], a[72:75], v[56:59]
ds_read_b64_tr_b16 a[242:243], v4 offset:3712
v_mfma_f32_16x16x32_bf16 v[52:55], a[244:247], a[68:71], v[52:55]
buffer_load_dwordx4 a[48:51], v2, s[24:27], s47 offen offset:0 sc1
v_mfma_f32_16x16x32_bf16 v[48:51], a[244:247], a[64:67], v[48:51]
s_waitcnt lgkmcnt(4)
v_mfma_f32_16x16x32_bf16 v[64:67], a[248:251], a[64:67], v[64:67]
ds_read_b64_tr_b16 a[244:245], v4 offset:160
v_add_u32 v5, 86016, v3
v_mfma_f32_16x16x32_bf16 v[68:71], a[248:251], a[68:71], v[68:71]
ds_read_b64_tr_b16 a[246:247], v4 offset:3744
v_mfma_f32_16x16x32_bf16 v[72:75], a[248:251], a[72:75], v[72:75]
v_mfma_f32_16x16x32_bf16 v[76:79], a[248:251], a[76:79], v[76:79]
s_waitcnt lgkmcnt(4)
v_mfma_f32_16x16x32_bf16 v[92:95], a[252:255], a[76:79], v[92:95]
ds_read_b64_tr_b16 a[248:249], v4 offset:192
v_mfma_f32_16x16x32_bf16 v[88:91], a[252:255], a[72:75], v[88:91]
ds_read_b64_tr_b16 a[250:251], v4 offset:3776
v_mfma_f32_16x16x32_bf16 v[84:87], a[252:255], a[68:71], v[84:87]
v_mfma_f32_16x16x32_bf16 v[80:83], a[252:255], a[64:67], v[80:83]
s_waitcnt lgkmcnt(4)
v_mfma_f32_16x16x32_bf16 v[96:99], a[240:243], a[64:67], v[96:99]
ds_read_b64_tr_b16 a[252:253], v4 offset:14336
v_mfma_f32_16x16x32_bf16 v[100:103], a[240:243], a[68:71], v[100:103]
ds_read_b64_tr_b16 a[254:255], v4 offset:17920
v_mfma_f32_16x16x32_bf16 v[104:107], a[240:243], a[72:75], v[104:107]
v_mfma_f32_16x16x32_bf16 v[108:111], a[240:243], a[76:79], v[108:111]
s_waitcnt lgkmcnt(4)
v_mfma_f32_16x16x32_bf16 v[124:127], a[244:247], a[76:79], v[124:127]
ds_read_b64_tr_b16 a[240:241], v4 offset:14368
v_mfma_f32_16x16x32_bf16 v[120:123], a[244:247], a[72:75], v[120:123]
ds_read_b64_tr_b16 a[242:243], v4 offset:17952
v_mfma_f32_16x16x32_bf16 v[116:119], a[244:247], a[68:71], v[116:119]
buffer_load_dwordx4 a[52:55], v2, s[24:27], s47 offen offset:1024 sc1
v_mfma_f32_16x16x32_bf16 v[112:115], a[244:247], a[64:67], v[112:115]
s_waitcnt lgkmcnt(4)
v_mfma_f32_16x16x32_bf16 v[128:131], a[248:251], a[64:67], v[128:131]
ds_read_b64_tr_b16 a[244:245], v4 offset:14400
v_mfma_f32_16x16x32_bf16 v[132:135], a[248:251], a[68:71], v[132:135]
ds_read_b64_tr_b16 a[246:247], v4 offset:17984
v_mfma_f32_16x16x32_bf16 v[136:139], a[248:251], a[72:75], v[136:139]
v_mfma_f32_16x16x32_bf16 v[140:143], a[248:251], a[76:79], v[140:143]
s_waitcnt lgkmcnt(4)
v_mfma_f32_16x16x32_bf16 v[156:159], a[252:255], a[76:79], v[156:159]
ds_read_b64_tr_b16 a[248:249], v4 offset:14432
v_mfma_f32_16x16x32_bf16 v[152:155], a[252:255], a[72:75], v[152:155]
ds_read_b64_tr_b16 a[250:251], v4 offset:18016
v_mfma_f32_16x16x32_bf16 v[148:151], a[252:255], a[68:71], v[148:151]
v_mfma_f32_16x16x32_bf16 v[144:147], a[252:255], a[64:67], v[144:147]
s_waitcnt lgkmcnt(4)
v_mfma_f32_16x16x32_bf16 v[160:163], a[240:243], a[64:67], v[160:163]
ds_read_b64_tr_b16 a[252:253], v4 offset:14464
v_mfma_f32_16x16x32_bf16 v[164:167], a[240:243], a[68:71], v[164:167]
ds_read_b64_tr_b16 a[254:255], v4 offset:18048
v_mfma_f32_16x16x32_bf16 v[168:171], a[240:243], a[72:75], v[168:171]
buffer_load_dwordx4 a[56:59], v2, s[24:27], s47 offen offset:2048 sc1
v_mfma_f32_16x16x32_bf16 v[172:175], a[240:243], a[76:79], v[172:175]
s_waitcnt lgkmcnt(4)
v_mfma_f32_16x16x32_bf16 v[188:191], a[244:247], a[76:79], v[188:191]
ds_read_b64_tr_b16 a[240:241], v4 offset:14496
v_mfma_f32_16x16x32_bf16 v[184:187], a[244:247], a[72:75], v[184:187]
ds_read_b64_tr_b16 a[242:243], v4 offset:18080
v_mfma_f32_16x16x32_bf16 v[180:183], a[244:247], a[68:71], v[180:183]
s_add_u32 s54, s1, s101
s_and_b32 s54, s54, 3
s_and_b32 s55, s1, 12
v_mfma_f32_16x16x32_bf16 v[176:179], a[244:247], a[64:67], v[176:179]
s_waitcnt lgkmcnt(4)
s_or_b32 s54, s54, s55
s_mul_i32 s54, s54, 458752
v_mfma_f32_16x16x32_bf16 v[192:195], a[248:251], a[64:67], v[192:195]
ds_read_b64_tr_b16 a[244:245], v4 offset:14528
s_add_u32 s54, s54, s48
v_mfma_f32_16x16x32_bf16 v[196:199], a[248:251], a[68:71], v[196:199]
ds_read_b64_tr_b16 a[246:247], v4 offset:18112
s_add_u32 s1, s1, 1
v_mfma_f32_16x16x32_bf16 v[200:203], a[248:251], a[72:75], v[200:203]
s_add_u32 m0, s57, 28672
v_mfma_f32_16x16x32_bf16 v[204:207], a[248:251], a[76:79], v[204:207]
s_waitcnt lgkmcnt(4)
v_mfma_f32_16x16x32_bf16 v[220:223], a[252:255], a[76:79], v[220:223]
ds_read_b64_tr_b16 a[248:249], v4 offset:7168
v_mfma_f32_16x16x32_bf16 v[216:219], a[252:255], a[72:75], v[216:219]
ds_read_b64_tr_b16 a[250:251], v4 offset:10752
v_mfma_f32_16x16x32_bf16 v[212:215], a[252:255], a[68:71], v[212:215]
v_mfma_f32_16x16x32_bf16 v[208:211], a[252:255], a[64:67], v[208:211]
s_waitcnt lgkmcnt(4)
v_mfma_f32_16x16x32_bf16 v[224:227], a[240:243], a[64:67], v[224:227]
ds_read_b64_tr_b16 a[252:253], v4 offset:7200
v_mfma_f32_16x16x32_bf16 v[228:231], a[240:243], a[68:71], v[228:231]
ds_read_b64_tr_b16 a[254:255], v4 offset:10784
v_mfma_f32_16x16x32_bf16 v[232:235], a[240:243], a[72:75], v[232:235]
buffer_load_dwordx4 a[60:63], v2, s[24:27], s47 offen offset:3072 sc1
v_mfma_f32_16x16x32_bf16 v[236:239], a[240:243], a[76:79], v[236:239]
s_waitcnt lgkmcnt(4)
s_add_u32 s47, s2, s101
s_and_b32 s47, s47, 3
v_mfma_f32_16x16x32_bf16 v[252:255], a[244:247], a[76:79], v[252:255]
ds_read_b64_tr_b16 a[240:241], v4 offset:7232
s_and_b32 s56, s2, 12
v_mfma_f32_16x16x32_bf16 v[248:251], a[244:247], a[72:75], v[248:251]
ds_read_b64_tr_b16 a[242:243], v4 offset:10816
s_or_b32 s47, s47, s56
v_mfma_f32_16x16x32_bf16 v[244:247], a[244:247], a[68:71], v[244:247]
s_lshl_b32 s47, s47, 13
s_add_u32 s47, s47, s58
s_add_u32 s2, s2, 1
v_mfma_f32_16x16x32_bf16 v[240:243], a[244:247], a[64:67], v[240:243]
s_waitcnt lgkmcnt(4)
v_mfma_f32_16x16x32_bf16 v[32:35], a[248:251], a[80:83], v[32:35]
ds_read_b64_tr_b16 a[244:245], v4 offset:7264
v_mfma_f32_16x16x32_bf16 v[36:39], a[248:251], a[84:87], v[36:39]
ds_read_b64_tr_b16 a[246:247], v4 offset:10848
v_mfma_f32_16x16x32_bf16 v[40:43], a[248:251], a[88:91], v[40:43]
buffer_load_dwordx4 a[64:67], v2, s[24:27], s47 offen offset:0 sc1
v_mfma_f32_16x16x32_bf16 v[44:47], a[248:251], a[92:95], v[44:47]
s_waitcnt lgkmcnt(4)
v_mfma_f32_16x16x32_bf16 v[60:63], a[252:255], a[92:95], v[60:63]
ds_read_b64_tr_b16 a[248:249], v4 offset:7296
v_mfma_f32_16x16x32_bf16 v[56:59], a[252:255], a[88:91], v[56:59]
ds_read_b64_tr_b16 a[250:251], v4 offset:10880
v_mfma_f32_16x16x32_bf16 v[52:55], a[252:255], a[84:87], v[52:55]
buffer_load_dwordx4 a[68:71], v2, s[24:27], s47 offen offset:1024 sc1
v_mfma_f32_16x16x32_bf16 v[48:51], a[252:255], a[80:83], v[48:51]
s_waitcnt lgkmcnt(4)
v_mfma_f32_16x16x32_bf16 v[64:67], a[240:243], a[80:83], v[64:67]
ds_read_b64_tr_b16 a[252:253], v4 offset:7328
v_mfma_f32_16x16x32_bf16 v[68:71], a[240:243], a[84:87], v[68:71]
ds_read_b64_tr_b16 a[254:255], v4 offset:10912
v_mfma_f32_16x16x32_bf16 v[72:75], a[240:243], a[88:91], v[72:75]
v_mfma_f32_16x16x32_bf16 v[76:79], a[240:243], a[92:95], v[76:79]
s_waitcnt vmcnt(21)
s_barrier
s_waitcnt lgkmcnt(4)
v_mfma_f32_16x16x32_bf16 v[92:95], a[244:247], a[92:95], v[92:95]
ds_read_b64_tr_b16 a[240:241], v4 offset:7360
v_mfma_f32_16x16x32_bf16 v[88:91], a[244:247], a[88:91], v[88:91]
ds_read_b64_tr_b16 a[242:243], v4 offset:10944
v_mfma_f32_16x16x32_bf16 v[84:87], a[244:247], a[84:87], v[84:87]
buffer_load_dwordx4 a[72:75], v2, s[24:27], s47 offen offset:2048 sc1
v_mfma_f32_16x16x32_bf16 v[80:83], a[244:247], a[80:83], v[80:83]
s_waitcnt lgkmcnt(4)
v_mfma_f32_16x16x32_bf16 v[96:99], a[248:251], a[80:83], v[96:99]
ds_read_b64_tr_b16 a[244:245], v4 offset:21504
v_mfma_f32_16x16x32_bf16 v[100:103], a[248:251], a[84:87], v[100:103]
ds_read_b64_tr_b16 a[246:247], v4 offset:25088
v_mfma_f32_16x16x32_bf16 v[104:107], a[248:251], a[88:91], v[104:107]
buffer_load_dwordx4 a[76:79], v2, s[24:27], s47 offen offset:3072 sc1
v_mfma_f32_16x16x32_bf16 v[108:111], a[248:251], a[92:95], v[108:111]
s_waitcnt lgkmcnt(4)
s_add_u32 s47, s47, 4096
v_mfma_f32_16x16x32_bf16 v[124:127], a[252:255], a[92:95], v[124:127]
ds_read_b64_tr_b16 a[248:249], v4 offset:21536
v_mfma_f32_16x16x32_bf16 v[120:123], a[252:255], a[88:91], v[120:123]
ds_read_b64_tr_b16 a[250:251], v4 offset:25120
v_mfma_f32_16x16x32_bf16 v[116:119], a[252:255], a[84:87], v[116:119]
buffer_load_dwordx4 v6, s[28:31], s54 offen lds
v_mfma_f32_16x16x32_bf16 v[112:115], a[252:255], a[80:83], v[112:115]
s_waitcnt lgkmcnt(4)
s_add_u32 m0, s57, 29696
v_mfma_f32_16x16x32_bf16 v[128:131], a[240:243], a[80:83], v[128:131]
ds_read_b64_tr_b16 a[252:253], v4 offset:21568
v_mfma_f32_16x16x32_bf16 v[132:135], a[240:243], a[84:87], v[132:135]
ds_read_b64_tr_b16 a[254:255], v4 offset:25152
v_mfma_f32_16x16x32_bf16 v[136:139], a[240:243], a[88:91], v[136:139]
buffer_load_dwordx4 v7, s[28:31], s54 offen lds
v_mfma_f32_16x16x32_bf16 v[140:143], a[240:243], a[92:95], v[140:143]
s_waitcnt lgkmcnt(4)
s_add_u32 m0, s57, 30720
v_mfma_f32_16x16x32_bf16 v[156:159], a[244:247], a[92:95], v[156:159]
ds_read_b64_tr_b16 a[240:241], v4 offset:21600
v_mfma_f32_16x16x32_bf16 v[152:155], a[244:247], a[88:91], v[152:155]
ds_read_b64_tr_b16 a[242:243], v4 offset:25184
v_mfma_f32_16x16x32_bf16 v[148:151], a[244:247], a[84:87], v[148:151]
buffer_load_dwordx4 v8, s[28:31], s54 offen lds
v_mfma_f32_16x16x32_bf16 v[144:147], a[244:247], a[80:83], v[144:147]
s_waitcnt lgkmcnt(4)
s_add_u32 m0, s57, 31744
v_mfma_f32_16x16x32_bf16 v[160:163], a[248:251], a[80:83], v[160:163]
ds_read_b64_tr_b16 a[244:245], v4 offset:21632
v_mfma_f32_16x16x32_bf16 v[164:167], a[248:251], a[84:87], v[164:167]
ds_read_b64_tr_b16 a[246:247], v4 offset:25216
v_mfma_f32_16x16x32_bf16 v[168:171], a[248:251], a[88:91], v[168:171]
v_mfma_f32_16x16x32_bf16 v[172:175], a[248:251], a[92:95], v[172:175]
s_waitcnt lgkmcnt(4)
v_mfma_f32_16x16x32_bf16 v[188:191], a[252:255], a[92:95], v[188:191]
ds_read_b64_tr_b16 a[248:249], v4 offset:21664
v_mfma_f32_16x16x32_bf16 v[184:187], a[252:255], a[88:91], v[184:187]
ds_read_b64_tr_b16 a[250:251], v4 offset:25248
v_mfma_f32_16x16x32_bf16 v[180:183], a[252:255], a[84:87], v[180:183]
buffer_load_dwordx4 v9, s[28:31], s54 offen lds
v_mfma_f32_16x16x32_bf16 v[176:179], a[252:255], a[80:83], v[176:179]
s_waitcnt lgkmcnt(4)
s_add_u32 m0, s57, 32768
v_mfma_f32_16x16x32_bf16 v[192:195], a[240:243], a[80:83], v[192:195]
ds_read_b64_tr_b16 a[252:253], v4 offset:21696
v_mfma_f32_16x16x32_bf16 v[196:199], a[240:243], a[84:87], v[196:199]
ds_read_b64_tr_b16 a[254:255], v4 offset:25280
v_mfma_f32_16x16x32_bf16 v[200:203], a[240:243], a[88:91], v[200:203]
buffer_load_dwordx4 v10, s[28:31], s54 offen lds
v_mfma_f32_16x16x32_bf16 v[204:207], a[240:243], a[92:95], v[204:207]
s_waitcnt lgkmcnt(4)
s_add_u32 m0, s57, 33792
v_mfma_f32_16x16x32_bf16 v[220:223], a[244:247], a[92:95], v[220:223]
ds_read_b64_tr_b16 a[240:241], v5 offset:0
v_mfma_f32_16x16x32_bf16 v[216:219], a[244:247], a[88:91], v[216:219]
ds_read_b64_tr_b16 a[242:243], v5 offset:3584
v_mfma_f32_16x16x32_bf16 v[212:215], a[244:247], a[84:87], v[212:215]
buffer_load_dwordx4 v11, s[28:31], s54 offen lds
v_mfma_f32_16x16x32_bf16 v[208:211], a[244:247], a[80:83], v[208:211]
s_waitcnt lgkmcnt(4)
s_add_u32 m0, s57, 34816
v_mfma_f32_16x16x32_bf16 v[224:227], a[248:251], a[80:83], v[224:227]
ds_read_b64_tr_b16 a[244:245], v5 offset:32
v_mfma_f32_16x16x32_bf16 v[228:231], a[248:251], a[84:87], v[228:231]
ds_read_b64_tr_b16 a[246:247], v5 offset:3616
v_mfma_f32_16x16x32_bf16 v[232:235], a[248:251], a[88:91], v[232:235]
buffer_load_dwordx4 v12, s[28:31], s54 offen lds
v_mfma_f32_16x16x32_bf16 v[236:239], a[248:251], a[92:95], v[236:239]
s_waitcnt lgkmcnt(4)
v_mfma_f32_16x16x32_bf16 v[252:255], a[252:255], a[92:95], v[252:255]
ds_read_b64_tr_b16 a[248:249], v5 offset:64
v_mfma_f32_16x16x32_bf16 v[248:251], a[252:255], a[88:91], v[248:251]
ds_read_b64_tr_b16 a[250:251], v5 offset:3648
v_mfma_f32_16x16x32_bf16 v[244:247], a[252:255], a[84:87], v[244:247]
v_mfma_f32_16x16x32_bf16 v[240:243], a[252:255], a[80:83], v[240:243]
s_waitcnt lgkmcnt(4)
v_mfma_f32_16x16x32_bf16 v[32:35], a[240:243], a[96:99], v[32:35]
ds_read_b64_tr_b16 a[252:253], v5 offset:96
v_mfma_f32_16x16x32_bf16 v[36:39], a[240:243], a[100:103], v[36:39]
ds_read_b64_tr_b16 a[254:255], v5 offset:3680
v_mfma_f32_16x16x32_bf16 v[40:43], a[240:243], a[104:107], v[40:43]
v_mfma_f32_16x16x32_bf16 v[44:47], a[240:243], a[108:111], v[44:47]
s_waitcnt lgkmcnt(4)
v_mfma_f32_16x16x32_bf16 v[60:63], a[244:247], a[108:111], v[60:63]
ds_read_b64_tr_b16 a[240:241], v5 offset:128
v_mfma_f32_16x16x32_bf16 v[56:59], a[244:247], a[104:107], v[56:59]
ds_read_b64_tr_b16 a[242:243], v5 offset:3712
v_mfma_f32_16x16x32_bf16 v[52:55], a[244:247], a[100:103], v[52:55]
buffer_load_dwordx4 a[80:83], v2, s[24:27], s47 offen offset:0 sc1
v_mfma_f32_16x16x32_bf16 v[48:51], a[244:247], a[96:99], v[48:51]
s_waitcnt lgkmcnt(4)
v_mfma_f32_16x16x32_bf16 v[64:67], a[248:251], a[96:99], v[64:67]
ds_read_b64_tr_b16 a[244:245], v5 offset:160
v_add_u32 v4, 0, v3
v_mfma_f32_16x16x32_bf16 v[68:71], a[248:251], a[100:103], v[68:71]
ds_read_b64_tr_b16 a[246:247], v5 offset:3744
v_mfma_f32_16x16x32_bf16 v[72:75], a[248:251], a[104:107], v[72:75]
v_mfma_f32_16x16x32_bf16 v[76:79], a[248:251], a[108:111], v[76:79]
s_waitcnt lgkmcnt(4)
v_mfma_f32_16x16x32_bf16 v[92:95], a[252:255], a[108:111], v[92:95]
ds_read_b64_tr_b16 a[248:249], v5 offset:192
v_mfma_f32_16x16x32_bf16 v[88:91], a[252:255], a[104:107], v[88:91]
ds_read_b64_tr_b16 a[250:251], v5 offset:3776
v_mfma_f32_16x16x32_bf16 v[84:87], a[252:255], a[100:103], v[84:87]
v_mfma_f32_16x16x32_bf16 v[80:83], a[252:255], a[96:99], v[80:83]
s_waitcnt lgkmcnt(4)
v_mfma_f32_16x16x32_bf16 v[96:99], a[240:243], a[96:99], v[96:99]
ds_read_b64_tr_b16 a[252:253], v5 offset:14336
v_mfma_f32_16x16x32_bf16 v[100:103], a[240:243], a[100:103], v[100:103]
ds_read_b64_tr_b16 a[254:255], v5 offset:17920
v_mfma_f32_16x16x32_bf16 v[104:107], a[240:243], a[104:107], v[104:107]
v_mfma_f32_16x16x32_bf16 v[108:111], a[240:243], a[108:111], v[108:111]
s_waitcnt lgkmcnt(4)
v_mfma_f32_16x16x32_bf16 v[124:127], a[244:247], a[108:111], v[124:127]
ds_read_b64_tr_b16 a[240:241], v5 offset:14368
v_mfma_f32_16x16x32_bf16 v[120:123], a[244:247], a[104:107], v[120:123]
ds_read_b64_tr_b16 a[242:243], v5 offset:17952
v_mfma_f32_16x16x32_bf16 v[116:119], a[244:247], a[100:103], v[116:119]
buffer_load_dwordx4 a[84:87], v2, s[24:27], s47 offen offset:1024 sc1
v_mfma_f32_16x16x32_bf16 v[112:115], a[244:247], a[96:99], v[112:115]
s_waitcnt lgkmcnt(4)
v_mfma_f32_16x16x32_bf16 v[128:131], a[248:251], a[96:99], v[128:131]
ds_read_b64_tr_b16 a[244:245], v5 offset:14400
v_mfma_f32_16x16x32_bf16 v[132:135], a[248:251], a[100:103], v[132:135]
ds_read_b64_tr_b16 a[246:247], v5 offset:17984
v_mfma_f32_16x16x32_bf16 v[136:139], a[248:251], a[104:107], v[136:139]
v_mfma_f32_16x16x32_bf16 v[140:143], a[248:251], a[108:111], v[140:143]
s_waitcnt lgkmcnt(4)
v_mfma_f32_16x16x32_bf16 v[156:159], a[252:255], a[108:111], v[156:159]
ds_read_b64_tr_b16 a[248:249], v5 offset:14432
v_mfma_f32_16x16x32_bf16 v[152:155], a[252:255], a[104:107], v[152:155]
ds_read_b64_tr_b16 a[250:251], v5 offset:18016
v_mfma_f32_16x16x32_bf16 v[148:151], a[252:255], a[100:103], v[148:151]
v_mfma_f32_16x16x32_bf16 v[144:147], a[252:255], a[96:99], v[144:147]
s_waitcnt lgkmcnt(4)
v_mfma_f32_16x16x32_bf16 v[160:163], a[240:243], a[96:99], v[160:163]
ds_read_b64_tr_b16 a[252:253], v5 offset:14464
v_mfma_f32_16x16x32_bf16 v[164:167], a[240:243], a[100:103], v[164:167]
ds_read_b64_tr_b16 a[254:255], v5 offset:18048
v_mfma_f32_16x16x32_bf16 v[168:171], a[240:243], a[104:107], v[168:171]
buffer_load_dwordx4 a[88:91], v2, s[24:27], s47 offen offset:2048 sc1
v_mfma_f32_16x16x32_bf16 v[172:175], a[240:243], a[108:111], v[172:175]
s_waitcnt lgkmcnt(4)
v_mfma_f32_16x16x32_bf16 v[188:191], a[244:247], a[108:111], v[188:191]
ds_read_b64_tr_b16 a[240:241], v5 offset:14496
v_mfma_f32_16x16x32_bf16 v[184:187], a[244:247], a[104:107], v[184:187]
ds_read_b64_tr_b16 a[242:243], v5 offset:18080
v_mfma_f32_16x16x32_bf16 v[180:183], a[244:247], a[100:103], v[180:183]
s_add_u32 s54, s1, s101
s_and_b32 s54, s54, 3
s_and_b32 s55, s1, 12
v_mfma_f32_16x16x32_bf16 v[176:179], a[244:247], a[96:99], v[176:179]
s_waitcnt lgkmcnt(4)
s_or_b32 s54, s54, s55
s_mul_i32 s54, s54, 458752
v_mfma_f32_16x16x32_bf16 v[192:195], a[248:251], a[96:99], v[192:195]
ds_read_b64_tr_b16 a[244:245], v5 offset:14528
s_add_u32 s54, s54, s48
v_mfma_f32_16x16x32_bf16 v[196:199], a[248:251], a[100:103], v[196:199]
ds_read_b64_tr_b16 a[246:247], v5 offset:18112
s_add_u32 s1, s1, 1
v_mfma_f32_16x16x32_bf16 v[200:203], a[248:251], a[104:107], v[200:203]
s_add_u32 m0, s57, 57344
v_mfma_f32_16x16x32_bf16 v[204:207], a[248:251], a[108:111], v[204:207]
s_waitcnt lgkmcnt(4)
v_mfma_f32_16x16x32_bf16 v[220:223], a[252:255], a[108:111], v[220:223]
ds_read_b64_tr_b16 a[248:249], v5 offset:7168
v_mfma_f32_16x16x32_bf16 v[216:219], a[252:255], a[104:107], v[216:219]
ds_read_b64_tr_b16 a[250:251], v5 offset:10752
v_mfma_f32_16x16x32_bf16 v[212:215], a[252:255], a[100:103], v[212:215]
v_mfma_f32_16x16x32_bf16 v[208:211], a[252:255], a[96:99], v[208:211]
s_waitcnt lgkmcnt(4)
v_mfma_f32_16x16x32_bf16 v[224:227], a[240:243], a[96:99], v[224:227]
ds_read_b64_tr_b16 a[252:253], v5 offset:7200
v_mfma_f32_16x16x32_bf16 v[228:231], a[240:243], a[100:103], v[228:231]
ds_read_b64_tr_b16 a[254:255], v5 offset:10784
v_mfma_f32_16x16x32_bf16 v[232:235], a[240:243], a[104:107], v[232:235]
buffer_load_dwordx4 a[92:95], v2, s[24:27], s47 offen offset:3072 sc1
v_mfma_f32_16x16x32_bf16 v[236:239], a[240:243], a[108:111], v[236:239]
s_waitcnt lgkmcnt(4)
s_add_u32 s47, s2, s101
s_and_b32 s47, s47, 3
v_mfma_f32_16x16x32_bf16 v[252:255], a[244:247], a[108:111], v[252:255]
ds_read_b64_tr_b16 a[240:241], v5 offset:7232
s_and_b32 s56, s2, 12
v_mfma_f32_16x16x32_bf16 v[248:251], a[244:247], a[104:107], v[248:251]
ds_read_b64_tr_b16 a[242:243], v5 offset:10816
s_or_b32 s47, s47, s56
v_mfma_f32_16x16x32_bf16 v[244:247], a[244:247], a[100:103], v[244:247]
s_lshl_b32 s47, s47, 13
s_add_u32 s47, s47, s58
s_add_u32 s2, s2, 1
v_mfma_f32_16x16x32_bf16 v[240:243], a[244:247], a[96:99], v[240:243]
s_waitcnt lgkmcnt(4)
v_mfma_f32_16x16x32_bf16 v[32:35], a[248:251], a[112:115], v[32:35]
ds_read_b64_tr_b16 a[244:245], v5 offset:7264
v_mfma_f32_16x16x32_bf16 v[36:39], a[248:251], a[116:119], v[36:39]
ds_read_b64_tr_b16 a[246:247], v5 offset:10848
v_mfma_f32_16x16x32_bf16 v[40:43], a[248:251], a[120:123], v[40:43]
buffer_load_dwordx4 a[96:99], v2, s[24:27], s47 offen offset:0 sc1
v_mfma_f32_16x16x32_bf16 v[44:47], a[248:251], a[124:127], v[44:47]
s_waitcnt lgkmcnt(4)
v_mfma_f32_16x16x32_bf16 v[60:63], a[252:255], a[124:127], v[60:63]
ds_read_b64_tr_b16 a[248:249], v5 offset:7296
v_mfma_f32_16x16x32_bf16 v[56:59], a[252:255], a[120:123], v[56:59]
ds_read_b64_tr_b16 a[250:251], v5 offset:10880
v_mfma_f32_16x16x32_bf16 v[52:55], a[252:255], a[116:119], v[52:55]
buffer_load_dwordx4 a[100:103], v2, s[24:27], s47 offen offset:1024 sc1
v_mfma_f32_16x16x32_bf16 v[48:51], a[252:255], a[112:115], v[48:51]
s_waitcnt lgkmcnt(4)
v_mfma_f32_16x16x32_bf16 v[64:67], a[240:243], a[112:115], v[64:67]
ds_read_b64_tr_b16 a[252:253], v5 offset:7328
v_mfma_f32_16x16x32_bf16 v[68:71], a[240:243], a[116:119], v[68:71]
ds_read_b64_tr_b16 a[254:255], v5 offset:10912
v_mfma_f32_16x16x32_bf16 v[72:75], a[240:243], a[120:123], v[72:75]
v_mfma_f32_16x16x32_bf16 v[76:79], a[240:243], a[124:127], v[76:79]
s_waitcnt vmcnt(21)
s_barrier
s_waitcnt lgkmcnt(4)
v_mfma_f32_16x16x32_bf16 v[92:95], a[244:247], a[124:127], v[92:95]
ds_read_b64_tr_b16 a[240:241], v5 offset:7360
v_mfma_f32_16x16x32_bf16 v[88:91], a[244:247], a[120:123], v[88:91]
ds_read_b64_tr_b16 a[242:243], v5 offset:10944
v_mfma_f32_16x16x32_bf16 v[84:87], a[244:247], a[116:119], v[84:87]
buffer_load_dwordx4 a[104:107], v2, s[24:27], s47 offen offset:2048 sc1
v_mfma_f32_16x16x32_bf16 v[80:83], a[244:247], a[112:115], v[80:83]
s_waitcnt lgkmcnt(4)
v_mfma_f32_16x16x32_bf16 v[96:99], a[248:251], a[112:115], v[96:99]
ds_read_b64_tr_b16 a[244:245], v5 offset:21504
v_mfma_f32_16x16x32_bf16 v[100:103], a[248:251], a[116:119], v[100:103]
ds_read_b64_tr_b16 a[246:247], v5 offset:25088
v_mfma_f32_16x16x32_bf16 v[104:107], a[248:251], a[120:123], v[104:107]
buffer_load_dwordx4 a[108:111], v2, s[24:27], s47 offen offset:3072 sc1
v_mfma_f32_16x16x32_bf16 v[108:111], a[248:251], a[124:127], v[108:111]
s_waitcnt lgkmcnt(4)
s_add_u32 s47, s47, 4096
v_mfma_f32_16x16x32_bf16 v[124:127], a[252:255], a[124:127], v[124:127]
ds_read_b64_tr_b16 a[248:249], v5 offset:21536
v_mfma_f32_16x16x32_bf16 v[120:123], a[252:255], a[120:123], v[120:123]
ds_read_b64_tr_b16 a[250:251], v5 offset:25120
v_mfma_f32_16x16x32_bf16 v[116:119], a[252:255], a[116:119], v[116:119]
buffer_load_dwordx4 v6, s[28:31], s54 offen lds
v_mfma_f32_16x16x32_bf16 v[112:115], a[252:255], a[112:115], v[112:115]
s_waitcnt lgkmcnt(4)
s_add_u32 m0, s57, 58368
v_mfma_f32_16x16x32_bf16 v[128:131], a[240:243], a[112:115], v[128:131]
ds_read_b64_tr_b16 a[252:253], v5 offset:21568
v_mfma_f32_16x16x32_bf16 v[132:135], a[240:243], a[116:119], v[132:135]
ds_read_b64_tr_b16 a[254:255], v5 offset:25152
v_mfma_f32_16x16x32_bf16 v[136:139], a[240:243], a[120:123], v[136:139]
buffer_load_dwordx4 v7, s[28:31], s54 offen lds
v_mfma_f32_16x16x32_bf16 v[140:143], a[240:243], a[124:127], v[140:143]
s_waitcnt lgkmcnt(4)
s_add_u32 m0, s57, 59392
v_mfma_f32_16x16x32_bf16 v[156:159], a[244:247], a[124:127], v[156:159]
ds_read_b64_tr_b16 a[240:241], v5 offset:21600
v_mfma_f32_16x16x32_bf16 v[152:155], a[244:247], a[120:123], v[152:155]
ds_read_b64_tr_b16 a[242:243], v5 offset:25184
v_mfma_f32_16x16x32_bf16 v[148:151], a[244:247], a[116:119], v[148:151]
buffer_load_dwordx4 v8, s[28:31], s54 offen lds
v_mfma_f32_16x16x32_bf16 v[144:147], a[244:247], a[112:115], v[144:147]
s_waitcnt lgkmcnt(4)
s_add_u32 m0, s57, 60416
v_mfma_f32_16x16x32_bf16 v[160:163], a[248:251], a[112:115], v[160:163]
ds_read_b64_tr_b16 a[244:245], v5 offset:21632
v_mfma_f32_16x16x32_bf16 v[164:167], a[248:251], a[116:119], v[164:167]
ds_read_b64_tr_b16 a[246:247], v5 offset:25216
v_mfma_f32_16x16x32_bf16 v[168:171], a[248:251], a[120:123], v[168:171]
v_mfma_f32_16x16x32_bf16 v[172:175], a[248:251], a[124:127], v[172:175]
s_waitcnt lgkmcnt(4)
v_mfma_f32_16x16x32_bf16 v[188:191], a[252:255], a[124:127], v[188:191]
ds_read_b64_tr_b16 a[248:249], v5 offset:21664
v_mfma_f32_16x16x32_bf16 v[184:187], a[252:255], a[120:123], v[184:187]
ds_read_b64_tr_b16 a[250:251], v5 offset:25248
v_mfma_f32_16x16x32_bf16 v[180:183], a[252:255], a[116:119], v[180:183]
buffer_load_dwordx4 v9, s[28:31], s54 offen lds
v_mfma_f32_16x16x32_bf16 v[176:179], a[252:255], a[112:115], v[176:179]
s_waitcnt lgkmcnt(4)
s_add_u32 m0, s57, 61440
v_mfma_f32_16x16x32_bf16 v[192:195], a[240:243], a[112:115], v[192:195]
ds_read_b64_tr_b16 a[252:253], v5 offset:21696
v_mfma_f32_16x16x32_bf16 v[196:199], a[240:243], a[116:119], v[196:199]
ds_read_b64_tr_b16 a[254:255], v5 offset:25280
v_mfma_f32_16x16x32_bf16 v[200:203], a[240:243], a[120:123], v[200:203]
buffer_load_dwordx4 v10, s[28:31], s54 offen lds
v_mfma_f32_16x16x32_bf16 v[204:207], a[240:243], a[124:127], v[204:207]
s_waitcnt lgkmcnt(4)
s_add_u32 m0, s57, 62464
v_mfma_f32_16x16x32_bf16 v[220:223], a[244:247], a[124:127], v[220:223]
ds_read_b64_tr_b16 a[240:241], v4 offset:0
v_mfma_f32_16x16x32_bf16 v[216:219], a[244:247], a[120:123], v[216:219]
ds_read_b64_tr_b16 a[242:243], v4 offset:3584
v_mfma_f32_16x16x32_bf16 v[212:215], a[244:247], a[116:119], v[212:215]
buffer_load_dwordx4 v11, s[28:31], s54 offen lds
v_mfma_f32_16x16x32_bf16 v[208:211], a[244:247], a[112:115], v[208:211]
s_waitcnt lgkmcnt(4)
s_add_u32 m0, s57, 63488
v_mfma_f32_16x16x32_bf16 v[224:227], a[248:251], a[112:115], v[224:227]
ds_read_b64_tr_b16 a[244:245], v4 offset:32
v_mfma_f32_16x16x32_bf16 v[228:231], a[248:251], a[116:119], v[228:231]
ds_read_b64_tr_b16 a[246:247], v4 offset:3616
v_mfma_f32_16x16x32_bf16 v[232:235], a[248:251], a[120:123], v[232:235]
buffer_load_dwordx4 v12, s[28:31], s54 offen lds
v_mfma_f32_16x16x32_bf16 v[236:239], a[248:251], a[124:127], v[236:239]
s_waitcnt lgkmcnt(4)
v_mfma_f32_16x16x32_bf16 v[252:255], a[252:255], a[124:127], v[252:255]
ds_read_b64_tr_b16 a[248:249], v4 offset:64
v_mfma_f32_16x16x32_bf16 v[248:251], a[252:255], a[120:123], v[248:251]
ds_read_b64_tr_b16 a[250:251], v4 offset:3648
v_mfma_f32_16x16x32_bf16 v[244:247], a[252:255], a[116:119], v[244:247]
v_mfma_f32_16x16x32_bf16 v[240:243], a[252:255], a[112:115], v[240:243]
s_waitcnt lgkmcnt(4)
s_sub_u32 s46, s46, 1
s_cmp_lg_u32 s46, 0
s_cbranch_scc1 GUL_7
v_mfma_f32_16x16x32_bf16 v[32:35], a[240:243], a[0:3], v[32:35]
s_nop 0
ds_read_b64_tr_b16 a[252:253], v4 offset:96
v_mfma_f32_16x16x32_bf16 v[36:39], a[240:243], a[4:7], v[36:39]
ds_read_b64_tr_b16 a[254:255], v4 offset:3680
v_mfma_f32_16x16x32_bf16 v[40:43], a[240:243], a[8:11], v[40:43]
v_mfma_f32_16x16x32_bf16 v[44:47], a[240:243], a[12:15], v[44:47]
s_waitcnt lgkmcnt(4)
v_mfma_f32_16x16x32_bf16 v[60:63], a[244:247], a[12:15], v[60:63]
ds_read_b64_tr_b16 a[240:241], v4 offset:128
v_mfma_f32_16x16x32_bf16 v[56:59], a[244:247], a[8:11], v[56:59]
ds_read_b64_tr_b16 a[242:243], v4 offset:3712
v_mfma_f32_16x16x32_bf16 v[52:55], a[244:247], a[4:7], v[52:55]
buffer_load_dwordx4 a[112:115], v2, s[24:27], s47 offen offset:0 sc1
v_mfma_f32_16x16x32_bf16 v[48:51], a[244:247], a[0:3], v[48:51]
s_waitcnt lgkmcnt(4)
s_cmp_eq_u32 s99, 0
s_cbranch_scc0 INV_8
buffer_inv sc1
INV_8:
v_mfma_f32_16x16x32_bf16 v[64:67], a[248:251], a[0:3], v[64:67]
ds_read_b64_tr_b16 a[244:245], v4 offset:160
v_add_u32 v5, 28672, v3
v_mfma_f32_16x16x32_bf16 v[68:71], a[248:251], a[4:7], v[68:71]
ds_read_b64_tr_b16 a[246:247], v4 offset:3744
v_mfma_f32_16x16x32_bf16 v[72:75], a[248:251], a[8:11], v[72:75]
v_mfma_f32_16x16x32_bf16 v[76:79], a[248:251], a[12:15], v[76:79]
s_waitcnt lgkmcnt(4)
v_mfma_f32_16x16x32_bf16 v[92:95], a[252:255], a[12:15], v[92:95]
ds_read_b64_tr_b16 a[248:249], v4 offset:192
v_mfma_f32_16x16x32_bf16 v[88:91], a[252:255], a[8:11], v[88:91]
ds_read_b64_tr_b16 a[250:251], v4 offset:3776
v_mfma_f32_16x16x32_bf16 v[84:87], a[252:255], a[4:7], v[84:87]
v_mfma_f32_16x16x32_bf16 v[80:83], a[252:255], a[0:3], v[80:83]
s_waitcnt lgkmcnt(4)
v_mfma_f32_16x16x32_bf16 v[96:99], a[240:243], a[0:3], v[96:99]
ds_read_b64_tr_b16 a[252:253], v4 offset:14336
v_mfma_f32_16x16x32_bf16 v[100:103], a[240:243], a[4:7], v[100:103]
ds_read_b64_tr_b16 a[254:255], v4 offset:17920
v_mfma_f32_16x16x32_bf16 v[104:107], a[240:243], a[8:11], v[104:107]
v_mfma_f32_16x16x32_bf16 v[108:111], a[240:243], a[12:15], v[108:111]
s_waitcnt lgkmcnt(4)
v_mfma_f32_16x16x32_bf16 v[124:127], a[244:247], a[12:15], v[124:127]
ds_read_b64_tr_b16 a[240:241], v4 offset:14368
v_mfma_f32_16x16x32_bf16 v[120:123], a[244:247], a[8:11], v[120:123]
ds_read_b64_tr_b16 a[242:243], v4 offset:17952
v_mfma_f32_16x16x32_bf16 v[116:119], a[244:247], a[4:7], v[116:119]
buffer_load_dwordx4 a[116:119], v2, s[24:27], s47 offen offset:1024 sc1
v_mfma_f32_16x16x32_bf16 v[112:115], a[244:247], a[0:3], v[112:115]
s_waitcnt lgkmcnt(4)
v_mfma_f32_16x16x32_bf16 v[128:131], a[248:251], a[0:3], v[128:131]
ds_read_b64_tr_b16 a[244:245], v4 offset:14400
v_mfma_f32_16x16x32_bf16 v[132:135], a[248:251], a[4:7], v[132:135]
ds_read_b64_tr_b16 a[246:247], v4 offset:17984
v_mfma_f32_16x16x32_bf16 v[136:139], a[248:251], a[8:11], v[136:139]
v_mfma_f32_16x16x32_bf16 v[140:143], a[248:251], a[12:15], v[140:143]
s_waitcnt lgkmcnt(4)
v_mfma_f32_16x16x32_bf16 v[156:159], a[252:255], a[12:15], v[156:159]
ds_read_b64_tr_b16 a[248:249], v4 offset:14432
v_mfma_f32_16x16x32_bf16 v[152:155], a[252:255], a[8:11], v[152:155]
ds_read_b64_tr_b16 a[250:251], v4 offset:18016
v_mfma_f32_16x16x32_bf16 v[148:151], a[252:255], a[4:7], v[148:151]
v_mfma_f32_16x16x32_bf16 v[144:147], a[252:255], a[0:3], v[144:147]
s_waitcnt lgkmcnt(4)
v_mfma_f32_16x16x32_bf16 v[160:163], a[240:243], a[0:3], v[160:163]
ds_read_b64_tr_b16 a[252:253], v4 offset:14464
v_mfma_f32_16x16x32_bf16 v[164:167], a[240:243], a[4:7], v[164:167]
ds_read_b64_tr_b16 a[254:255], v4 offset:18048
v_mfma_f32_16x16x32_bf16 v[168:171], a[240:243], a[8:11], v[168:171]
buffer_load_dwordx4 a[120:123], v2, s[24:27], s47 offen offset:2048 sc1
v_mfma_f32_16x16x32_bf16 v[172:175], a[240:243], a[12:15], v[172:175]
s_waitcnt lgkmcnt(4)
v_mfma_f32_16x16x32_bf16 v[188:191], a[244:247], a[12:15], v[188:191]
ds_read_b64_tr_b16 a[240:241], v4 offset:14496
v_mfma_f32_16x16x32_bf16 v[184:187], a[244:247], a[8:11], v[184:187]
ds_read_b64_tr_b16 a[242:243], v4 offset:18080
v_mfma_f32_16x16x32_bf16 v[180:183], a[244:247], a[4:7], v[180:183]
s_add_u32 s54, s101, 3
s_and_b32 s54, s54, 3
s_mul_i32 s54, s54, 458752
v_mfma_f32_16x16x32_bf16 v[176:179], a[244:247], a[0:3], v[176:179]
s_waitcnt lgkmcnt(4)
s_add_u32 s54, s54, s48
s_add_u32 s54, s54, 5505024
v_mfma_f32_16x16x32_bf16 v[192:195], a[248:251], a[0:3], v[192:195]
ds_read_b64_tr_b16 a[244:245], v4 offset:14528
s_add_u32 m0, s57, 86016
v_mfma_f32_16x16x32_bf16 v[196:199], a[248:251], a[4:7], v[196:199]
ds_read_b64_tr_b16 a[246:247], v4 offset:18112
v_mfma_f32_16x16x32_bf16 v[200:203], a[248:251], a[8:11], v[200:203]
v_mfma_f32_16x16x32_bf16 v[204:207], a[248:251], a[12:15], v[204:207]
s_waitcnt lgkmcnt(4)
v_mfma_f32_16x16x32_bf16 v[220:223], a[252:255], a[12:15], v[220:223]
ds_read_b64_tr_b16 a[248:249], v4 offset:7168
v_mfma_f32_16x16x32_bf16 v[216:219], a[252:255], a[8:11], v[216:219]
ds_read_b64_tr_b16 a[250:251], v4 offset:10752
v_mfma_f32_16x16x32_bf16 v[212:215], a[252:255], a[4:7], v[212:215]
v_mfma_f32_16x16x32_bf16 v[208:211], a[252:255], a[0:3], v[208:211]
s_waitcnt lgkmcnt(4)
v_mfma_f32_16x16x32_bf16 v[224:227], a[240:243], a[0:3], v[224:227]
ds_read_b64_tr_b16 a[252:253], v4 offset:7200
v_mfma_f32_16x16x32_bf16 v[228:231], a[240:243], a[4:7], v[228:231]
ds_read_b64_tr_b16 a[254:255], v4 offset:10784
v_mfma_f32_16x16x32_bf16 v[232:235], a[240:243], a[8:11], v[232:235]
buffer_load_dwordx4 a[124:127], v2, s[24:27], s47 offen offset:3072 sc1
v_mfma_f32_16x16x32_bf16 v[236:239], a[240:243], a[12:15], v[236:239]
s_waitcnt lgkmcnt(4)
v_mfma_f32_16x16x32_bf16 v[252:255], a[244:247], a[12:15], v[252:255]
ds_read_b64_tr_b16 a[240:241], v4 offset:7232
v_mfma_f32_16x16x32_bf16 v[248:251], a[244:247], a[8:11], v[248:251]
ds_read_b64_tr_b16 a[242:243], v4 offset:10816
v_mfma_f32_16x16x32_bf16 v[244:247], a[244:247], a[4:7], v[244:247]
v_mfma_f32_16x16x32_bf16 v[240:243], a[244:247], a[0:3], v[240:243]
s_waitcnt lgkmcnt(4)
v_mfma_f32_16x16x32_bf16 v[32:35], a[248:251], a[16:19], v[32:35]
ds_read_b64_tr_b16 a[244:245], v4 offset:7264
v_mfma_f32_16x16x32_bf16 v[36:39], a[248:251], a[20:23], v[36:39]
ds_read_b64_tr_b16 a[246:247], v4 offset:10848
v_mfma_f32_16x16x32_bf16 v[40:43], a[248:251], a[24:27], v[40:43]
v_mfma_f32_16x16x32_bf16 v[44:47], a[248:251], a[28:31], v[44:47]
s_waitcnt lgkmcnt(4)
v_mfma_f32_16x16x32_bf16 v[60:63], a[252:255], a[28:31], v[60:63]
ds_read_b64_tr_b16 a[248:249], v4 offset:7296
v_mfma_f32_16x16x32_bf16 v[56:59], a[252:255], a[24:27], v[56:59]
ds_read_b64_tr_b16 a[250:251], v4 offset:10880
v_mfma_f32_16x16x32_bf16 v[52:55], a[252:255], a[20:23], v[52:55]
v_mfma_f32_16x16x32_bf16 v[48:51], a[252:255], a[16:19], v[48:51]
s_waitcnt lgkmcnt(4)
s_cmp_eq_u32 s99, 1
s_cbranch_scc0 INV_9
buffer_inv sc1
INV_9:
v_mfma_f32_16x16x32_bf16 v[64:67], a[240:243], a[16:19], v[64:67]
ds_read_b64_tr_b16 a[252:253], v4 offset:7328
v_mfma_f32_16x16x32_bf16 v[68:71], a[240:243], a[20:23], v[68:71]
ds_read_b64_tr_b16 a[254:255], v4 offset:10912
v_mfma_f32_16x16x32_bf16 v[72:75], a[240:243], a[24:27], v[72:75]
v_mfma_f32_16x16x32_bf16 v[76:79], a[240:243], a[28:31], v[76:79]
s_waitcnt vmcnt(19)
s_barrier
s_waitcnt lgkmcnt(4)
v_mfma_f32_16x16x32_bf16 v[92:95], a[244:247], a[28:31], v[92:95]
ds_read_b64_tr_b16 a[240:241], v4 offset:7360
v_mfma_f32_16x16x32_bf16 v[88:91], a[244:247], a[24:27], v[88:91]
ds_read_b64_tr_b16 a[242:243], v4 offset:10944
v_mfma_f32_16x16x32_bf16 v[84:87], a[244:247], a[20:23], v[84:87]
v_mfma_f32_16x16x32_bf16 v[80:83], a[244:247], a[16:19], v[80:83]
s_waitcnt lgkmcnt(4)
v_mfma_f32_16x16x32_bf16 v[96:99], a[248:251], a[16:19], v[96:99]
ds_read_b64_tr_b16 a[244:245], v4 offset:21504
v_mfma_f32_16x16x32_bf16 v[100:103], a[248:251], a[20:23], v[100:103]
ds_read_b64_tr_b16 a[246:247], v4 offset:25088
v_mfma_f32_16x16x32_bf16 v[104:107], a[248:251], a[24:27], v[104:107]
v_mfma_f32_16x16x32_bf16 v[108:111], a[248:251], a[28:31], v[108:111]
s_waitcnt lgkmcnt(4)
v_mfma_f32_16x16x32_bf16 v[124:127], a[252:255], a[28:31], v[124:127]
ds_read_b64_tr_b16 a[248:249], v4 offset:21536
v_mfma_f32_16x16x32_bf16 v[120:123], a[252:255], a[24:27], v[120:123]
ds_read_b64_tr_b16 a[250:251], v4 offset:25120
v_mfma_f32_16x16x32_bf16 v[116:119], a[252:255], a[20:23], v[116:119]
buffer_load_dwordx4 v6, s[28:31], s54 offen lds
v_mfma_f32_16x16x32_bf16 v[112:115], a[252:255], a[16:19], v[112:115]
s_waitcnt lgkmcnt(4)
s_add_u32 m0, s57, 87040
v_mfma_f32_16x16x32_bf16 v[128:131], a[240:243], a[16:19], v[128:131]
ds_read_b64_tr_b16 a[252:253], v4 offset:21568
v_mfma_f32_16x16x32_bf16 v[132:135], a[240:243], a[20:23], v[132:135]
ds_read_b64_tr_b16 a[254:255], v4 offset:25152
v_mfma_f32_16x16x32_bf16 v[136:139], a[240:243], a[24:27], v[136:139]
buffer_load_dwordx4 v7, s[28:31], s54 offen lds
v_mfma_f32_16x16x32_bf16 v[140:143], a[240:243], a[28:31], v[140:143]
s_waitcnt lgkmcnt(4)
s_add_u32 m0, s57, 88064
s_add_u32 s47, s0, 0
v_mfma_f32_16x16x32_bf16 v[156:159], a[244:247], a[28:31], v[156:159]
ds_read_b64_tr_b16 a[240:241], v4 offset:21600
s_and_b32 s47, s47, 7
v_mfma_f32_16x16x32_bf16 v[152:155], a[244:247], a[24:27], v[152:155]
ds_read_b64_tr_b16 a[242:243], v4 offset:25184
s_mul_i32 s47, s47, 8192
v_mfma_f32_16x16x32_bf16 v[148:151], a[244:247], a[20:23], v[148:151]
buffer_load_dwordx4 v8, s[28:31], s54 offen lds
v_mfma_f32_16x16x32_bf16 v[144:147], a[244:247], a[16:19], v[144:147]
s_waitcnt lgkmcnt(4)
s_add_u32 m0, s57, 89088
s_add_u32 s47, s47, s58
v_mfma_f32_16x16x32_bf16 v[160:163], a[248:251], a[16:19], v[160:163]
ds_read_b64_tr_b16 a[244:245], v4 offset:21632
v_mfma_f32_16x16x32_bf16 v[164:167], a[248:251], a[20:23], v[164:167]
ds_read_b64_tr_b16 a[246:247], v4 offset:25216
v_mfma_f32_16x16x32_bf16 v[168:171], a[248:251], a[24:27], v[168:171]
v_mfma_f32_16x16x32_bf16 v[172:175], a[248:251], a[28:31], v[172:175]
s_waitcnt lgkmcnt(4)
v_mfma_f32_16x16x32_bf16 v[188:191], a[252:255], a[28:31], v[188:191]
ds_read_b64_tr_b16 a[248:249], v4 offset:21664
v_mfma_f32_16x16x32_bf16 v[184:187], a[252:255], a[24:27], v[184:187]
ds_read_b64_tr_b16 a[250:251], v4 offset:25248
v_mfma_f32_16x16x32_bf16 v[180:183], a[252:255], a[20:23], v[180:183]
buffer_load_dwordx4 v9, s[28:31], s54 offen lds
v_mfma_f32_16x16x32_bf16 v[176:179], a[252:255], a[16:19], v[176:179]
s_waitcnt lgkmcnt(4)
s_add_u32 m0, s57, 90112
v_mfma_f32_16x16x32_bf16 v[192:195], a[240:243], a[16:19], v[192:195]
ds_read_b64_tr_b16 a[252:253], v4 offset:21696
v_mfma_f32_16x16x32_bf16 v[196:199], a[240:243], a[20:23], v[196:199]
ds_read_b64_tr_b16 a[254:255], v4 offset:25280
v_mfma_f32_16x16x32_bf16 v[200:203], a[240:243], a[24:27], v[200:203]
buffer_load_dwordx4 v10, s[28:31], s54 offen lds
v_mfma_f32_16x16x32_bf16 v[204:207], a[240:243], a[28:31], v[204:207]
s_waitcnt lgkmcnt(4)
s_add_u32 m0, s57, 91136
v_mfma_f32_16x16x32_bf16 v[220:223], a[244:247], a[28:31], v[220:223]
ds_read_b64_tr_b16 a[240:241], v5 offset:0
v_mfma_f32_16x16x32_bf16 v[216:219], a[244:247], a[24:27], v[216:219]
ds_read_b64_tr_b16 a[242:243], v5 offset:3584
v_mfma_f32_16x16x32_bf16 v[212:215], a[244:247], a[20:23], v[212:215]
buffer_load_dwordx4 v11, s[28:31], s54 offen lds
v_mfma_f32_16x16x32_bf16 v[208:211], a[244:247], a[16:19], v[208:211]
s_waitcnt lgkmcnt(4)
s_add_u32 m0, s57, 92160
v_mfma_f32_16x16x32_bf16 v[224:227], a[248:251], a[16:19], v[224:227]
ds_read_b64_tr_b16 a[244:245], v5 offset:32
v_mfma_f32_16x16x32_bf16 v[228:231], a[248:251], a[20:23], v[228:231]
ds_read_b64_tr_b16 a[246:247], v5 offset:3616
v_mfma_f32_16x16x32_bf16 v[232:235], a[248:251], a[24:27], v[232:235]
buffer_load_dwordx4 v12, s[28:31], s54 offen lds
v_mfma_f32_16x16x32_bf16 v[236:239], a[248:251], a[28:31], v[236:239]
s_waitcnt lgkmcnt(4)
v_mfma_f32_16x16x32_bf16 v[252:255], a[252:255], a[28:31], v[252:255]
ds_read_b64_tr_b16 a[248:249], v5 offset:64
v_mfma_f32_16x16x32_bf16 v[248:251], a[252:255], a[24:27], v[248:251]
ds_read_b64_tr_b16 a[250:251], v5 offset:3648
v_mfma_f32_16x16x32_bf16 v[244:247], a[252:255], a[20:23], v[244:247]
v_mfma_f32_16x16x32_bf16 v[240:243], a[252:255], a[16:19], v[240:243]
s_waitcnt lgkmcnt(4)
v_mfma_f32_16x16x32_bf16 v[32:35], a[240:243], a[32:35], v[32:35]
ds_read_b64_tr_b16 a[252:253], v5 offset:96
v_mfma_f32_16x16x32_bf16 v[36:39], a[240:243], a[36:39], v[36:39]
ds_read_b64_tr_b16 a[254:255], v5 offset:3680
v_mfma_f32_16x16x32_bf16 v[40:43], a[240:243], a[40:43], v[40:43]
s_mov_b32 s36, s49
s_mov_b32 s37, s50
s_mov_b32 s38, KTEXT1-KTEXT0
v_mfma_f32_16x16x32_bf16 v[44:47], a[240:243], a[44:47], v[44:47]
s_waitcnt lgkmcnt(4)
s_mov_b32 s39, 0x20000
s_lshr_b32 s69, s98, 3
v_mfma_f32_16x16x32_bf16 v[60:63], a[244:247], a[44:47], v[60:63]
ds_read_b64_tr_b16 a[240:241], v5 offset:128
s_lshl_b32 s69, s69, 2
v_mfma_f32_16x16x32_bf16 v[56:59], a[244:247], a[40:43], v[56:59]
ds_read_b64_tr_b16 a[242:243], v5 offset:3712
s_add_u32 s69, s69, s43
v_mfma_f32_16x16x32_bf16 v[52:55], a[244:247], a[36:39], v[52:55]
buffer_load_dwordx4 a[128:131], v2, s[24:27], s47 offen offset:0 sc1
v_mfma_f32_16x16x32_bf16 v[48:51], a[244:247], a[32:35], v[48:51]
s_waitcnt lgkmcnt(4)
s_cmp_eq_u32 s99, 2
s_cbranch_scc0 INV_10
buffer_inv sc1
INV_10:
v_mfma_f32_16x16x32_bf16 v[64:67], a[248:251], a[32:35], v[64:67]
ds_read_b64_tr_b16 a[244:245], v5 offset:160
s_and_b32 s69, s69, 63
v_mfma_f32_16x16x32_bf16 v[68:71], a[248:251], a[36:39], v[68:71]
ds_read_b64_tr_b16 a[246:247], v5 offset:3744
s_lshl_b32 s69, s69, 10
v_mfma_f32_16x16x32_bf16 v[72:75], a[248:251], a[40:43], v[72:75]
s_add_u32 s69, s69, KQ-KTEXT0
v_add_u32 v4, 57344, v3
v_mfma_f32_16x16x32_bf16 v[76:79], a[248:251], a[44:47], v[76:79]
s_waitcnt lgkmcnt(4)
v_mfma_f32_16x16x32_bf16 v[92:95], a[252:255], a[44:47], v[92:95]
ds_read_b64_tr_b16 a[248:249], v5 offset:192
v_mfma_f32_16x16x32_bf16 v[88:91], a[252:255], a[40:43], v[88:91]
ds_read_b64_tr_b16 a[250:251], v5 offset:3776
v_mfma_f32_16x16x32_bf16 v[84:87], a[252:255], a[36:39], v[84:87]
v_mfma_f32_16x16x32_bf16 v[80:83], a[252:255], a[32:35], v[80:83]
s_waitcnt lgkmcnt(4)
v_mfma_f32_16x16x32_bf16 v[96:99], a[240:243], a[32:35], v[96:99]
ds_read_b64_tr_b16 a[252:253], v5 offset:14336
v_mfma_f32_16x16x32_bf16 v[100:103], a[240:243], a[36:39], v[100:103]
ds_read_b64_tr_b16 a[254:255], v5 offset:17920
v_mfma_f32_16x16x32_bf16 v[104:107], a[240:243], a[40:43], v[104:107]
v_mfma_f32_16x16x32_bf16 v[108:111], a[240:243], a[44:47], v[108:111]
s_waitcnt lgkmcnt(4)
v_mfma_f32_16x16x32_bf16 v[124:127], a[244:247], a[44:47], v[124:127]
ds_read_b64_tr_b16 a[240:241], v5 offset:14368
v_mfma_f32_16x16x32_bf16 v[120:123], a[244:247], a[40:43], v[120:123]
ds_read_b64_tr_b16 a[242:243], v5 offset:17952
v_mfma_f32_16x16x32_bf16 v[116:119], a[244:247], a[36:39], v[116:119]
buffer_load_dwordx4 a[132:135], v2, s[24:27], s47 offen offset:1024 sc1
v_mfma_f32_16x16x32_bf16 v[112:115], a[244:247], a[32:35], v[112:115]
s_waitcnt lgkmcnt(4)
v_mfma_f32_16x16x32_bf16 v[128:131], a[248:251], a[32:35], v[128:131]
ds_read_b64_tr_b16 a[244:245], v5 offset:14400
v_mfma_f32_16x16x32_bf16 v[132:135], a[248:251], a[36:39], v[132:135]
ds_read_b64_tr_b16 a[246:247], v5 offset:17984
v_mfma_f32_16x16x32_bf16 v[136:139], a[248:251], a[40:43], v[136:139]
v_mfma_f32_16x16x32_bf16 v[140:143], a[248:251], a[44:47], v[140:143]
s_waitcnt lgkmcnt(4)
v_mfma_f32_16x16x32_bf16 v[156:159], a[252:255], a[44:47], v[156:159]
ds_read_b64_tr_b16 a[248:249], v5 offset:14432
v_mfma_f32_16x16x32_bf16 v[152:155], a[252:255], a[40:43], v[152:155]
ds_read_b64_tr_b16 a[250:251], v5 offset:18016
v_mfma_f32_16x16x32_bf16 v[148:151], a[252:255], a[36:39], v[148:151]
v_mfma_f32_16x16x32_bf16 v[144:147], a[252:255], a[32:35], v[144:147]
s_waitcnt lgkmcnt(4)
v_mfma_f32_16x16x32_bf16 v[160:163], a[240:243], a[32:35], v[160:163]
ds_read_b64_tr_b16 a[252:253], v5 offset:14464
v_mfma_f32_16x16x32_bf16 v[164:167], a[240:243], a[36:39], v[164:167]
ds_read_b64_tr_b16 a[254:255], v5 offset:18048
v_mfma_f32_16x16x32_bf16 v[168:171], a[240:243], a[40:43], v[168:171]
buffer_load_dwordx4 a[136:139], v2, s[24:27], s47 offen offset:2048 sc1
v_mfma_f32_16x16x32_bf16 v[172:175], a[240:243], a[44:47], v[172:175]
s_waitcnt lgkmcnt(4)
v_mfma_f32_16x16x32_bf16 v[188:191], a[244:247], a[44:47], v[188:191]
ds_read_b64_tr_b16 a[240:241], v5 offset:14496
v_mfma_f32_16x16x32_bf16 v[184:187], a[244:247], a[40:43], v[184:187]
ds_read_b64_tr_b16 a[242:243], v5 offset:18080
v_mfma_f32_16x16x32_bf16 v[180:183], a[244:247], a[36:39], v[180:183]
s_add_u32 s54, s0, 0
s_and_b32 s54, s54, 7
s_mul_i32 s54, s54, 524288
v_mfma_f32_16x16x32_bf16 v[176:179], a[244:247], a[32:35], v[176:179]
s_waitcnt lgkmcnt(4)
s_add_u32 s54, s54, s85
s_add_u32 m0, s70, 114688
v_mfma_f32_16x16x32_bf16 v[192:195], a[248:251], a[32:35], v[192:195]
ds_read_b64_tr_b16 a[244:245], v5 offset:14528
v_mfma_f32_16x16x32_bf16 v[196:199], a[248:251], a[36:39], v[196:199]
ds_read_b64_tr_b16 a[246:247], v5 offset:18112
v_mfma_f32_16x16x32_bf16 v[200:203], a[248:251], a[40:43], v[200:203]
buffer_load_dwordx4 v[6:9], v2, s[36:39], s69 offen
v_mfma_f32_16x16x32_bf16 v[204:207], a[248:251], a[44:47], v[204:207]
s_waitcnt lgkmcnt(4)
v_mfma_f32_16x16x32_bf16 v[220:223], a[252:255], a[44:47], v[220:223]
ds_read_b64_tr_b16 a[248:249], v5 offset:7168
v_mfma_f32_16x16x32_bf16 v[216:219], a[252:255], a[40:43], v[216:219]
ds_read_b64_tr_b16 a[250:251], v5 offset:10752
v_mfma_f32_16x16x32_bf16 v[212:215], a[252:255], a[36:39], v[212:215]
v_mfma_f32_16x16x32_bf16 v[208:211], a[252:255], a[32:35], v[208:211]
s_waitcnt lgkmcnt(4)
v_mfma_f32_16x16x32_bf16 v[224:227], a[240:243], a[32:35], v[224:227]
ds_read_b64_tr_b16 a[252:253], v5 offset:7200
v_mfma_f32_16x16x32_bf16 v[228:231], a[240:243], a[36:39], v[228:231]
ds_read_b64_tr_b16 a[254:255], v5 offset:10784
v_mfma_f32_16x16x32_bf16 v[232:235], a[240:243], a[40:43], v[232:235]
buffer_load_dwordx4 a[140:143], v2, s[24:27], s47 offen offset:3072 sc1
v_mfma_f32_16x16x32_bf16 v[236:239], a[240:243], a[44:47], v[236:239]
s_waitcnt lgkmcnt(4)
s_add_u32 s47, s47, 4096
v_mfma_f32_16x16x32_bf16 v[252:255], a[244:247], a[44:47], v[252:255]
ds_read_b64_tr_b16 a[240:241], v5 offset:7232
v_mfma_f32_16x16x32_bf16 v[248:251], a[244:247], a[40:43], v[248:251]
ds_read_b64_tr_b16 a[242:243], v5 offset:10816
v_mfma_f32_16x16x32_bf16 v[244:247], a[244:247], a[36:39], v[244:247]
v_mfma_f32_16x16x32_bf16 v[240:243], a[244:247], a[32:35], v[240:243]
s_waitcnt lgkmcnt(4)
v_mfma_f32_16x16x32_bf16 v[32:35], a[248:251], a[48:51], v[32:35]
ds_read_b64_tr_b16 a[244:245], v5 offset:7264
v_mfma_f32_16x16x32_bf16 v[36:39], a[248:251], a[52:55], v[36:39]
ds_read_b64_tr_b16 a[246:247], v5 offset:10848
v_mfma_f32_16x16x32_bf16 v[40:43], a[248:251], a[56:59], v[40:43]
buffer_load_dwordx4 a[144:147], v2, s[24:27], s47 offen offset:0 sc1
v_mfma_f32_16x16x32_bf16 v[44:47], a[248:251], a[60:63], v[44:47]
s_waitcnt lgkmcnt(4)
v_mfma_f32_16x16x32_bf16 v[60:63], a[252:255], a[60:63], v[60:63]
ds_read_b64_tr_b16 a[248:249], v5 offset:7296
v_mfma_f32_16x16x32_bf16 v[56:59], a[252:255], a[56:59], v[56:59]
ds_read_b64_tr_b16 a[250:251], v5 offset:10880
v_mfma_f32_16x16x32_bf16 v[52:55], a[252:255], a[52:55], v[52:55]
buffer_load_dwordx4 a[148:151], v2, s[24:27], s47 offen offset:1024 sc1
v_mfma_f32_16x16x32_bf16 v[48:51], a[252:255], a[48:51], v[48:51]
s_waitcnt lgkmcnt(4)
s_cmp_eq_u32 s99, 3
s_cbranch_scc0 INV_11
buffer_inv sc1
INV_11:
v_mfma_f32_16x16x32_bf16 v[64:67], a[240:243], a[48:51], v[64:67]
ds_read_b64_tr_b16 a[252:253], v5 offset:7328
v_mfma_f32_16x16x32_bf16 v[68:71], a[240:243], a[52:55], v[68:71]
ds_read_b64_tr_b16 a[254:255], v5 offset:10912
v_mfma_f32_16x16x32_bf16 v[72:75], a[240:243], a[56:59], v[72:75]
v_mfma_f32_16x16x32_bf16 v[76:79], a[240:243], a[60:63], v[76:79]
s_waitcnt vmcnt(18)
s_barrier
s_waitcnt lgkmcnt(4)
v_mfma_f32_16x16x32_bf16 v[92:95], a[244:247], a[60:63], v[92:95]
ds_read_b64_tr_b16 a[240:241], v5 offset:7360
v_mfma_f32_16x16x32_bf16 v[88:91], a[244:247], a[56:59], v[88:91]
ds_read_b64_tr_b16 a[242:243], v5 offset:10944
v_mfma_f32_16x16x32_bf16 v[84:87], a[244:247], a[52:55], v[84:87]
buffer_load_dwordx4 a[152:155], v2, s[24:27], s47 offen offset:2048 sc1
v_mfma_f32_16x16x32_bf16 v[80:83], a[244:247], a[48:51], v[80:83]
s_waitcnt lgkmcnt(4)
v_mfma_f32_16x16x32_bf16 v[96:99], a[248:251], a[48:51], v[96:99]
ds_read_b64_tr_b16 a[244:245], v5 offset:21504
v_mfma_f32_16x16x32_bf16 v[100:103], a[248:251], a[52:55], v[100:103]
ds_read_b64_tr_b16 a[246:247], v5 offset:25088
v_mfma_f32_16x16x32_bf16 v[104:107], a[248:251], a[56:59], v[104:107]
buffer_load_dwordx4 a[156:159], v2, s[24:27], s47 offen offset:3072 sc1
v_mfma_f32_16x16x32_bf16 v[108:111], a[248:251], a[60:63], v[108:111]
s_waitcnt lgkmcnt(4)
v_mfma_f32_16x16x32_bf16 v[124:127], a[252:255], a[60:63], v[124:127]
ds_read_b64_tr_b16 a[248:249], v5 offset:21536
v_mfma_f32_16x16x32_bf16 v[120:123], a[252:255], a[56:59], v[120:123]
ds_read_b64_tr_b16 a[250:251], v5 offset:25120
v_mfma_f32_16x16x32_bf16 v[116:119], a[252:255], a[52:55], v[116:119]
v_mfma_f32_16x16x32_bf16 v[112:115], a[252:255], a[48:51], v[112:115]
s_waitcnt lgkmcnt(4)
v_mfma_f32_16x16x32_bf16 v[128:131], a[240:243], a[48:51], v[128:131]
ds_read_b64_tr_b16 a[252:253], v5 offset:21568
v_mfma_f32_16x16x32_bf16 v[132:135], a[240:243], a[52:55], v[132:135]
ds_read_b64_tr_b16 a[254:255], v5 offset:25152
v_mfma_f32_16x16x32_bf16 v[136:139], a[240:243], a[56:59], v[136:139]
buffer_load_dwordx4 v13, s[88:91], s54 offen lds
v_mfma_f32_16x16x32_bf16 v[140:143], a[240:243], a[60:63], v[140:143]
s_waitcnt lgkmcnt(4)
s_add_u32 m0, s70, 115584
s_add_u32 s47, s0, 1
v_mfma_f32_16x16x32_bf16 v[156:159], a[244:247], a[60:63], v[156:159]
ds_read_b64_tr_b16 a[240:241], v5 offset:21600
s_and_b32 s47, s47, 7
v_mfma_f32_16x16x32_bf16 v[152:155], a[244:247], a[56:59], v[152:155]
ds_read_b64_tr_b16 a[242:243], v5 offset:25184
s_mul_i32 s47, s47, 8192
v_mfma_f32_16x16x32_bf16 v[148:151], a[244:247], a[52:55], v[148:151]
s_add_u32 s47, s47, s58
v_mfma_f32_16x16x32_bf16 v[144:147], a[244:247], a[48:51], v[144:147]
s_waitcnt lgkmcnt(4)
v_mfma_f32_16x16x32_bf16 v[160:163], a[248:251], a[48:51], v[160:163]
ds_read_b64_tr_b16 a[244:245], v5 offset:21632
v_mfma_f32_16x16x32_bf16 v[164:167], a[248:251], a[52:55], v[164:167]
ds_read_b64_tr_b16 a[246:247], v5 offset:25216
v_mfma_f32_16x16x32_bf16 v[168:171], a[248:251], a[56:59], v[168:171]
buffer_load_dwordx4 v13, s[88:91], s54 offen offset:128 lds
v_mfma_f32_16x16x32_bf16 v[172:175], a[248:251], a[60:63], v[172:175]
s_waitcnt lgkmcnt(4)
s_add_u32 s54, s54, 65536
s_add_u32 m0, s70, 116736
v_mfma_f32_16x16x32_bf16 v[188:191], a[252:255], a[60:63], v[188:191]
ds_read_b64_tr_b16 a[248:249], v5 offset:21664
v_mfma_f32_16x16x32_bf16 v[184:187], a[252:255], a[56:59], v[184:187]
ds_read_b64_tr_b16 a[250:251], v5 offset:25248
v_mfma_f32_16x16x32_bf16 v[180:183], a[252:255], a[52:55], v[180:183]
v_mfma_f32_16x16x32_bf16 v[176:179], a[252:255], a[48:51], v[176:179]
s_waitcnt lgkmcnt(4)
v_mfma_f32_16x16x32_bf16 v[192:195], a[240:243], a[48:51], v[192:195]
ds_read_b64_tr_b16 a[252:253], v5 offset:21696
v_mfma_f32_16x16x32_bf16 v[196:199], a[240:243], a[52:55], v[196:199]
ds_read_b64_tr_b16 a[254:255], v5 offset:25280
v_mfma_f32_16x16x32_bf16 v[200:203], a[240:243], a[56:59], v[200:203]
buffer_load_dwordx4 v13, s[88:91], s54 offen lds
v_mfma_f32_16x16x32_bf16 v[204:207], a[240:243], a[60:63], v[204:207]
s_waitcnt lgkmcnt(4)
s_add_u32 m0, s70, 117632
v_mfma_f32_16x16x32_bf16 v[220:223], a[244:247], a[60:63], v[220:223]
ds_read_b64_tr_b16 a[240:241], v4 offset:0
v_mfma_f32_16x16x32_bf16 v[216:219], a[244:247], a[56:59], v[216:219]
ds_read_b64_tr_b16 a[242:243], v4 offset:3584
v_mfma_f32_16x16x32_bf16 v[212:215], a[244:247], a[52:55], v[212:215]
v_mfma_f32_16x16x32_bf16 v[208:211], a[244:247], a[48:51], v[208:211]
s_waitcnt lgkmcnt(4)
v_mfma_f32_16x16x32_bf16 v[224:227], a[248:251], a[48:51], v[224:227]
ds_read_b64_tr_b16 a[244:245], v4 offset:32
v_mfma_f32_16x16x32_bf16 v[228:231], a[248:251], a[52:55], v[228:231]
ds_read_b64_tr_b16 a[246:247], v4 offset:3616
v_mfma_f32_16x16x32_bf16 v[232:235], a[248:251], a[56:59], v[232:235]
buffer_load_dwordx4 v13, s[88:91], s54 offen offset:128 lds
v_mfma_f32_16x16x32_bf16 v[236:239], a[248:251], a[60:63], v[236:239]
s_waitcnt lgkmcnt(4)
v_mfma_f32_16x16x32_bf16 v[252:255], a[252:255], a[60:63], v[252:255]
ds_read_b64_tr_b16 a[248:249], v4 offset:64
v_mfma_f32_16x16x32_bf16 v[248:251], a[252:255], a[56:59], v[248:251]
ds_read_b64_tr_b16 a[250:251], v4 offset:3648
v_mfma_f32_16x16x32_bf16 v[244:247], a[252:255], a[52:55], v[244:247]
v_mfma_f32_16x16x32_bf16 v[240:243], a[252:255], a[48:51], v[240:243]
s_waitcnt lgkmcnt(4)
v_mfma_f32_16x16x32_bf16 v[32:35], a[240:243], a[64:67], v[32:35]
ds_read_b64_tr_b16 a[252:253], v4 offset:96
v_mfma_f32_16x16x32_bf16 v[36:39], a[240:243], a[68:71], v[36:39]
ds_read_b64_tr_b16 a[254:255], v4 offset:3680
v_mfma_f32_16x16x32_bf16 v[40:43], a[240:243], a[72:75], v[40:43]
v_mfma_f32_16x16x32_bf16 v[44:47], a[240:243], a[76:79], v[44:47]
s_waitcnt lgkmcnt(4)
v_mfma_f32_16x16x32_bf16 v[60:63], a[244:247], a[76:79], v[60:63]
ds_read_b64_tr_b16 a[240:241], v4 offset:128
v_mfma_f32_16x16x32_bf16 v[56:59], a[244:247], a[72:75], v[56:59]
ds_read_b64_tr_b16 a[242:243], v4 offset:3712
v_mfma_f32_16x16x32_bf16 v[52:55], a[244:247], a[68:71], v[52:55]
buffer_load_dwordx4 a[160:163], v2, s[24:27], s47 offen offset:0 sc1
v_mfma_f32_16x16x32_bf16 v[48:51], a[244:247], a[64:67], v[48:51]
s_waitcnt lgkmcnt(4)
s_cmp_eq_u32 s99, 4
s_cbranch_scc0 INV_12
buffer_inv sc1
INV_12:
v_mfma_f32_16x16x32_bf16 v[64:67], a[248:251], a[64:67], v[64:67]
ds_read_b64_tr_b16 a[244:245], v4 offset:160
v_add_u32 v5, 86016, v3
v_mfma_f32_16x16x32_bf16 v[68:71], a[248:251], a[68:71], v[68:71]
ds_read_b64_tr_b16 a[246:247], v4 offset:3744
v_mfma_f32_16x16x32_bf16 v[72:75], a[248:251], a[72:75], v[72:75]
v_mfma_f32_16x16x32_bf16 v[76:79], a[248:251], a[76:79], v[76:79]
s_waitcnt lgkmcnt(4)
v_mfma_f32_16x16x32_bf16 v[92:95], a[252:255], a[76:79], v[92:95]
ds_read_b64_tr_b16 a[248:249], v4 offset:192
v_mfma_f32_16x16x32_bf16 v[88:91], a[252:255], a[72:75], v[88:91]
ds_read_b64_tr_b16 a[250:251], v4 offset:3776
v_mfma_f32_16x16x32_bf16 v[84:87], a[252:255], a[68:71], v[84:87]
v_mfma_f32_16x16x32_bf16 v[80:83], a[252:255], a[64:67], v[80:83]
s_waitcnt lgkmcnt(4)
v_mfma_f32_16x16x32_bf16 v[96:99], a[240:243], a[64:67], v[96:99]
ds_read_b64_tr_b16 a[252:253], v4 offset:14336
v_mfma_f32_16x16x32_bf16 v[100:103], a[240:243], a[68:71], v[100:103]
ds_read_b64_tr_b16 a[254:255], v4 offset:17920
v_mfma_f32_16x16x32_bf16 v[104:107], a[240:243], a[72:75], v[104:107]
v_mfma_f32_16x16x32_bf16 v[108:111], a[240:243], a[76:79], v[108:111]
s_waitcnt lgkmcnt(4)
v_mfma_f32_16x16x32_bf16 v[124:127], a[244:247], a[76:79], v[124:127]
ds_read_b64_tr_b16 a[240:241], v4 offset:14368
v_mfma_f32_16x16x32_bf16 v[120:123], a[244:247], a[72:75], v[120:123]
ds_read_b64_tr_b16 a[242:243], v4 offset:17952
v_mfma_f32_16x16x32_bf16 v[116:119], a[244:247], a[68:71], v[116:119]
buffer_load_dwordx4 a[164:167], v2, s[24:27], s47 offen offset:1024 sc1
v_mfma_f32_16x16x32_bf16 v[112:115], a[244:247], a[64:67], v[112:115]
s_waitcnt lgkmcnt(4)
v_mfma_f32_16x16x32_bf16 v[128:131], a[248:251], a[64:67], v[128:131]
ds_read_b64_tr_b16 a[244:245], v4 offset:14400
v_mfma_f32_16x16x32_bf16 v[132:135], a[248:251], a[68:71], v[132:135]
ds_read_b64_tr_b16 a[246:247], v4 offset:17984
v_mfma_f32_16x16x32_bf16 v[136:139], a[248:251], a[72:75], v[136:139]
v_mfma_f32_16x16x32_bf16 v[140:143], a[248:251], a[76:79], v[140:143]
s_waitcnt lgkmcnt(4)
v_mfma_f32_16x16x32_bf16 v[156:159], a[252:255], a[76:79], v[156:159]
ds_read_b64_tr_b16 a[248:249], v4 offset:14432
v_mfma_f32_16x16x32_bf16 v[152:155], a[252:255], a[72:75], v[152:155]
ds_read_b64_tr_b16 a[250:251], v4 offset:18016
v_mfma_f32_16x16x32_bf16 v[148:151], a[252:255], a[68:71], v[148:151]
v_mfma_f32_16x16x32_bf16 v[144:147], a[252:255], a[64:67], v[144:147]
s_waitcnt lgkmcnt(4)
v_mfma_f32_16x16x32_bf16 v[160:163], a[240:243], a[64:67], v[160:163]
ds_read_b64_tr_b16 a[252:253], v4 offset:14464
v_mfma_f32_16x16x32_bf16 v[164:167], a[240:243], a[68:71], v[164:167]
ds_read_b64_tr_b16 a[254:255], v4 offset:18048
v_mfma_f32_16x16x32_bf16 v[168:171], a[240:243], a[72:75], v[168:171]
buffer_load_dwordx4 a[168:171], v2, s[24:27], s47 offen offset:2048 sc1
v_mfma_f32_16x16x32_bf16 v[172:175], a[240:243], a[76:79], v[172:175]
s_waitcnt lgkmcnt(4)
v_mfma_f32_16x16x32_bf16 v[188:191], a[244:247], a[76:79], v[188:191]
ds_read_b64_tr_b16 a[240:241], v4 offset:14496
v_mfma_f32_16x16x32_bf16 v[184:187], a[244:247], a[72:75], v[184:187]
ds_read_b64_tr_b16 a[242:243], v4 offset:18080
v_mfma_f32_16x16x32_bf16 v[180:183], a[244:247], a[68:71], v[180:183]
s_add_u32 s54, s0, 1
s_and_b32 s54, s54, 7
s_mul_i32 s54, s54, 524288
v_mfma_f32_16x16x32_bf16 v[176:179], a[244:247], a[64:67], v[176:179]
s_waitcnt lgkmcnt(4)
s_add_u32 s54, s54, s85
s_add_u32 m0, s70, 131072
v_mfma_f32_16x16x32_bf16 v[192:195], a[248:251], a[64:67], v[192:195]
ds_read_b64_tr_b16 a[244:245], v4 offset:14528
v_mfma_f32_16x16x32_bf16 v[196:199], a[248:251], a[68:71], v[196:199]
ds_read_b64_tr_b16 a[246:247], v4 offset:18112
v_mfma_f32_16x16x32_bf16 v[200:203], a[248:251], a[72:75], v[200:203]
v_mfma_f32_16x16x32_bf16 v[204:207], a[248:251], a[76:79], v[204:207]
s_waitcnt lgkmcnt(4)
v_mfma_f32_16x16x32_bf16 v[220:223], a[252:255], a[76:79], v[220:223]
ds_read_b64_tr_b16 a[248:249], v4 offset:7168
v_mfma_f32_16x16x32_bf16 v[216:219], a[252:255], a[72:75], v[216:219]
ds_read_b64_tr_b16 a[250:251], v4 offset:10752
v_mfma_f32_16x16x32_bf16 v[212:215], a[252:255], a[68:71], v[212:215]
v_mfma_f32_16x16x32_bf16 v[208:211], a[252:255], a[64:67], v[208:211]
s_waitcnt lgkmcnt(4)
v_mfma_f32_16x16x32_bf16 v[224:227], a[240:243], a[64:67], v[224:227]
ds_read_b64_tr_b16 a[252:253], v4 offset:7200
v_mfma_f32_16x16x32_bf16 v[228:231], a[240:243], a[68:71], v[228:231]
ds_read_b64_tr_b16 a[254:255], v4 offset:10784
v_mfma_f32_16x16x32_bf16 v[232:235], a[240:243], a[72:75], v[232:235]
buffer_load_dwordx4 a[172:175], v2, s[24:27], s47 offen offset:3072 sc1
v_mfma_f32_16x16x32_bf16 v[236:239], a[240:243], a[76:79], v[236:239]
s_waitcnt lgkmcnt(4)
s_add_u32 s47, s47, 4096
v_mfma_f32_16x16x32_bf16 v[252:255], a[244:247], a[76:79], v[252:255]
ds_read_b64_tr_b16 a[240:241], v4 offset:7232
v_mfma_f32_16x16x32_bf16 v[248:251], a[244:247], a[72:75], v[248:251]
ds_read_b64_tr_b16 a[242:243], v4 offset:10816
v_mfma_f32_16x16x32_bf16 v[244:247], a[244:247], a[68:71], v[244:247]
v_mfma_f32_16x16x32_bf16 v[240:243], a[244:247], a[64:67], v[240:243]
s_waitcnt lgkmcnt(4)
v_mfma_f32_16x16x32_bf16 v[32:35], a[248:251], a[80:83], v[32:35]
ds_read_b64_tr_b16 a[244:245], v4 offset:7264
v_mfma_f32_16x16x32_bf16 v[36:39], a[248:251], a[84:87], v[36:39]
ds_read_b64_tr_b16 a[246:247], v4 offset:10848
v_mfma_f32_16x16x32_bf16 v[40:43], a[248:251], a[88:91], v[40:43]
buffer_load_dwordx4 a[176:179], v2, s[24:27], s47 offen offset:0 sc1
v_mfma_f32_16x16x32_bf16 v[44:47], a[248:251], a[92:95], v[44:47]
s_waitcnt lgkmcnt(4)
v_mfma_f32_16x16x32_bf16 v[60:63], a[252:255], a[92:95], v[60:63]
ds_read_b64_tr_b16 a[248:249], v4 offset:7296
v_mfma_f32_16x16x32_bf16 v[56:59], a[252:255], a[88:91], v[56:59]
ds_read_b64_tr_b16 a[250:251], v4 offset:10880
v_mfma_f32_16x16x32_bf16 v[52:55], a[252:255], a[84:87], v[52:55]
buffer_load_dwordx4 a[180:183], v2, s[24:27], s47 offen offset:1024 sc1
v_mfma_f32_16x16x32_bf16 v[48:51], a[252:255], a[80:83], v[48:51]
s_waitcnt lgkmcnt(4)
s_cmp_eq_u32 s99, 5
s_cbranch_scc0 INV_13
buffer_inv sc1
INV_13:
v_mfma_f32_16x16x32_bf16 v[64:67], a[240:243], a[80:83], v[64:67]
ds_read_b64_tr_b16 a[252:253], v4 offset:7328
v_mfma_f32_16x16x32_bf16 v[68:71], a[240:243], a[84:87], v[68:71]
ds_read_b64_tr_b16 a[254:255], v4 offset:10912
v_mfma_f32_16x16x32_bf16 v[72:75], a[240:243], a[88:91], v[72:75]
v_mfma_f32_16x16x32_bf16 v[76:79], a[240:243], a[92:95], v[76:79]
s_waitcnt vmcnt(19)
s_barrier
s_waitcnt lgkmcnt(4)
v_mfma_f32_16x16x32_bf16 v[92:95], a[244:247], a[92:95], v[92:95]
ds_read_b64_tr_b16 a[240:241], v4 offset:7360
v_mfma_f32_16x16x32_bf16 v[88:91], a[244:247], a[88:91], v[88:91]
ds_read_b64_tr_b16 a[242:243], v4 offset:10944
v_mfma_f32_16x16x32_bf16 v[84:87], a[244:247], a[84:87], v[84:87]
buffer_load_dwordx4 a[184:187], v2, s[24:27], s47 offen offset:2048 sc1
v_mfma_f32_16x16x32_bf16 v[80:83], a[244:247], a[80:83], v[80:83]
s_waitcnt lgkmcnt(4)
v_mfma_f32_16x16x32_bf16 v[96:99], a[248:251], a[80:83], v[96:99]
ds_read_b64_tr_b16 a[244:245], v4 offset:21504
v_mfma_f32_16x16x32_bf16 v[100:103], a[248:251], a[84:87], v[100:103]
ds_read_b64_tr_b16 a[246:247], v4 offset:25088
v_mfma_f32_16x16x32_bf16 v[104:107], a[248:251], a[88:91], v[104:107]
buffer_load_dwordx4 a[188:191], v2, s[24:27], s47 offen offset:3072 sc1
v_mfma_f32_16x16x32_bf16 v[108:111], a[248:251], a[92:95], v[108:111]
s_waitcnt lgkmcnt(4)
v_mfma_f32_16x16x32_bf16 v[124:127], a[252:255], a[92:95], v[124:127]
ds_read_b64_tr_b16 a[248:249], v4 offset:21536
v_mfma_f32_16x16x32_bf16 v[120:123], a[252:255], a[88:91], v[120:123]
ds_read_b64_tr_b16 a[250:251], v4 offset:25120
v_mfma_f32_16x16x32_bf16 v[116:119], a[252:255], a[84:87], v[116:119]
buffer_load_dwordx4 v13, s[88:91], s54 offen lds
v_mfma_f32_16x16x32_bf16 v[112:115], a[252:255], a[80:83], v[112:115]
s_waitcnt lgkmcnt(4)
s_add_u32 m0, s70, 131968
v_mfma_f32_16x16x32_bf16 v[128:131], a[240:243], a[80:83], v[128:131]
ds_read_b64_tr_b16 a[252:253], v4 offset:21568
v_mfma_f32_16x16x32_bf16 v[132:135], a[240:243], a[84:87], v[132:135]
ds_read_b64_tr_b16 a[254:255], v4 offset:25152
v_mfma_f32_16x16x32_bf16 v[136:139], a[240:243], a[88:91], v[136:139]
buffer_load_dwordx4 v13, s[88:91], s54 offen offset:128 lds
v_mfma_f32_16x16x32_bf16 v[140:143], a[240:243], a[92:95], v[140:143]
s_waitcnt lgkmcnt(4)
s_add_u32 s54, s54, 65536
s_add_u32 m0, s70, 133120
v_mfma_f32_16x16x32_bf16 v[156:159], a[244:247], a[92:95], v[156:159]
ds_read_b64_tr_b16 a[240:241], v4 offset:21600
s_add_u32 s47, s0, 2
v_mfma_f32_16x16x32_bf16 v[152:155], a[244:247], a[88:91], v[152:155]
ds_read_b64_tr_b16 a[242:243], v4 offset:25184
s_and_b32 s47, s47, 7
v_mfma_f32_16x16x32_bf16 v[148:151], a[244:247], a[84:87], v[148:151]
buffer_load_dwordx4 v13, s[88:91], s54 offen lds
v_mfma_f32_16x16x32_bf16 v[144:147], a[244:247], a[80:83], v[144:147]
s_waitcnt lgkmcnt(4)
s_add_u32 m0, s70, 134016
s_mul_i32 s47, s47, 8192
v_mfma_f32_16x16x32_bf16 v[160:163], a[248:251], a[80:83], v[160:163]
ds_read_b64_tr_b16 a[244:245], v4 offset:21632
s_add_u32 s47, s47, s58
v_mfma_f32_16x16x32_bf16 v[164:167], a[248:251], a[84:87], v[164:167]
ds_read_b64_tr_b16 a[246:247], v4 offset:25216
v_mfma_f32_16x16x32_bf16 v[168:171], a[248:251], a[88:91], v[168:171]
buffer_load_dwordx4 v13, s[88:91], s54 offen offset:128 lds
v_mfma_f32_16x16x32_bf16 v[172:175], a[248:251], a[92:95], v[172:175]
s_waitcnt lgkmcnt(4)
s_add_u32 s54, s0, 2
s_and_b32 s54, s54, 7
v_mfma_f32_16x16x32_bf16 v[188:191], a[252:255], a[92:95], v[188:191]
s_mul_i32 s54, s54, 524288
s_add_u32 s54, s54, s85
s_add_u32 m0, s70, 147456
v_mfma_f32_16x16x32_bf16 v[184:187], a[252:255], a[88:91], v[184:187]
ds_read_b64_tr_b16 a[248:249], v4 offset:21664
v_mfma_f32_16x16x32_bf16 v[180:183], a[252:255], a[84:87], v[180:183]
buffer_load_dwordx4 v13, s[88:91], s54 offen lds
ds_read_b64_tr_b16 a[250:251], v4 offset:25248
v_mfma_f32_16x16x32_bf16 v[176:179], a[252:255], a[80:83], v[176:179]
s_waitcnt lgkmcnt(4)
s_add_u32 m0, s70, 148352
v_mfma_f32_16x16x32_bf16 v[192:195], a[240:243], a[80:83], v[192:195]
ds_read_b64_tr_b16 a[252:253], v4 offset:21696
v_mfma_f32_16x16x32_bf16 v[196:199], a[240:243], a[84:87], v[196:199]
ds_read_b64_tr_b16 a[254:255], v4 offset:25280
v_mfma_f32_16x16x32_bf16 v[200:203], a[240:243], a[88:91], v[200:203]
buffer_load_dwordx4 v13, s[88:91], s54 offen offset:128 lds
v_mfma_f32_16x16x32_bf16 v[204:207], a[240:243], a[92:95], v[204:207]
s_waitcnt lgkmcnt(4)
s_add_u32 s54, s54, 65536
s_add_u32 m0, s70, 149504
v_mfma_f32_16x16x32_bf16 v[220:223], a[244:247], a[92:95], v[220:223]
ds_read_b64_tr_b16 a[240:241], v5 offset:0
v_mfma_f32_16x16x32_bf16 v[216:219], a[244:247], a[88:91], v[216:219]
ds_read_b64_tr_b16 a[242:243], v5 offset:3584
v_mfma_f32_16x16x32_bf16 v[212:215], a[244:247], a[84:87], v[212:215]
buffer_load_dwordx4 v13, s[88:91], s54 offen lds
v_mfma_f32_16x16x32_bf16 v[208:211], a[244:247], a[80:83], v[208:211]
s_waitcnt lgkmcnt(4)
s_add_u32 m0, s70, 150400
v_mfma_f32_16x16x32_bf16 v[224:227], a[248:251], a[80:83], v[224:227]
ds_read_b64_tr_b16 a[244:245], v5 offset:32
v_mfma_f32_16x16x32_bf16 v[228:231], a[248:251], a[84:87], v[228:231]
ds_read_b64_tr_b16 a[246:247], v5 offset:3616
v_mfma_f32_16x16x32_bf16 v[232:235], a[248:251], a[88:91], v[232:235]
buffer_load_dwordx4 v13, s[88:91], s54 offen offset:128 lds
v_mfma_f32_16x16x32_bf16 v[236:239], a[248:251], a[92:95], v[236:239]
s_waitcnt lgkmcnt(4)
v_mfma_f32_16x16x32_bf16 v[252:255], a[252:255], a[92:95], v[252:255]
ds_read_b64_tr_b16 a[248:249], v5 offset:64
v_mfma_f32_16x16x32_bf16 v[248:251], a[252:255], a[88:91], v[248:251]
ds_read_b64_tr_b16 a[250:251], v5 offset:3648
v_mfma_f32_16x16x32_bf16 v[244:247], a[252:255], a[84:87], v[244:247]
v_mfma_f32_16x16x32_bf16 v[240:243], a[252:255], a[80:83], v[240:243]
s_waitcnt lgkmcnt(4)
v_mfma_f32_16x16x32_bf16 v[32:35], a[240:243], a[96:99], v[32:35]
ds_read_b64_tr_b16 a[252:253], v5 offset:96
v_mfma_f32_16x16x32_bf16 v[36:39], a[240:243], a[100:103], v[36:39]
ds_read_b64_tr_b16 a[254:255], v5 offset:3680
v_mfma_f32_16x16x32_bf16 v[40:43], a[240:243], a[104:107], v[40:43]
v_mfma_f32_16x16x32_bf16 v[44:47], a[240:243], a[108:111], v[44:47]
s_waitcnt lgkmcnt(4)
v_mfma_f32_16x16x32_bf16 v[60:63], a[244:247], a[108:111], v[60:63]
ds_read_b64_tr_b16 a[240:241], v5 offset:128
v_mfma_f32_16x16x32_bf16 v[56:59], a[244:247], a[104:107], v[56:59]
ds_read_b64_tr_b16 a[242:243], v5 offset:3712
v_mfma_f32_16x16x32_bf16 v[52:55], a[244:247], a[100:103], v[52:55]
buffer_load_dwordx4 a[192:195], v2, s[24:27], s47 offen offset:0 sc1
v_mfma_f32_16x16x32_bf16 v[48:51], a[244:247], a[96:99], v[48:51]
s_waitcnt lgkmcnt(4)
s_cmp_eq_u32 s99, 6
s_cbranch_scc0 INV_14
buffer_inv sc1
INV_14:
v_mfma_f32_16x16x32_bf16 v[64:67], a[248:251], a[96:99], v[64:67]
ds_read_b64_tr_b16 a[244:245], v5 offset:160
v_mfma_f32_16x16x32_bf16 v[68:71], a[248:251], a[100:103], v[68:71]
ds_read_b64_tr_b16 a[246:247], v5 offset:3744
v_mfma_f32_16x16x32_bf16 v[72:75], a[248:251], a[104:107], v[72:75]
v_mfma_f32_16x16x32_bf16 v[76:79], a[248:251], a[108:111], v[76:79]
s_waitcnt lgkmcnt(4)
v_mfma_f32_16x16x32_bf16 v[92:95], a[252:255], a[108:111], v[92:95]
ds_read_b64_tr_b16 a[248:249], v5 offset:192
v_mfma_f32_16x16x32_bf16 v[88:91], a[252:255], a[104:107], v[88:91]
ds_read_b64_tr_b16 a[250:251], v5 offset:3776
v_mfma_f32_16x16x32_bf16 v[84:87], a[252:255], a[100:103], v[84:87]
v_mfma_f32_16x16x32_bf16 v[80:83], a[252:255], a[96:99], v[80:83]
s_waitcnt lgkmcnt(4)
v_mfma_f32_16x16x32_bf16 v[96:99], a[240:243], a[96:99], v[96:99]
ds_read_b64_tr_b16 a[252:253], v5 offset:14336
v_mfma_f32_16x16x32_bf16 v[100:103], a[240:243], a[100:103], v[100:103]
ds_read_b64_tr_b16 a[254:255], v5 offset:17920
v_mfma_f32_16x16x32_bf16 v[104:107], a[240:243], a[104:107], v[104:107]
v_mfma_f32_16x16x32_bf16 v[108:111], a[240:243], a[108:111], v[108:111]
s_waitcnt lgkmcnt(4)
v_mfma_f32_16x16x32_bf16 v[124:127], a[244:247], a[108:111], v[124:127]
ds_read_b64_tr_b16 a[240:241], v5 offset:14368
v_mfma_f32_16x16x32_bf16 v[120:123], a[244:247], a[104:107], v[120:123]
ds_read_b64_tr_b16 a[242:243], v5 offset:17952
v_mfma_f32_16x16x32_bf16 v[116:119], a[244:247], a[100:103], v[116:119]
buffer_load_dwordx4 a[196:199], v2, s[24:27], s47 offen offset:1024 sc1
v_mfma_f32_16x16x32_bf16 v[112:115], a[244:247], a[96:99], v[112:115]
s_waitcnt lgkmcnt(4)
v_mfma_f32_16x16x32_bf16 v[128:131], a[248:251], a[96:99], v[128:131]
ds_read_b64_tr_b16 a[244:245], v5 offset:14400
v_mfma_f32_16x16x32_bf16 v[132:135], a[248:251], a[100:103], v[132:135]
ds_read_b64_tr_b16 a[246:247], v5 offset:17984
v_mfma_f32_16x16x32_bf16 v[136:139], a[248:251], a[104:107], v[136:139]
v_mfma_f32_16x16x32_bf16 v[140:143], a[248:251], a[108:111], v[140:143]
s_waitcnt lgkmcnt(4)
v_mfma_f32_16x16x32_bf16 v[156:159], a[252:255], a[108:111], v[156:159]
ds_read_b64_tr_b16 a[248:249], v5 offset:14432
v_mfma_f32_16x16x32_bf16 v[152:155], a[252:255], a[104:107], v[152:155]
ds_read_b64_tr_b16 a[250:251], v5 offset:18016
v_mfma_f32_16x16x32_bf16 v[148:151], a[252:255], a[100:103], v[148:151]
v_mfma_f32_16x16x32_bf16 v[144:147], a[252:255], a[96:99], v[144:147]
s_waitcnt lgkmcnt(4)
v_mfma_f32_16x16x32_bf16 v[160:163], a[240:243], a[96:99], v[160:163]
ds_read_b64_tr_b16 a[252:253], v5 offset:14464
v_mfma_f32_16x16x32_bf16 v[164:167], a[240:243], a[100:103], v[164:167]
ds_read_b64_tr_b16 a[254:255], v5 offset:18048
v_mfma_f32_16x16x32_bf16 v[168:171], a[240:243], a[104:107], v[168:171]
buffer_load_dwordx4 a[200:203], v2, s[24:27], s47 offen offset:2048 sc1
v_mfma_f32_16x16x32_bf16 v[172:175], a[240:243], a[108:111], v[172:175]
s_waitcnt lgkmcnt(4)
v_mfma_f32_16x16x32_bf16 v[188:191], a[244:247], a[108:111], v[188:191]
ds_read_b64_tr_b16 a[240:241], v5 offset:14496
v_mfma_f32_16x16x32_bf16 v[184:187], a[244:247], a[104:107], v[184:187]
ds_read_b64_tr_b16 a[242:243], v5 offset:18080
v_mfma_f32_16x16x32_bf16 v[180:183], a[244:247], a[100:103], v[180:183]
s_add_u32 s54, s0, 3
s_and_b32 s54, s54, 7
s_mul_i32 s54, s54, 524288
v_mfma_f32_16x16x32_bf16 v[176:179], a[244:247], a[96:99], v[176:179]
s_waitcnt lgkmcnt(4)
s_add_u32 s54, s54, s85
s_add_u32 m0, s70, 0
v_mfma_f32_16x16x32_bf16 v[192:195], a[248:251], a[96:99], v[192:195]
ds_read_b64_tr_b16 a[244:245], v5 offset:14528
v_mfma_f32_16x16x32_bf16 v[196:199], a[248:251], a[100:103], v[196:199]
ds_read_b64_tr_b16 a[246:247], v5 offset:18112
v_mfma_f32_16x16x32_bf16 v[200:203], a[248:251], a[104:107], v[200:203]
v_mfma_f32_16x16x32_bf16 v[204:207], a[248:251], a[108:111], v[204:207]
s_waitcnt lgkmcnt(4)
v_mfma_f32_16x16x32_bf16 v[220:223], a[252:255], a[108:111], v[220:223]
ds_read_b64_tr_b16 a[248:249], v5 offset:7168
v_mfma_f32_16x16x32_bf16 v[216:219], a[252:255], a[104:107], v[216:219]
ds_read_b64_tr_b16 a[250:251], v5 offset:10752
v_mfma_f32_16x16x32_bf16 v[212:215], a[252:255], a[100:103], v[212:215]
v_mfma_f32_16x16x32_bf16 v[208:211], a[252:255], a[96:99], v[208:211]
s_waitcnt lgkmcnt(4)
v_mfma_f32_16x16x32_bf16 v[224:227], a[240:243], a[96:99], v[224:227]
ds_read_b64_tr_b16 a[252:253], v5 offset:7200
v_mfma_f32_16x16x32_bf16 v[228:231], a[240:243], a[100:103], v[228:231]
ds_read_b64_tr_b16 a[254:255], v5 offset:10784
v_mfma_f32_16x16x32_bf16 v[232:235], a[240:243], a[104:107], v[232:235]
buffer_load_dwordx4 a[204:207], v2, s[24:27], s47 offen offset:3072 sc1
v_mfma_f32_16x16x32_bf16 v[236:239], a[240:243], a[108:111], v[236:239]
s_waitcnt lgkmcnt(4)
s_add_u32 s47, s47, 4096
v_mfma_f32_16x16x32_bf16 v[252:255], a[244:247], a[108:111], v[252:255]
ds_read_b64_tr_b16 a[240:241], v5 offset:7232
v_mfma_f32_16x16x32_bf16 v[248:251], a[244:247], a[104:107], v[248:251]
ds_read_b64_tr_b16 a[242:243], v5 offset:10816
v_mfma_f32_16x16x32_bf16 v[244:247], a[244:247], a[100:103], v[244:247]
v_mfma_f32_16x16x32_bf16 v[240:243], a[244:247], a[96:99], v[240:243]
s_waitcnt lgkmcnt(4)
v_mfma_f32_16x16x32_bf16 v[32:35], a[248:251], a[112:115], v[32:35]
ds_read_b64_tr_b16 a[244:245], v5 offset:7264
v_add_u32 v18, 114688, v14
v_mfma_f32_16x16x32_bf16 v[36:39], a[248:251], a[116:119], v[36:39]
ds_read_b64_tr_b16 a[246:247], v5 offset:10848
v_add_u32 v19, 114688, v15
v_mfma_f32_16x16x32_bf16 v[40:43], a[248:251], a[120:123], v[40:43]
buffer_load_dwordx4 a[208:211], v2, s[24:27], s47 offen offset:0 sc1
v_mfma_f32_16x16x32_bf16 v[44:47], a[248:251], a[124:127], v[44:47]
s_waitcnt lgkmcnt(4)
v_add_u32 v20, 114688, v16
v_add_u32 v21, 114688, v17
v_mfma_f32_16x16x32_bf16 v[60:63], a[252:255], a[124:127], v[60:63]
ds_read_b64_tr_b16 a[248:249], v5 offset:7296
v_mfma_f32_16x16x32_bf16 v[56:59], a[252:255], a[120:123], v[56:59]
ds_read_b64_tr_b16 a[250:251], v5 offset:10880
v_mfma_f32_16x16x32_bf16 v[52:55], a[252:255], a[116:119], v[52:55]
buffer_load_dwordx4 a[212:215], v2, s[24:27], s47 offen offset:1024 sc1
v_mfma_f32_16x16x32_bf16 v[48:51], a[252:255], a[112:115], v[48:51]
s_waitcnt lgkmcnt(4)
s_cmp_eq_u32 s99, 7
s_cbranch_scc0 INV_15
buffer_inv sc1
INV_15:
v_mfma_f32_16x16x32_bf16 v[64:67], a[240:243], a[112:115], v[64:67]
ds_read_b64_tr_b16 a[252:253], v5 offset:7328
v_mfma_f32_16x16x32_bf16 v[68:71], a[240:243], a[116:119], v[68:71]
ds_read_b64_tr_b16 a[254:255], v5 offset:10912
v_mfma_f32_16x16x32_bf16 v[72:75], a[240:243], a[120:123], v[72:75]
v_mfma_f32_16x16x32_bf16 v[76:79], a[240:243], a[124:127], v[76:79]
s_waitcnt vmcnt(22)
s_barrier
s_waitcnt lgkmcnt(4)
v_mfma_f32_16x16x32_bf16 v[92:95], a[244:247], a[124:127], v[92:95]
ds_read_b64_tr_b16 a[240:241], v5 offset:7360
v_mfma_f32_16x16x32_bf16 v[88:91], a[244:247], a[120:123], v[88:91]
ds_read_b64_tr_b16 a[242:243], v5 offset:10944
v_mfma_f32_16x16x32_bf16 v[84:87], a[244:247], a[116:119], v[84:87]
buffer_load_dwordx4 a[216:219], v2, s[24:27], s47 offen offset:2048 sc1
v_mfma_f32_16x16x32_bf16 v[80:83], a[244:247], a[112:115], v[80:83]
s_waitcnt lgkmcnt(4)
v_mfma_f32_16x16x32_bf16 v[96:99], a[248:251], a[112:115], v[96:99]
ds_read_b64_tr_b16 a[244:245], v5 offset:21504
v_mfma_f32_16x16x32_bf16 v[100:103], a[248:251], a[116:119], v[100:103]
ds_read_b64_tr_b16 a[246:247], v5 offset:25088
v_mfma_f32_16x16x32_bf16 v[104:107], a[248:251], a[120:123], v[104:107]
buffer_load_dwordx4 a[220:223], v2, s[24:27], s47 offen offset:3072 sc1
v_mfma_f32_16x16x32_bf16 v[108:111], a[248:251], a[124:127], v[108:111]
s_waitcnt lgkmcnt(4)
v_mfma_f32_16x16x32_bf16 v[124:127], a[252:255], a[124:127], v[124:127]
ds_read_b64_tr_b16 a[248:249], v5 offset:21536
v_mfma_f32_16x16x32_bf16 v[120:123], a[252:255], a[120:123], v[120:123]
ds_read_b64_tr_b16 a[250:251], v5 offset:25120
v_mfma_f32_16x16x32_bf16 v[116:119], a[252:255], a[116:119], v[116:119]
v_mfma_f32_16x16x32_bf16 v[112:115], a[252:255], a[112:115], v[112:115]
s_waitcnt lgkmcnt(4)
v_mfma_f32_16x16x32_bf16 v[128:131], a[240:243], a[112:115], v[128:131]
ds_read_b64_tr_b16 a[252:253], v5 offset:21568
v_mfma_f32_16x16x32_bf16 v[132:135], a[240:243], a[116:119], v[132:135]
ds_read_b64_tr_b16 a[254:255], v5 offset:25152
v_mfma_f32_16x16x32_bf16 v[136:139], a[240:243], a[120:123], v[136:139]
buffer_load_dwordx4 v13, s[88:91], s54 offen lds
v_mfma_f32_16x16x32_bf16 v[140:143], a[240:243], a[124:127], v[140:143]
s_waitcnt lgkmcnt(4)
s_add_u32 m0, s70, 896
v_mfma_f32_16x16x32_bf16 v[156:159], a[244:247], a[124:127], v[156:159]
ds_read_b64_tr_b16 a[240:241], v5 offset:21600
v_mfma_f32_16x16x32_bf16 v[152:155], a[244:247], a[120:123], v[152:155]
ds_read_b64_tr_b16 a[242:243], v5 offset:25184
v_mfma_f32_16x16x32_bf16 v[148:151], a[244:247], a[116:119], v[148:151]
v_mfma_f32_16x16x32_bf16 v[144:147], a[244:247], a[112:115], v[144:147]
s_waitcnt lgkmcnt(4)
v_mfma_f32_16x16x32_bf16 v[160:163], a[248:251], a[112:115], v[160:163]
ds_read_b64_tr_b16 a[244:245], v5 offset:21632
v_mfma_f32_16x16x32_bf16 v[164:167], a[248:251], a[116:119], v[164:167]
ds_read_b64_tr_b16 a[246:247], v5 offset:25216
v_mfma_f32_16x16x32_bf16 v[168:171], a[248:251], a[120:123], v[168:171]
buffer_load_dwordx4 v13, s[88:91], s54 offen offset:128 lds
v_mfma_f32_16x16x32_bf16 v[172:175], a[248:251], a[124:127], v[172:175]
s_waitcnt lgkmcnt(4)
s_add_u32 s54, s54, 65536
s_add_u32 m0, s70, 2048
v_mfma_f32_16x16x32_bf16 v[188:191], a[252:255], a[124:127], v[188:191]
ds_read_b64_tr_b16 a[248:249], v5 offset:21664
v_mfma_f32_16x16x32_bf16 v[184:187], a[252:255], a[120:123], v[184:187]
ds_read_b64_tr_b16 a[250:251], v5 offset:25248
v_mfma_f32_16x16x32_bf16 v[180:183], a[252:255], a[116:119], v[180:183]
v_mfma_f32_16x16x32_bf16 v[176:179], a[252:255], a[112:115], v[176:179]
s_waitcnt lgkmcnt(4)
v_mfma_f32_16x16x32_bf16 v[192:195], a[240:243], a[112:115], v[192:195]
ds_read_b64_tr_b16 a[252:253], v5 offset:21696
v_mfma_f32_16x16x32_bf16 v[196:199], a[240:243], a[116:119], v[196:199]
ds_read_b64_tr_b16 a[254:255], v5 offset:25280
v_mfma_f32_16x16x32_bf16 v[200:203], a[240:243], a[120:123], v[200:203]
buffer_load_dwordx4 v13, s[88:91], s54 offen lds
v_mfma_f32_16x16x32_bf16 v[204:207], a[240:243], a[124:127], v[204:207]
s_waitcnt lgkmcnt(4)
s_add_u32 m0, s70, 2944
v_mfma_f32_16x16x32_bf16 v[220:223], a[244:247], a[124:127], v[220:223]
ds_read_b64_tr_b16 a[240:241], v18 offset:0
v_mfma_f32_16x16x32_bf16 v[216:219], a[244:247], a[120:123], v[216:219]
ds_read_b64_tr_b16 a[242:243], v18 offset:4096
v_mfma_f32_16x16x32_bf16 v[212:215], a[244:247], a[116:119], v[212:215]
v_mfma_f32_16x16x32_bf16 v[208:211], a[244:247], a[112:115], v[208:211]
s_waitcnt lgkmcnt(4)
v_mfma_f32_16x16x32_bf16 v[224:227], a[248:251], a[112:115], v[224:227]
ds_read_b64_tr_b16 a[244:245], v19 offset:0
v_mfma_f32_16x16x32_bf16 v[228:231], a[248:251], a[116:119], v[228:231]
ds_read_b64_tr_b16 a[246:247], v19 offset:4096
v_mfma_f32_16x16x32_bf16 v[232:235], a[248:251], a[120:123], v[232:235]
buffer_load_dwordx4 v13, s[88:91], s54 offen offset:128 lds
v_mfma_f32_16x16x32_bf16 v[236:239], a[248:251], a[124:127], v[236:239]
s_waitcnt lgkmcnt(4)
v_mfma_f32_16x16x32_bf16 v[252:255], a[252:255], a[124:127], v[252:255]
ds_read_b64_tr_b16 a[248:249], v20 offset:0
v_mfma_f32_16x16x32_bf16 v[248:251], a[252:255], a[120:123], v[248:251]
ds_read_b64_tr_b16 a[250:251], v20 offset:4096
v_mfma_f32_16x16x32_bf16 v[244:247], a[252:255], a[116:119], v[244:247]
v_mfma_f32_16x16x32_bf16 v[240:243], a[252:255], a[112:115], v[240:243]
s_waitcnt lgkmcnt(4)
KQ:
v_mfma_f32_16x16x32_bf16 a[0:3], a[240:243], a[128:131], 0
ds_read_b64_tr_b16 a[252:253], v21 offset:0
s_add_u32 s47, s0, 3
v_mfma_f32_16x16x32_bf16 a[4:7], a[240:243], a[132:135], 0
ds_read_b64_tr_b16 a[254:255], v21 offset:4096
s_and_b32 s47, s47, 7
v_mfma_f32_16x16x32_bf16 a[8:11], a[240:243], a[136:139], 0
s_mul_i32 s47, s47, 8192
s_add_u32 s47, s47, s58
v_mov_b32 v31, 0xbfb8aa3b
v_mfma_f32_16x16x32_bf16 a[12:15], a[240:243], a[140:143], 0
s_waitcnt lgkmcnt(4)
s_add_u32 s32, s22, 6291456
s_addc_u32 s33, s23, 0
v_mfma_f32_16x16x32_bf16 a[28:31], a[244:247], a[140:143], 0
ds_read_b64_tr_b16 a[240:241], v18 offset:1024
s_and_b32 s33, s33, 0xffff
v_mfma_f32_16x16x32_bf16 a[24:27], a[244:247], a[136:139], 0
ds_read_b64_tr_b16 a[242:243], v18 offset:5120
s_mov_b32 s34, 14680064
v_mfma_f32_16x16x32_bf16 a[20:23], a[244:247], a[132:135], 0
buffer_load_dwordx4 a[224:227], v2, s[24:27], s47 offen offset:0 sc1
v_mfma_f32_16x16x32_bf16 a[16:19], a[244:247], a[128:131], 0
s_waitcnt lgkmcnt(4)
s_mov_b32 s35, 0x20000
v_mfma_f32_16x16x32_bf16 a[32:35], a[248:251], a[128:131], 0
ds_read_b64_tr_b16 a[244:245], v19 offset:1024
v_mfma_f32_16x16x32_bf16 a[36:39], a[248:251], a[132:135], 0
ds_read_b64_tr_b16 a[246:247], v19 offset:5120
v_mfma_f32_16x16x32_bf16 a[40:43], a[248:251], a[136:139], 0
v_mfma_f32_16x16x32_bf16 a[44:47], a[248:251], a[140:143], 0
s_waitcnt lgkmcnt(4)
v_mfma_f32_16x16x32_bf16 a[60:63], a[252:255], a[140:143], 0
ds_read_b64_tr_b16 a[248:249], v20 offset:1024
v_mul_f32 v32, v31, v32
v_mfma_f32_16x16x32_bf16 a[56:59], a[252:255], a[136:139], 0
ds_read_b64_tr_b16 a[250:251], v20 offset:5120
v_add_u32 v22, 131072, v14
v_mfma_f32_16x16x32_bf16 a[52:55], a[252:255], a[132:135], 0
buffer_load_dwordx4 a[228:231], v2, s[24:27], s47 offen offset:1024 sc1
v_mfma_f32_16x16x32_bf16 a[48:51], a[252:255], a[128:131], 0
s_waitcnt lgkmcnt(4)
v_add_u32 v23, 131072, v15
v_add_u32 v24, 131072, v16
v_mfma_f32_16x16x32_bf16 a[64:67], a[240:243], a[128:131], 0
ds_read_b64_tr_b16 a[252:253], v21 offset:1024
v_add_u32 v25, 131072, v17
v_mfma_f32_16x16x32_bf16 a[68:71], a[240:243], a[132:135], 0
ds_read_b64_tr_b16 a[254:255], v21 offset:5120
v_mul_f32 v33, v31, v33
v_mfma_f32_16x16x32_bf16 a[72:75], a[240:243], a[136:139], 0
v_exp_f32 v32, v32
v_mfma_f32_16x16x32_bf16 a[76:79], a[240:243], a[140:143], 0
s_waitcnt lgkmcnt(4)
s_add_u32 s54, s0, 4
s_and_b32 s54, s54, 7
v_mfma_f32_16x16x32_bf16 a[92:95], a[244:247], a[140:143], 0
ds_read_b64_tr_b16 a[240:241], v18 offset:8192
s_mul_i32 s54, s54, 524288
v_mfma_f32_16x16x32_bf16 a[88:91], a[244:247], a[136:139], 0
ds_read_b64_tr_b16 a[242:243], v18 offset:12288
s_add_u32 s54, s54, s85
v_mfma_f32_16x16x32_bf16 a[84:87], a[244:247], a[132:135], 0
buffer_load_dwordx4 a[232:235], v2, s[24:27], s47 offen offset:2048 sc1
v_mfma_f32_16x16x32_bf16 a[80:83], a[244:247], a[128:131], 0
s_waitcnt lgkmcnt(4)
s_add_u32 m0, s70, 16384
v_mul_f32 v34, v31, v34
v_mfma_f32_16x16x32_bf16 a[96:99], a[248:251], a[128:131], 0
ds_read_b64_tr_b16 a[244:245], v19 offset:8192
v_mul_f32 v35, v31, v35
v_mfma_f32_16x16x32_bf16 a[100:103], a[248:251], a[132:135], 0
ds_read_b64_tr_b16 a[246:247], v19 offset:12288
v_add_f32 v32, 1.0, v32
v_mfma_f32_16x16x32_bf16 a[104:107], a[248:251], a[136:139], 0
v_exp_f32 v33, v33
v_mfma_f32_16x16x32_bf16 a[108:111], a[248:251], a[140:143], 0
s_waitcnt lgkmcnt(4)
v_exp_f32 v34, v34
v_mfma_f32_16x16x32_bf16 a[124:127], a[252:255], a[140:143], 0
ds_read_b64_tr_b16 a[248:249], v20 offset:8192
v_add_f32 v33, 1.0, v33
v_mfma_f32_16x16x32_bf16 a[120:123], a[252:255], a[136:139], 0
ds_read_b64_tr_b16 a[250:251], v20 offset:12288
v_add_f32 v34, 1.0, v34
v_mfma_f32_16x16x32_bf16 a[116:119], a[252:255], a[132:135], 0
buffer_load_dwordx4 a[236:239], v2, s[24:27], s47 offen offset:3072 sc1
v_mfma_f32_16x16x32_bf16 a[112:115], a[252:255], a[128:131], 0
s_waitcnt lgkmcnt(4)
s_add_u32 s47, s47, 4096
v_mfma_f32_16x16x32_bf16 a[0:3], a[240:243], a[144:147], a[0:3]
ds_read_b64_tr_b16 a[252:253], v21 offset:8192
v_mul_f32 v48, v31, v48
v_mfma_f32_16x16x32_bf16 a[4:7], a[240:243], a[148:151], a[4:7]
ds_read_b64_tr_b16 a[254:255], v21 offset:12288
v_mul_f32 v49, v31, v49
v_mfma_f32_16x16x32_bf16 a[8:11], a[240:243], a[152:155], a[8:11]
buffer_load_dwordx4 a[128:131], v2, s[24:27], s47 offen offset:0 sc1
v_mfma_f32_16x16x32_bf16 a[12:15], a[240:243], a[156:159], a[12:15]
s_waitcnt vmcnt(21)
s_barrier
s_waitcnt lgkmcnt(4)
v_mfma_f32_16x16x32_bf16 a[28:31], a[244:247], a[156:159], a[28:31]
ds_read_b64_tr_b16 a[240:241], v18 offset:9216
v_mul_f32 v50, v31, v50
v_mfma_f32_16x16x32_bf16 a[24:27], a[244:247], a[152:155], a[24:27]
ds_read_b64_tr_b16 a[242:243], v18 offset:13312
v_mul_f32 v51, v31, v51
v_mfma_f32_16x16x32_bf16 a[20:23], a[244:247], a[148:151], a[20:23]
buffer_load_dwordx4 a[132:135], v2, s[24:27], s47 offen offset:1024 sc1
v_mfma_f32_16x16x32_bf16 a[16:19], a[244:247], a[144:147], a[16:19]
s_waitcnt lgkmcnt(4)
v_exp_f32 v35, v35
v_mfma_f32_16x16x32_bf16 a[32:35], a[248:251], a[144:147], a[32:35]
ds_read_b64_tr_b16 a[244:245], v19 offset:9216
v_add_f32 v35, 1.0, v35
v_mfma_f32_16x16x32_bf16 a[36:39], a[248:251], a[148:151], a[36:39]
ds_read_b64_tr_b16 a[246:247], v19 offset:13312
v_mfma_f32_16x16x32_bf16 a[40:43], a[248:251], a[152:155], a[40:43]
buffer_load_dwordx4 a[136:139], v2, s[24:27], s47 offen offset:2048 sc1
v_mfma_f32_16x16x32_bf16 a[44:47], a[248:251], a[156:159], a[44:47]
s_waitcnt lgkmcnt(4)
v_rcp_f32 v32, v32
v_mfma_f32_16x16x32_bf16 a[60:63], a[252:255], a[156:159], a[60:63]
ds_read_b64_tr_b16 a[248:249], v20 offset:9216
v_mul_f32 v32, v32, v144
v_mfma_f32_16x16x32_bf16 a[56:59], a[252:255], a[152:155], a[56:59]
ds_read_b64_tr_b16 a[250:251], v20 offset:13312
v_mul_f32 v36, v31, v36
v_mfma_f32_16x16x32_bf16 a[52:55], a[252:255], a[148:151], a[52:55]
buffer_load_dwordx4 a[140:143], v2, s[24:27], s47 offen offset:3072 sc1
v_mfma_f32_16x16x32_bf16 a[48:51], a[252:255], a[144:147], a[48:51]
s_waitcnt lgkmcnt(4)
s_add_u32 s47, s0, 4
s_and_b32 s47, s47, 7
v_mfma_f32_16x16x32_bf16 a[64:67], a[240:243], a[144:147], a[64:67]
ds_read_b64_tr_b16 a[252:253], v21 offset:9216
s_mul_i32 s47, s47, 8192
v_mfma_f32_16x16x32_bf16 a[68:71], a[240:243], a[148:151], a[68:71]
ds_read_b64_tr_b16 a[254:255], v21 offset:13312
s_add_u32 s47, s47, s58
v_mfma_f32_16x16x32_bf16 a[72:75], a[240:243], a[152:155], a[72:75]
buffer_load_dwordx4 v13, s[88:91], s54 offen lds
v_mfma_f32_16x16x32_bf16 a[76:79], a[240:243], a[156:159], a[76:79]
s_waitcnt lgkmcnt(4)
s_add_u32 m0, s70, 17280
v_mul_f32 v37, v31, v37
v_mfma_f32_16x16x32_bf16 a[92:95], a[244:247], a[156:159], a[92:95]
ds_read_b64_tr_b16 a[240:241], v22 offset:0
v_mul_f32 v38, v31, v38
v_mfma_f32_16x16x32_bf16 a[88:91], a[244:247], a[152:155], a[88:91]
ds_read_b64_tr_b16 a[242:243], v22 offset:4096
v_mul_f32 v39, v31, v39
v_mfma_f32_16x16x32_bf16 a[84:87], a[244:247], a[148:151], a[84:87]
buffer_load_dwordx4 v13, s[88:91], s54 offen offset:128 lds
v_mfma_f32_16x16x32_bf16 a[80:83], a[244:247], a[144:147], a[80:83]
s_waitcnt lgkmcnt(4)
s_add_u32 s54, s54, 65536
s_add_u32 m0, s70, 18432
v_mfma_f32_16x16x32_bf16 a[96:99], a[248:251], a[144:147], a[96:99]
ds_read_b64_tr_b16 a[244:245], v23 offset:0
v_mfma_f32_16x16x32_bf16 a[100:103], a[248:251], a[148:151], a[100:103]
ds_read_b64_tr_b16 a[246:247], v23 offset:4096
v_mfma_f32_16x16x32_bf16 a[104:107], a[248:251], a[152:155], a[104:107]
buffer_load_dwordx4 v13, s[88:91], s54 offen lds
v_mfma_f32_16x16x32_bf16 a[108:111], a[248:251], a[156:159], a[108:111]
s_waitcnt lgkmcnt(4)
s_add_u32 m0, s70, 19328
v_mfma_f32_16x16x32_bf16 a[124:127], a[252:255], a[156:159], a[124:127]
ds_read_b64_tr_b16 a[248:249], v24 offset:0
v_mfma_f32_16x16x32_bf16 a[120:123], a[252:255], a[152:155], a[120:123]
ds_read_b64_tr_b16 a[250:251], v24 offset:4096
v_mfma_f32_16x16x32_bf16 a[116:119], a[252:255], a[148:151], a[116:119]
buffer_load_dwordx4 v13, s[88:91], s54 offen offset:128 lds
v_mfma_f32_16x16x32_bf16 a[112:115], a[252:255], a[144:147], a[112:115]
s_waitcnt lgkmcnt(4)
v_mfma_f32_16x16x32_bf16 a[0:3], a[240:243], a[160:163], a[0:3]
ds_read_b64_tr_b16 a[252:253], v25 offset:0
v_mfma_f32_16x16x32_bf16 a[4:7], a[240:243], a[164:167], a[4:7]
ds_read_b64_tr_b16 a[254:255], v25 offset:4096
v_mfma_f32_16x16x32_bf16 a[8:11], a[240:243], a[168:171], a[8:11]
v_rcp_f32 v33, v33
v_mfma_f32_16x16x32_bf16 a[12:15], a[240:243], a[172:175], a[12:15]
s_waitcnt lgkmcnt(4)
v_rcp_f32 v34, v34
v_mfma_f32_16x16x32_bf16 a[28:31], a[244:247], a[172:175], a[28:31]
ds_read_b64_tr_b16 a[240:241], v22 offset:1024
v_mul_f32 v33, v33, v145
v_mfma_f32_16x16x32_bf16 a[24:27], a[244:247], a[168:171], a[24:27]
ds_read_b64_tr_b16 a[242:243], v22 offset:5120
v_mul_f32 v34, v34, v146
v_mfma_f32_16x16x32_bf16 a[20:23], a[244:247], a[164:167], a[20:23]
buffer_load_dwordx4 a[144:147], v2, s[24:27], s47 offen offset:0 sc1
v_mfma_f32_16x16x32_bf16 a[16:19], a[244:247], a[160:163], a[16:19]
s_waitcnt lgkmcnt(4)
v_rcp_f32 v35, v35
v_mfma_f32_16x16x32_bf16 a[32:35], a[248:251], a[160:163], a[32:35]
ds_read_b64_tr_b16 a[244:245], v23 offset:1024
v_mul_f32 v35, v35, v147
v_mfma_f32_16x16x32_bf16 a[36:39], a[248:251], a[164:167], a[36:39]
ds_read_b64_tr_b16 a[246:247], v23 offset:5120
v_cvt_pk_bf16_f32 v144, v32, v33
v_mfma_f32_16x16x32_bf16 a[40:43], a[248:251], a[168:171], a[40:43]
v_exp_f32 v48, v48
v_mfma_f32_16x16x32_bf16 a[44:47], a[248:251], a[172:175], a[44:47]
s_waitcnt lgkmcnt(4)
v_exp_f32 v49, v49
v_mfma_f32_16x16x32_bf16 a[60:63], a[252:255], a[172:175], a[60:63]
ds_read_b64_tr_b16 a[248:249], v24 offset:1024
v_add_f32 v48, 1.0, v48
v_mfma_f32_16x16x32_bf16 a[56:59], a[252:255], a[168:171], a[56:59]
ds_read_b64_tr_b16 a[250:251], v24 offset:5120
v_add_u32 v18, 147456, v14
v_mfma_f32_16x16x32_bf16 a[52:55], a[252:255], a[164:167], a[52:55]
buffer_load_dwordx4 a[148:151], v2, s[24:27], s47 offen offset:1024 sc1
v_mfma_f32_16x16x32_bf16 a[48:51], a[252:255], a[160:163], a[48:51]
s_waitcnt lgkmcnt(4)
v_add_u32 v19, 147456, v15
v_add_u32 v20, 147456, v16
v_mfma_f32_16x16x32_bf16 a[64:67], a[240:243], a[160:163], a[64:67]
ds_read_b64_tr_b16 a[252:253], v25 offset:1024
v_add_u32 v21, 147456, v17
v_mfma_f32_16x16x32_bf16 a[68:71], a[240:243], a[164:167], a[68:71]
ds_read_b64_tr_b16 a[254:255], v25 offset:5120
v_add_f32 v49, 1.0, v49
v_mfma_f32_16x16x32_bf16 a[72:75], a[240:243], a[168:171], a[72:75]
v_exp_f32 v50, v50
v_mfma_f32_16x16x32_bf16 a[76:79], a[240:243], a[172:175], a[76:79]
s_waitcnt lgkmcnt(4)
s_add_u32 s54, s0, 5
s_and_b32 s54, s54, 7
v_mfma_f32_16x16x32_bf16 a[92:95], a[244:247], a[172:175], a[92:95]
ds_read_b64_tr_b16 a[240:241], v22 offset:8192
s_mul_i32 s54, s54, 524288
v_mfma_f32_16x16x32_bf16 a[88:91], a[244:247], a[168:171], a[88:91]
ds_read_b64_tr_b16 a[242:243], v22 offset:12288
s_add_u32 s54, s54, s85
v_mfma_f32_16x16x32_bf16 a[84:87], a[244:247], a[164:167], a[84:87]
buffer_load_dwordx4 a[152:155], v2, s[24:27], s47 offen offset:2048 sc1
v_mfma_f32_16x16x32_bf16 a[80:83], a[244:247], a[160:163], a[80:83]
s_waitcnt lgkmcnt(4)
s_add_u32 m0, s70, 32768
v_add_f32 v50, 1.0, v50
v_mfma_f32_16x16x32_bf16 a[96:99], a[248:251], a[160:163], a[96:99]
ds_read_b64_tr_b16 a[244:245], v23 offset:8192
v_cvt_pk_bf16_f32 v145, v34, v35
v_mfma_f32_16x16x32_bf16 a[100:103], a[248:251], a[164:167], a[100:103]
ds_read_b64_tr_b16 a[246:247], v23 offset:12288
v_mul_f32 v52, v31, v52
v_mfma_f32_16x16x32_bf16 a[104:107], a[248:251], a[168:171], a[104:107]
v_exp_f32 v51, v51
v_mfma_f32_16x16x32_bf16 a[108:111], a[248:251], a[172:175], a[108:111]
s_waitcnt lgkmcnt(4)
v_rcp_f32 v48, v48
v_mfma_f32_16x16x32_bf16 a[124:127], a[252:255], a[172:175], a[124:127]
ds_read_b64_tr_b16 a[248:249], v24 offset:8192
v_add_f32 v51, 1.0, v51
v_mfma_f32_16x16x32_bf16 a[120:123], a[252:255], a[168:171], a[120:123]
ds_read_b64_tr_b16 a[250:251], v24 offset:12288
v_mul_f32 v48, v48, v160
v_mfma_f32_16x16x32_bf16 a[116:119], a[252:255], a[164:167], a[116:119]
buffer_load_dwordx4 a[156:159], v2, s[24:27], s47 offen offset:3072 sc1
v_mfma_f32_16x16x32_bf16 a[112:115], a[252:255], a[160:163], a[112:115]
s_waitcnt lgkmcnt(4)
s_add_u32 s47, s47, 4096
v_mfma_f32_16x16x32_bf16 a[0:3], a[240:243], a[176:179], a[0:3]
ds_read_b64_tr_b16 a[252:253], v25 offset:8192
v_mul_f32 v53, v31, v53
v_mfma_f32_16x16x32_bf16 a[4:7], a[240:243], a[180:183], a[4:7]
ds_read_b64_tr_b16 a[254:255], v25 offset:12288
v_mul_f32 v54, v31, v54
v_mfma_f32_16x16x32_bf16 a[8:11], a[240:243], a[184:187], a[8:11]
buffer_load_dwordx4 a[160:163], v2, s[24:27], s47 offen offset:0 sc1
v_mfma_f32_16x16x32_bf16 a[12:15], a[240:243], a[188:191], a[12:15]
s_waitcnt vmcnt(29)
s_barrier
s_waitcnt lgkmcnt(4)
v_mfma_f32_16x16x32_bf16 a[28:31], a[244:247], a[188:191], a[28:31]
ds_read_b64_tr_b16 a[240:241], v22 offset:9216
v_mul_f32 v55, v31, v55
v_mfma_f32_16x16x32_bf16 a[24:27], a[244:247], a[184:187], a[24:27]
ds_read_b64_tr_b16 a[242:243], v22 offset:13312
v_mfma_f32_16x16x32_bf16 a[20:23], a[244:247], a[180:183], a[20:23]
buffer_load_dwordx4 a[164:167], v2, s[24:27], s47 offen offset:1024 sc1
v_mfma_f32_16x16x32_bf16 a[16:19], a[244:247], a[176:179], a[16:19]
s_waitcnt lgkmcnt(4)
v_rcp_f32 v49, v49
v_mfma_f32_16x16x32_bf16 a[32:35], a[248:251], a[176:179], a[32:35]
ds_read_b64_tr_b16 a[244:245], v23 offset:9216
v_mul_f32 v49, v49, v161
v_mfma_f32_16x16x32_bf16 a[36:39], a[248:251], a[180:183], a[36:39]
ds_read_b64_tr_b16 a[246:247], v23 offset:13312
v_cvt_pk_bf16_f32 v146, v48, v49
v_mfma_f32_16x16x32_bf16 a[40:43], a[248:251], a[184:187], a[40:43]
buffer_load_dwordx4 a[168:171], v2, s[24:27], s47 offen offset:2048 sc1
v_mfma_f32_16x16x32_bf16 a[44:47], a[248:251], a[188:191], a[44:47]
s_waitcnt lgkmcnt(4)
v_rcp_f32 v50, v50
v_mfma_f32_16x16x32_bf16 a[60:63], a[252:255], a[188:191], a[60:63]
ds_read_b64_tr_b16 a[248:249], v24 offset:9216
v_mul_f32 v50, v50, v162
v_mfma_f32_16x16x32_bf16 a[56:59], a[252:255], a[184:187], a[56:59]
ds_read_b64_tr_b16 a[250:251], v24 offset:13312
v_mul_f32 v40, v31, v40
v_mfma_f32_16x16x32_bf16 a[52:55], a[252:255], a[180:183], a[52:55]
buffer_load_dwordx4 a[172:175], v2, s[24:27], s47 offen offset:3072 sc1
v_mfma_f32_16x16x32_bf16 a[48:51], a[252:255], a[176:179], a[48:51]
s_waitcnt lgkmcnt(4)
v_rcp_f32 v51, v51
v_mfma_f32_16x16x32_bf16 a[64:67], a[240:243], a[176:179], a[64:67]
ds_read_b64_tr_b16 a[252:253], v25 offset:9216
s_add_u32 s47, s0, 5
v_mfma_f32_16x16x32_bf16 a[68:71], a[240:243], a[180:183], a[68:71]
ds_read_b64_tr_b16 a[254:255], v25 offset:13312
s_and_b32 s47, s47, 7
v_mfma_f32_16x16x32_bf16 a[72:75], a[240:243], a[184:187], a[72:75]
buffer_load_dwordx4 v13, s[88:91], s54 offen lds
v_mfma_f32_16x16x32_bf16 a[76:79], a[240:243], a[188:191], a[76:79]
s_waitcnt lgkmcnt(4)
s_add_u32 m0, s70, 33664
s_mul_i32 s47, s47, 8192
v_mfma_f32_16x16x32_bf16 a[92:95], a[244:247], a[188:191], a[92:95]
ds_read_b64_tr_b16 a[240:241], v18 offset:0
s_add_u32 s47, s47, s58
v_mfma_f32_16x16x32_bf16 a[88:91], a[244:247], a[184:187], a[88:91]
ds_read_b64_tr_b16 a[242:243], v18 offset:4096
v_mul_f32 v51, v51, v163
v_mfma_f32_16x16x32_bf16 a[84:87], a[244:247], a[180:183], a[84:87]
buffer_load_dwordx4 v13, s[88:91], s54 offen offset:128 lds
v_mfma_f32_16x16x32_bf16 a[80:83], a[244:247], a[176:179], a[80:83]
s_waitcnt lgkmcnt(4)
s_add_u32 s54, s54, 65536
s_add_u32 m0, s70, 34816
v_mfma_f32_16x16x32_bf16 a[96:99], a[248:251], a[176:179], a[96:99]
ds_read_b64_tr_b16 a[244:245], v19 offset:0
v_cvt_pk_bf16_f32 v147, v50, v51
v_mfma_f32_16x16x32_bf16 a[100:103], a[248:251], a[180:183], a[100:103]
ds_read_b64_tr_b16 a[246:247], v19 offset:4096
v_mul_f32 v41, v31, v41
v_mfma_f32_16x16x32_bf16 a[104:107], a[248:251], a[184:187], a[104:107]
buffer_load_dwordx4 v13, s[88:91], s54 offen lds
v_mfma_f32_16x16x32_bf16 a[108:111], a[248:251], a[188:191], a[108:111]
s_waitcnt lgkmcnt(4)
s_add_u32 m0, s70, 35712
v_mul_f32 v42, v31, v42
v_mfma_f32_16x16x32_bf16 a[124:127], a[252:255], a[188:191], a[124:127]
ds_read_b64_tr_b16 a[248:249], v20 offset:0
v_mul_f32 v43, v31, v43
v_mfma_f32_16x16x32_bf16 a[120:123], a[252:255], a[184:187], a[120:123]
ds_read_b64_tr_b16 a[250:251], v20 offset:4096
v_mfma_f32_16x16x32_bf16 a[116:119], a[252:255], a[180:183], a[116:119]
buffer_load_dwordx4 v13, s[88:91], s54 offen offset:128 lds
v_mfma_f32_16x16x32_bf16 a[112:115], a[252:255], a[176:179], a[112:115]
s_waitcnt lgkmcnt(4)
s_waitcnt vmcnt(32)
v_mfma_f32_16x16x32_bf16 a[0:3], a[240:243], a[192:195], a[0:3]
ds_read_b64_tr_b16 a[252:253], v21 offset:0
v_mfma_f32_16x16x32_bf16 a[4:7], a[240:243], a[196:199], a[4:7]
ds_read_b64_tr_b16 a[254:255], v21 offset:4096
v_mfma_f32_16x16x32_bf16 a[8:11], a[240:243], a[200:203], a[8:11]
v_exp_f32 v36, v36
v_mfma_f32_16x16x32_bf16 a[12:15], a[240:243], a[204:207], a[12:15]
s_waitcnt lgkmcnt(4)
v_exp_f32 v37, v37
v_mfma_f32_16x16x32_bf16 a[28:31], a[244:247], a[204:207], a[28:31]
ds_read_b64_tr_b16 a[240:241], v18 offset:1024
v_add_f32 v36, 1.0, v36
v_mfma_f32_16x16x32_bf16 a[24:27], a[244:247], a[200:203], a[24:27]
ds_read_b64_tr_b16 a[242:243], v18 offset:5120
v_add_f32 v37, 1.0, v37
v_mfma_f32_16x16x32_bf16 a[20:23], a[244:247], a[196:199], a[20:23]
buffer_load_dwordx4 a[176:179], v2, s[24:27], s47 offen offset:0 sc1
v_mfma_f32_16x16x32_bf16 a[16:19], a[244:247], a[192:195], a[16:19]
s_waitcnt lgkmcnt(4)
v_exp_f32 v38, v38
v_mfma_f32_16x16x32_bf16 a[32:35], a[248:251], a[192:195], a[32:35]
ds_read_b64_tr_b16 a[244:245], v19 offset:1024
v_add_f32 v38, 1.0, v38
v_mfma_f32_16x16x32_bf16 a[36:39], a[248:251], a[196:199], a[36:39]
ds_read_b64_tr_b16 a[246:247], v19 offset:5120
v_mul_f32 v56, v31, v56
v_mfma_f32_16x16x32_bf16 a[40:43], a[248:251], a[200:203], a[40:43]
buffer_store_dwordx4 v[144:147], v2, s[32:35], s86 offen offset:0 sc1
v_mfma_f32_16x16x32_bf16 a[44:47], a[248:251], a[204:207], a[44:47]
s_waitcnt lgkmcnt(4)
v_exp_f32 v39, v39
v_mfma_f32_16x16x32_bf16 a[60:63], a[252:255], a[204:207], a[60:63]
ds_read_b64_tr_b16 a[248:249], v20 offset:1024
v_add_f32 v39, 1.0, v39
v_mfma_f32_16x16x32_bf16 a[56:59], a[252:255], a[200:203], a[56:59]
ds_read_b64_tr_b16 a[250:251], v20 offset:5120
v_add_u32 v22, 0, v14
v_mfma_f32_16x16x32_bf16 a[52:55], a[252:255], a[196:199], a[52:55]
buffer_load_dwordx4 a[180:183], v2, s[24:27], s47 offen offset:1024 sc1
v_mfma_f32_16x16x32_bf16 a[48:51], a[252:255], a[192:195], a[48:51]
s_waitcnt lgkmcnt(4)
v_rcp_f32 v36, v36
v_mfma_f32_16x16x32_bf16 a[64:67], a[240:243], a[192:195], a[64:67]
ds_read_b64_tr_b16 a[252:253], v21 offset:1024
v_mul_f32 v36, v36, v148
v_mfma_f32_16x16x32_bf16 a[68:71], a[240:243], a[196:199], a[68:71]
ds_read_b64_tr_b16 a[254:255], v21 offset:5120
v_add_u32 v23, 0, v15
v_mfma_f32_16x16x32_bf16 a[72:75], a[240:243], a[200:203], a[72:75]
v_rcp_f32 v37, v37
v_mfma_f32_16x16x32_bf16 a[76:79], a[240:243], a[204:207], a[76:79]
s_waitcnt lgkmcnt(4)
v_rcp_f32 v38, v38
v_mfma_f32_16x16x32_bf16 a[92:95], a[244:247], a[204:207], a[92:95]
ds_read_b64_tr_b16 a[240:241], v18 offset:8192
v_mul_f32 v37, v37, v149
v_mfma_f32_16x16x32_bf16 a[88:91], a[244:247], a[200:203], a[88:91]
ds_read_b64_tr_b16 a[242:243], v18 offset:12288
v_mul_f32 v38, v38, v150
v_mfma_f32_16x16x32_bf16 a[84:87], a[244:247], a[196:199], a[84:87]
buffer_load_dwordx4 a[184:187], v2, s[24:27], s47 offen offset:2048 sc1
v_mfma_f32_16x16x32_bf16 a[80:83], a[244:247], a[192:195], a[80:83]
s_waitcnt lgkmcnt(4)
v_rcp_f32 v39, v39
v_mfma_f32_16x16x32_bf16 a[96:99], a[248:251], a[192:195], a[96:99]
ds_read_b64_tr_b16 a[244:245], v19 offset:8192
v_mul_f32 v39, v39, v151
v_mfma_f32_16x16x32_bf16 a[100:103], a[248:251], a[196:199], a[100:103]
ds_read_b64_tr_b16 a[246:247], v19 offset:12288
s_add_u32 s54, s0, 6
v_mfma_f32_16x16x32_bf16 a[104:107], a[248:251], a[200:203], a[104:107]
s_and_b32 s54, s54, 7
s_mul_i32 s54, s54, 524288
s_add_u32 s54, s54, s85
v_mfma_f32_16x16x32_bf16 a[108:111], a[248:251], a[204:207], a[108:111]
s_waitcnt lgkmcnt(4)
s_add_u32 m0, s70, 49152
v_add_u32 v24, 0, v16
v_mfma_f32_16x16x32_bf16 a[124:127], a[252:255], a[204:207], a[124:127]
ds_read_b64_tr_b16 a[248:249], v20 offset:8192
v_add_u32 v25, 0, v17
v_mfma_f32_16x16x32_bf16 a[120:123], a[252:255], a[200:203], a[120:123]
ds_read_b64_tr_b16 a[250:251], v20 offset:12288
v_cvt_pk_bf16_f32 v148, v36, v37
v_mfma_f32_16x16x32_bf16 a[116:119], a[252:255], a[196:199], a[116:119]
buffer_load_dwordx4 a[188:191], v2, s[24:27], s47 offen offset:3072 sc1
v_mfma_f32_16x16x32_bf16 a[112:115], a[252:255], a[192:195], a[112:115]
s_waitcnt lgkmcnt(4)
s_waitcnt vmcnt(33)
s_add_u32 s47, s47, 4096
v_mfma_f32_16x16x32_bf16 a[0:3], a[240:243], a[208:211], a[0:3]
ds_read_b64_tr_b16 a[252:253], v21 offset:8192
v_cvt_pk_bf16_f32 v149, v38, v39
v_mfma_f32_16x16x32_bf16 a[4:7], a[240:243], a[212:215], a[4:7]
ds_read_b64_tr_b16 a[254:255], v21 offset:12288
v_mul_f32 v57, v31, v57
v_mfma_f32_16x16x32_bf16 a[8:11], a[240:243], a[216:219], a[8:11]
buffer_load_dwordx4 a[192:195], v2, s[24:27], s47 offen offset:0 sc1
v_mfma_f32_16x16x32_bf16 a[12:15], a[240:243], a[220:223], a[12:15]
s_waitcnt vmcnt(30)
s_barrier
s_waitcnt lgkmcnt(4)
v_mfma_f32_16x16x32_bf16 a[28:31], a[244:247], a[220:223], a[28:31]
ds_read_b64_tr_b16 a[240:241], v18 offset:9216
v_mul_f32 v58, v31, v58
v_mfma_f32_16x16x32_bf16 a[24:27], a[244:247], a[216:219], a[24:27]
ds_read_b64_tr_b16 a[242:243], v18 offset:13312
v_mul_f32 v59, v31, v59
v_mfma_f32_16x16x32_bf16 a[20:23], a[244:247], a[212:215], a[20:23]
buffer_load_dwordx4 a[196:199], v2, s[24:27], s47 offen offset:1024 sc1
v_mfma_f32_16x16x32_bf16 a[16:19], a[244:247], a[208:211], a[16:19]
s_waitcnt lgkmcnt(4)
v_exp_f32 v52, v52
v_mfma_f32_16x16x32_bf16 a[32:35], a[248:251], a[208:211], a[32:35]
ds_read_b64_tr_b16 a[244:245], v19 offset:9216
v_add_f32 v52, 1.0, v52
v_mfma_f32_16x16x32_bf16 a[36:39], a[248:251], a[212:215], a[36:39]
ds_read_b64_tr_b16 a[246:247], v19 offset:13312
v_mul_f32 v44, v31, v44
v_mfma_f32_16x16x32_bf16 a[40:43], a[248:251], a[216:219], a[40:43]
buffer_load_dwordx4 a[200:203], v2, s[24:27], s47 offen offset:2048 sc1
v_mfma_f32_16x16x32_bf16 a[44:47], a[248:251], a[220:223], a[44:47]
s_waitcnt lgkmcnt(4)
v_exp_f32 v53, v53
v_mfma_f32_16x16x32_bf16 a[60:63], a[252:255], a[220:223], a[60:63]
ds_read_b64_tr_b16 a[248:249], v20 offset:9216
v_add_f32 v53, 1.0, v53
v_mfma_f32_16x16x32_bf16 a[56:59], a[252:255], a[216:219], a[56:59]
ds_read_b64_tr_b16 a[250:251], v20 offset:13312
v_mul_f32 v45, v31, v45
v_mfma_f32_16x16x32_bf16 a[52:55], a[252:255], a[212:215], a[52:55]
buffer_load_dwordx4 a[204:207], v2, s[24:27], s47 offen offset:3072 sc1
v_mfma_f32_16x16x32_bf16 a[48:51], a[252:255], a[208:211], a[48:51]
s_waitcnt lgkmcnt(4)
v_exp_f32 v54, v54
v_mfma_f32_16x16x32_bf16 a[64:67], a[240:243], a[208:211], a[64:67]
ds_read_b64_tr_b16 a[252:253], v21 offset:9216
v_add_f32 v54, 1.0, v54
v_mfma_f32_16x16x32_bf16 a[68:71], a[240:243], a[212:215], a[68:71]
ds_read_b64_tr_b16 a[254:255], v21 offset:13312
s_add_u32 s47, s0, 6
v_mfma_f32_16x16x32_bf16 a[72:75], a[240:243], a[216:219], a[72:75]
buffer_load_dwordx4 v13, s[88:91], s54 offen lds
v_mfma_f32_16x16x32_bf16 a[76:79], a[240:243], a[220:223], a[76:79]
s_waitcnt lgkmcnt(4)
s_add_u32 m0, s70, 50048
s_and_b32 s47, s47, 7
v_mfma_f32_16x16x32_bf16 a[92:95], a[244:247], a[220:223], a[92:95]
ds_read_b64_tr_b16 a[240:241], v22 offset:0
s_mul_i32 s47, s47, 8192
v_mfma_f32_16x16x32_bf16 a[88:91], a[244:247], a[216:219], a[88:91]
ds_read_b64_tr_b16 a[242:243], v22 offset:4096
s_add_u32 s47, s47, s58
v_mfma_f32_16x16x32_bf16 a[84:87], a[244:247], a[212:215], a[84:87]
buffer_load_dwordx4 v13, s[88:91], s54 offen offset:128 lds
v_mfma_f32_16x16x32_bf16 a[80:83], a[244:247], a[208:211], a[80:83]
s_waitcnt lgkmcnt(4)
s_add_u32 s54, s54, 65536
s_add_u32 m0, s70, 51200
v_mfma_f32_16x16x32_bf16 a[96:99], a[248:251], a[208:211], a[96:99]
ds_read_b64_tr_b16 a[244:245], v23 offset:0
v_mul_f32 v46, v31, v46
v_mfma_f32_16x16x32_bf16 a[100:103], a[248:251], a[212:215], a[100:103]
ds_read_b64_tr_b16 a[246:247], v23 offset:4096
v_mul_f32 v47, v31, v47
v_mfma_f32_16x16x32_bf16 a[104:107], a[248:251], a[216:219], a[104:107]
buffer_load_dwordx4 v13, s[88:91], s54 offen lds
v_mfma_f32_16x16x32_bf16 a[108:111], a[248:251], a[220:223], a[108:111]
s_waitcnt lgkmcnt(4)
s_add_u32 m0, s70, 52096
v_mfma_f32_16x16x32_bf16 a[124:127], a[252:255], a[220:223], a[124:127]
ds_read_b64_tr_b16 a[248:249], v24 offset:0
v_mfma_f32_16x16x32_bf16 a[120:123], a[252:255], a[216:219], a[120:123]
ds_read_b64_tr_b16 a[250:251], v24 offset:4096
v_mfma_f32_16x16x32_bf16 a[116:119], a[252:255], a[212:215], a[116:119]
buffer_load_dwordx4 v13, s[88:91], s54 offen offset:128 lds
v_mfma_f32_16x16x32_bf16 a[112:115], a[252:255], a[208:211], a[112:115]
s_waitcnt lgkmcnt(4)
s_waitcnt vmcnt(33)
v_mfma_f32_16x16x32_bf16 a[0:3], a[240:243], a[224:227], a[0:3]
v_exp_f32 v55, v55
v_mfma_f32_16x16x32_bf16 a[4:7], a[240:243], a[228:231], a[4:7]
v_rcp_f32 v52, v52
v_rcp_f32 v53, v53
v_rcp_f32 v54, v54
v_add_f32 v55, 1.0, v55
v_rcp_f32 v55, v55
v_mfma_f32_16x16x32_bf16 a[8:11], a[240:243], a[232:235], a[8:11]
ds_read_b64_tr_b16 a[252:253], v25 offset:0
ds_read_b64_tr_b16 a[254:255], v25 offset:4096
v_mul_f32 v52, v52, v164
v_mul_f32 v53, v53, v165
v_mul_f32 v54, v54, v166
v_mul_f32 v55, v55, v167
v_cvt_pk_bf16_f32 v150, v52, v53
v_cvt_pk_bf16_f32 v151, v54, v55
v_mfma_f32_16x16x32_bf16 a[12:15], a[240:243], a[236:239], a[12:15]
s_waitcnt lgkmcnt(4)
v_exp_f32 v40, v40
v_mfma_f32_16x16x32_bf16 a[28:31], a[244:247], a[236:239], a[28:31]
ds_read_b64_tr_b16 a[240:241], v22 offset:1024
v_add_f32 v40, 1.0, v40
v_mfma_f32_16x16x32_bf16 a[24:27], a[244:247], a[232:235], a[24:27]
ds_read_b64_tr_b16 a[242:243], v22 offset:5120
v_mul_f32 v60, v31, v60
v_mfma_f32_16x16x32_bf16 a[20:23], a[244:247], a[228:231], a[20:23]
buffer_load_dwordx4 a[208:211], v2, s[24:27], s47 offen offset:0 sc1
v_mfma_f32_16x16x32_bf16 a[16:19], a[244:247], a[224:227], a[16:19]
s_waitcnt lgkmcnt(4)
v_exp_f32 v41, v41
v_mfma_f32_16x16x32_bf16 a[32:35], a[248:251], a[224:227], a[32:35]
ds_read_b64_tr_b16 a[244:245], v23 offset:1024
v_add_f32 v41, 1.0, v41
v_mfma_f32_16x16x32_bf16 a[36:39], a[248:251], a[228:231], a[36:39]
ds_read_b64_tr_b16 a[246:247], v23 offset:5120
v_mul_f32 v61, v31, v61
v_mfma_f32_16x16x32_bf16 a[40:43], a[248:251], a[232:235], a[40:43]
buffer_store_dwordx4 v[148:151], v2, s[32:35], s86 offen offset:1024 sc1
v_mfma_f32_16x16x32_bf16 a[44:47], a[248:251], a[236:239], a[44:47]
s_waitcnt lgkmcnt(4)
v_exp_f32 v42, v42
v_mfma_f32_16x16x32_bf16 a[60:63], a[252:255], a[236:239], a[60:63]
ds_read_b64_tr_b16 a[248:249], v24 offset:1024
v_add_f32 v42, 1.0, v42
v_mfma_f32_16x16x32_bf16 a[56:59], a[252:255], a[232:235], a[56:59]
ds_read_b64_tr_b16 a[250:251], v24 offset:5120
v_add_u32 v18, 16384, v14
v_mfma_f32_16x16x32_bf16 a[52:55], a[252:255], a[228:231], a[52:55]
buffer_load_dwordx4 a[212:215], v2, s[24:27], s47 offen offset:1024 sc1
v_mfma_f32_16x16x32_bf16 a[48:51], a[252:255], a[224:227], a[48:51]
s_waitcnt lgkmcnt(4)
v_exp_f32 v43, v43
v_mfma_f32_16x16x32_bf16 a[64:67], a[240:243], a[224:227], a[64:67]
ds_read_b64_tr_b16 a[252:253], v25 offset:1024
v_add_f32 v43, 1.0, v43
v_mfma_f32_16x16x32_bf16 a[68:71], a[240:243], a[228:231], a[68:71]
ds_read_b64_tr_b16 a[254:255], v25 offset:5120
v_add_u32 v19, 16384, v15
v_mfma_f32_16x16x32_bf16 a[72:75], a[240:243], a[232:235], a[72:75]
v_rcp_f32 v40, v40
v_mfma_f32_16x16x32_bf16 a[76:79], a[240:243], a[236:239], a[76:79]
s_waitcnt lgkmcnt(4)
v_rcp_f32 v41, v41
v_mfma_f32_16x16x32_bf16 a[92:95], a[244:247], a[236:239], a[92:95]
ds_read_b64_tr_b16 a[240:241], v22 offset:8192
v_mul_f32 v40, v40, v152
v_mfma_f32_16x16x32_bf16 a[88:91], a[244:247], a[232:235], a[88:91]
ds_read_b64_tr_b16 a[242:243], v22 offset:12288
v_mul_f32 v41, v41, v153
v_mfma_f32_16x16x32_bf16 a[84:87], a[244:247], a[228:231], a[84:87]
buffer_load_dwordx4 a[216:219], v2, s[24:27], s47 offen offset:2048 sc1
v_mfma_f32_16x16x32_bf16 a[80:83], a[244:247], a[224:227], a[80:83]
s_waitcnt lgkmcnt(4)
v_rcp_f32 v42, v42
v_mfma_f32_16x16x32_bf16 a[96:99], a[248:251], a[224:227], a[96:99]
ds_read_b64_tr_b16 a[244:245], v23 offset:8192
v_mul_f32 v42, v42, v154
v_mfma_f32_16x16x32_bf16 a[100:103], a[248:251], a[228:231], a[100:103]
ds_read_b64_tr_b16 a[246:247], v23 offset:12288
s_add_u32 s54, s0, 7
v_mfma_f32_16x16x32_bf16 a[104:107], a[248:251], a[232:235], a[104:107]
s_and_b32 s54, s54, 7
v_rcp_f32 v43, v43
v_mfma_f32_16x16x32_bf16 a[108:111], a[248:251], a[236:239], a[108:111]
s_waitcnt lgkmcnt(4)
s_mul_i32 s54, s54, 524288
v_mul_f32 v43, v43, v155
v_mfma_f32_16x16x32_bf16 a[124:127], a[252:255], a[236:239], a[124:127]
ds_read_b64_tr_b16 a[248:249], v24 offset:8192
s_add_u32 s54, s54, s85
v_mfma_f32_16x16x32_bf16 a[120:123], a[252:255], a[232:235], a[120:123]
ds_read_b64_tr_b16 a[250:251], v24 offset:12288
s_add_u32 m0, s70, 65536
v_mfma_f32_16x16x32_bf16 a[116:119], a[252:255], a[228:231], a[116:119]
buffer_load_dwordx4 a[220:223], v2, s[24:27], s47 offen offset:3072 sc1
v_mfma_f32_16x16x32_bf16 a[112:115], a[252:255], a[224:227], a[112:115]
s_waitcnt lgkmcnt(4)
s_waitcnt vmcnt(34)
s_add_u32 s47, s47, 4096
v_mfma_f32_16x16x32_bf16 a[0:3], a[240:243], a[128:131], a[0:3]
ds_read_b64_tr_b16 a[252:253], v25 offset:8192
v_add_u32 v20, 16384, v16
v_mfma_f32_16x16x32_bf16 a[4:7], a[240:243], a[132:135], a[4:7]
ds_read_b64_tr_b16 a[254:255], v25 offset:12288
v_add_u32 v21, 16384, v17
v_mfma_f32_16x16x32_bf16 a[8:11], a[240:243], a[136:139], a[8:11]
buffer_load_dwordx4 a[224:227], v2, s[24:27], s47 offen offset:0 sc1
v_mfma_f32_16x16x32_bf16 a[12:15], a[240:243], a[140:143], a[12:15]
s_waitcnt vmcnt(31)
s_barrier
s_waitcnt lgkmcnt(4)
v_mfma_f32_16x16x32_bf16 a[28:31], a[244:247], a[140:143], a[28:31]
ds_read_b64_tr_b16 a[240:241], v22 offset:9216
v_cvt_pk_bf16_f32 v152, v40, v41
v_mfma_f32_16x16x32_bf16 a[24:27], a[244:247], a[136:139], a[24:27]
ds_read_b64_tr_b16 a[242:243], v22 offset:13312
v_cvt_pk_bf16_f32 v153, v42, v43
v_mfma_f32_16x16x32_bf16 a[20:23], a[244:247], a[132:135], a[20:23]
buffer_load_dwordx4 a[228:231], v2, s[24:27], s47 offen offset:1024 sc1
v_mfma_f32_16x16x32_bf16 a[16:19], a[244:247], a[128:131], a[16:19]
s_waitcnt lgkmcnt(4)
v_exp_f32 v56, v56
v_mfma_f32_16x16x32_bf16 a[32:35], a[248:251], a[128:131], a[32:35]
ds_read_b64_tr_b16 a[244:245], v23 offset:9216
v_add_f32 v56, 1.0, v56
v_mfma_f32_16x16x32_bf16 a[36:39], a[248:251], a[132:135], a[36:39]
ds_read_b64_tr_b16 a[246:247], v23 offset:13312
v_mul_f32 v62, v31, v62
v_mfma_f32_16x16x32_bf16 a[40:43], a[248:251], a[136:139], a[40:43]
buffer_load_dwordx4 a[232:235], v2, s[24:27], s47 offen offset:2048 sc1
v_mfma_f32_16x16x32_bf16 a[44:47], a[248:251], a[140:143], a[44:47]
s_waitcnt lgkmcnt(4)
v_exp_f32 v57, v57
v_mfma_f32_16x16x32_bf16 a[60:63], a[252:255], a[140:143], a[60:63]
ds_read_b64_tr_b16 a[248:249], v24 offset:9216
v_add_f32 v57, 1.0, v57
v_mfma_f32_16x16x32_bf16 a[56:59], a[252:255], a[136:139], a[56:59]
ds_read_b64_tr_b16 a[250:251], v24 offset:13312
v_mul_f32 v63, v31, v63
v_mfma_f32_16x16x32_bf16 a[52:55], a[252:255], a[132:135], a[52:55]
buffer_load_dwordx4 a[236:239], v2, s[24:27], s47 offen offset:3072 sc1
v_mfma_f32_16x16x32_bf16 a[48:51], a[252:255], a[128:131], a[48:51]
s_waitcnt lgkmcnt(4)
v_exp_f32 v58, v58
v_mfma_f32_16x16x32_bf16 a[64:67], a[240:243], a[128:131], a[64:67]
ds_read_b64_tr_b16 a[252:253], v25 offset:9216
v_add_f32 v58, 1.0, v58
v_mfma_f32_16x16x32_bf16 a[68:71], a[240:243], a[132:135], a[68:71]
ds_read_b64_tr_b16 a[254:255], v25 offset:13312
s_add_u32 s47, s0, 7
v_mfma_f32_16x16x32_bf16 a[72:75], a[240:243], a[136:139], a[72:75]
buffer_load_dwordx4 v13, s[88:91], s54 offen lds
v_mfma_f32_16x16x32_bf16 a[76:79], a[240:243], a[140:143], a[76:79]
s_waitcnt lgkmcnt(4)
s_add_u32 m0, s70, 66432
s_and_b32 s47, s47, 7
v_mfma_f32_16x16x32_bf16 a[92:95], a[244:247], a[140:143], a[92:95]
ds_read_b64_tr_b16 a[240:241], v18 offset:0
s_mul_i32 s47, s47, 8192
v_mfma_f32_16x16x32_bf16 a[88:91], a[244:247], a[136:139], a[88:91]
ds_read_b64_tr_b16 a[242:243], v18 offset:4096
s_add_u32 s47, s47, s58
v_mfma_f32_16x16x32_bf16 a[84:87], a[244:247], a[132:135], a[84:87]
buffer_load_dwordx4 v13, s[88:91], s54 offen offset:128 lds
v_mfma_f32_16x16x32_bf16 a[80:83], a[244:247], a[128:131], a[80:83]
s_waitcnt lgkmcnt(4)
s_add_u32 s54, s54, 65536
s_add_u32 m0, s70, 67584
v_mfma_f32_16x16x32_bf16 a[96:99], a[248:251], a[128:131], a[96:99]
ds_read_b64_tr_b16 a[244:245], v19 offset:0
v_mul_f32 v64, v31, v64
v_mfma_f32_16x16x32_bf16 a[100:103], a[248:251], a[132:135], a[100:103]
ds_read_b64_tr_b16 a[246:247], v19 offset:4096
v_mul_f32 v65, v31, v65
v_mfma_f32_16x16x32_bf16 a[104:107], a[248:251], a[136:139], a[104:107]
buffer_load_dwordx4 v13, s[88:91], s54 offen lds
v_mfma_f32_16x16x32_bf16 a[108:111], a[248:251], a[140:143], a[108:111]
s_waitcnt lgkmcnt(4)
s_add_u32 m0, s70, 68480
v_mul_f32 v66, v31, v66
v_mfma_f32_16x16x32_bf16 a[124:127], a[252:255], a[140:143], a[124:127]
v_exp_f32 v59, v59
v_mfma_f32_16x16x32_bf16 a[120:123], a[252:255], a[136:139], a[120:123]
ds_read_b64_tr_b16 a[248:249], v20 offset:0
v_add_f32 v59, 1.0, v59
v_mfma_f32_16x16x32_bf16 a[116:119], a[252:255], a[132:135], a[116:119]
buffer_load_dwordx4 v13, s[88:91], s54 offen offset:128 lds
ds_read_b64_tr_b16 a[250:251], v20 offset:4096
v_rcp_f32 v56, v56
v_rcp_f32 v57, v57
v_rcp_f32 v58, v58
v_rcp_f32 v59, v59
v_mfma_f32_16x16x32_bf16 a[112:115], a[252:255], a[128:131], a[112:115]
s_waitcnt lgkmcnt(4)
s_waitcnt vmcnt(34)
v_mul_f32 v56, v56, v168
v_mul_f32 v57, v57, v169
v_mul_f32 v58, v58, v170
v_mul_f32 v59, v59, v171
v_cvt_pk_bf16_f32 v154, v56, v57
v_cvt_pk_bf16_f32 v155, v58, v59
v_mfma_f32_16x16x32_bf16 a[0:3], a[240:243], a[144:147], a[0:3]
ds_read_b64_tr_b16 a[252:253], v21 offset:0
v_mul_f32 v67, v31, v67
v_mfma_f32_16x16x32_bf16 a[4:7], a[240:243], a[148:151], a[4:7]
ds_read_b64_tr_b16 a[254:255], v21 offset:4096
v_mul_f32 v80, v31, v80
v_mfma_f32_16x16x32_bf16 a[8:11], a[240:243], a[152:155], a[8:11]
buffer_store_dwordx4 v[152:155], v2, s[32:35], s86 offen offset:2048 sc1
v_mfma_f32_16x16x32_bf16 a[12:15], a[240:243], a[156:159], a[12:15]
s_waitcnt lgkmcnt(4)
v_exp_f32 v44, v44
v_mfma_f32_16x16x32_bf16 a[28:31], a[244:247], a[156:159], a[28:31]
ds_read_b64_tr_b16 a[240:241], v18 offset:1024
v_add_f32 v44, 1.0, v44
v_mfma_f32_16x16x32_bf16 a[24:27], a[244:247], a[152:155], a[24:27]
ds_read_b64_tr_b16 a[242:243], v18 offset:5120
v_mul_f32 v81, v31, v81
v_mfma_f32_16x16x32_bf16 a[20:23], a[244:247], a[148:151], a[20:23]
buffer_load_dwordx4 a[128:131], v2, s[24:27], s47 offen offset:0 sc1
v_mfma_f32_16x16x32_bf16 a[16:19], a[244:247], a[144:147], a[16:19]
s_waitcnt lgkmcnt(4)
v_exp_f32 v45, v45
v_mfma_f32_16x16x32_bf16 a[32:35], a[248:251], a[144:147], a[32:35]
ds_read_b64_tr_b16 a[244:245], v19 offset:1024
v_add_f32 v45, 1.0, v45
v_mfma_f32_16x16x32_bf16 a[36:39], a[248:251], a[148:151], a[36:39]
ds_read_b64_tr_b16 a[246:247], v19 offset:5120
v_mul_f32 v82, v31, v82
v_mfma_f32_16x16x32_bf16 a[40:43], a[248:251], a[152:155], a[40:43]
v_exp_f32 v46, v46
v_mfma_f32_16x16x32_bf16 a[44:47], a[248:251], a[156:159], a[44:47]
s_waitcnt lgkmcnt(4)
v_exp_f32 v47, v47
v_mfma_f32_16x16x32_bf16 a[60:63], a[252:255], a[156:159], a[60:63]
ds_read_b64_tr_b16 a[248:249], v20 offset:1024
v_add_f32 v46, 1.0, v46
v_mfma_f32_16x16x32_bf16 a[56:59], a[252:255], a[152:155], a[56:59]
ds_read_b64_tr_b16 a[250:251], v20 offset:5120
v_add_f32 v47, 1.0, v47
v_mfma_f32_16x16x32_bf16 a[52:55], a[252:255], a[148:151], a[52:55]
buffer_load_dwordx4 a[132:135], v2, s[24:27], s47 offen offset:1024 sc1
v_mfma_f32_16x16x32_bf16 a[48:51], a[252:255], a[144:147], a[48:51]
s_waitcnt lgkmcnt(4)
v_rcp_f32 v44, v44
v_mfma_f32_16x16x32_bf16 a[64:67], a[240:243], a[144:147], a[64:67]
ds_read_b64_tr_b16 a[252:253], v21 offset:1024
v_mul_f32 v44, v44, v156
v_mfma_f32_16x16x32_bf16 a[68:71], a[240:243], a[148:151], a[68:71]
ds_read_b64_tr_b16 a[254:255], v21 offset:5120
v_add_u32 v22, 32768, v14
v_mfma_f32_16x16x32_bf16 a[72:75], a[240:243], a[152:155], a[72:75]
v_rcp_f32 v45, v45
v_mfma_f32_16x16x32_bf16 a[76:79], a[240:243], a[156:159], a[76:79]
s_waitcnt lgkmcnt(4)
v_rcp_f32 v46, v46
v_mfma_f32_16x16x32_bf16 a[92:95], a[244:247], a[156:159], a[92:95]
ds_read_b64_tr_b16 a[240:241], v18 offset:8192
v_mul_f32 v45, v45, v157
v_mfma_f32_16x16x32_bf16 a[88:91], a[244:247], a[152:155], a[88:91]
ds_read_b64_tr_b16 a[242:243], v18 offset:12288
v_mul_f32 v46, v46, v158
v_mfma_f32_16x16x32_bf16 a[84:87], a[244:247], a[148:151], a[84:87]
buffer_load_dwordx4 a[136:139], v2, s[24:27], s47 offen offset:2048 sc1
v_mfma_f32_16x16x32_bf16 a[80:83], a[244:247], a[144:147], a[80:83]
s_waitcnt lgkmcnt(4)
v_rcp_f32 v47, v47
v_mfma_f32_16x16x32_bf16 a[96:99], a[248:251], a[144:147], a[96:99]
ds_read_b64_tr_b16 a[244:245], v19 offset:8192
v_mul_f32 v47, v47, v159
v_mfma_f32_16x16x32_bf16 a[100:103], a[248:251], a[148:151], a[100:103]
ds_read_b64_tr_b16 a[246:247], v19 offset:12288
s_add_u32 s54, s0, 0
v_mfma_f32_16x16x32_bf16 a[104:107], a[248:251], a[152:155], a[104:107]
s_and_b32 s54, s54, 7
s_mul_i32 s54, s54, 524288
s_add_u32 s54, s54, s85
v_mfma_f32_16x16x32_bf16 a[108:111], a[248:251], a[156:159], a[108:111]
s_waitcnt lgkmcnt(4)
s_add_u32 s54, s54, 4194304
s_add_u32 m0, s70, 81920
v_mfma_f32_16x16x32_bf16 a[124:127], a[252:255], a[156:159], a[124:127]
ds_read_b64_tr_b16 a[248:249], v20 offset:8192
v_add_u32 v23, 32768, v15
v_mfma_f32_16x16x32_bf16 a[120:123], a[252:255], a[152:155], a[120:123]
ds_read_b64_tr_b16 a[250:251], v20 offset:12288
v_add_u32 v24, 32768, v16
v_mfma_f32_16x16x32_bf16 a[116:119], a[252:255], a[148:151], a[116:119]
buffer_load_dwordx4 a[140:143], v2, s[24:27], s47 offen offset:3072 sc1
v_mfma_f32_16x16x32_bf16 a[112:115], a[252:255], a[144:147], a[112:115]
s_waitcnt lgkmcnt(4)
s_waitcnt vmcnt(35)
s_add_u32 s47, s47, 4096
v_mfma_f32_16x16x32_bf16 a[0:3], a[240:243], a[160:163], a[0:3]
ds_read_b64_tr_b16 a[252:253], v21 offset:8192
v_cvt_pk_bf16_f32 v156, v44, v45
v_mfma_f32_16x16x32_bf16 a[4:7], a[240:243], a[164:167], a[4:7]
ds_read_b64_tr_b16 a[254:255], v21 offset:12288
v_cvt_pk_bf16_f32 v157, v46, v47
v_mfma_f32_16x16x32_bf16 a[8:11], a[240:243], a[168:171], a[8:11]
buffer_load_dwordx4 a[144:147], v2, s[24:27], s47 offen offset:0 sc1
v_mfma_f32_16x16x32_bf16 a[12:15], a[240:243], a[172:175], a[12:15]
s_waitcnt vmcnt(32)
s_barrier
s_waitcnt lgkmcnt(4)
v_mfma_f32_16x16x32_bf16 a[28:31], a[244:247], a[172:175], a[28:31]
ds_read_b64_tr_b16 a[240:241], v18 offset:9216
v_add_u32 v25, 32768, v17
v_mfma_f32_16x16x32_bf16 a[24:27], a[244:247], a[168:171], a[24:27]
ds_read_b64_tr_b16 a[242:243], v18 offset:13312
v_mul_f32 v83, v31, v83
v_mfma_f32_16x16x32_bf16 a[20:23], a[244:247], a[164:167], a[20:23]
buffer_load_dwordx4 a[148:151], v2, s[24:27], s47 offen offset:1024 sc1
v_mfma_f32_16x16x32_bf16 a[16:19], a[244:247], a[160:163], a[16:19]
s_waitcnt lgkmcnt(4)
v_exp_f32 v60, v60
v_mfma_f32_16x16x32_bf16 a[32:35], a[248:251], a[160:163], a[32:35]
ds_read_b64_tr_b16 a[244:245], v19 offset:9216
v_add_f32 v60, 1.0, v60
v_mfma_f32_16x16x32_bf16 a[36:39], a[248:251], a[164:167], a[36:39]
ds_read_b64_tr_b16 a[246:247], v19 offset:13312
v_mul_f32 v68, v31, v68
v_mfma_f32_16x16x32_bf16 a[40:43], a[248:251], a[168:171], a[40:43]
buffer_load_dwordx4 a[152:155], v2, s[24:27], s47 offen offset:2048 sc1
v_mfma_f32_16x16x32_bf16 a[44:47], a[248:251], a[172:175], a[44:47]
s_waitcnt lgkmcnt(4)
v_exp_f32 v61, v61
v_mfma_f32_16x16x32_bf16 a[60:63], a[252:255], a[172:175], a[60:63]
ds_read_b64_tr_b16 a[248:249], v20 offset:9216
v_add_f32 v61, 1.0, v61
v_mfma_f32_16x16x32_bf16 a[56:59], a[252:255], a[168:171], a[56:59]
ds_read_b64_tr_b16 a[250:251], v20 offset:13312
v_mul_f32 v69, v31, v69
v_mfma_f32_16x16x32_bf16 a[52:55], a[252:255], a[164:167], a[52:55]
buffer_load_dwordx4 a[156:159], v2, s[24:27], s47 offen offset:3072 sc1
v_mfma_f32_16x16x32_bf16 a[48:51], a[252:255], a[160:163], a[48:51]
s_waitcnt lgkmcnt(4)
v_exp_f32 v62, v62
v_mfma_f32_16x16x32_bf16 a[64:67], a[240:243], a[160:163], a[64:67]
ds_read_b64_tr_b16 a[252:253], v21 offset:9216
v_add_f32 v62, 1.0, v62
v_mfma_f32_16x16x32_bf16 a[68:71], a[240:243], a[164:167], a[68:71]
ds_read_b64_tr_b16 a[254:255], v21 offset:13312
s_add_u32 s47, s0, 0
v_mfma_f32_16x16x32_bf16 a[72:75], a[240:243], a[168:171], a[72:75]
buffer_load_dwordx4 v13, s[88:91], s54 offen lds
v_mfma_f32_16x16x32_bf16 a[76:79], a[240:243], a[172:175], a[76:79]
s_waitcnt lgkmcnt(4)
s_add_u32 m0, s70, 82816
s_and_b32 s47, s47, 7
v_mfma_f32_16x16x32_bf16 a[92:95], a[244:247], a[172:175], a[92:95]
ds_read_b64_tr_b16 a[240:241], v22 offset:0
s_mul_i32 s47, s47, 8192
v_mfma_f32_16x16x32_bf16 a[88:91], a[244:247], a[168:171], a[88:91]
ds_read_b64_tr_b16 a[242:243], v22 offset:4096
s_add_u32 s47, s47, s58
v_mfma_f32_16x16x32_bf16 a[84:87], a[244:247], a[164:167], a[84:87]
buffer_load_dwordx4 v13, s[88:91], s54 offen offset:128 lds
v_mfma_f32_16x16x32_bf16 a[80:83], a[244:247], a[160:163], a[80:83]
s_waitcnt lgkmcnt(4)
s_add_u32 s54, s54, 65536
s_add_u32 m0, s70, 83968
v_mfma_f32_16x16x32_bf16 a[96:99], a[248:251], a[160:163], a[96:99]
ds_read_b64_tr_b16 a[244:245], v23 offset:0
s_add_u32 s47, s47, 65536
v_mfma_f32_16x16x32_bf16 a[100:103], a[248:251], a[164:167], a[100:103]
ds_read_b64_tr_b16 a[246:247], v23 offset:4096
v_mul_f32 v70, v31, v70
v_mfma_f32_16x16x32_bf16 a[104:107], a[248:251], a[168:171], a[104:107]
buffer_load_dwordx4 v13, s[88:91], s54 offen lds
v_exp_f32 v63, v63
v_mfma_f32_16x16x32_bf16 a[108:111], a[248:251], a[172:175], a[108:111]
s_waitcnt lgkmcnt(4)
v_rcp_f32 v60, v60
v_rcp_f32 v61, v61
v_rcp_f32 v62, v62
v_add_f32 v63, 1.0, v63
v_rcp_f32 v63, v63
v_mfma_f32_16x16x32_bf16 a[124:127], a[252:255], a[172:175], a[124:127]
s_add_u32 m0, s70, 84864
v_mul_f32 v60, v60, v172
v_mul_f32 v61, v61, v173
v_mul_f32 v62, v62, v174
v_mul_f32 v63, v63, v175
v_cvt_pk_bf16_f32 v158, v60, v61
v_cvt_pk_bf16_f32 v159, v62, v63
v_mfma_f32_16x16x32_bf16 a[120:123], a[252:255], a[168:171], a[120:123]
ds_read_b64_tr_b16 a[248:249], v24 offset:0
v_mul_f32 v71, v31, v71
v_mfma_f32_16x16x32_bf16 a[116:119], a[252:255], a[164:167], a[116:119]
buffer_load_dwordx4 v13, s[88:91], s54 offen offset:128 lds
ds_read_b64_tr_b16 a[250:251], v24 offset:4096
v_mfma_f32_16x16x32_bf16 a[112:115], a[252:255], a[160:163], a[112:115]
s_waitcnt lgkmcnt(4)
s_waitcnt vmcnt(34)
v_mul_f32 v84, v31, v84
v_mfma_f32_16x16x32_bf16 a[0:3], a[240:243], a[176:179], a[0:3]
ds_read_b64_tr_b16 a[252:253], v25 offset:0
v_mul_f32 v85, v31, v85
v_mfma_f32_16x16x32_bf16 a[4:7], a[240:243], a[180:183], a[4:7]
ds_read_b64_tr_b16 a[254:255], v25 offset:4096
v_mul_f32 v86, v31, v86
v_mfma_f32_16x16x32_bf16 a[8:11], a[240:243], a[184:187], a[8:11]
buffer_store_dwordx4 v[156:159], v2, s[32:35], s86 offen offset:3072 sc1
v_mfma_f32_16x16x32_bf16 a[12:15], a[240:243], a[188:191], a[12:15]
s_waitcnt lgkmcnt(4)
s_add_u32 s86, s86, 4096
v_mul_f32 v87, v31, v87
v_mfma_f32_16x16x32_bf16 a[28:31], a[244:247], a[188:191], a[28:31]
ds_read_b64_tr_b16 a[240:241], v22 offset:1024
v_mfma_f32_16x16x32_bf16 a[24:27], a[244:247], a[184:187], a[24:27]
ds_read_b64_tr_b16 a[242:243], v22 offset:5120
v_mfma_f32_16x16x32_bf16 a[20:23], a[244:247], a[180:183], a[20:23]
buffer_load_dwordx4 a[160:163], v2, s[24:27], s47 offen offset:0 sc1
v_mfma_f32_16x16x32_bf16 a[16:19], a[244:247], a[176:179], a[16:19]
s_waitcnt lgkmcnt(4)
v_exp_f32 v64, v64
v_mfma_f32_16x16x32_bf16 a[32:35], a[248:251], a[176:179], a[32:35]
ds_read_b64_tr_b16 a[244:245], v23 offset:1024
v_add_f32 v64, 1.0, v64
v_mfma_f32_16x16x32_bf16 a[36:39], a[248:251], a[180:183], a[36:39]
ds_read_b64_tr_b16 a[246:247], v23 offset:5120
v_mfma_f32_16x16x32_bf16 a[40:43], a[248:251], a[184:187], a[40:43]
v_exp_f32 v65, v65
v_mfma_f32_16x16x32_bf16 a[44:47], a[248:251], a[188:191], a[44:47]
s_waitcnt lgkmcnt(4)
v_exp_f32 v66, v66
v_mfma_f32_16x16x32_bf16 a[60:63], a[252:255], a[188:191], a[60:63]
ds_read_b64_tr_b16 a[248:249], v24 offset:1024
v_add_f32 v65, 1.0, v65
v_mfma_f32_16x16x32_bf16 a[56:59], a[252:255], a[184:187], a[56:59]
ds_read_b64_tr_b16 a[250:251], v24 offset:5120
v_add_f32 v66, 1.0, v66
v_mfma_f32_16x16x32_bf16 a[52:55], a[252:255], a[180:183], a[52:55]
buffer_load_dwordx4 a[164:167], v2, s[24:27], s47 offen offset:1024 sc1
v_mfma_f32_16x16x32_bf16 a[48:51], a[252:255], a[176:179], a[48:51]
s_waitcnt lgkmcnt(4)
v_exp_f32 v67, v67
v_mfma_f32_16x16x32_bf16 a[64:67], a[240:243], a[176:179], a[64:67]
ds_read_b64_tr_b16 a[252:253], v25 offset:1024
v_add_f32 v67, 1.0, v67
v_mfma_f32_16x16x32_bf16 a[68:71], a[240:243], a[180:183], a[68:71]
ds_read_b64_tr_b16 a[254:255], v25 offset:5120
v_add_u32 v18, 49152, v14
v_mfma_f32_16x16x32_bf16 a[72:75], a[240:243], a[184:187], a[72:75]
v_rcp_f32 v64, v64
v_mfma_f32_16x16x32_bf16 a[76:79], a[240:243], a[188:191], a[76:79]
s_waitcnt lgkmcnt(4)
v_rcp_f32 v65, v65
v_mfma_f32_16x16x32_bf16 a[92:95], a[244:247], a[188:191], a[92:95]
ds_read_b64_tr_b16 a[240:241], v22 offset:8192
v_mul_f32 v64, v64, v176
v_mfma_f32_16x16x32_bf16 a[88:91], a[244:247], a[184:187], a[88:91]
ds_read_b64_tr_b16 a[242:243], v22 offset:12288
v_mul_f32 v65, v65, v177
v_mfma_f32_16x16x32_bf16 a[84:87], a[244:247], a[180:183], a[84:87]
buffer_load_dwordx4 a[168:171], v2, s[24:27], s47 offen offset:2048 sc1
v_mfma_f32_16x16x32_bf16 a[80:83], a[244:247], a[176:179], a[80:83]
s_waitcnt lgkmcnt(4)
v_rcp_f32 v66, v66
v_mfma_f32_16x16x32_bf16 a[96:99], a[248:251], a[176:179], a[96:99]
s_add_u32 s54, s0, 1
v_rcp_f32 v67, v67
v_mfma_f32_16x16x32_bf16 a[100:103], a[248:251], a[180:183], a[100:103]
ds_read_b64_tr_b16 a[244:245], v23 offset:8192
v_mul_f32 v66, v66, v178
v_mfma_f32_16x16x32_bf16 a[104:107], a[248:251], a[184:187], a[104:107]
ds_read_b64_tr_b16 a[246:247], v23 offset:12288
v_mul_f32 v67, v67, v179
v_mfma_f32_16x16x32_bf16 a[108:111], a[248:251], a[188:191], a[108:111]
s_waitcnt lgkmcnt(4)
s_and_b32 s54, s54, 7
s_mul_i32 s54, s54, 524288
v_mfma_f32_16x16x32_bf16 a[124:127], a[252:255], a[188:191], a[124:127]
ds_read_b64_tr_b16 a[248:249], v24 offset:8192
s_add_u32 s54, s54, s85
v_mfma_f32_16x16x32_bf16 a[120:123], a[252:255], a[184:187], a[120:123]
ds_read_b64_tr_b16 a[250:251], v24 offset:12288
s_add_u32 s54, s54, 4194304
v_mfma_f32_16x16x32_bf16 a[116:119], a[252:255], a[180:183], a[116:119]
buffer_load_dwordx4 a[172:175], v2, s[24:27], s47 offen offset:3072 sc1
v_mfma_f32_16x16x32_bf16 a[112:115], a[252:255], a[176:179], a[112:115]
s_waitcnt lgkmcnt(4)
s_waitcnt vmcnt(35)
s_add_u32 s47, s47, 4096
v_mfma_f32_16x16x32_bf16 a[0:3], a[240:243], a[192:195], a[0:3]
ds_read_b64_tr_b16 a[252:253], v25 offset:8192
s_add_u32 m0, s70, 98304
v_mfma_f32_16x16x32_bf16 a[4:7], a[240:243], a[196:199], a[4:7]
ds_read_b64_tr_b16 a[254:255], v25 offset:12288
v_add_u32 v19, 49152, v15
v_mfma_f32_16x16x32_bf16 a[8:11], a[240:243], a[200:203], a[8:11]
buffer_load_dwordx4 a[176:179], v2, s[24:27], s47 offen offset:0 sc1
v_mfma_f32_16x16x32_bf16 a[12:15], a[240:243], a[204:207], a[12:15]
s_waitcnt vmcnt(32)
s_barrier
s_waitcnt lgkmcnt(4)
v_mfma_f32_16x16x32_bf16 a[28:31], a[244:247], a[204:207], a[28:31]
ds_read_b64_tr_b16 a[240:241], v22 offset:9216
v_cvt_pk_bf16_f32 v176, v64, v65
v_mfma_f32_16x16x32_bf16 a[24:27], a[244:247], a[200:203], a[24:27]
ds_read_b64_tr_b16 a[242:243], v22 offset:13312
v_cvt_pk_bf16_f32 v177, v66, v67
v_mfma_f32_16x16x32_bf16 a[20:23], a[244:247], a[196:199], a[20:23]
buffer_load_dwordx4 a[180:183], v2, s[24:27], s47 offen offset:1024 sc1
v_mfma_f32_16x16x32_bf16 a[16:19], a[244:247], a[192:195], a[16:19]
s_waitcnt lgkmcnt(4)
v_exp_f32 v80, v80
v_mfma_f32_16x16x32_bf16 a[32:35], a[248:251], a[192:195], a[32:35]
ds_read_b64_tr_b16 a[244:245], v23 offset:9216
v_add_f32 v80, 1.0, v80
v_mfma_f32_16x16x32_bf16 a[36:39], a[248:251], a[196:199], a[36:39]
ds_read_b64_tr_b16 a[246:247], v23 offset:13312
v_add_u32 v20, 49152, v16
v_mfma_f32_16x16x32_bf16 a[40:43], a[248:251], a[200:203], a[40:43]
buffer_load_dwordx4 a[184:187], v2, s[24:27], s47 offen offset:2048 sc1
v_mfma_f32_16x16x32_bf16 a[44:47], a[248:251], a[204:207], a[44:47]
s_waitcnt lgkmcnt(4)
v_exp_f32 v81, v81
v_mfma_f32_16x16x32_bf16 a[60:63], a[252:255], a[204:207], a[60:63]
ds_read_b64_tr_b16 a[248:249], v24 offset:9216
v_add_f32 v81, 1.0, v81
v_mfma_f32_16x16x32_bf16 a[56:59], a[252:255], a[200:203], a[56:59]
ds_read_b64_tr_b16 a[250:251], v24 offset:13312
v_add_u32 v21, 49152, v17
v_mfma_f32_16x16x32_bf16 a[52:55], a[252:255], a[196:199], a[52:55]
buffer_load_dwordx4 a[188:191], v2, s[24:27], s47 offen offset:3072 sc1
v_mfma_f32_16x16x32_bf16 a[48:51], a[252:255], a[192:195], a[48:51]
s_waitcnt lgkmcnt(4)
v_exp_f32 v82, v82
v_mfma_f32_16x16x32_bf16 a[64:67], a[240:243], a[192:195], a[64:67]
ds_read_b64_tr_b16 a[252:253], v25 offset:9216
v_add_f32 v82, 1.0, v82
v_mfma_f32_16x16x32_bf16 a[68:71], a[240:243], a[196:199], a[68:71]
ds_read_b64_tr_b16 a[254:255], v25 offset:13312
s_add_u32 s47, s0, 1
v_mfma_f32_16x16x32_bf16 a[72:75], a[240:243], a[200:203], a[72:75]
buffer_load_dwordx4 v13, s[88:91], s54 offen lds
v_mfma_f32_16x16x32_bf16 a[76:79], a[240:243], a[204:207], a[76:79]
s_waitcnt lgkmcnt(4)
s_add_u32 m0, s70, 99200
s_and_b32 s47, s47, 7
v_mfma_f32_16x16x32_bf16 a[92:95], a[244:247], a[204:207], a[92:95]
ds_read_b64_tr_b16 a[240:241], v18 offset:0
s_mul_i32 s47, s47, 8192
v_mfma_f32_16x16x32_bf16 a[88:91], a[244:247], a[200:203], a[88:91]
ds_read_b64_tr_b16 a[242:243], v18 offset:4096
s_add_u32 s47, s47, s58
v_mfma_f32_16x16x32_bf16 a[84:87], a[244:247], a[196:199], a[84:87]
buffer_load_dwordx4 v13, s[88:91], s54 offen offset:128 lds
v_mfma_f32_16x16x32_bf16 a[80:83], a[244:247], a[192:195], a[80:83]
s_waitcnt lgkmcnt(4)
v_exp_f32 v83, v83
v_mfma_f32_16x16x32_bf16 a[96:99], a[248:251], a[192:195], a[96:99]
s_add_u32 s54, s54, 65536
v_rcp_f32 v80, v80
v_rcp_f32 v81, v81
v_rcp_f32 v82, v82
v_add_f32 v83, 1.0, v83
v_rcp_f32 v83, v83
v_mfma_f32_16x16x32_bf16 a[100:103], a[248:251], a[196:199], a[100:103]
s_add_u32 m0, s70, 100352
v_mul_f32 v80, v80, v192
v_mul_f32 v81, v81, v193
v_mul_f32 v82, v82, v194
v_mul_f32 v83, v83, v195
v_cvt_pk_bf16_f32 v178, v80, v81
v_cvt_pk_bf16_f32 v179, v82, v83
v_mfma_f32_16x16x32_bf16 a[104:107], a[248:251], a[200:203], a[104:107]
buffer_load_dwordx4 v13, s[88:91], s54 offen lds
ds_read_b64_tr_b16 a[244:245], v19 offset:0
ds_read_b64_tr_b16 a[246:247], v19 offset:4096
v_mfma_f32_16x16x32_bf16 a[108:111], a[248:251], a[204:207], a[108:111]
s_waitcnt lgkmcnt(4)
s_add_u32 m0, s70, 101248
s_add_u32 s47, s47, 65536
v_mfma_f32_16x16x32_bf16 a[124:127], a[252:255], a[204:207], a[124:127]
ds_read_b64_tr_b16 a[248:249], v20 offset:0
v_mul_f32 v72, v31, v72
v_mfma_f32_16x16x32_bf16 a[120:123], a[252:255], a[200:203], a[120:123]
ds_read_b64_tr_b16 a[250:251], v20 offset:4096
v_mul_f32 v73, v31, v73
v_mfma_f32_16x16x32_bf16 a[116:119], a[252:255], a[196:199], a[116:119]
buffer_load_dwordx4 v13, s[88:91], s54 offen offset:128 lds
v_mfma_f32_16x16x32_bf16 a[112:115], a[252:255], a[192:195], a[112:115]
s_waitcnt lgkmcnt(4)
s_waitcnt vmcnt(34)
v_mul_f32 v74, v31, v74
v_mfma_f32_16x16x32_bf16 a[0:3], a[240:243], a[208:211], a[0:3]
ds_read_b64_tr_b16 a[252:253], v21 offset:0
v_mul_f32 v75, v31, v75
v_mfma_f32_16x16x32_bf16 a[4:7], a[240:243], a[212:215], a[4:7]
ds_read_b64_tr_b16 a[254:255], v21 offset:4096
v_mul_f32 v88, v31, v88
v_mfma_f32_16x16x32_bf16 a[8:11], a[240:243], a[216:219], a[8:11]
buffer_store_dwordx4 v[176:179], v2, s[32:35], s86 offen offset:0 sc1
v_mfma_f32_16x16x32_bf16 a[12:15], a[240:243], a[220:223], a[12:15]
s_waitcnt lgkmcnt(4)
v_exp_f32 v68, v68
v_mfma_f32_16x16x32_bf16 a[28:31], a[244:247], a[220:223], a[28:31]
ds_read_b64_tr_b16 a[240:241], v18 offset:1024
v_add_f32 v68, 1.0, v68
v_mfma_f32_16x16x32_bf16 a[24:27], a[244:247], a[216:219], a[24:27]
ds_read_b64_tr_b16 a[242:243], v18 offset:5120
v_mul_f32 v89, v31, v89
v_mfma_f32_16x16x32_bf16 a[20:23], a[244:247], a[212:215], a[20:23]
buffer_load_dwordx4 a[192:195], v2, s[24:27], s47 offen offset:0 sc1
v_mfma_f32_16x16x32_bf16 a[16:19], a[244:247], a[208:211], a[16:19]
s_waitcnt lgkmcnt(4)
v_exp_f32 v69, v69
v_mfma_f32_16x16x32_bf16 a[32:35], a[248:251], a[208:211], a[32:35]
ds_read_b64_tr_b16 a[244:245], v19 offset:1024
v_add_f32 v69, 1.0, v69
v_mfma_f32_16x16x32_bf16 a[36:39], a[248:251], a[212:215], a[36:39]
ds_read_b64_tr_b16 a[246:247], v19 offset:5120
v_mul_f32 v90, v31, v90
v_mfma_f32_16x16x32_bf16 a[40:43], a[248:251], a[216:219], a[40:43]
v_exp_f32 v70, v70
v_mfma_f32_16x16x32_bf16 a[44:47], a[248:251], a[220:223], a[44:47]
s_waitcnt lgkmcnt(4)
v_exp_f32 v71, v71
v_mfma_f32_16x16x32_bf16 a[60:63], a[252:255], a[220:223], a[60:63]
ds_read_b64_tr_b16 a[248:249], v20 offset:1024
v_add_f32 v70, 1.0, v70
v_mfma_f32_16x16x32_bf16 a[56:59], a[252:255], a[216:219], a[56:59]
ds_read_b64_tr_b16 a[250:251], v20 offset:5120
v_add_f32 v71, 1.0, v71
v_mfma_f32_16x16x32_bf16 a[52:55], a[252:255], a[212:215], a[52:55]
buffer_load_dwordx4 a[196:199], v2, s[24:27], s47 offen offset:1024 sc1
v_mfma_f32_16x16x32_bf16 a[48:51], a[252:255], a[208:211], a[48:51]
s_waitcnt lgkmcnt(4)
v_rcp_f32 v68, v68
v_mfma_f32_16x16x32_bf16 a[64:67], a[240:243], a[208:211], a[64:67]
ds_read_b64_tr_b16 a[252:253], v21 offset:1024
v_mul_f32 v68, v68, v180
v_mfma_f32_16x16x32_bf16 a[68:71], a[240:243], a[212:215], a[68:71]
ds_read_b64_tr_b16 a[254:255], v21 offset:5120
v_add_u32 v22, 65536, v14
v_mfma_f32_16x16x32_bf16 a[72:75], a[240:243], a[216:219], a[72:75]
v_rcp_f32 v69, v69
v_mfma_f32_16x16x32_bf16 a[76:79], a[240:243], a[220:223], a[76:79]
s_waitcnt lgkmcnt(4)
v_rcp_f32 v70, v70
v_mfma_f32_16x16x32_bf16 a[92:95], a[244:247], a[220:223], a[92:95]
ds_read_b64_tr_b16 a[240:241], v18 offset:8192
v_mul_f32 v69, v69, v181
v_mfma_f32_16x16x32_bf16 a[88:91], a[244:247], a[216:219], a[88:91]
ds_read_b64_tr_b16 a[242:243], v18 offset:12288
v_mul_f32 v70, v70, v182
v_mfma_f32_16x16x32_bf16 a[84:87], a[244:247], a[212:215], a[84:87]
buffer_load_dwordx4 a[200:203], v2, s[24:27], s47 offen offset:2048 sc1
v_rcp_f32 v71, v71
v_mfma_f32_16x16x32_bf16 a[80:83], a[244:247], a[208:211], a[80:83]
s_waitcnt lgkmcnt(4)
s_add_u32 s54, s0, 2
v_mul_f32 v71, v71, v183
v_mfma_f32_16x16x32_bf16 a[96:99], a[248:251], a[208:211], a[96:99]
ds_read_b64_tr_b16 a[244:245], v19 offset:8192
s_and_b32 s54, s54, 7
v_mfma_f32_16x16x32_bf16 a[100:103], a[248:251], a[212:215], a[100:103]
ds_read_b64_tr_b16 a[246:247], v19 offset:12288
s_mul_i32 s54, s54, 524288
v_mfma_f32_16x16x32_bf16 a[104:107], a[248:251], a[216:219], a[104:107]
s_add_u32 s54, s54, s85
s_add_u32 s54, s54, 4194304
s_add_u32 m0, s70, 114688
v_mfma_f32_16x16x32_bf16 a[108:111], a[248:251], a[220:223], a[108:111]
s_waitcnt lgkmcnt(4)
v_exp_f32 v84, v84
v_mfma_f32_16x16x32_bf16 a[124:127], a[252:255], a[220:223], a[124:127]
ds_read_b64_tr_b16 a[248:249], v20 offset:8192
v_add_f32 v84, 1.0, v84
v_mfma_f32_16x16x32_bf16 a[120:123], a[252:255], a[216:219], a[120:123]
ds_read_b64_tr_b16 a[250:251], v20 offset:12288
v_cvt_pk_bf16_f32 v180, v68, v69
v_mfma_f32_16x16x32_bf16 a[116:119], a[252:255], a[212:215], a[116:119]
buffer_load_dwordx4 a[204:207], v2, s[24:27], s47 offen offset:3072 sc1
v_mfma_f32_16x16x32_bf16 a[112:115], a[252:255], a[208:211], a[112:115]
s_waitcnt lgkmcnt(4)
s_waitcnt vmcnt(35)
s_add_u32 s47, s47, 4096
v_mfma_f32_16x16x32_bf16 a[0:3], a[240:243], a[224:227], a[0:3]
ds_read_b64_tr_b16 a[252:253], v21 offset:8192
v_cvt_pk_bf16_f32 v181, v70, v71
v_mfma_f32_16x16x32_bf16 a[4:7], a[240:243], a[228:231], a[4:7]
ds_read_b64_tr_b16 a[254:255], v21 offset:12288
v_add_u32 v23, 65536, v15
v_mfma_f32_16x16x32_bf16 a[8:11], a[240:243], a[232:235], a[8:11]
buffer_load_dwordx4 a[208:211], v2, s[24:27], s47 offen offset:0 sc1
v_mfma_f32_16x16x32_bf16 a[12:15], a[240:243], a[236:239], a[12:15]
s_waitcnt vmcnt(32)
s_barrier
s_waitcnt lgkmcnt(4)
v_mfma_f32_16x16x32_bf16 a[28:31], a[244:247], a[236:239], a[28:31]
ds_read_b64_tr_b16 a[240:241], v18 offset:9216
v_add_u32 v24, 65536, v16
v_mfma_f32_16x16x32_bf16 a[24:27], a[244:247], a[232:235], a[24:27]
ds_read_b64_tr_b16 a[242:243], v18 offset:13312
v_add_u32 v25, 65536, v17
v_mfma_f32_16x16x32_bf16 a[20:23], a[244:247], a[228:231], a[20:23]
buffer_load_dwordx4 a[212:215], v2, s[24:27], s47 offen offset:1024 sc1
v_mfma_f32_16x16x32_bf16 a[16:19], a[244:247], a[224:227], a[16:19]
s_waitcnt lgkmcnt(4)
v_exp_f32 v85, v85
v_mfma_f32_16x16x32_bf16 a[32:35], a[248:251], a[224:227], a[32:35]
ds_read_b64_tr_b16 a[244:245], v19 offset:9216
v_add_f32 v85, 1.0, v85
v_mfma_f32_16x16x32_bf16 a[36:39], a[248:251], a[228:231], a[36:39]
ds_read_b64_tr_b16 a[246:247], v19 offset:13312
s_mov_b32 s36, s49
v_mfma_f32_16x16x32_bf16 a[40:43], a[248:251], a[232:235], a[40:43]
buffer_load_dwordx4 a[216:219], v2, s[24:27], s47 offen offset:2048 sc1
v_mfma_f32_16x16x32_bf16 a[44:47], a[248:251], a[236:239], a[44:47]
s_waitcnt lgkmcnt(4)
v_exp_f32 v86, v86
v_mfma_f32_16x16x32_bf16 a[60:63], a[252:255], a[236:239], a[60:63]
ds_read_b64_tr_b16 a[248:249], v20 offset:9216
v_add_f32 v86, 1.0, v86
v_mfma_f32_16x16x32_bf16 a[56:59], a[252:255], a[232:235], a[56:59]
ds_read_b64_tr_b16 a[250:251], v20 offset:13312
s_mov_b32 s37, s50
v_mfma_f32_16x16x32_bf16 a[52:55], a[252:255], a[228:231], a[52:55]
buffer_load_dwordx4 a[220:223], v2, s[24:27], s47 offen offset:3072 sc1
v_mfma_f32_16x16x32_bf16 a[48:51], a[252:255], a[224:227], a[48:51]
s_waitcnt lgkmcnt(4)
v_exp_f32 v87, v87
v_mfma_f32_16x16x32_bf16 a[64:67], a[240:243], a[224:227], a[64:67]
ds_read_b64_tr_b16 a[252:253], v21 offset:9216
v_add_f32 v87, 1.0, v87
v_mfma_f32_16x16x32_bf16 a[68:71], a[240:243], a[228:231], a[68:71]
ds_read_b64_tr_b16 a[254:255], v21 offset:13312
s_mov_b32 s38, KTEXT1-KTEXT0
v_mfma_f32_16x16x32_bf16 a[72:75], a[240:243], a[232:235], a[72:75]
buffer_load_dwordx4 v13, s[88:91], s54 offen lds
v_mfma_f32_16x16x32_bf16 a[76:79], a[240:243], a[236:239], a[76:79]
s_waitcnt lgkmcnt(4)
s_add_u32 m0, s70, 115584
s_mov_b32 s39, 0x20000
v_mfma_f32_16x16x32_bf16 a[92:95], a[244:247], a[236:239], a[92:95]
s_lshr_b32 s69, s98, 3
v_rcp_f32 v84, v84
v_mfma_f32_16x16x32_bf16 a[88:91], a[244:247], a[232:235], a[88:91]
s_lshl_b32 s69, s69, 2
v_rcp_f32 v85, v85
v_rcp_f32 v86, v86
v_rcp_f32 v87, v87
v_mfma_f32_16x16x32_bf16 a[84:87], a[244:247], a[228:231], a[84:87]
buffer_load_dwordx4 v13, s[88:91], s54 offen offset:128 lds
ds_read_b64_tr_b16 a[240:241], v22 offset:0
ds_read_b64_tr_b16 a[242:243], v22 offset:4096
v_mul_f32 v84, v84, v196
v_mul_f32 v85, v85, v197
v_mul_f32 v86, v86, v198
v_mul_f32 v87, v87, v199
v_cvt_pk_bf16_f32 v182, v84, v85
v_cvt_pk_bf16_f32 v183, v86, v87
v_mfma_f32_16x16x32_bf16 a[80:83], a[244:247], a[224:227], a[80:83]
s_waitcnt lgkmcnt(4)
s_add_u32 s54, s54, 65536
s_add_u32 m0, s70, 116736
buffer_store_dwordx4 v[180:183], v2, s[32:35], s86 offen offset:1024 sc1
v_mfma_f32_16x16x32_bf16 a[96:99], a[248:251], a[224:227], a[96:99]
ds_read_b64_tr_b16 a[244:245], v23 offset:0
s_add_u32 s69, s69, s43
v_mfma_f32_16x16x32_bf16 a[100:103], a[248:251], a[228:231], a[100:103]
ds_read_b64_tr_b16 a[246:247], v23 offset:4096
s_and_b32 s69, s69, 63
v_mfma_f32_16x16x32_bf16 a[104:107], a[248:251], a[232:235], a[104:107]
buffer_load_dwordx4 v13, s[88:91], s54 offen lds
v_mfma_f32_16x16x32_bf16 a[108:111], a[248:251], a[236:239], a[108:111]
s_waitcnt lgkmcnt(4)
s_add_u32 m0, s70, 117632
s_lshl_b32 s69, s69, 10
v_mfma_f32_16x16x32_bf16 a[124:127], a[252:255], a[236:239], a[124:127]
ds_read_b64_tr_b16 a[248:249], v24 offset:0
s_add_u32 s69, s69, KA-KTEXT0
v_mfma_f32_16x16x32_bf16 a[120:123], a[252:255], a[232:235], a[120:123]
ds_read_b64_tr_b16 a[250:251], v24 offset:4096
s_add_u32 s47, s0, 2
v_mfma_f32_16x16x32_bf16 a[116:119], a[252:255], a[228:231], a[116:119]
buffer_load_dwordx4 v13, s[88:91], s54 offen offset:128 lds
v_mfma_f32_16x16x32_bf16 a[112:115], a[252:255], a[224:227], a[112:115]
s_waitcnt lgkmcnt(4)
s_waitcnt vmcnt(35)
s_and_b32 s47, s47, 7
v_mfma_f32_16x16x32_bf16 a[0:3], a[240:243], a[128:131], a[0:3]
ds_read_b64_tr_b16 a[252:253], v25 offset:0
s_mul_i32 s47, s47, 8192
v_mfma_f32_16x16x32_bf16 a[4:7], a[240:243], a[132:135], a[4:7]
ds_read_b64_tr_b16 a[254:255], v25 offset:4096
s_add_u32 s47, s47, s58
v_mfma_f32_16x16x32_bf16 a[8:11], a[240:243], a[136:139], a[8:11]
buffer_load_dwordx4 v[6:9], v2, s[36:39], s69 offen
v_mfma_f32_16x16x32_bf16 a[12:15], a[240:243], a[140:143], a[12:15]
s_waitcnt lgkmcnt(4)
s_add_u32 s47, s47, 65536
v_mul_f32 v91, v31, v91
v_mfma_f32_16x16x32_bf16 a[28:31], a[244:247], a[140:143], a[28:31]
ds_read_b64_tr_b16 a[240:241], v22 offset:1024
v_mul_f32 v76, v31, v76
v_mfma_f32_16x16x32_bf16 a[24:27], a[244:247], a[136:139], a[24:27]
ds_read_b64_tr_b16 a[242:243], v22 offset:5120
v_mul_f32 v77, v31, v77
v_mfma_f32_16x16x32_bf16 a[20:23], a[244:247], a[132:135], a[20:23]
buffer_load_dwordx4 a[224:227], v2, s[24:27], s47 offen offset:0 sc1
v_mfma_f32_16x16x32_bf16 a[16:19], a[244:247], a[128:131], a[16:19]
s_waitcnt lgkmcnt(4)
v_exp_f32 v72, v72
v_mfma_f32_16x16x32_bf16 a[32:35], a[248:251], a[128:131], a[32:35]
ds_read_b64_tr_b16 a[244:245], v23 offset:1024
v_add_f32 v72, 1.0, v72
v_mfma_f32_16x16x32_bf16 a[36:39], a[248:251], a[132:135], a[36:39]
ds_read_b64_tr_b16 a[246:247], v23 offset:5120
v_mul_f32 v78, v31, v78
v_mfma_f32_16x16x32_bf16 a[40:43], a[248:251], a[136:139], a[40:43]
v_exp_f32 v73, v73
v_mfma_f32_16x16x32_bf16 a[44:47], a[248:251], a[140:143], a[44:47]
s_waitcnt lgkmcnt(4)
v_exp_f32 v74, v74
v_mfma_f32_16x16x32_bf16 a[60:63], a[252:255], a[140:143], a[60:63]
ds_read_b64_tr_b16 a[248:249], v24 offset:1024
v_add_f32 v73, 1.0, v73
v_mfma_f32_16x16x32_bf16 a[56:59], a[252:255], a[136:139], a[56:59]
ds_read_b64_tr_b16 a[250:251], v24 offset:5120
v_add_f32 v74, 1.0, v74
v_mfma_f32_16x16x32_bf16 a[52:55], a[252:255], a[132:135], a[52:55]
buffer_load_dwordx4 a[228:231], v2, s[24:27], s47 offen offset:1024 sc1
v_mfma_f32_16x16x32_bf16 a[48:51], a[252:255], a[128:131], a[48:51]
s_waitcnt lgkmcnt(4)
v_exp_f32 v75, v75
v_mfma_f32_16x16x32_bf16 a[64:67], a[240:243], a[128:131], a[64:67]
ds_read_b64_tr_b16 a[252:253], v25 offset:1024
v_add_f32 v75, 1.0, v75
v_mfma_f32_16x16x32_bf16 a[68:71], a[240:243], a[132:135], a[68:71]
ds_read_b64_tr_b16 a[254:255], v25 offset:5120
v_add_u32 v18, 81920, v14
v_mfma_f32_16x16x32_bf16 a[72:75], a[240:243], a[136:139], a[72:75]
v_rcp_f32 v72, v72
v_mfma_f32_16x16x32_bf16 a[76:79], a[240:243], a[140:143], a[76:79]
s_waitcnt lgkmcnt(4)
v_rcp_f32 v73, v73
v_rcp_f32 v74, v74
v_rcp_f32 v75, v75
v_mfma_f32_16x16x32_bf16 a[92:95], a[244:247], a[140:143], a[92:95]
s_add_u32 s54, s0, 3
v_mul_f32 v72, v72, v184
v_mul_f32 v73, v73, v185
v_mul_f32 v74, v74, v186
v_mul_f32 v75, v75, v187
v_mfma_f32_16x16x32_bf16 a[88:91], a[244:247], a[136:139], a[88:91]
ds_read_b64_tr_b16 a[240:241], v22 offset:8192
s_and_b32 s54, s54, 7
v_mfma_f32_16x16x32_bf16 a[84:87], a[244:247], a[132:135], a[84:87]
buffer_load_dwordx4 a[232:235], v2, s[24:27], s47 offen offset:2048 sc1
ds_read_b64_tr_b16 a[242:243], v22 offset:12288
v_mfma_f32_16x16x32_bf16 a[80:83], a[244:247], a[128:131], a[80:83]
s_waitcnt lgkmcnt(4)
v_exp_f32 v88, v88
v_mfma_f32_16x16x32_bf16 a[96:99], a[248:251], a[128:131], a[96:99]
ds_read_b64_tr_b16 a[244:245], v23 offset:8192
s_mul_i32 s54, s54, 524288
v_mfma_f32_16x16x32_bf16 a[100:103], a[248:251], a[132:135], a[100:103]
ds_read_b64_tr_b16 a[246:247], v23 offset:12288
s_add_u32 s54, s54, s85
v_mfma_f32_16x16x32_bf16 a[104:107], a[248:251], a[136:139], a[104:107]
s_add_u32 s54, s54, 4194304
v_exp_f32 v89, v89
v_mfma_f32_16x16x32_bf16 a[108:111], a[248:251], a[140:143], a[108:111]
s_waitcnt lgkmcnt(4)
v_exp_f32 v90, v90
v_mfma_f32_16x16x32_bf16 a[124:127], a[252:255], a[140:143], a[124:127]
ds_read_b64_tr_b16 a[248:249], v24 offset:8192
s_add_u32 m0, s70, 131072
v_mfma_f32_16x16x32_bf16 a[120:123], a[252:255], a[136:139], a[120:123]
ds_read_b64_tr_b16 a[250:251], v24 offset:12288
v_add_f32 v88, 1.0, v88
v_mfma_f32_16x16x32_bf16 a[116:119], a[252:255], a[132:135], a[116:119]
buffer_load_dwordx4 a[236:239], v2, s[24:27], s47 offen offset:3072 sc1
v_mfma_f32_16x16x32_bf16 a[112:115], a[252:255], a[128:131], a[112:115]
s_waitcnt lgkmcnt(4)
s_waitcnt vmcnt(36)
s_add_u32 s47, s47, 4096
v_mfma_f32_16x16x32_bf16 a[0:3], a[240:243], a[144:147], a[0:3]
ds_read_b64_tr_b16 a[252:253], v25 offset:8192
v_add_f32 v89, 1.0, v89
v_mfma_f32_16x16x32_bf16 a[4:7], a[240:243], a[148:151], a[4:7]
ds_read_b64_tr_b16 a[254:255], v25 offset:12288
v_add_f32 v90, 1.0, v90
v_mfma_f32_16x16x32_bf16 a[8:11], a[240:243], a[152:155], a[8:11]
buffer_load_dwordx4 a[128:131], v2, s[24:27], s47 offen offset:0 sc1
v_mfma_f32_16x16x32_bf16 a[12:15], a[240:243], a[156:159], a[12:15]
s_waitcnt vmcnt(33)
s_barrier
s_waitcnt lgkmcnt(4)
v_mfma_f32_16x16x32_bf16 a[28:31], a[244:247], a[156:159], a[28:31]
ds_read_b64_tr_b16 a[240:241], v22 offset:9216
v_cvt_pk_bf16_f32 v184, v72, v73
v_mfma_f32_16x16x32_bf16 a[24:27], a[244:247], a[152:155], a[24:27]
ds_read_b64_tr_b16 a[242:243], v22 offset:13312
v_cvt_pk_bf16_f32 v185, v74, v75
v_mfma_f32_16x16x32_bf16 a[20:23], a[244:247], a[148:151], a[20:23]
buffer_load_dwordx4 a[132:135], v2, s[24:27], s47 offen offset:1024 sc1
v_mfma_f32_16x16x32_bf16 a[16:19], a[244:247], a[144:147], a[16:19]
s_waitcnt lgkmcnt(4)
v_exp_f32 v91, v91
v_mfma_f32_16x16x32_bf16 a[32:35], a[248:251], a[144:147], a[32:35]
ds_read_b64_tr_b16 a[244:245], v23 offset:9216
v_add_f32 v91, 1.0, v91
v_mfma_f32_16x16x32_bf16 a[36:39], a[248:251], a[148:151], a[36:39]
ds_read_b64_tr_b16 a[246:247], v23 offset:13312
v_add_u32 v19, 81920, v15
v_mfma_f32_16x16x32_bf16 a[40:43], a[248:251], a[152:155], a[40:43]
buffer_load_dwordx4 a[136:139], v2, s[24:27], s47 offen offset:2048 sc1
v_mfma_f32_16x16x32_bf16 a[44:47], a[248:251], a[156:159], a[44:47]
s_waitcnt lgkmcnt(4)
v_rcp_f32 v88, v88
v_mfma_f32_16x16x32_bf16 a[60:63], a[252:255], a[156:159], a[60:63]
ds_read_b64_tr_b16 a[248:249], v24 offset:9216
v_mul_f32 v88, v88, v200
v_mfma_f32_16x16x32_bf16 a[56:59], a[252:255], a[152:155], a[56:59]
ds_read_b64_tr_b16 a[250:251], v24 offset:13312
v_add_u32 v20, 81920, v16
v_mfma_f32_16x16x32_bf16 a[52:55], a[252:255], a[148:151], a[52:55]
buffer_load_dwordx4 a[140:143], v2, s[24:27], s47 offen offset:3072 sc1
v_mfma_f32_16x16x32_bf16 a[48:51], a[252:255], a[144:147], a[48:51]
s_waitcnt lgkmcnt(4)
v_rcp_f32 v89, v89
v_mfma_f32_16x16x32_bf16 a[64:67], a[240:243], a[144:147], a[64:67]
s_add_u32 s47, s0, 3
v_rcp_f32 v90, v90
v_mfma_f32_16x16x32_bf16 a[68:71], a[240:243], a[148:151], a[68:71]
s_and_b32 s47, s47, 7
v_rcp_f32 v91, v91
v_mfma_f32_16x16x32_bf16 a[72:75], a[240:243], a[152:155], a[72:75]
buffer_load_dwordx4 v13, s[88:91], s54 offen lds
ds_read_b64_tr_b16 a[252:253], v25 offset:9216
ds_read_b64_tr_b16 a[254:255], v25 offset:13312
v_mul_f32 v89, v89, v201
v_mul_f32 v90, v90, v202
v_mul_f32 v91, v91, v203
v_cvt_pk_bf16_f32 v186, v88, v89
v_cvt_pk_bf16_f32 v187, v90, v91
v_mfma_f32_16x16x32_bf16 a[76:79], a[240:243], a[156:159], a[76:79]
s_waitcnt lgkmcnt(4)
s_add_u32 m0, s70, 131968
v_add_u32 v21, 81920, v17
v_mfma_f32_16x16x32_bf16 a[92:95], a[244:247], a[156:159], a[92:95]
ds_read_b64_tr_b16 a[240:241], v18 offset:0
s_mul_i32 s47, s47, 8192
v_mfma_f32_16x16x32_bf16 a[88:91], a[244:247], a[152:155], a[88:91]
ds_read_b64_tr_b16 a[242:243], v18 offset:4096
s_add_u32 s47, s47, s58
v_mfma_f32_16x16x32_bf16 a[84:87], a[244:247], a[148:151], a[84:87]
buffer_load_dwordx4 v13, s[88:91], s54 offen offset:128 lds
v_mfma_f32_16x16x32_bf16 a[80:83], a[244:247], a[144:147], a[80:83]
s_waitcnt lgkmcnt(4)
s_add_u32 s54, s54, 65536
s_add_u32 m0, s70, 133120
buffer_store_dwordx4 v[184:187], v2, s[32:35], s86 offen offset:2048 sc1
v_mfma_f32_16x16x32_bf16 a[96:99], a[248:251], a[144:147], a[96:99]
ds_read_b64_tr_b16 a[244:245], v19 offset:0
s_add_u32 s47, s47, 65536
v_mfma_f32_16x16x32_bf16 a[100:103], a[248:251], a[148:151], a[100:103]
ds_read_b64_tr_b16 a[246:247], v19 offset:4096
v_mul_f32 v79, v31, v79
v_mfma_f32_16x16x32_bf16 a[104:107], a[248:251], a[152:155], a[104:107]
buffer_load_dwordx4 v13, s[88:91], s54 offen lds
v_mfma_f32_16x16x32_bf16 a[108:111], a[248:251], a[156:159], a[108:111]
s_waitcnt lgkmcnt(4)
s_add_u32 m0, s70, 134016
v_mul_f32 v92, v31, v92
v_mfma_f32_16x16x32_bf16 a[124:127], a[252:255], a[156:159], a[124:127]
ds_read_b64_tr_b16 a[248:249], v20 offset:0
v_mul_f32 v93, v31, v93
v_mfma_f32_16x16x32_bf16 a[120:123], a[252:255], a[152:155], a[120:123]
ds_read_b64_tr_b16 a[250:251], v20 offset:4096
v_mul_f32 v94, v31, v94
v_mfma_f32_16x16x32_bf16 a[116:119], a[252:255], a[148:151], a[116:119]
buffer_load_dwordx4 v13, s[88:91], s54 offen offset:128 lds
v_mfma_f32_16x16x32_bf16 a[112:115], a[252:255], a[144:147], a[112:115]
s_waitcnt lgkmcnt(4)
s_waitcnt vmcnt(36)
v_mul_f32 v95, v31, v95
v_mfma_f32_16x16x32_bf16 a[0:3], a[240:243], a[160:163], a[0:3]
ds_read_b64_tr_b16 a[252:253], v21 offset:0
v_mul_f32 v96, v31, v96
v_mfma_f32_16x16x32_bf16 a[4:7], a[240:243], a[164:167], a[4:7]
ds_read_b64_tr_b16 a[254:255], v21 offset:4096
v_mul_f32 v97, v31, v97
v_mfma_f32_16x16x32_bf16 a[8:11], a[240:243], a[168:171], a[8:11]
v_exp_f32 v76, v76
v_mfma_f32_16x16x32_bf16 a[12:15], a[240:243], a[172:175], a[12:15]
s_waitcnt lgkmcnt(4)
v_exp_f32 v77, v77
v_mfma_f32_16x16x32_bf16 a[28:31], a[244:247], a[172:175], a[28:31]
ds_read_b64_tr_b16 a[240:241], v18 offset:1024
v_add_f32 v76, 1.0, v76
v_mfma_f32_16x16x32_bf16 a[24:27], a[244:247], a[168:171], a[24:27]
ds_read_b64_tr_b16 a[242:243], v18 offset:5120
v_add_f32 v77, 1.0, v77
v_mfma_f32_16x16x32_bf16 a[20:23], a[244:247], a[164:167], a[20:23]
buffer_load_dwordx4 a[144:147], v2, s[24:27], s47 offen offset:0 sc1
v_mfma_f32_16x16x32_bf16 a[16:19], a[244:247], a[160:163], a[16:19]
s_waitcnt lgkmcnt(4)
v_exp_f32 v78, v78
v_mfma_f32_16x16x32_bf16 a[32:35], a[248:251], a[160:163], a[32:35]
ds_read_b64_tr_b16 a[244:245], v19 offset:1024
v_add_f32 v78, 1.0, v78
v_mfma_f32_16x16x32_bf16 a[36:39], a[248:251], a[164:167], a[36:39]
ds_read_b64_tr_b16 a[246:247], v19 offset:5120
v_mul_f32 v98, v31, v98
v_mfma_f32_16x16x32_bf16 a[40:43], a[248:251], a[168:171], a[40:43]
v_exp_f32 v79, v79
v_mfma_f32_16x16x32_bf16 a[44:47], a[248:251], a[172:175], a[44:47]
s_waitcnt lgkmcnt(4)
v_rcp_f32 v76, v76
v_mfma_f32_16x16x32_bf16 a[60:63], a[252:255], a[172:175], a[60:63]
ds_read_b64_tr_b16 a[248:249], v20 offset:1024
v_add_f32 v79, 1.0, v79
v_mfma_f32_16x16x32_bf16 a[56:59], a[252:255], a[168:171], a[56:59]
ds_read_b64_tr_b16 a[250:251], v20 offset:5120
v_mul_f32 v76, v76, v188
v_mfma_f32_16x16x32_bf16 a[52:55], a[252:255], a[164:167], a[52:55]
buffer_load_dwordx4 a[148:151], v2, s[24:27], s47 offen offset:1024 sc1
v_mfma_f32_16x16x32_bf16 a[48:51], a[252:255], a[160:163], a[48:51]
s_waitcnt lgkmcnt(4)
v_rcp_f32 v77, v77
v_mfma_f32_16x16x32_bf16 a[64:67], a[240:243], a[160:163], a[64:67]
v_rcp_f32 v78, v78
v_rcp_f32 v79, v79
v_mfma_f32_16x16x32_bf16 a[68:71], a[240:243], a[164:167], a[68:71]
v_mul_f32 v77, v77, v189
v_mul_f32 v78, v78, v190
v_mul_f32 v79, v79, v191
v_mfma_f32_16x16x32_bf16 a[72:75], a[240:243], a[168:171], a[72:75]
ds_read_b64_tr_b16 a[252:253], v21 offset:1024
ds_read_b64_tr_b16 a[254:255], v21 offset:5120
v_cvt_pk_bf16_f32 v188, v76, v77
v_mfma_f32_16x16x32_bf16 a[76:79], a[240:243], a[172:175], a[76:79]
s_waitcnt lgkmcnt(4)
v_exp_f32 v92, v92
v_mfma_f32_16x16x32_bf16 a[92:95], a[244:247], a[172:175], a[92:95]
ds_read_b64_tr_b16 a[240:241], v18 offset:8192
v_add_f32 v92, 1.0, v92
v_mfma_f32_16x16x32_bf16 a[88:91], a[244:247], a[168:171], a[88:91]
ds_read_b64_tr_b16 a[242:243], v18 offset:12288
v_cvt_pk_bf16_f32 v189, v78, v79
v_mfma_f32_16x16x32_bf16 a[84:87], a[244:247], a[164:167], a[84:87]
buffer_load_dwordx4 a[152:155], v2, s[24:27], s47 offen offset:2048 sc1
v_mfma_f32_16x16x32_bf16 a[80:83], a[244:247], a[160:163], a[80:83]
s_waitcnt lgkmcnt(4)
v_exp_f32 v93, v93
v_mfma_f32_16x16x32_bf16 a[96:99], a[248:251], a[160:163], a[96:99]
ds_read_b64_tr_b16 a[244:245], v19 offset:8192
v_add_f32 v93, 1.0, v93
v_mfma_f32_16x16x32_bf16 a[100:103], a[248:251], a[164:167], a[100:103]
ds_read_b64_tr_b16 a[246:247], v19 offset:12288
s_add_u32 s54, s0, 4
v_mfma_f32_16x16x32_bf16 a[104:107], a[248:251], a[168:171], a[104:107]
s_and_b32 s54, s54, 7
v_exp_f32 v94, v94
v_mfma_f32_16x16x32_bf16 a[108:111], a[248:251], a[172:175], a[108:111]
s_waitcnt lgkmcnt(4)
v_exp_f32 v95, v95
v_mfma_f32_16x16x32_bf16 a[124:127], a[252:255], a[172:175], a[124:127]
ds_read_b64_tr_b16 a[248:249], v20 offset:8192
v_add_f32 v94, 1.0, v94
v_mfma_f32_16x16x32_bf16 a[120:123], a[252:255], a[168:171], a[120:123]
ds_read_b64_tr_b16 a[250:251], v20 offset:12288
v_add_f32 v95, 1.0, v95
v_mfma_f32_16x16x32_bf16 a[116:119], a[252:255], a[164:167], a[116:119]
buffer_load_dwordx4 a[156:159], v2, s[24:27], s47 offen offset:3072 sc1
v_mfma_f32_16x16x32_bf16 a[112:115], a[252:255], a[160:163], a[112:115]
s_waitcnt lgkmcnt(4)
s_waitcnt vmcnt(36)
s_add_u32 s47, s47, 4096
v_mfma_f32_16x16x32_bf16 a[0:3], a[240:243], a[176:179], a[0:3]
ds_read_b64_tr_b16 a[252:253], v21 offset:8192
s_mul_i32 s54, s54, 524288
v_mfma_f32_16x16x32_bf16 a[4:7], a[240:243], a[180:183], a[4:7]
ds_read_b64_tr_b16 a[254:255], v21 offset:12288
s_add_u32 s54, s54, s85
v_mfma_f32_16x16x32_bf16 a[8:11], a[240:243], a[184:187], a[8:11]
buffer_load_dwordx4 a[160:163], v2, s[24:27], s47 offen offset:0 sc1
v_mfma_f32_16x16x32_bf16 a[12:15], a[240:243], a[188:191], a[12:15]
s_waitcnt vmcnt(33)
s_barrier
s_waitcnt lgkmcnt(4)
v_mfma_f32_16x16x32_bf16 a[28:31], a[244:247], a[188:191], a[28:31]
ds_read_b64_tr_b16 a[240:241], v18 offset:9216
s_add_u32 s54, s54, 4194304
v_mfma_f32_16x16x32_bf16 a[24:27], a[244:247], a[184:187], a[24:27]
ds_read_b64_tr_b16 a[242:243], v18 offset:13312
s_add_u32 m0, s70, 147456
v_mfma_f32_16x16x32_bf16 a[20:23], a[244:247], a[180:183], a[20:23]
buffer_load_dwordx4 a[164:167], v2, s[24:27], s47 offen offset:1024 sc1
v_mfma_f32_16x16x32_bf16 a[16:19], a[244:247], a[176:179], a[16:19]
s_waitcnt lgkmcnt(4)
v_rcp_f32 v92, v92
v_mfma_f32_16x16x32_bf16 a[32:35], a[248:251], a[176:179], a[32:35]
ds_read_b64_tr_b16 a[244:245], v19 offset:9216
v_mul_f32 v92, v92, v204
v_mfma_f32_16x16x32_bf16 a[36:39], a[248:251], a[180:183], a[36:39]
ds_read_b64_tr_b16 a[246:247], v19 offset:13312
v_add_u32 v22, 98304, v14
v_mfma_f32_16x16x32_bf16 a[40:43], a[248:251], a[184:187], a[40:43]
buffer_load_dwordx4 a[168:171], v2, s[24:27], s47 offen offset:2048 sc1
v_mfma_f32_16x16x32_bf16 a[44:47], a[248:251], a[188:191], a[44:47]
s_waitcnt lgkmcnt(4)
v_rcp_f32 v93, v93
v_mfma_f32_16x16x32_bf16 a[60:63], a[252:255], a[188:191], a[60:63]
ds_read_b64_tr_b16 a[248:249], v20 offset:9216
v_mul_f32 v93, v93, v205
v_mfma_f32_16x16x32_bf16 a[56:59], a[252:255], a[184:187], a[56:59]
ds_read_b64_tr_b16 a[250:251], v20 offset:13312
v_cvt_pk_bf16_f32 v190, v92, v93
v_mfma_f32_16x16x32_bf16 a[52:55], a[252:255], a[180:183], a[52:55]
buffer_load_dwordx4 a[172:175], v2, s[24:27], s47 offen offset:3072 sc1
v_rcp_f32 v94, v94
v_rcp_f32 v95, v95
v_mfma_f32_16x16x32_bf16 a[48:51], a[252:255], a[176:179], a[48:51]
s_waitcnt lgkmcnt(4)
v_mul_f32 v94, v94, v206
v_mul_f32 v95, v95, v207
v_cvt_pk_bf16_f32 v191, v94, v95
v_mfma_f32_16x16x32_bf16 a[64:67], a[240:243], a[176:179], a[64:67]
ds_read_b64_tr_b16 a[252:253], v21 offset:9216
v_add_u32 v23, 98304, v15
v_mfma_f32_16x16x32_bf16 a[68:71], a[240:243], a[180:183], a[68:71]
ds_read_b64_tr_b16 a[254:255], v21 offset:13312
v_add_u32 v24, 98304, v16
v_mfma_f32_16x16x32_bf16 a[72:75], a[240:243], a[184:187], a[72:75]
buffer_load_dwordx4 v13, s[88:91], s54 offen lds
v_mfma_f32_16x16x32_bf16 a[76:79], a[240:243], a[188:191], a[76:79]
s_waitcnt lgkmcnt(4)
s_add_u32 m0, s70, 148352
v_add_u32 v25, 98304, v17
v_mfma_f32_16x16x32_bf16 a[92:95], a[244:247], a[188:191], a[92:95]
ds_read_b64_tr_b16 a[240:241], v22 offset:0
s_add_u32 s47, s0, 4
v_mfma_f32_16x16x32_bf16 a[88:91], a[244:247], a[184:187], a[88:91]
ds_read_b64_tr_b16 a[242:243], v22 offset:4096
s_and_b32 s47, s47, 7
v_mfma_f32_16x16x32_bf16 a[84:87], a[244:247], a[180:183], a[84:87]
buffer_load_dwordx4 v13, s[88:91], s54 offen offset:128 lds
v_mfma_f32_16x16x32_bf16 a[80:83], a[244:247], a[176:179], a[80:83]
s_waitcnt lgkmcnt(4)
s_add_u32 s54, s54, 65536
s_add_u32 m0, s70, 149504
buffer_store_dwordx4 v[188:191], v2, s[32:35], s86 offen offset:3072 sc1
v_mfma_f32_16x16x32_bf16 a[96:99], a[248:251], a[176:179], a[96:99]
ds_read_b64_tr_b16 a[244:245], v23 offset:0
s_add_u32 s86, s86, 4096
v_mfma_f32_16x16x32_bf16 a[100:103], a[248:251], a[180:183], a[100:103]
ds_read_b64_tr_b16 a[246:247], v23 offset:4096
s_mul_i32 s47, s47, 8192
v_mfma_f32_16x16x32_bf16 a[104:107], a[248:251], a[184:187], a[104:107]
buffer_load_dwordx4 v13, s[88:91], s54 offen lds
v_mfma_f32_16x16x32_bf16 a[108:111], a[248:251], a[188:191], a[108:111]
s_waitcnt lgkmcnt(4)
s_add_u32 m0, s70, 150400
s_add_u32 s47, s47, s58
v_mfma_f32_16x16x32_bf16 a[124:127], a[252:255], a[188:191], a[124:127]
ds_read_b64_tr_b16 a[248:249], v24 offset:0
s_add_u32 s47, s47, 65536
v_mfma_f32_16x16x32_bf16 a[120:123], a[252:255], a[184:187], a[120:123]
ds_read_b64_tr_b16 a[250:251], v24 offset:4096
v_mul_f32 v99, v31, v99
v_mfma_f32_16x16x32_bf16 a[116:119], a[252:255], a[180:183], a[116:119]
buffer_load_dwordx4 v13, s[88:91], s54 offen offset:128 lds
v_mfma_f32_16x16x32_bf16 a[112:115], a[252:255], a[176:179], a[112:115]
s_waitcnt lgkmcnt(4)
s_waitcnt vmcnt(36)
v_mul_f32 v112, v31, v112
v_mfma_f32_16x16x32_bf16 a[0:3], a[240:243], a[192:195], a[0:3]
ds_read_b64_tr_b16 a[252:253], v25 offset:0
v_mul_f32 v113, v31, v113
v_mfma_f32_16x16x32_bf16 a[4:7], a[240:243], a[196:199], a[4:7]
ds_read_b64_tr_b16 a[254:255], v25 offset:4096
v_mul_f32 v114, v31, v114
v_mfma_f32_16x16x32_bf16 a[8:11], a[240:243], a[200:203], a[8:11]
v_exp_f32 v96, v96
v_mfma_f32_16x16x32_bf16 a[12:15], a[240:243], a[204:207], a[12:15]
s_waitcnt lgkmcnt(4)
v_exp_f32 v97, v97
v_mfma_f32_16x16x32_bf16 a[28:31], a[244:247], a[204:207], a[28:31]
ds_read_b64_tr_b16 a[240:241], v22 offset:1024
v_add_f32 v96, 1.0, v96
v_mfma_f32_16x16x32_bf16 a[24:27], a[244:247], a[200:203], a[24:27]
ds_read_b64_tr_b16 a[242:243], v22 offset:5120
v_add_f32 v97, 1.0, v97
v_mfma_f32_16x16x32_bf16 a[20:23], a[244:247], a[196:199], a[20:23]
buffer_load_dwordx4 a[176:179], v2, s[24:27], s47 offen offset:0 sc1
v_mfma_f32_16x16x32_bf16 a[16:19], a[244:247], a[192:195], a[16:19]
s_waitcnt lgkmcnt(4)
v_exp_f32 v98, v98
v_mfma_f32_16x16x32_bf16 a[32:35], a[248:251], a[192:195], a[32:35]
ds_read_b64_tr_b16 a[244:245], v23 offset:1024
v_add_f32 v98, 1.0, v98
v_mfma_f32_16x16x32_bf16 a[36:39], a[248:251], a[196:199], a[36:39]
ds_read_b64_tr_b16 a[246:247], v23 offset:5120
v_mul_f32 v115, v31, v115
v_mfma_f32_16x16x32_bf16 a[40:43], a[248:251], a[200:203], a[40:43]
v_exp_f32 v99, v99
v_mfma_f32_16x16x32_bf16 a[44:47], a[248:251], a[204:207], a[44:47]
s_waitcnt lgkmcnt(4)
v_rcp_f32 v96, v96
v_mfma_f32_16x16x32_bf16 a[60:63], a[252:255], a[204:207], a[60:63]
v_rcp_f32 v97, v97
v_mfma_f32_16x16x32_bf16 a[56:59], a[252:255], a[200:203], a[56:59]
v_rcp_f32 v98, v98
v_add_f32 v99, 1.0, v99
v_rcp_f32 v99, v99
v_mfma_f32_16x16x32_bf16 a[52:55], a[252:255], a[196:199], a[52:55]
buffer_load_dwordx4 a[180:183], v2, s[24:27], s47 offen offset:1024 sc1
ds_read_b64_tr_b16 a[248:249], v24 offset:1024
ds_read_b64_tr_b16 a[250:251], v24 offset:5120
v_mul_f32 v96, v96, v208
v_mul_f32 v97, v97, v209
v_mul_f32 v98, v98, v210
v_mul_f32 v99, v99, v211
v_mfma_f32_16x16x32_bf16 a[48:51], a[252:255], a[192:195], a[48:51]
s_waitcnt lgkmcnt(4)
v_exp_f32 v112, v112
v_mfma_f32_16x16x32_bf16 a[64:67], a[240:243], a[192:195], a[64:67]
ds_read_b64_tr_b16 a[252:253], v25 offset:1024
v_add_f32 v112, 1.0, v112
v_mfma_f32_16x16x32_bf16 a[68:71], a[240:243], a[196:199], a[68:71]
ds_read_b64_tr_b16 a[254:255], v25 offset:5120
v_cvt_pk_bf16_f32 v208, v96, v97
v_mfma_f32_16x16x32_bf16 a[72:75], a[240:243], a[200:203], a[72:75]
v_exp_f32 v113, v113
v_mfma_f32_16x16x32_bf16 a[76:79], a[240:243], a[204:207], a[76:79]
s_waitcnt lgkmcnt(4)
v_exp_f32 v114, v114
v_mfma_f32_16x16x32_bf16 a[92:95], a[244:247], a[204:207], a[92:95]
ds_read_b64_tr_b16 a[240:241], v22 offset:8192
v_add_f32 v113, 1.0, v113
v_mfma_f32_16x16x32_bf16 a[88:91], a[244:247], a[200:203], a[88:91]
ds_read_b64_tr_b16 a[242:243], v22 offset:12288
v_add_f32 v114, 1.0, v114
v_mfma_f32_16x16x32_bf16 a[84:87], a[244:247], a[196:199], a[84:87]
buffer_load_dwordx4 a[184:187], v2, s[24:27], s47 offen offset:2048 sc1
v_mfma_f32_16x16x32_bf16 a[80:83], a[244:247], a[192:195], a[80:83]
s_waitcnt lgkmcnt(4)
v_exp_f32 v115, v115
v_mfma_f32_16x16x32_bf16 a[96:99], a[248:251], a[192:195], a[96:99]
ds_read_b64_tr_b16 a[244:245], v23 offset:8192
v_add_f32 v115, 1.0, v115
v_mfma_f32_16x16x32_bf16 a[100:103], a[248:251], a[196:199], a[100:103]
ds_read_b64_tr_b16 a[246:247], v23 offset:12288
v_cvt_pk_bf16_f32 v209, v98, v99
v_mfma_f32_16x16x32_bf16 a[104:107], a[248:251], a[200:203], a[104:107]
s_add_u32 s54, s0, 5
v_rcp_f32 v112, v112
v_mfma_f32_16x16x32_bf16 a[108:111], a[248:251], a[204:207], a[108:111]
s_waitcnt lgkmcnt(4)
v_rcp_f32 v113, v113
v_mfma_f32_16x16x32_bf16 a[124:127], a[252:255], a[204:207], a[124:127]
ds_read_b64_tr_b16 a[248:249], v24 offset:8192
v_mul_f32 v112, v112, v224
v_mfma_f32_16x16x32_bf16 a[120:123], a[252:255], a[200:203], a[120:123]
ds_read_b64_tr_b16 a[250:251], v24 offset:12288
v_mul_f32 v113, v113, v225
v_mfma_f32_16x16x32_bf16 a[116:119], a[252:255], a[196:199], a[116:119]
buffer_load_dwordx4 a[188:191], v2, s[24:27], s47 offen offset:3072 sc1
v_mfma_f32_16x16x32_bf16 a[112:115], a[252:255], a[192:195], a[112:115]
s_waitcnt lgkmcnt(4)
s_waitcnt vmcnt(36)
s_add_u32 s47, s47, 4096
v_mfma_f32_16x16x32_bf16 a[0:3], a[240:243], a[208:211], a[0:3]
ds_read_b64_tr_b16 a[252:253], v25 offset:8192
v_cvt_pk_bf16_f32 v210, v112, v113
v_mfma_f32_16x16x32_bf16 a[4:7], a[240:243], a[212:215], a[4:7]
ds_read_b64_tr_b16 a[254:255], v25 offset:12288
s_and_b32 s54, s54, 7
v_mfma_f32_16x16x32_bf16 a[8:11], a[240:243], a[216:219], a[8:11]
buffer_load_dwordx4 a[192:195], v2, s[24:27], s47 offen offset:0 sc1
v_mfma_f32_16x16x32_bf16 a[12:15], a[240:243], a[220:223], a[12:15]
s_waitcnt vmcnt(32)
s_barrier
s_waitcnt lgkmcnt(4)
v_mfma_f32_16x16x32_bf16 a[28:31], a[244:247], a[220:223], a[28:31]
ds_read_b64_tr_b16 a[240:241], v22 offset:9216
s_mul_i32 s54, s54, 524288
v_mfma_f32_16x16x32_bf16 a[24:27], a[244:247], a[216:219], a[24:27]
ds_read_b64_tr_b16 a[242:243], v22 offset:13312
s_add_u32 s54, s54, s85
v_mfma_f32_16x16x32_bf16 a[20:23], a[244:247], a[212:215], a[20:23]
buffer_load_dwordx4 a[196:199], v2, s[24:27], s47 offen offset:1024 sc1
v_mfma_f32_16x16x32_bf16 a[16:19], a[244:247], a[208:211], a[16:19]
s_waitcnt lgkmcnt(4)
v_rcp_f32 v114, v114
v_mfma_f32_16x16x32_bf16 a[32:35], a[248:251], a[208:211], a[32:35]
ds_read_b64_tr_b16 a[244:245], v23 offset:9216
v_mul_f32 v114, v114, v226
v_mfma_f32_16x16x32_bf16 a[36:39], a[248:251], a[212:215], a[36:39]
ds_read_b64_tr_b16 a[246:247], v23 offset:13312
s_add_u32 s54, s54, 4194304
v_mfma_f32_16x16x32_bf16 a[40:43], a[248:251], a[216:219], a[40:43]
buffer_load_dwordx4 a[200:203], v2, s[24:27], s47 offen offset:2048 sc1
v_mfma_f32_16x16x32_bf16 a[44:47], a[248:251], a[220:223], a[44:47]
s_waitcnt lgkmcnt(4)
v_rcp_f32 v115, v115
v_mfma_f32_16x16x32_bf16 a[60:63], a[252:255], a[220:223], a[60:63]
s_add_u32 m0, s70, 0
v_mul_f32 v115, v115, v227
v_cvt_pk_bf16_f32 v211, v114, v115
v_mfma_f32_16x16x32_bf16 a[56:59], a[252:255], a[216:219], a[56:59]
ds_read_b64_tr_b16 a[248:249], v24 offset:9216
v_add_u32 v18, 114688, v14
v_mfma_f32_16x16x32_bf16 a[52:55], a[252:255], a[212:215], a[52:55]
buffer_load_dwordx4 a[204:207], v2, s[24:27], s47 offen offset:3072 sc1
ds_read_b64_tr_b16 a[250:251], v24 offset:13312
v_mfma_f32_16x16x32_bf16 a[48:51], a[252:255], a[208:211], a[48:51]
s_waitcnt lgkmcnt(4)
v_add_u32 v19, 114688, v15
v_add_u32 v20, 114688, v16
v_mfma_f32_16x16x32_bf16 a[64:67], a[240:243], a[208:211], a[64:67]
ds_read_b64_tr_b16 a[252:253], v25 offset:9216
v_add_u32 v21, 114688, v17
v_mfma_f32_16x16x32_bf16 a[68:71], a[240:243], a[212:215], a[68:71]
ds_read_b64_tr_b16 a[254:255], v25 offset:13312
s_add_u32 s47, s0, 5
v_mfma_f32_16x16x32_bf16 a[72:75], a[240:243], a[216:219], a[72:75]
buffer_load_dwordx4 v13, s[88:91], s54 offen lds
v_mfma_f32_16x16x32_bf16 a[76:79], a[240:243], a[220:223], a[76:79]
s_waitcnt lgkmcnt(4)
s_add_u32 m0, s70, 896
s_and_b32 s47, s47, 7
v_mfma_f32_16x16x32_bf16 a[92:95], a[244:247], a[220:223], a[92:95]
ds_read_b64_tr_b16 a[240:241], v18 offset:0
s_mul_i32 s47, s47, 8192
v_mfma_f32_16x16x32_bf16 a[88:91], a[244:247], a[216:219], a[88:91]
ds_read_b64_tr_b16 a[242:243], v18 offset:4096
s_add_u32 s47, s47, s58
v_mfma_f32_16x16x32_bf16 a[84:87], a[244:247], a[212:215], a[84:87]
buffer_load_dwordx4 v13, s[88:91], s54 offen offset:128 lds
v_mfma_f32_16x16x32_bf16 a[80:83], a[244:247], a[208:211], a[80:83]
s_waitcnt lgkmcnt(4)
s_add_u32 s54, s54, 65536
s_add_u32 m0, s70, 2048
buffer_store_dwordx4 v[208:211], v2, s[32:35], s86 offen offset:0 sc1
v_mfma_f32_16x16x32_bf16 a[96:99], a[248:251], a[208:211], a[96:99]
ds_read_b64_tr_b16 a[244:245], v19 offset:0
s_add_u32 s47, s47, 65536
v_mfma_f32_16x16x32_bf16 a[100:103], a[248:251], a[212:215], a[100:103]
ds_read_b64_tr_b16 a[246:247], v19 offset:4096
v_mul_f32 v100, v31, v100
v_mfma_f32_16x16x32_bf16 a[104:107], a[248:251], a[216:219], a[104:107]
buffer_load_dwordx4 v13, s[88:91], s54 offen lds
v_mfma_f32_16x16x32_bf16 a[108:111], a[248:251], a[220:223], a[108:111]
s_waitcnt lgkmcnt(4)
s_add_u32 m0, s70, 2944
v_mul_f32 v101, v31, v101
v_mfma_f32_16x16x32_bf16 a[124:127], a[252:255], a[220:223], a[124:127]
ds_read_b64_tr_b16 a[248:249], v20 offset:0
v_mul_f32 v102, v31, v102
v_mfma_f32_16x16x32_bf16 a[120:123], a[252:255], a[216:219], a[120:123]
ds_read_b64_tr_b16 a[250:251], v20 offset:4096
v_mul_f32 v103, v31, v103
v_mfma_f32_16x16x32_bf16 a[116:119], a[252:255], a[212:215], a[116:119]
buffer_load_dwordx4 v13, s[88:91], s54 offen offset:128 lds
v_mfma_f32_16x16x32_bf16 a[112:115], a[252:255], a[208:211], a[112:115]
s_waitcnt lgkmcnt(4)
s_waitcnt vmcnt(35)
v_mul_f32 v116, v31, v116
v_mfma_f32_16x16x32_bf16 a[0:3], a[240:243], a[224:227], a[0:3]
ds_read_b64_tr_b16 a[252:253], v21 offset:0
v_mul_f32 v117, v31, v117
v_mfma_f32_16x16x32_bf16 a[4:7], a[240:243], a[228:231], a[4:7]
ds_read_b64_tr_b16 a[254:255], v21 offset:4096
v_mul_f32 v118, v31, v118
v_mfma_f32_16x16x32_bf16 a[8:11], a[240:243], a[232:235], a[8:11]
v_exp_f32 v100, v100
v_mfma_f32_16x16x32_bf16 a[12:15], a[240:243], a[236:239], a[12:15]
s_waitcnt lgkmcnt(4)
v_exp_f32 v101, v101
v_mfma_f32_16x16x32_bf16 a[28:31], a[244:247], a[236:239], a[28:31]
ds_read_b64_tr_b16 a[240:241], v18 offset:1024
v_add_f32 v100, 1.0, v100
v_mfma_f32_16x16x32_bf16 a[24:27], a[244:247], a[232:235], a[24:27]
ds_read_b64_tr_b16 a[242:243], v18 offset:5120
v_add_f32 v101, 1.0, v101
v_mfma_f32_16x16x32_bf16 a[20:23], a[244:247], a[228:231], a[20:23]
buffer_load_dwordx4 a[208:211], v2, s[24:27], s47 offen offset:0 sc1
v_mfma_f32_16x16x32_bf16 a[16:19], a[244:247], a[224:227], a[16:19]
s_waitcnt lgkmcnt(4)
v_exp_f32 v102, v102
v_mfma_f32_16x16x32_bf16 a[32:35], a[248:251], a[224:227], a[32:35]
v_exp_f32 v103, v103
v_mfma_f32_16x16x32_bf16 a[36:39], a[248:251], a[228:231], a[36:39]
ds_read_b64_tr_b16 a[244:245], v19 offset:1024
v_add_f32 v102, 1.0, v102
v_mfma_f32_16x16x32_bf16 a[40:43], a[248:251], a[232:235], a[40:43]
ds_read_b64_tr_b16 a[246:247], v19 offset:5120
v_add_f32 v103, 1.0, v103
v_rcp_f32 v100, v100
v_rcp_f32 v101, v101
v_rcp_f32 v102, v102
v_rcp_f32 v103, v103
v_mfma_f32_16x16x32_bf16 a[44:47], a[248:251], a[236:239], a[44:47]
s_waitcnt lgkmcnt(4)
v_mul_f32 v100, v100, v212
v_mul_f32 v101, v101, v213
v_mul_f32 v102, v102, v214
v_mul_f32 v103, v103, v215
v_mfma_f32_16x16x32_bf16 a[60:63], a[252:255], a[236:239], a[60:63]
ds_read_b64_tr_b16 a[248:249], v20 offset:1024
v_mul_f32 v119, v31, v119
v_mfma_f32_16x16x32_bf16 a[56:59], a[252:255], a[232:235], a[56:59]
ds_read_b64_tr_b16 a[250:251], v20 offset:5120
v_cvt_pk_bf16_f32 v212, v100, v101
v_mfma_f32_16x16x32_bf16 a[52:55], a[252:255], a[228:231], a[52:55]
buffer_load_dwordx4 a[212:215], v2, s[24:27], s47 offen offset:1024 sc1
v_mfma_f32_16x16x32_bf16 a[48:51], a[252:255], a[224:227], a[48:51]
s_waitcnt lgkmcnt(4)
v_exp_f32 v116, v116
v_mfma_f32_16x16x32_bf16 a[64:67], a[240:243], a[224:227], a[64:67]
ds_read_b64_tr_b16 a[252:253], v21 offset:1024
v_add_f32 v116, 1.0, v116
v_mfma_f32_16x16x32_bf16 a[68:71], a[240:243], a[228:231], a[68:71]
ds_read_b64_tr_b16 a[254:255], v21 offset:5120
v_cvt_pk_bf16_f32 v213, v102, v103
v_mfma_f32_16x16x32_bf16 a[72:75], a[240:243], a[232:235], a[72:75]
v_exp_f32 v117, v117
v_mfma_f32_16x16x32_bf16 a[76:79], a[240:243], a[236:239], a[76:79]
s_waitcnt lgkmcnt(4)
v_exp_f32 v118, v118
v_mfma_f32_16x16x32_bf16 a[92:95], a[244:247], a[236:239], a[92:95]
ds_read_b64_tr_b16 a[240:241], v18 offset:8192
v_add_f32 v117, 1.0, v117
v_mfma_f32_16x16x32_bf16 a[88:91], a[244:247], a[232:235], a[88:91]
ds_read_b64_tr_b16 a[242:243], v18 offset:12288
v_add_f32 v118, 1.0, v118
v_mfma_f32_16x16x32_bf16 a[84:87], a[244:247], a[228:231], a[84:87]
buffer_load_dwordx4 a[216:219], v2, s[24:27], s47 offen offset:2048 sc1
v_mfma_f32_16x16x32_bf16 a[80:83], a[244:247], a[224:227], a[80:83]
s_waitcnt lgkmcnt(4)
v_exp_f32 v119, v119
v_mfma_f32_16x16x32_bf16 a[96:99], a[248:251], a[224:227], a[96:99]
ds_read_b64_tr_b16 a[244:245], v19 offset:8192
v_add_f32 v119, 1.0, v119
v_mfma_f32_16x16x32_bf16 a[100:103], a[248:251], a[228:231], a[100:103]
ds_read_b64_tr_b16 a[246:247], v19 offset:12288
s_add_u32 s54, s0, 6
v_mfma_f32_16x16x32_bf16 a[104:107], a[248:251], a[232:235], a[104:107]
s_and_b32 s54, s54, 7
v_rcp_f32 v116, v116
v_mfma_f32_16x16x32_bf16 a[108:111], a[248:251], a[236:239], a[108:111]
s_waitcnt lgkmcnt(4)
v_rcp_f32 v117, v117
v_mfma_f32_16x16x32_bf16 a[124:127], a[252:255], a[236:239], a[124:127]
ds_read_b64_tr_b16 a[248:249], v20 offset:8192
v_mul_f32 v116, v116, v228
v_mfma_f32_16x16x32_bf16 a[120:123], a[252:255], a[232:235], a[120:123]
ds_read_b64_tr_b16 a[250:251], v20 offset:12288
v_mul_f32 v117, v117, v229
v_mfma_f32_16x16x32_bf16 a[116:119], a[252:255], a[228:231], a[116:119]
buffer_load_dwordx4 a[220:223], v2, s[24:27], s47 offen offset:3072 sc1
v_mfma_f32_16x16x32_bf16 a[112:115], a[252:255], a[224:227], a[112:115]
s_waitcnt lgkmcnt(4)
s_waitcnt vmcnt(35)
s_add_u32 s47, s47, 4096
v_mfma_f32_16x16x32_bf16 a[0:3], a[240:243], a[128:131], a[0:3]
ds_read_b64_tr_b16 a[252:253], v21 offset:8192
v_cvt_pk_bf16_f32 v214, v116, v117
v_mfma_f32_16x16x32_bf16 a[4:7], a[240:243], a[132:135], a[4:7]
ds_read_b64_tr_b16 a[254:255], v21 offset:12288
s_mul_i32 s54, s54, 524288
v_mfma_f32_16x16x32_bf16 a[8:11], a[240:243], a[136:139], a[8:11]
buffer_load_dwordx4 a[224:227], v2, s[24:27], s47 offen offset:0 sc1
v_mfma_f32_16x16x32_bf16 a[12:15], a[240:243], a[140:143], a[12:15]
s_waitcnt vmcnt(31)
s_barrier
s_waitcnt lgkmcnt(4)
v_mfma_f32_16x16x32_bf16 a[28:31], a[244:247], a[140:143], a[28:31]
ds_read_b64_tr_b16 a[240:241], v18 offset:9216
s_add_u32 s54, s54, s85
v_mfma_f32_16x16x32_bf16 a[24:27], a[244:247], a[136:139], a[24:27]
ds_read_b64_tr_b16 a[242:243], v18 offset:13312
s_add_u32 s54, s54, 4194304
v_mfma_f32_16x16x32_bf16 a[20:23], a[244:247], a[132:135], a[20:23]
buffer_load_dwordx4 a[228:231], v2, s[24:27], s47 offen offset:1024 sc1
v_mfma_f32_16x16x32_bf16 a[16:19], a[244:247], a[128:131], a[16:19]
s_waitcnt lgkmcnt(4)
v_rcp_f32 v118, v118
v_mfma_f32_16x16x32_bf16 a[32:35], a[248:251], a[128:131], a[32:35]
s_add_u32 m0, s70, 16384
v_rcp_f32 v119, v119
v_mfma_f32_16x16x32_bf16 a[36:39], a[248:251], a[132:135], a[36:39]
v_mul_f32 v118, v118, v230
v_mul_f32 v119, v119, v231
v_cvt_pk_bf16_f32 v215, v118, v119
v_mfma_f32_16x16x32_bf16 a[40:43], a[248:251], a[136:139], a[40:43]
buffer_load_dwordx4 a[232:235], v2, s[24:27], s47 offen offset:2048 sc1
ds_read_b64_tr_b16 a[244:245], v19 offset:9216
ds_read_b64_tr_b16 a[246:247], v19 offset:13312
v_mfma_f32_16x16x32_bf16 a[44:47], a[248:251], a[140:143], a[44:47]
s_waitcnt lgkmcnt(4)
buffer_store_dwordx4 v[212:215], v2, s[32:35], s86 offen offset:1024 sc1
v_add_u32 v22, 131072, v14
v_add_u32 v23, 131072, v15
v_mfma_f32_16x16x32_bf16 a[60:63], a[252:255], a[140:143], a[60:63]
ds_read_b64_tr_b16 a[248:249], v20 offset:9216
v_add_u32 v24, 131072, v16
v_mfma_f32_16x16x32_bf16 a[56:59], a[252:255], a[136:139], a[56:59]
ds_read_b64_tr_b16 a[250:251], v20 offset:13312
v_add_u32 v25, 131072, v17
v_mfma_f32_16x16x32_bf16 a[52:55], a[252:255], a[132:135], a[52:55]
buffer_load_dwordx4 a[236:239], v2, s[24:27], s47 offen offset:3072 sc1
v_mfma_f32_16x16x32_bf16 a[48:51], a[252:255], a[128:131], a[48:51]
s_waitcnt lgkmcnt(4)
s_add_u32 s47, s0, 6
s_and_b32 s47, s47, 7
v_mfma_f32_16x16x32_bf16 a[64:67], a[240:243], a[128:131], a[64:67]
ds_read_b64_tr_b16 a[252:253], v21 offset:9216
s_mul_i32 s47, s47, 8192
v_mfma_f32_16x16x32_bf16 a[68:71], a[240:243], a[132:135], a[68:71]
ds_read_b64_tr_b16 a[254:255], v21 offset:13312
s_add_u32 s47, s47, s58
v_mfma_f32_16x16x32_bf16 a[72:75], a[240:243], a[136:139], a[72:75]
buffer_load_dwordx4 v13, s[88:91], s54 offen lds
v_mfma_f32_16x16x32_bf16 a[76:79], a[240:243], a[140:143], a[76:79]
s_waitcnt lgkmcnt(4)
s_add_u32 m0, s70, 17280
s_add_u32 s47, s47, 65536
v_mfma_f32_16x16x32_bf16 a[92:95], a[244:247], a[140:143], a[92:95]
ds_read_b64_tr_b16 a[240:241], v22 offset:0
v_mul_f32 v104, v31, v104
v_mfma_f32_16x16x32_bf16 a[88:91], a[244:247], a[136:139], a[88:91]
ds_read_b64_tr_b16 a[242:243], v22 offset:4096
v_mul_f32 v105, v31, v105
v_mfma_f32_16x16x32_bf16 a[84:87], a[244:247], a[132:135], a[84:87]
buffer_load_dwordx4 v13, s[88:91], s54 offen offset:128 lds
v_mfma_f32_16x16x32_bf16 a[80:83], a[244:247], a[128:131], a[80:83]
s_waitcnt lgkmcnt(4)
s_add_u32 s54, s54, 65536
s_add_u32 m0, s70, 18432
v_mfma_f32_16x16x32_bf16 a[96:99], a[248:251], a[128:131], a[96:99]
ds_read_b64_tr_b16 a[244:245], v23 offset:0
v_mul_f32 v106, v31, v106
v_mfma_f32_16x16x32_bf16 a[100:103], a[248:251], a[132:135], a[100:103]
ds_read_b64_tr_b16 a[246:247], v23 offset:4096
v_mul_f32 v107, v31, v107
v_mfma_f32_16x16x32_bf16 a[104:107], a[248:251], a[136:139], a[104:107]
buffer_load_dwordx4 v13, s[88:91], s54 offen lds
v_mfma_f32_16x16x32_bf16 a[108:111], a[248:251], a[140:143], a[108:111]
s_waitcnt lgkmcnt(4)
s_add_u32 m0, s70, 19328
v_mul_f32 v120, v31, v120
v_mfma_f32_16x16x32_bf16 a[124:127], a[252:255], a[140:143], a[124:127]
ds_read_b64_tr_b16 a[248:249], v24 offset:0
v_mul_f32 v121, v31, v121
v_mfma_f32_16x16x32_bf16 a[120:123], a[252:255], a[136:139], a[120:123]
ds_read_b64_tr_b16 a[250:251], v24 offset:4096
v_mul_f32 v122, v31, v122
v_mfma_f32_16x16x32_bf16 a[116:119], a[252:255], a[132:135], a[116:119]
buffer_load_dwordx4 v13, s[88:91], s54 offen offset:128 lds
v_mfma_f32_16x16x32_bf16 a[112:115], a[252:255], a[128:131], a[112:115]
s_waitcnt lgkmcnt(4)
s_waitcnt vmcnt(35)
v_mul_f32 v123, v31, v123
v_mfma_f32_16x16x32_bf16 a[0:3], a[240:243], a[144:147], a[0:3]
ds_read_b64_tr_b16 a[252:253], v25 offset:0
v_mul_f32 v108, v31, v108
v_mfma_f32_16x16x32_bf16 a[4:7], a[240:243], a[148:151], a[4:7]
ds_read_b64_tr_b16 a[254:255], v25 offset:4096
v_mul_f32 v109, v31, v109
v_mfma_f32_16x16x32_bf16 a[8:11], a[240:243], a[152:155], a[8:11]
v_exp_f32 v104, v104
v_mfma_f32_16x16x32_bf16 a[12:15], a[240:243], a[156:159], a[12:15]
s_waitcnt lgkmcnt(4)
v_exp_f32 v105, v105
v_mfma_f32_16x16x32_bf16 a[28:31], a[244:247], a[156:159], a[28:31]
v_exp_f32 v106, v106
v_mfma_f32_16x16x32_bf16 a[24:27], a[244:247], a[152:155], a[24:27]
v_exp_f32 v107, v107
v_mfma_f32_16x16x32_bf16 a[20:23], a[244:247], a[148:151], a[20:23]
buffer_load_dwordx4 a[128:131], v2, s[24:27], s47 offen offset:0 sc1
ds_read_b64_tr_b16 a[240:241], v22 offset:1024
ds_read_b64_tr_b16 a[242:243], v22 offset:5120
v_add_f32 v104, 1.0, v104
v_rcp_f32 v104, v104
v_add_f32 v105, 1.0, v105
v_rcp_f32 v105, v105
v_add_f32 v106, 1.0, v106
v_rcp_f32 v106, v106
v_add_f32 v107, 1.0, v107
v_rcp_f32 v107, v107
v_mfma_f32_16x16x32_bf16 a[16:19], a[244:247], a[144:147], a[16:19]
s_waitcnt lgkmcnt(4)
v_mul_f32 v104, v104, v216
v_mul_f32 v105, v105, v217
v_mul_f32 v106, v106, v218
v_mul_f32 v107, v107, v219
v_mfma_f32_16x16x32_bf16 a[32:35], a[248:251], a[144:147], a[32:35]
ds_read_b64_tr_b16 a[244:245], v23 offset:1024
v_cvt_pk_bf16_f32 v216, v104, v105
v_mfma_f32_16x16x32_bf16 a[36:39], a[248:251], a[148:151], a[36:39]
ds_read_b64_tr_b16 a[246:247], v23 offset:5120
v_cvt_pk_bf16_f32 v217, v106, v107
v_mfma_f32_16x16x32_bf16 a[40:43], a[248:251], a[152:155], a[40:43]
v_exp_f32 v120, v120
v_mfma_f32_16x16x32_bf16 a[44:47], a[248:251], a[156:159], a[44:47]
s_waitcnt lgkmcnt(4)
v_exp_f32 v121, v121
v_mfma_f32_16x16x32_bf16 a[60:63], a[252:255], a[156:159], a[60:63]
ds_read_b64_tr_b16 a[248:249], v24 offset:1024
v_add_f32 v120, 1.0, v120
v_mfma_f32_16x16x32_bf16 a[56:59], a[252:255], a[152:155], a[56:59]
ds_read_b64_tr_b16 a[250:251], v24 offset:5120
v_add_f32 v121, 1.0, v121
v_mfma_f32_16x16x32_bf16 a[52:55], a[252:255], a[148:151], a[52:55]
buffer_load_dwordx4 a[132:135], v2, s[24:27], s47 offen offset:1024 sc1
v_mfma_f32_16x16x32_bf16 a[48:51], a[252:255], a[144:147], a[48:51]
s_waitcnt lgkmcnt(4)
v_exp_f32 v122, v122
v_mfma_f32_16x16x32_bf16 a[64:67], a[240:243], a[144:147], a[64:67]
ds_read_b64_tr_b16 a[252:253], v25 offset:1024
v_add_f32 v122, 1.0, v122
v_mfma_f32_16x16x32_bf16 a[68:71], a[240:243], a[148:151], a[68:71]
ds_read_b64_tr_b16 a[254:255], v25 offset:5120
v_add_u32 v18, 147456, v14
v_mfma_f32_16x16x32_bf16 a[72:75], a[240:243], a[152:155], a[72:75]
v_exp_f32 v123, v123
v_mfma_f32_16x16x32_bf16 a[76:79], a[240:243], a[156:159], a[76:79]
s_waitcnt lgkmcnt(4)
v_rcp_f32 v120, v120
v_mfma_f32_16x16x32_bf16 a[92:95], a[244:247], a[156:159], a[92:95]
ds_read_b64_tr_b16 a[240:241], v22 offset:8192
v_add_f32 v123, 1.0, v123
v_mfma_f32_16x16x32_bf16 a[88:91], a[244:247], a[152:155], a[88:91]
ds_read_b64_tr_b16 a[242:243], v22 offset:12288
v_mul_f32 v120, v120, v232
v_mfma_f32_16x16x32_bf16 a[84:87], a[244:247], a[148:151], a[84:87]
buffer_load_dwordx4 a[136:139], v2, s[24:27], s47 offen offset:2048 sc1
v_mfma_f32_16x16x32_bf16 a[80:83], a[244:247], a[144:147], a[80:83]
s_waitcnt lgkmcnt(4)
v_rcp_f32 v121, v121
v_mfma_f32_16x16x32_bf16 a[96:99], a[248:251], a[144:147], a[96:99]
ds_read_b64_tr_b16 a[244:245], v23 offset:8192
v_mul_f32 v121, v121, v233
v_mfma_f32_16x16x32_bf16 a[100:103], a[248:251], a[148:151], a[100:103]
ds_read_b64_tr_b16 a[246:247], v23 offset:12288
v_cvt_pk_bf16_f32 v218, v120, v121
v_mfma_f32_16x16x32_bf16 a[104:107], a[248:251], a[152:155], a[104:107]
s_add_u32 s54, s0, 7
v_rcp_f32 v122, v122
v_mfma_f32_16x16x32_bf16 a[108:111], a[248:251], a[156:159], a[108:111]
s_waitcnt lgkmcnt(4)
v_rcp_f32 v123, v123
v_mfma_f32_16x16x32_bf16 a[124:127], a[252:255], a[156:159], a[124:127]
ds_read_b64_tr_b16 a[248:249], v24 offset:8192
v_mul_f32 v122, v122, v234
v_mfma_f32_16x16x32_bf16 a[120:123], a[252:255], a[152:155], a[120:123]
ds_read_b64_tr_b16 a[250:251], v24 offset:12288
v_mul_f32 v123, v123, v235
v_mfma_f32_16x16x32_bf16 a[116:119], a[252:255], a[148:151], a[116:119]
buffer_load_dwordx4 a[140:143], v2, s[24:27], s47 offen offset:3072 sc1
v_mfma_f32_16x16x32_bf16 a[112:115], a[252:255], a[144:147], a[112:115]
s_waitcnt lgkmcnt(4)
s_waitcnt vmcnt(35)
s_add_u32 s47, s47, 4096
v_mfma_f32_16x16x32_bf16 a[0:3], a[240:243], a[160:163], a[0:3]
ds_read_b64_tr_b16 a[252:253], v25 offset:8192
v_cvt_pk_bf16_f32 v219, v122, v123
v_mfma_f32_16x16x32_bf16 a[4:7], a[240:243], a[164:167], a[4:7]
ds_read_b64_tr_b16 a[254:255], v25 offset:12288
s_and_b32 s54, s54, 7
v_mfma_f32_16x16x32_bf16 a[8:11], a[240:243], a[168:171], a[8:11]
buffer_load_dwordx4 a[144:147], v2, s[24:27], s47 offen offset:0 sc1
v_mfma_f32_16x16x32_bf16 a[12:15], a[240:243], a[172:175], a[12:15]
s_waitcnt vmcnt(31)
s_barrier
s_waitcnt lgkmcnt(4)
v_mfma_f32_16x16x32_bf16 a[28:31], a[244:247], a[172:175], a[28:31]
ds_read_b64_tr_b16 a[240:241], v22 offset:9216
s_mul_i32 s54, s54, 524288
v_mfma_f32_16x16x32_bf16 a[24:27], a[244:247], a[168:171], a[24:27]
ds_read_b64_tr_b16 a[242:243], v22 offset:13312
s_add_u32 s54, s54, s85
v_mfma_f32_16x16x32_bf16 a[20:23], a[244:247], a[164:167], a[20:23]
buffer_load_dwordx4 a[148:151], v2, s[24:27], s47 offen offset:1024 sc1
v_mfma_f32_16x16x32_bf16 a[16:19], a[244:247], a[160:163], a[16:19]
s_waitcnt lgkmcnt(4)
s_add_u32 s54, s54, 4194304
s_add_u32 m0, s70, 32768
v_mfma_f32_16x16x32_bf16 a[32:35], a[248:251], a[160:163], a[32:35]
ds_read_b64_tr_b16 a[244:245], v23 offset:9216
v_add_u32 v19, 147456, v15
v_mfma_f32_16x16x32_bf16 a[36:39], a[248:251], a[164:167], a[36:39]
ds_read_b64_tr_b16 a[246:247], v23 offset:13312
v_add_u32 v20, 147456, v16
v_mfma_f32_16x16x32_bf16 a[40:43], a[248:251], a[168:171], a[40:43]
buffer_load_dwordx4 a[152:155], v2, s[24:27], s47 offen offset:2048 sc1
v_mfma_f32_16x16x32_bf16 a[44:47], a[248:251], a[172:175], a[44:47]
s_waitcnt lgkmcnt(4)
buffer_store_dwordx4 v[216:219], v2, s[32:35], s86 offen offset:2048 sc1
v_add_u32 v21, 147456, v17
v_mul_f32 v110, v31, v110
v_mfma_f32_16x16x32_bf16 a[60:63], a[252:255], a[172:175], a[60:63]
ds_read_b64_tr_b16 a[248:249], v24 offset:9216
v_mul_f32 v111, v31, v111
v_mfma_f32_16x16x32_bf16 a[56:59], a[252:255], a[168:171], a[56:59]
ds_read_b64_tr_b16 a[250:251], v24 offset:13312
v_mul_f32 v124, v31, v124
v_mfma_f32_16x16x32_bf16 a[52:55], a[252:255], a[164:167], a[52:55]
buffer_load_dwordx4 a[156:159], v2, s[24:27], s47 offen offset:3072 sc1
v_mfma_f32_16x16x32_bf16 a[48:51], a[252:255], a[160:163], a[48:51]
s_waitcnt lgkmcnt(4)
v_exp_f32 v108, v108
v_mfma_f32_16x16x32_bf16 a[64:67], a[240:243], a[160:163], a[64:67]
ds_read_b64_tr_b16 a[252:253], v25 offset:9216
v_add_f32 v108, 1.0, v108
v_mfma_f32_16x16x32_bf16 a[68:71], a[240:243], a[164:167], a[68:71]
ds_read_b64_tr_b16 a[254:255], v25 offset:13312
s_add_u32 s47, s0, 7
v_mfma_f32_16x16x32_bf16 a[72:75], a[240:243], a[168:171], a[72:75]
buffer_load_dwordx4 v13, s[88:91], s54 offen lds
v_mfma_f32_16x16x32_bf16 a[76:79], a[240:243], a[172:175], a[76:79]
s_waitcnt lgkmcnt(4)
s_add_u32 m0, s70, 33664
s_and_b32 s47, s47, 7
v_mfma_f32_16x16x32_bf16 a[92:95], a[244:247], a[172:175], a[92:95]
ds_read_b64_tr_b16 a[240:241], v18 offset:0
s_mul_i32 s47, s47, 8192
v_mfma_f32_16x16x32_bf16 a[88:91], a[244:247], a[168:171], a[88:91]
ds_read_b64_tr_b16 a[242:243], v18 offset:4096
s_add_u32 s47, s47, s58
v_mfma_f32_16x16x32_bf16 a[84:87], a[244:247], a[164:167], a[84:87]
buffer_load_dwordx4 v13, s[88:91], s54 offen offset:128 lds
v_mfma_f32_16x16x32_bf16 a[80:83], a[244:247], a[160:163], a[80:83]
s_waitcnt lgkmcnt(4)
s_add_u32 s54, s54, 65536
s_add_u32 m0, s70, 34816
v_mfma_f32_16x16x32_bf16 a[96:99], a[248:251], a[160:163], a[96:99]
ds_read_b64_tr_b16 a[244:245], v19 offset:0
s_add_u32 s47, s47, 65536
v_mfma_f32_16x16x32_bf16 a[100:103], a[248:251], a[164:167], a[100:103]
ds_read_b64_tr_b16 a[246:247], v19 offset:4096
v_mul_f32 v125, v31, v125
v_mfma_f32_16x16x32_bf16 a[104:107], a[248:251], a[168:171], a[104:107]
buffer_load_dwordx4 v13, s[88:91], s54 offen lds
v_mfma_f32_16x16x32_bf16 a[108:111], a[248:251], a[172:175], a[108:111]
s_waitcnt lgkmcnt(4)
s_add_u32 m0, s70, 35712
v_mul_f32 v126, v31, v126
v_mfma_f32_16x16x32_bf16 a[124:127], a[252:255], a[172:175], a[124:127]
ds_read_b64_tr_b16 a[248:249], v20 offset:0
v_mul_f32 v127, v31, v127
v_mfma_f32_16x16x32_bf16 a[120:123], a[252:255], a[168:171], a[120:123]
ds_read_b64_tr_b16 a[250:251], v20 offset:4096
v_mul_f32 v128, v31, v128
v_mfma_f32_16x16x32_bf16 a[116:119], a[252:255], a[164:167], a[116:119]
buffer_load_dwordx4 v13, s[88:91], s54 offen offset:128 lds
v_mfma_f32_16x16x32_bf16 a[112:115], a[252:255], a[160:163], a[112:115]
s_waitcnt lgkmcnt(4)
s_waitcnt vmcnt(35)
v_mul_f32 v129, v31, v129
v_mfma_f32_16x16x32_bf16 a[0:3], a[240:243], a[176:179], a[0:3]
ds_read_b64_tr_b16 a[252:253], v21 offset:0
v_mul_f32 v130, v31, v130
v_mfma_f32_16x16x32_bf16 a[4:7], a[240:243], a[180:183], a[4:7]
ds_read_b64_tr_b16 a[254:255], v21 offset:4096
v_mul_f32 v131, v31, v131
v_mfma_f32_16x16x32_bf16 a[8:11], a[240:243], a[184:187], a[8:11]
v_exp_f32 v109, v109
v_exp_f32 v110, v110
v_exp_f32 v111, v111
v_mfma_f32_16x16x32_bf16 a[12:15], a[240:243], a[188:191], a[12:15]
s_waitcnt lgkmcnt(4)
v_rcp_f32 v108, v108
v_add_f32 v109, 1.0, v109
v_rcp_f32 v109, v109
v_add_f32 v110, 1.0, v110
v_rcp_f32 v110, v110
v_add_f32 v111, 1.0, v111
v_rcp_f32 v111, v111
v_mfma_f32_16x16x32_bf16 a[28:31], a[244:247], a[188:191], a[28:31]
v_mul_f32 v108, v108, v220
v_mul_f32 v109, v109, v221
v_mul_f32 v110, v110, v222
v_mul_f32 v111, v111, v223
v_mfma_f32_16x16x32_bf16 a[24:27], a[244:247], a[184:187], a[24:27]
ds_read_b64_tr_b16 a[240:241], v18 offset:1024
v_cvt_pk_bf16_f32 v220, v108, v109
v_mfma_f32_16x16x32_bf16 a[20:23], a[244:247], a[180:183], a[20:23]
buffer_load_dwordx4 a[160:163], v2, s[24:27], s47 offen offset:0 sc1
ds_read_b64_tr_b16 a[242:243], v18 offset:5120
v_mfma_f32_16x16x32_bf16 a[16:19], a[244:247], a[176:179], a[16:19]
s_waitcnt lgkmcnt(4)
v_exp_f32 v124, v124
v_mfma_f32_16x16x32_bf16 a[32:35], a[248:251], a[176:179], a[32:35]
ds_read_b64_tr_b16 a[244:245], v19 offset:1024
v_add_f32 v124, 1.0, v124
v_mfma_f32_16x16x32_bf16 a[36:39], a[248:251], a[180:183], a[36:39]
ds_read_b64_tr_b16 a[246:247], v19 offset:5120
v_cvt_pk_bf16_f32 v221, v110, v111
v_mfma_f32_16x16x32_bf16 a[40:43], a[248:251], a[184:187], a[40:43]
v_exp_f32 v125, v125
v_mfma_f32_16x16x32_bf16 a[44:47], a[248:251], a[188:191], a[44:47]
s_waitcnt lgkmcnt(4)
v_exp_f32 v126, v126
v_mfma_f32_16x16x32_bf16 a[60:63], a[252:255], a[188:191], a[60:63]
ds_read_b64_tr_b16 a[248:249], v20 offset:1024
v_add_f32 v125, 1.0, v125
v_mfma_f32_16x16x32_bf16 a[56:59], a[252:255], a[184:187], a[56:59]
ds_read_b64_tr_b16 a[250:251], v20 offset:5120
v_add_f32 v126, 1.0, v126
v_mfma_f32_16x16x32_bf16 a[52:55], a[252:255], a[180:183], a[52:55]
buffer_load_dwordx4 a[164:167], v2, s[24:27], s47 offen offset:1024 sc1
v_mfma_f32_16x16x32_bf16 a[48:51], a[252:255], a[176:179], a[48:51]
s_waitcnt lgkmcnt(4)
v_exp_f32 v127, v127
v_mfma_f32_16x16x32_bf16 a[64:67], a[240:243], a[176:179], a[64:67]
ds_read_b64_tr_b16 a[252:253], v21 offset:1024
v_add_f32 v127, 1.0, v127
v_mfma_f32_16x16x32_bf16 a[68:71], a[240:243], a[180:183], a[68:71]
ds_read_b64_tr_b16 a[254:255], v21 offset:5120
v_add_u32 v22, 0, v14
v_mfma_f32_16x16x32_bf16 a[72:75], a[240:243], a[184:187], a[72:75]
v_rcp_f32 v124, v124
v_mfma_f32_16x16x32_bf16 a[76:79], a[240:243], a[188:191], a[76:79]
s_waitcnt lgkmcnt(4)
v_rcp_f32 v125, v125
v_mfma_f32_16x16x32_bf16 a[92:95], a[244:247], a[188:191], a[92:95]
ds_read_b64_tr_b16 a[240:241], v18 offset:8192
v_mul_f32 v124, v124, v236
v_mfma_f32_16x16x32_bf16 a[88:91], a[244:247], a[184:187], a[88:91]
ds_read_b64_tr_b16 a[242:243], v18 offset:12288
v_mul_f32 v125, v125, v237
v_mfma_f32_16x16x32_bf16 a[84:87], a[244:247], a[180:183], a[84:87]
buffer_load_dwordx4 a[168:171], v2, s[24:27], s47 offen offset:2048 sc1
v_mfma_f32_16x16x32_bf16 a[80:83], a[244:247], a[176:179], a[80:83]
s_waitcnt lgkmcnt(4)
v_rcp_f32 v126, v126
v_mfma_f32_16x16x32_bf16 a[96:99], a[248:251], a[176:179], a[96:99]
ds_read_b64_tr_b16 a[244:245], v19 offset:8192
v_mul_f32 v126, v126, v238
v_mfma_f32_16x16x32_bf16 a[100:103], a[248:251], a[180:183], a[100:103]
ds_read_b64_tr_b16 a[246:247], v19 offset:12288
v_cvt_pk_bf16_f32 v222, v124, v125
v_mfma_f32_16x16x32_bf16 a[104:107], a[248:251], a[184:187], a[104:107]
s_add_u32 s54, s0, 0
v_rcp_f32 v127, v127
v_mfma_f32_16x16x32_bf16 a[108:111], a[248:251], a[188:191], a[108:111]
s_waitcnt lgkmcnt(4)
v_mul_f32 v127, v127, v239
v_cvt_pk_bf16_f32 v223, v126, v127
v_mfma_f32_16x16x32_bf16 a[124:127], a[252:255], a[188:191], a[124:127]
ds_read_b64_tr_b16 a[248:249], v20 offset:8192
s_and_b32 s54, s54, 7
v_mfma_f32_16x16x32_bf16 a[120:123], a[252:255], a[184:187], a[120:123]
ds_read_b64_tr_b16 a[250:251], v20 offset:12288
s_mul_i32 s54, s54, 32768
v_mfma_f32_16x16x32_bf16 a[116:119], a[252:255], a[180:183], a[116:119]
buffer_load_dwordx4 a[172:175], v2, s[24:27], s47 offen offset:3072 sc1
v_mfma_f32_16x16x32_bf16 a[112:115], a[252:255], a[176:179], a[112:115]
s_waitcnt lgkmcnt(4)
s_waitcnt vmcnt(35)
s_add_u32 s47, s47, 4096
v_mfma_f32_16x16x32_bf16 a[0:3], a[240:243], a[192:195], a[0:3]
ds_read_b64_tr_b16 a[252:253], v21 offset:8192
s_add_u32 s54, s54, s71
v_mfma_f32_16x16x32_bf16 a[4:7], a[240:243], a[196:199], a[4:7]
ds_read_b64_tr_b16 a[254:255], v21 offset:12288
s_add_u32 m0, s70, 49152
v_mfma_f32_16x16x32_bf16 a[8:11], a[240:243], a[200:203], a[8:11]
buffer_load_dwordx4 a[176:179], v2, s[24:27], s47 offen offset:0 sc1
v_mfma_f32_16x16x32_bf16 a[12:15], a[240:243], a[204:207], a[12:15]
s_waitcnt vmcnt(31)
s_barrier
s_waitcnt lgkmcnt(4)
v_mfma_f32_16x16x32_bf16 a[28:31], a[244:247], a[204:207], a[28:31]
ds_read_b64_tr_b16 a[240:241], v18 offset:9216
v_add_u32 v23, 0, v15
v_mfma_f32_16x16x32_bf16 a[24:27], a[244:247], a[200:203], a[24:27]
ds_read_b64_tr_b16 a[242:243], v18 offset:13312
v_add_u32 v24, 0, v16
v_mfma_f32_16x16x32_bf16 a[20:23], a[244:247], a[196:199], a[20:23]
buffer_load_dwordx4 a[180:183], v2, s[24:27], s47 offen offset:1024 sc1
v_mfma_f32_16x16x32_bf16 a[16:19], a[244:247], a[192:195], a[16:19]
s_waitcnt lgkmcnt(4)
v_add_u32 v25, 0, v17
v_bfe_u32 v29, v1, 1, 2
v_mfma_f32_16x16x32_bf16 a[32:35], a[248:251], a[192:195], a[32:35]
ds_read_b64_tr_b16 a[244:245], v19 offset:9216
v_bfe_u32 v30, v1, 4, 2
v_mfma_f32_16x16x32_bf16 a[36:39], a[248:251], a[196:199], a[36:39]
ds_read_b64_tr_b16 a[246:247], v19 offset:13312
v_xor_b32 v29, v29, v30
v_mfma_f32_16x16x32_bf16 a[40:43], a[248:251], a[200:203], a[40:43]
buffer_load_dwordx4 a[184:187], v2, s[24:27], s47 offen offset:2048 sc1
v_mfma_f32_16x16x32_bf16 a[44:47], a[248:251], a[204:207], a[44:47]
s_waitcnt lgkmcnt(4)
buffer_store_dwordx4 v[220:223], v2, s[32:35], s86 offen offset:3072 sc1
v_exp_f32 v128, v128
v_mfma_f32_16x16x32_bf16 a[60:63], a[252:255], a[204:207], a[60:63]
ds_read_b64_tr_b16 a[248:249], v20 offset:9216
s_add_u32 s86, s86, 4096
v_mfma_f32_16x16x32_bf16 a[56:59], a[252:255], a[200:203], a[56:59]
ds_read_b64_tr_b16 a[250:251], v20 offset:13312
v_add_f32 v128, 1.0, v128
v_mfma_f32_16x16x32_bf16 a[52:55], a[252:255], a[196:199], a[52:55]
buffer_load_dwordx4 a[188:191], v2, s[24:27], s47 offen offset:3072 sc1
v_mfma_f32_16x16x32_bf16 a[48:51], a[252:255], a[192:195], a[48:51]
s_waitcnt lgkmcnt(4)
v_exp_f32 v129, v129
v_mfma_f32_16x16x32_bf16 a[64:67], a[240:243], a[192:195], a[64:67]
ds_read_b64_tr_b16 a[252:253], v21 offset:9216
v_add_f32 v129, 1.0, v129
v_mfma_f32_16x16x32_bf16 a[68:71], a[240:243], a[196:199], a[68:71]
ds_read_b64_tr_b16 a[254:255], v21 offset:13312
v_and_b32 v30, 1, v1
v_mfma_f32_16x16x32_bf16 a[72:75], a[240:243], a[200:203], a[72:75]
buffer_load_dwordx4 v2, s[72:75], s54 offen lds
v_mfma_f32_16x16x32_bf16 a[76:79], a[240:243], a[204:207], a[76:79]
s_waitcnt lgkmcnt(4)
v_exp_f32 v130, v130
v_mfma_f32_16x16x32_bf16 a[92:95], a[244:247], a[204:207], a[92:95]
ds_read_b64_tr_b16 a[240:241], v22 offset:0
v_add_f32 v130, 1.0, v130
v_mfma_f32_16x16x32_bf16 a[88:91], a[244:247], a[200:203], a[88:91]
ds_read_b64_tr_b16 a[242:243], v22 offset:4096
v_lshl_add_u32 v29, v29, 1, v30
v_mfma_f32_16x16x32_bf16 a[84:87], a[244:247], a[196:199], a[84:87]
buffer_load_dwordx4 v2, s[72:75], s54 offen offset:1024 lds
v_mfma_f32_16x16x32_bf16 a[80:83], a[244:247], a[192:195], a[80:83]
s_waitcnt lgkmcnt(4)
v_exp_f32 v131, v131
v_mfma_f32_16x16x32_bf16 a[96:99], a[248:251], a[192:195], a[96:99]
ds_read_b64_tr_b16 a[244:245], v23 offset:0
v_add_f32 v131, 1.0, v131
v_mfma_f32_16x16x32_bf16 a[100:103], a[248:251], a[196:199], a[100:103]
ds_read_b64_tr_b16 a[246:247], v23 offset:4096
v_lshrrev_b32 v30, 3, v1
v_mfma_f32_16x16x32_bf16 a[104:107], a[248:251], a[200:203], a[104:107]
buffer_load_dwordx4 v2, s[72:75], s54 offen offset:2048 lds
v_mfma_f32_16x16x32_bf16 a[108:111], a[248:251], a[204:207], a[108:111]
s_waitcnt lgkmcnt(4)
v_rcp_f32 v128, v128
v_mfma_f32_16x16x32_bf16 a[124:127], a[252:255], a[204:207], a[124:127]
ds_read_b64_tr_b16 a[248:249], v24 offset:0
v_mul_f32 v128, v128, v240
v_mfma_f32_16x16x32_bf16 a[120:123], a[252:255], a[200:203], a[120:123]
ds_read_b64_tr_b16 a[250:251], v24 offset:4096
v_mul_u32_u24 v13, 2048, v30
v_mfma_f32_16x16x32_bf16 a[116:119], a[252:255], a[196:199], a[116:119]
buffer_load_dwordx4 v2, s[72:75], s54 offen offset:3072 lds
v_mfma_f32_16x16x32_bf16 a[112:115], a[252:255], a[192:195], a[112:115]
s_waitcnt lgkmcnt(4)
s_waitcnt vmcnt(35)
v_lshl_add_u32 v13, v29, 4, v13
v_mfma_f32_16x16x32_bf16 a[0:3], a[240:243], a[208:211], a[0:3]
s_mov_b32 s88, s18
v_rcp_f32 v129, v129
v_rcp_f32 v130, v130
v_rcp_f32 v131, v131
v_mfma_f32_16x16x32_bf16 a[4:7], a[240:243], a[212:215], a[4:7]
s_mov_b32 s89, s19
v_mul_f32 v129, v129, v241
v_mul_f32 v130, v130, v242
v_mul_f32 v131, v131, v243
v_cvt_pk_bf16_f32 v240, v128, v129
v_cvt_pk_bf16_f32 v241, v130, v131
v_mfma_f32_16x16x32_bf16 a[8:11], a[240:243], a[216:219], a[8:11]
ds_read_b64_tr_b16 a[252:253], v25 offset:0
s_and_b32 s89, s89, 0xffff
ds_read_b64_tr_b16 a[254:255], v25 offset:4096
buffer_store_dwordx2 v[240:241], v2, s[32:35], s87 offen offset:0 sc1
v_mfma_f32_16x16x32_bf16 a[12:15], a[240:243], a[220:223], a[12:15]
s_waitcnt lgkmcnt(4)
s_mov_b32 s90, 7340032
s_mov_b32 s91, 0x20000
v_mfma_f32_16x16x32_bf16 a[28:31], a[244:247], a[220:223], a[28:31]
ds_read_b64_tr_b16 a[240:241], v22 offset:1024
s_mov_b32 s76, s12
v_mfma_f32_16x16x32_bf16 a[24:27], a[244:247], a[216:219], a[24:27]
ds_read_b64_tr_b16 a[242:243], v22 offset:5120
s_mov_b32 s77, s13
v_mfma_f32_16x16x32_bf16 a[20:23], a[244:247], a[212:215], a[20:23]
s_and_b32 s77, s77, 0xffff
s_mov_b32 s78, 8388608
s_mov_b32 s79, 0x20000
v_mfma_f32_16x16x32_bf16 a[16:19], a[244:247], a[208:211], a[16:19]
s_waitcnt lgkmcnt(4)
s_mul_i32 s85, s59, 1835008
s_lshl_b32 s51, s43, 15
v_mfma_f32_16x16x32_bf16 a[32:35], a[248:251], a[208:211], a[32:35]
ds_read_b64_tr_b16 a[244:245], v23 offset:1024
s_lshl_b32 s52, s60, 8
v_mfma_f32_16x16x32_bf16 a[36:39], a[248:251], a[212:215], a[36:39]
ds_read_b64_tr_b16 a[246:247], v23 offset:5120
s_add_u32 s51, s51, s52
v_mfma_f32_16x16x32_bf16 a[40:43], a[248:251], a[216:219], a[40:43]
s_add_u32 s85, s85, s51
s_lshl_b32 s80, s59, 21
s_add_u32 s80, s80, s51
v_mfma_f32_16x16x32_bf16 a[44:47], a[248:251], a[220:223], a[44:47]
s_waitcnt lgkmcnt(4)
v_mul_f32 v132, v31, v132
v_mul_f32 v133, v31, v133
v_mfma_f32_16x16x32_bf16 a[60:63], a[252:255], a[220:223], a[60:63]
ds_read_b64_tr_b16 a[248:249], v24 offset:1024
v_mul_f32 v134, v31, v134
v_mfma_f32_16x16x32_bf16 a[56:59], a[252:255], a[216:219], a[56:59]
ds_read_b64_tr_b16 a[250:251], v24 offset:5120
v_mul_f32 v135, v31, v135
v_mfma_f32_16x16x32_bf16 a[52:55], a[252:255], a[212:215], a[52:55]
v_exp_f32 v132, v132
v_mfma_f32_16x16x32_bf16 a[48:51], a[252:255], a[208:211], a[48:51]
s_waitcnt lgkmcnt(4)
v_exp_f32 v133, v133
v_mfma_f32_16x16x32_bf16 a[64:67], a[240:243], a[208:211], a[64:67]
ds_read_b64_tr_b16 a[252:253], v25 offset:1024
v_add_f32 v132, 1.0, v132
v_mfma_f32_16x16x32_bf16 a[68:71], a[240:243], a[212:215], a[68:71]
ds_read_b64_tr_b16 a[254:255], v25 offset:5120
v_add_f32 v133, 1.0, v133
v_mfma_f32_16x16x32_bf16 a[72:75], a[240:243], a[216:219], a[72:75]
v_exp_f32 v134, v134
v_mfma_f32_16x16x32_bf16 a[76:79], a[240:243], a[220:223], a[76:79]
s_waitcnt lgkmcnt(4)
v_exp_f32 v135, v135
v_mfma_f32_16x16x32_bf16 a[92:95], a[244:247], a[220:223], a[92:95]
ds_read_b64_tr_b16 a[240:241], v22 offset:8192
v_add_f32 v134, 1.0, v134
v_mfma_f32_16x16x32_bf16 a[88:91], a[244:247], a[216:219], a[88:91]
ds_read_b64_tr_b16 a[242:243], v22 offset:12288
v_add_f32 v135, 1.0, v135
v_mfma_f32_16x16x32_bf16 a[84:87], a[244:247], a[212:215], a[84:87]
s_add_u32 s54, s0, 0
v_rcp_f32 v132, v132
v_mfma_f32_16x16x32_bf16 a[80:83], a[244:247], a[208:211], a[80:83]
s_waitcnt lgkmcnt(4)
v_rcp_f32 v133, v133
v_mfma_f32_16x16x32_bf16 a[96:99], a[248:251], a[208:211], a[96:99]
ds_read_b64_tr_b16 a[244:245], v23 offset:8192
v_mul_f32 v132, v132, v244
v_mfma_f32_16x16x32_bf16 a[100:103], a[248:251], a[212:215], a[100:103]
ds_read_b64_tr_b16 a[246:247], v23 offset:12288
v_mul_f32 v133, v133, v245
v_mfma_f32_16x16x32_bf16 a[104:107], a[248:251], a[216:219], a[104:107]
s_and_b32 s54, s54, 7
v_rcp_f32 v134, v134
v_mfma_f32_16x16x32_bf16 a[108:111], a[248:251], a[220:223], a[108:111]
s_waitcnt lgkmcnt(4)
v_rcp_f32 v135, v135
v_mfma_f32_16x16x32_bf16 a[124:127], a[252:255], a[220:223], a[124:127]
ds_read_b64_tr_b16 a[248:249], v24 offset:8192
v_mul_f32 v134, v134, v246
v_mfma_f32_16x16x32_bf16 a[120:123], a[252:255], a[216:219], a[120:123]
ds_read_b64_tr_b16 a[250:251], v24 offset:12288
v_mul_f32 v135, v135, v247
v_mfma_f32_16x16x32_bf16 a[116:119], a[252:255], a[212:215], a[116:119]
s_mul_i32 s54, s54, 32768
v_cvt_pk_bf16_f32 v244, v132, v133
v_cvt_pk_bf16_f32 v245, v134, v135
v_mfma_f32_16x16x32_bf16 a[112:115], a[252:255], a[208:211], a[112:115]
s_waitcnt lgkmcnt(4)
s_waitcnt vmcnt(31)
v_mov_b32 v28, 0x3e0293ee
v_mfma_f32_16x16x32_bf16 a[0:3], a[240:243], a[224:227], a[0:3]
ds_read_b64_tr_b16 a[252:253], v25 offset:8192
s_add_u32 s54, s54, s71
v_mfma_f32_16x16x32_bf16 a[4:7], a[240:243], a[228:231], a[4:7]
ds_read_b64_tr_b16 a[254:255], v25 offset:12288
s_add_u32 s54, s54, 16384
v_mfma_f32_16x16x32_bf16 a[8:11], a[240:243], a[232:235], a[8:11]
buffer_store_dwordx2 v[244:245], v2, s[32:35], s87 offen offset:1024 sc1
v_mfma_f32_16x16x32_bf16 a[12:15], a[240:243], a[236:239], a[12:15]
s_waitcnt vmcnt(15)
s_barrier
s_waitcnt lgkmcnt(4)
v_mfma_f32_16x16x32_bf16 a[28:31], a[244:247], a[236:239], a[28:31]
ds_read_b64_tr_b16 a[240:241], v22 offset:9216
s_add_u32 m0, s70, 65536
v_mfma_f32_16x16x32_bf16 a[24:27], a[244:247], a[232:235], a[24:27]
ds_read_b64_tr_b16 a[242:243], v22 offset:13312
v_add_u32 v18, 16384, v14
v_mfma_f32_16x16x32_bf16 a[20:23], a[244:247], a[228:231], a[20:23]
v_add_u32 v19, 16384, v15
v_add_u32 v20, 16384, v16
v_mfma_f32_16x16x32_bf16 a[16:19], a[244:247], a[224:227], a[16:19]
s_waitcnt lgkmcnt(4)
v_mul_f32 v136, v31, v136
v_mul_f32 v137, v31, v137
v_mfma_f32_16x16x32_bf16 a[32:35], a[248:251], a[224:227], a[32:35]
ds_read_b64_tr_b16 a[244:245], v23 offset:9216
v_mul_f32 v138, v31, v138
v_mfma_f32_16x16x32_bf16 a[36:39], a[248:251], a[228:231], a[36:39]
ds_read_b64_tr_b16 a[246:247], v23 offset:13312
v_mul_f32 v139, v31, v139
v_mfma_f32_16x16x32_bf16 a[40:43], a[248:251], a[232:235], a[40:43]
v_exp_f32 v136, v136
v_mfma_f32_16x16x32_bf16 a[44:47], a[248:251], a[236:239], a[44:47]
s_waitcnt lgkmcnt(4)
v_exp_f32 v137, v137
v_mfma_f32_16x16x32_bf16 a[60:63], a[252:255], a[236:239], a[60:63]
ds_read_b64_tr_b16 a[248:249], v24 offset:9216
v_add_f32 v136, 1.0, v136
v_mfma_f32_16x16x32_bf16 a[56:59], a[252:255], a[232:235], a[56:59]
ds_read_b64_tr_b16 a[250:251], v24 offset:13312
v_add_f32 v137, 1.0, v137
v_mfma_f32_16x16x32_bf16 a[52:55], a[252:255], a[228:231], a[52:55]
v_exp_f32 v138, v138
v_mfma_f32_16x16x32_bf16 a[48:51], a[252:255], a[224:227], a[48:51]
s_waitcnt lgkmcnt(4)
v_exp_f32 v139, v139
v_mfma_f32_16x16x32_bf16 a[64:67], a[240:243], a[224:227], a[64:67]
ds_read_b64_tr_b16 a[252:253], v25 offset:9216
v_add_f32 v138, 1.0, v138
v_mfma_f32_16x16x32_bf16 a[68:71], a[240:243], a[228:231], a[68:71]
ds_read_b64_tr_b16 a[254:255], v25 offset:13312
v_add_f32 v139, 1.0, v139
v_mfma_f32_16x16x32_bf16 a[72:75], a[240:243], a[232:235], a[72:75]
buffer_load_dwordx4 v2, s[72:75], s54 offen lds
v_mfma_f32_16x16x32_bf16 a[76:79], a[240:243], a[236:239], a[76:79]
s_waitcnt lgkmcnt(4)
v_add_u32 v22, 32768, v14
v_add_u32 v21, 16384, v17
v_mfma_f32_16x16x32_bf16 a[92:95], a[244:247], a[236:239], a[92:95]
ds_read_b64_tr_b16 a[240:241], v18 offset:0
v_add_u32 v23, 32768, v15
v_mfma_f32_16x16x32_bf16 a[88:91], a[244:247], a[232:235], a[88:91]
ds_read_b64_tr_b16 a[242:243], v18 offset:4096
v_mul_f32 v140, v31, v140
v_mfma_f32_16x16x32_bf16 a[84:87], a[244:247], a[228:231], a[84:87]
buffer_load_dwordx4 v2, s[72:75], s54 offen offset:1024 lds
v_mfma_f32_16x16x32_bf16 a[80:83], a[244:247], a[224:227], a[80:83]
s_waitcnt lgkmcnt(4)
v_rcp_f32 v136, v136
v_mfma_f32_16x16x32_bf16 a[96:99], a[248:251], a[224:227], a[96:99]
ds_read_b64_tr_b16 a[244:245], v18 offset:8192
v_mul_f32 v136, v136, v248
v_mfma_f32_16x16x32_bf16 a[100:103], a[248:251], a[228:231], a[100:103]
ds_read_b64_tr_b16 a[246:247], v18 offset:12288
v_mul_f32 v141, v31, v141
v_mfma_f32_16x16x32_bf16 a[104:107], a[248:251], a[232:235], a[104:107]
buffer_load_dwordx4 v2, s[72:75], s54 offen offset:2048 lds
v_mfma_f32_16x16x32_bf16 a[108:111], a[248:251], a[236:239], a[108:111]
s_waitcnt lgkmcnt(4)
v_rcp_f32 v137, v137
v_mfma_f32_16x16x32_bf16 a[124:127], a[252:255], a[236:239], a[124:127]
v_rcp_f32 v138, v138
v_mfma_f32_16x16x32_bf16 a[120:123], a[252:255], a[232:235], a[120:123]
v_rcp_f32 v139, v139
v_mfma_f32_16x16x32_bf16 a[116:119], a[252:255], a[228:231], a[116:119]
buffer_load_dwordx4 v2, s[72:75], s54 offen offset:3072 lds
ds_read_b64_tr_b16 a[248:249], v22 offset:0
ds_read_b64_tr_b16 a[250:251], v22 offset:4096
v_mul_f32 v137, v137, v249
v_mul_f32 v138, v138, v250
v_mul_f32 v139, v139, v251
v_cvt_pk_bf16_f32 v248, v136, v137
v_cvt_pk_bf16_f32 v249, v138, v139
v_mfma_f32_16x16x32_bf16 a[112:115], a[252:255], a[224:227], a[112:115]
s_waitcnt lgkmcnt(4)
v_mul_f32 v142, v31, v142
v_mfma_f32_16x16x32_bf16 a[0:3], a[240:243], a[128:131], a[0:3]
ds_read_b64_tr_b16 a[252:253], v22 offset:8192
v_mul_f32 v143, v31, v143
v_mfma_f32_16x16x32_bf16 a[4:7], a[240:243], a[132:135], a[4:7]
ds_read_b64_tr_b16 a[254:255], v22 offset:12288
v_add_u32 v24, 32768, v16
v_mfma_f32_16x16x32_bf16 a[8:11], a[240:243], a[136:139], a[8:11]
buffer_store_dwordx2 v[248:249], v2, s[32:35], s87 offen offset:2048 sc1
v_mfma_f32_16x16x32_bf16 a[12:15], a[240:243], a[140:143], a[12:15]
s_waitcnt lgkmcnt(4)
v_add_u32 v25, 32768, v17
v_mfma_f32_16x16x32_bf16 a[12:15], a[244:247], a[156:159], a[12:15]
ds_read_b64_tr_b16 a[240:241], v19 offset:0
v_mfma_f32_16x16x32_bf16 a[8:11], a[244:247], a[152:155], a[8:11]
ds_read_b64_tr_b16 a[242:243], v19 offset:4096
v_mfma_f32_16x16x32_bf16 a[4:7], a[244:247], a[148:151], a[4:7]
v_exp_f32 v140, v140
v_mfma_f32_16x16x32_bf16 a[0:3], a[244:247], a[144:147], a[0:3]
s_waitcnt lgkmcnt(4)
s_waitcnt vmcnt(16)
v_add_f32 v140, 1.0, v140
v_mfma_f32_16x16x32_bf16 a[0:3], a[248:251], a[160:163], a[0:3]
ds_read_b64_tr_b16 a[244:245], v19 offset:8192
v_mfma_f32_16x16x32_bf16 a[4:7], a[248:251], a[164:167], a[4:7]
ds_read_b64_tr_b16 a[246:247], v19 offset:12288
v_mfma_f32_16x16x32_bf16 a[8:11], a[248:251], a[168:171], a[8:11]
v_exp_f32 v141, v141
v_mfma_f32_16x16x32_bf16 a[12:15], a[248:251], a[172:175], a[12:15]
s_waitcnt lgkmcnt(4)
s_waitcnt vmcnt(11)
v_add_f32 v141, 1.0, v141
v_mfma_f32_16x16x32_bf16 a[12:15], a[252:255], a[188:191], a[12:15]
ds_read_b64_tr_b16 a[248:249], v23 offset:0
v_mfma_f32_16x16x32_bf16 a[8:11], a[252:255], a[184:187], a[8:11]
ds_read_b64_tr_b16 a[250:251], v23 offset:4096
v_mfma_f32_16x16x32_bf16 a[4:7], a[252:255], a[180:183], a[4:7]
v_exp_f32 v142, v142
v_mfma_f32_16x16x32_bf16 a[0:3], a[252:255], a[176:179], a[0:3]
s_waitcnt lgkmcnt(4)
v_exp_f32 v143, v143
v_mfma_f32_16x16x32_bf16 a[16:19], a[240:243], a[128:131], a[16:19]
ds_read_b64_tr_b16 a[252:253], v23 offset:8192
v_add_f32 v142, 1.0, v142
v_mfma_f32_16x16x32_bf16 a[20:23], a[240:243], a[132:135], a[20:23]
ds_read_b64_tr_b16 a[254:255], v23 offset:12288
v_add_f32 v143, 1.0, v143
v_mfma_f32_16x16x32_bf16 a[24:27], a[240:243], a[136:139], a[24:27]
v_rcp_f32 v140, v140
v_mfma_f32_16x16x32_bf16 a[28:31], a[240:243], a[140:143], a[28:31]
s_waitcnt lgkmcnt(4)
v_rcp_f32 v141, v141
v_mfma_f32_16x16x32_bf16 a[28:31], a[244:247], a[156:159], a[28:31]
ds_read_b64_tr_b16 a[240:241], v20 offset:0
v_mul_f32 v140, v140, v252
v_mfma_f32_16x16x32_bf16 a[24:27], a[244:247], a[152:155], a[24:27]
ds_read_b64_tr_b16 a[242:243], v20 offset:4096
v_mul_f32 v141, v141, v253
v_mfma_f32_16x16x32_bf16 a[20:23], a[244:247], a[148:151], a[20:23]
s_add_u32 s54, s0, 1
v_rcp_f32 v142, v142
v_mfma_f32_16x16x32_bf16 a[16:19], a[244:247], a[144:147], a[16:19]
s_waitcnt lgkmcnt(4)
v_rcp_f32 v143, v143
v_mfma_f32_16x16x32_bf16 a[16:19], a[248:251], a[160:163], a[16:19]
ds_read_b64_tr_b16 a[244:245], v20 offset:8192
v_mul_f32 v142, v142, v254
v_mfma_f32_16x16x32_bf16 a[20:23], a[248:251], a[164:167], a[20:23]
ds_read_b64_tr_b16 a[246:247], v20 offset:12288
v_mul_f32 v143, v143, v255
v_mfma_f32_16x16x32_bf16 a[24:27], a[248:251], a[168:171], a[24:27]
s_and_b32 s54, s54, 7
v_cvt_pk_bf16_f32 v252, v140, v141
v_cvt_pk_bf16_f32 v253, v142, v143
v_mfma_f32_16x16x32_bf16 a[28:31], a[248:251], a[172:175], a[28:31]
s_waitcnt lgkmcnt(4)
s_mul_i32 s54, s54, 32768
s_add_u32 s54, s54, s71
v_mfma_f32_16x16x32_bf16 a[28:31], a[252:255], a[188:191], a[28:31]
ds_read_b64_tr_b16 a[248:249], v24 offset:0
s_add_u32 m0, s70, 81920
v_mfma_f32_16x16x32_bf16 a[24:27], a[252:255], a[184:187], a[24:27]
ds_read_b64_tr_b16 a[250:251], v24 offset:4096
v_mfma_f32_16x16x32_bf16 a[20:23], a[252:255], a[180:183], a[20:23]
buffer_store_dwordx2 v[252:253], v2, s[32:35], s87 offen offset:3072 sc1
v_mfma_f32_16x16x32_bf16 a[16:19], a[252:255], a[176:179], a[16:19]
s_waitcnt lgkmcnt(4)
v_mfma_f32_16x16x32_bf16 a[32:35], a[240:243], a[128:131], a[32:35]
ds_read_b64_tr_b16 a[252:253], v24 offset:8192
v_mfma_f32_16x16x32_bf16 a[36:39], a[240:243], a[132:135], a[36:39]
ds_read_b64_tr_b16 a[254:255], v24 offset:12288
v_mfma_f32_16x16x32_bf16 a[40:43], a[240:243], a[136:139], a[40:43]
v_accvgpr_read_b32 v160, a0
v_accvgpr_read_b32 v161, a1
v_mfma_f32_16x16x32_bf16 a[44:47], a[240:243], a[140:143], a[44:47]
s_waitcnt lgkmcnt(4)
v_accvgpr_read_b32 v162, a2
v_accvgpr_read_b32 v163, a3
v_mfma_f32_16x16x32_bf16 a[44:47], a[244:247], a[156:159], a[44:47]
ds_read_b64_tr_b16 a[240:241], v21 offset:0
v_accvgpr_read_b32 v164, a16
v_mfma_f32_16x16x32_bf16 a[40:43], a[244:247], a[152:155], a[40:43]
ds_read_b64_tr_b16 a[242:243], v21 offset:4096
v_accvgpr_read_b32 v165, a17
v_mfma_f32_16x16x32_bf16 a[36:39], a[244:247], a[148:151], a[36:39]
v_accvgpr_read_b32 v166, a18
v_accvgpr_read_b32 v167, a19
v_mfma_f32_16x16x32_bf16 a[32:35], a[244:247], a[144:147], a[32:35]
s_waitcnt lgkmcnt(4)
v_mul_f32 v160, v28, v160
v_mul_f32 v161, v28, v161
v_mfma_f32_16x16x32_bf16 a[32:35], a[248:251], a[160:163], a[32:35]
ds_read_b64_tr_b16 a[244:245], v21 offset:8192
v_mul_f32 v162, v28, v162
v_mfma_f32_16x16x32_bf16 a[36:39], a[248:251], a[164:167], a[36:39]
ds_read_b64_tr_b16 a[246:247], v21 offset:12288
v_mul_f32 v163, v28, v163
v_mfma_f32_16x16x32_bf16 a[40:43], a[248:251], a[168:171], a[40:43]
v_mul_f32 v164, v28, v164
v_mul_f32 v165, v28, v165
v_mfma_f32_16x16x32_bf16 a[44:47], a[248:251], a[172:175], a[44:47]
s_waitcnt lgkmcnt(4)
v_mul_f32 v166, v28, v166
v_mul_f32 v167, v28, v167
v_mfma_f32_16x16x32_bf16 a[44:47], a[252:255], a[188:191], a[44:47]
ds_read_b64_tr_b16 a[248:249], v25 offset:0
v_cvt_pk_bf16_f32 v32, v160, v161
v_mfma_f32_16x16x32_bf16 a[40:43], a[252:255], a[184:187], a[40:43]
ds_read_b64_tr_b16 a[250:251], v25 offset:4096
v_cvt_pk_bf16_f32 v33, v162, v163
v_mfma_f32_16x16x32_bf16 a[36:39], a[252:255], a[180:183], a[36:39]
v_cvt_pk_bf16_f32 v34, v164, v165
v_cvt_pk_bf16_f32 v35, v166, v167
v_mfma_f32_16x16x32_bf16 a[32:35], a[252:255], a[176:179], a[32:35]
s_waitcnt lgkmcnt(4)
v_accvgpr_read_b32 v168, a4
v_accvgpr_read_b32 v169, a5
v_mfma_f32_16x16x32_bf16 a[48:51], a[240:243], a[128:131], a[48:51]
ds_read_b64_tr_b16 a[252:253], v25 offset:8192
v_accvgpr_read_b32 v170, a6
v_mfma_f32_16x16x32_bf16 a[52:55], a[240:243], a[132:135], a[52:55]
ds_read_b64_tr_b16 a[254:255], v25 offset:12288
v_accvgpr_read_b32 v171, a7
v_mfma_f32_16x16x32_bf16 a[56:59], a[240:243], a[136:139], a[56:59]
buffer_load_dwordx4 v2, s[72:75], s54 offen lds
v_mfma_f32_16x16x32_bf16 a[60:63], a[240:243], a[140:143], a[60:63]
s_waitcnt lgkmcnt(4)
v_accvgpr_read_b32 v172, a20
v_accvgpr_read_b32 v173, a21
v_mfma_f32_16x16x32_bf16 a[60:63], a[244:247], a[156:159], a[60:63]
ds_read_b64_tr_b16 a[240:241], v18 offset:1024
v_accvgpr_read_b32 v174, a22
v_mfma_f32_16x16x32_bf16 a[56:59], a[244:247], a[152:155], a[56:59]
ds_read_b64_tr_b16 a[242:243], v18 offset:5120
v_accvgpr_read_b32 v175, a23
v_mfma_f32_16x16x32_bf16 a[52:55], a[244:247], a[148:151], a[52:55]
buffer_load_dwordx4 v2, s[72:75], s54 offen offset:1024 lds
v_mfma_f32_16x16x32_bf16 a[48:51], a[244:247], a[144:147], a[48:51]
s_waitcnt lgkmcnt(4)
v_mul_f32 v168, v28, v168
v_mul_f32 v169, v28, v169
v_mfma_f32_16x16x32_bf16 a[48:51], a[248:251], a[160:163], a[48:51]
ds_read_b64_tr_b16 a[244:245], v18 offset:9216
v_mul_f32 v170, v28, v170
v_mfma_f32_16x16x32_bf16 a[52:55], a[248:251], a[164:167], a[52:55]
ds_read_b64_tr_b16 a[246:247], v18 offset:13312
v_mul_f32 v171, v28, v171
v_mfma_f32_16x16x32_bf16 a[56:59], a[248:251], a[168:171], a[56:59]
buffer_load_dwordx4 v2, s[72:75], s54 offen offset:2048 lds
v_mul_f32 v172, v28, v172
v_mul_f32 v173, v28, v173
v_mul_f32 v174, v28, v174
v_mul_f32 v175, v28, v175
v_mfma_f32_16x16x32_bf16 a[60:63], a[248:251], a[172:175], a[60:63]
s_waitcnt lgkmcnt(4)
v_cvt_pk_bf16_f32 v36, v168, v169
v_cvt_pk_bf16_f32 v37, v170, v171
v_cvt_pk_bf16_f32 v38, v172, v173
v_cvt_pk_bf16_f32 v39, v174, v175
v_mfma_f32_16x16x32_bf16 a[60:63], a[252:255], a[188:191], a[60:63]
ds_read_b64_tr_b16 a[248:249], v22 offset:1024
v_accvgpr_read_b32 v160, a8
v_mfma_f32_16x16x32_bf16 a[56:59], a[252:255], a[184:187], a[56:59]
ds_read_b64_tr_b16 a[250:251], v22 offset:5120
v_accvgpr_read_b32 v161, a9
v_mfma_f32_16x16x32_bf16 a[52:55], a[252:255], a[180:183], a[52:55]
buffer_load_dwordx4 v2, s[72:75], s54 offen offset:3072 lds
v_mfma_f32_16x16x32_bf16 a[48:51], a[252:255], a[176:179], a[48:51]
s_waitcnt lgkmcnt(4)
v_accvgpr_read_b32 v162, a10
v_accvgpr_read_b32 v163, a11
v_mfma_f32_16x16x32_bf16 a[64:67], a[240:243], a[128:131], a[64:67]
ds_read_b64_tr_b16 a[252:253], v22 offset:9216
v_accvgpr_read_b32 v164, a24
v_mfma_f32_16x16x32_bf16 a[68:71], a[240:243], a[132:135], a[68:71]
ds_read_b64_tr_b16 a[254:255], v22 offset:13312
v_accvgpr_read_b32 v165, a25
v_mfma_f32_16x16x32_bf16 a[72:75], a[240:243], a[136:139], a[72:75]
v_accvgpr_read_b32 v166, a26
v_accvgpr_read_b32 v167, a27
v_mfma_f32_16x16x32_bf16 a[76:79], a[240:243], a[140:143], a[76:79]
s_waitcnt lgkmcnt(4)
v_mul_f32 v160, v28, v160
v_mul_f32 v161, v28, v161
v_mfma_f32_16x16x32_bf16 a[76:79], a[244:247], a[156:159], a[76:79]
v_mul_f32 v162, v28, v162
v_mul_f32 v163, v28, v163
v_mfma_f32_16x16x32_bf16 a[72:75], a[244:247], a[152:155], a[72:75]
v_mul_f32 v164, v28, v164
v_mul_f32 v165, v28, v165
v_mul_f32 v166, v28, v166
v_mul_f32 v167, v28, v167
v_mfma_f32_16x16x32_bf16 a[68:71], a[244:247], a[148:151], a[68:71]
ds_read_b64_tr_b16 a[240:241], v19 offset:1024
ds_read_b64_tr_b16 a[242:243], v19 offset:5120
v_cvt_pk_bf16_f32 v40, v160, v161
v_cvt_pk_bf16_f32 v41, v162, v163
v_cvt_pk_bf16_f32 v42, v164, v165
v_cvt_pk_bf16_f32 v43, v166, v167
v_mfma_f32_16x16x32_bf16 a[64:67], a[244:247], a[144:147], a[64:67]
s_waitcnt lgkmcnt(4)
v_accvgpr_read_b32 v160, a32
v_accvgpr_read_b32 v161, a33
v_mfma_f32_16x16x32_bf16 a[64:67], a[248:251], a[160:163], a[64:67]
ds_read_b64_tr_b16 a[244:245], v19 offset:9216
v_accvgpr_read_b32 v162, a34
v_mfma_f32_16x16x32_bf16 a[68:71], a[248:251], a[164:167], a[68:71]
ds_read_b64_tr_b16 a[246:247], v19 offset:13312
v_accvgpr_read_b32 v163, a35
v_mfma_f32_16x16x32_bf16 a[72:75], a[248:251], a[168:171], a[72:75]
v_accvgpr_read_b32 v164, a48
v_accvgpr_read_b32 v165, a49
v_mfma_f32_16x16x32_bf16 a[76:79], a[248:251], a[172:175], a[76:79]
s_waitcnt lgkmcnt(4)
v_accvgpr_read_b32 v166, a50
v_accvgpr_read_b32 v167, a51
v_mfma_f32_16x16x32_bf16 a[76:79], a[252:255], a[188:191], a[76:79]
ds_read_b64_tr_b16 a[248:249], v23 offset:1024
v_accvgpr_read_b32 v168, a12
v_mfma_f32_16x16x32_bf16 a[72:75], a[252:255], a[184:187], a[72:75]
ds_read_b64_tr_b16 a[250:251], v23 offset:5120
v_accvgpr_read_b32 v169, a13
v_mfma_f32_16x16x32_bf16 a[68:71], a[252:255], a[180:183], a[68:71]
v_accvgpr_read_b32 v170, a14
v_accvgpr_read_b32 v171, a15
v_mfma_f32_16x16x32_bf16 a[64:67], a[252:255], a[176:179], a[64:67]
s_waitcnt lgkmcnt(4)
v_accvgpr_read_b32 v172, a28
v_accvgpr_read_b32 v173, a29
v_accvgpr_read_b32 v174, a30
v_accvgpr_read_b32 v175, a31
v_mul_f32 v160, v28, v160
v_mul_f32 v161, v28, v161
v_mul_f32 v162, v28, v162
v_mul_f32 v163, v28, v163
v_mul_f32 v164, v28, v164
v_mul_f32 v165, v28, v165
v_mul_f32 v166, v28, v166
v_mul_f32 v167, v28, v167
v_mfma_f32_16x16x32_bf16 a[80:83], a[240:243], a[128:131], a[80:83]
v_cvt_pk_bf16_f32 v48, v160, v161
v_cvt_pk_bf16_f32 v49, v162, v163
v_cvt_pk_bf16_f32 v50, v164, v165
v_cvt_pk_bf16_f32 v51, v166, v167
v_mul_f32 v168, v28, v168
v_mul_f32 v169, v28, v169
v_mul_f32 v170, v28, v170
v_mul_f32 v171, v28, v171
v_mul_f32 v172, v28, v172
v_mul_f32 v173, v28, v173
v_mul_f32 v174, v28, v174
v_mul_f32 v175, v28, v175
v_mfma_f32_16x16x32_bf16 a[84:87], a[240:243], a[132:135], a[84:87]
v_cvt_pk_bf16_f32 v44, v168, v169
v_cvt_pk_bf16_f32 v45, v170, v171
v_cvt_pk_bf16_f32 v46, v172, v173
v_cvt_pk_bf16_f32 v47, v174, v175
v_mfma_f32_16x16x32_bf16 a[88:91], a[240:243], a[136:139], a[88:91]
ds_read_b64_tr_b16 a[252:253], v23 offset:9216
ds_read_b64_tr_b16 a[254:255], v23 offset:13312
v_accvgpr_read_b32 v168, a36
v_mfma_f32_16x16x32_bf16 a[92:95], a[240:243], a[140:143], a[92:95]
s_waitcnt lgkmcnt(4)
v_accvgpr_read_b32 v169, a37
v_accvgpr_read_b32 v170, a38
v_mfma_f32_16x16x32_bf16 a[92:95], a[244:247], a[156:159], a[92:95]
ds_read_b64_tr_b16 a[240:241], v20 offset:1024
v_accvgpr_read_b32 v171, a39
v_mfma_f32_16x16x32_bf16 a[88:91], a[244:247], a[152:155], a[88:91]
ds_read_b64_tr_b16 a[242:243], v20 offset:5120
v_accvgpr_read_b32 v172, a52
v_mfma_f32_16x16x32_bf16 a[84:87], a[244:247], a[148:151], a[84:87]
s_add_u32 s54, s0, 1
v_accvgpr_read_b32 v173, a53
v_accvgpr_read_b32 v174, a54
v_mfma_f32_16x16x32_bf16 a[80:83], a[244:247], a[144:147], a[80:83]
s_waitcnt lgkmcnt(4)
v_accvgpr_read_b32 v175, a55
v_mul_f32 v168, v28, v168
v_mfma_f32_16x16x32_bf16 a[80:83], a[248:251], a[160:163], a[80:83]
ds_read_b64_tr_b16 a[244:245], v20 offset:9216
v_mul_f32 v169, v28, v169
v_mfma_f32_16x16x32_bf16 a[84:87], a[248:251], a[164:167], a[84:87]
ds_read_b64_tr_b16 a[246:247], v20 offset:13312
v_mul_f32 v170, v28, v170
v_mfma_f32_16x16x32_bf16 a[88:91], a[248:251], a[168:171], a[88:91]
s_and_b32 s54, s54, 7
v_mul_f32 v171, v28, v171
v_mul_f32 v172, v28, v172
v_mul_f32 v173, v28, v173
v_mul_f32 v174, v28, v174
v_mul_f32 v175, v28, v175
v_mfma_f32_16x16x32_bf16 a[92:95], a[248:251], a[172:175], a[92:95]
s_waitcnt lgkmcnt(4)
v_cvt_pk_bf16_f32 v52, v168, v169
v_cvt_pk_bf16_f32 v53, v170, v171
v_cvt_pk_bf16_f32 v54, v172, v173
v_cvt_pk_bf16_f32 v55, v174, v175
v_mfma_f32_16x16x32_bf16 a[92:95], a[252:255], a[188:191], a[92:95]
ds_read_b64_tr_b16 a[248:249], v24 offset:1024
v_accvgpr_read_b32 v160, a40
v_mfma_f32_16x16x32_bf16 a[88:91], a[252:255], a[184:187], a[88:91]
ds_read_b64_tr_b16 a[250:251], v24 offset:5120
v_accvgpr_read_b32 v161, a41
v_mfma_f32_16x16x32_bf16 a[84:87], a[252:255], a[180:183], a[84:87]
s_mul_i32 s54, s54, 32768
v_accvgpr_read_b32 v162, a42
v_accvgpr_read_b32 v163, a43
v_mfma_f32_16x16x32_bf16 a[80:83], a[252:255], a[176:179], a[80:83]
s_waitcnt lgkmcnt(4)
v_accvgpr_read_b32 v164, a56
v_accvgpr_read_b32 v165, a57
v_mfma_f32_16x16x32_bf16 a[96:99], a[240:243], a[128:131], a[96:99]
ds_read_b64_tr_b16 a[252:253], v24 offset:9216
v_accvgpr_read_b32 v166, a58
v_mfma_f32_16x16x32_bf16 a[100:103], a[240:243], a[132:135], a[100:103]
ds_read_b64_tr_b16 a[254:255], v24 offset:13312
v_accvgpr_read_b32 v167, a59
v_mfma_f32_16x16x32_bf16 a[104:107], a[240:243], a[136:139], a[104:107]
s_add_u32 s54, s54, s71
v_mul_f32 v160, v28, v160
v_mul_f32 v161, v28, v161
v_mfma_f32_16x16x32_bf16 a[108:111], a[240:243], a[140:143], a[108:111]
s_waitcnt vmcnt(12)
s_barrier
s_waitcnt lgkmcnt(4)
v_mfma_f32_16x16x32_bf16 a[108:111], a[244:247], a[156:159], a[108:111]
s_add_u32 s54, s54, 16384
v_mul_f32 v162, v28, v162
v_mul_f32 v163, v28, v163
v_mfma_f32_16x16x32_bf16 a[104:107], a[244:247], a[152:155], a[104:107]
s_add_u32 m0, s70, 98304
v_mul_f32 v164, v28, v164
v_mul_f32 v165, v28, v165
v_mul_f32 v166, v28, v166
v_mul_f32 v167, v28, v167
v_mfma_f32_16x16x32_bf16 a[100:103], a[244:247], a[148:151], a[100:103]
ds_read_b64_tr_b16 a[240:241], v21 offset:1024
ds_read_b64_tr_b16 a[242:243], v21 offset:5120
v_cvt_pk_bf16_f32 v56, v160, v161
v_cvt_pk_bf16_f32 v57, v162, v163
v_cvt_pk_bf16_f32 v58, v164, v165
v_cvt_pk_bf16_f32 v59, v166, v167
v_mfma_f32_16x16x32_bf16 a[96:99], a[244:247], a[144:147], a[96:99]
s_waitcnt lgkmcnt(4)
v_accvgpr_read_b32 v168, a44
v_accvgpr_read_b32 v169, a45
v_mfma_f32_16x16x32_bf16 a[96:99], a[248:251], a[160:163], a[96:99]
ds_read_b64_tr_b16 a[244:245], v21 offset:9216
v_accvgpr_read_b32 v170, a46
v_mfma_f32_16x16x32_bf16 a[100:103], a[248:251], a[164:167], a[100:103]
ds_read_b64_tr_b16 a[246:247], v21 offset:13312
v_accvgpr_read_b32 v171, a47
v_mfma_f32_16x16x32_bf16 a[104:107], a[248:251], a[168:171], a[104:107]
v_accvgpr_read_b32 v172, a60
v_accvgpr_read_b32 v173, a61
v_mfma_f32_16x16x32_bf16 a[108:111], a[248:251], a[172:175], a[108:111]
s_waitcnt lgkmcnt(4)
v_accvgpr_read_b32 v174, a62
v_accvgpr_read_b32 v175, a63
v_mfma_f32_16x16x32_bf16 a[108:111], a[252:255], a[188:191], a[108:111]
ds_read_b64_tr_b16 a[248:249], v25 offset:1024
v_mul_f32 v168, v28, v168
v_mfma_f32_16x16x32_bf16 a[104:107], a[252:255], a[184:187], a[104:107]
ds_read_b64_tr_b16 a[250:251], v25 offset:5120
v_mul_f32 v169, v28, v169
v_mfma_f32_16x16x32_bf16 a[100:103], a[252:255], a[180:183], a[100:103]
v_mul_f32 v170, v28, v170
v_mul_f32 v171, v28, v171
v_mfma_f32_16x16x32_bf16 a[96:99], a[252:255], a[176:179], a[96:99]
s_waitcnt lgkmcnt(4)
v_mul_f32 v172, v28, v172
v_mul_f32 v173, v28, v173
v_mfma_f32_16x16x32_bf16 a[112:115], a[240:243], a[128:131], a[112:115]
v_mul_f32 v174, v28, v174
v_mul_f32 v175, v28, v175
v_mfma_f32_16x16x32_bf16 a[116:119], a[240:243], a[132:135], a[116:119]
buffer_load_dwordx4 v2, s[72:75], s54 offen lds
v_cvt_pk_bf16_f32 v60, v168, v169
v_cvt_pk_bf16_f32 v61, v170, v171
v_cvt_pk_bf16_f32 v62, v172, v173
v_cvt_pk_bf16_f32 v63, v174, v175
v_mfma_f32_16x16x32_bf16 a[120:123], a[240:243], a[136:139], a[120:123]
ds_read_b64_tr_b16 a[252:253], v25 offset:9216
ds_read_b64_tr_b16 a[254:255], v25 offset:13312
v_mfma_f32_16x16x32_bf16 a[124:127], a[240:243], a[140:143], a[124:127]
s_waitcnt lgkmcnt(4)
buffer_load_dwordx4 v2, s[72:75], s54 offen offset:1024 lds
v_mfma_f32_16x16x32_bf16 a[124:127], a[244:247], a[156:159], a[124:127]
v_mfma_f32_16x16x32_bf16 a[120:123], a[244:247], a[152:155], a[120:123]
buffer_load_dwordx4 v2, s[72:75], s54 offen offset:2048 lds
v_mfma_f32_16x16x32_bf16 a[116:119], a[244:247], a[148:151], a[116:119]
v_mfma_f32_16x16x32_bf16 a[112:115], a[244:247], a[144:147], a[112:115]
s_waitcnt lgkmcnt(2)
buffer_load_dwordx4 v2, s[72:75], s54 offen offset:3072 lds
v_mfma_f32_16x16x32_bf16 a[112:115], a[248:251], a[160:163], a[112:115]
s_add_u32 s54, s0, 2
s_and_b32 s54, s54, 7
s_mul_i32 s54, s54, 32768
s_add_u32 s54, s54, s71
s_add_u32 m0, s70, 114688
v_mfma_f32_16x16x32_bf16 a[116:119], a[248:251], a[164:167], a[116:119]
buffer_load_dwordx4 v2, s[72:75], s54 offen lds
v_mfma_f32_16x16x32_bf16 a[120:123], a[248:251], a[168:171], a[120:123]
v_mfma_f32_16x16x32_bf16 a[124:127], a[248:251], a[172:175], a[124:127]
s_waitcnt lgkmcnt(0)
buffer_load_dwordx4 v2, s[72:75], s54 offen offset:1024 lds
v_mfma_f32_16x16x32_bf16 a[124:127], a[252:255], a[188:191], a[124:127]
v_mfma_f32_16x16x32_bf16 a[120:123], a[252:255], a[184:187], a[120:123]
buffer_load_dwordx4 v2, s[72:75], s54 offen offset:2048 lds
v_mfma_f32_16x16x32_bf16 a[116:119], a[252:255], a[180:183], a[116:119]
v_mfma_f32_16x16x32_bf16 a[112:115], a[252:255], a[176:179], a[112:115]
buffer_load_dwordx4 v2, s[72:75], s54 offen offset:3072 lds
v_mov_b32 v28, 0x3e0293ee
v_mov_b32 v29, 0x3e0293ee
v_accvgpr_read_b32 v96, a64
v_accvgpr_read_b32 v97, a65
v_accvgpr_read_b32 v98, a66
v_accvgpr_read_b32 v99, a67
v_accvgpr_read_b32 v100, a80
v_accvgpr_read_b32 v101, a81
v_accvgpr_read_b32 v102, a82
v_accvgpr_read_b32 v103, a83
v_pk_mul_f32 v[96:97], v[96:97], v[28:29]
v_pk_mul_f32 v[98:99], v[98:99], v[28:29]
v_pk_mul_f32 v[100:101], v[100:101], v[28:29]
v_pk_mul_f32 v[102:103], v[102:103], v[28:29]
v_cvt_pk_bf16_f32 v64, v96, v97
v_cvt_pk_bf16_f32 v65, v98, v99
v_cvt_pk_bf16_f32 v66, v100, v101
v_cvt_pk_bf16_f32 v67, v102, v103
v_accvgpr_read_b32 v104, a68
v_accvgpr_read_b32 v105, a69
v_accvgpr_read_b32 v106, a70
v_accvgpr_read_b32 v107, a71
v_accvgpr_read_b32 v108, a84
v_accvgpr_read_b32 v109, a85
v_accvgpr_read_b32 v110, a86
v_accvgpr_read_b32 v111, a87
v_pk_mul_f32 v[104:105], v[104:105], v[28:29]
v_pk_mul_f32 v[106:107], v[106:107], v[28:29]
v_pk_mul_f32 v[108:109], v[108:109], v[28:29]
v_pk_mul_f32 v[110:111], v[110:111], v[28:29]
v_cvt_pk_bf16_f32 v68, v104, v105
v_cvt_pk_bf16_f32 v69, v106, v107
v_cvt_pk_bf16_f32 v70, v108, v109
v_cvt_pk_bf16_f32 v71, v110, v111
v_accvgpr_read_b32 v112, a72
v_accvgpr_read_b32 v113, a73
v_accvgpr_read_b32 v114, a74
v_accvgpr_read_b32 v115, a75
v_accvgpr_read_b32 v116, a88
v_accvgpr_read_b32 v117, a89
v_accvgpr_read_b32 v118, a90
v_accvgpr_read_b32 v119, a91
v_pk_mul_f32 v[112:113], v[112:113], v[28:29]
v_pk_mul_f32 v[114:115], v[114:115], v[28:29]
v_pk_mul_f32 v[116:117], v[116:117], v[28:29]
v_pk_mul_f32 v[118:119], v[118:119], v[28:29]
v_cvt_pk_bf16_f32 v72, v112, v113
v_cvt_pk_bf16_f32 v73, v114, v115
v_cvt_pk_bf16_f32 v74, v116, v117
v_cvt_pk_bf16_f32 v75, v118, v119
v_accvgpr_read_b32 v120, a76
v_accvgpr_read_b32 v121, a77
v_accvgpr_read_b32 v122, a78
v_accvgpr_read_b32 v123, a79
v_accvgpr_read_b32 v124, a92
v_accvgpr_read_b32 v125, a93
v_accvgpr_read_b32 v126, a94
v_accvgpr_read_b32 v127, a95
v_pk_mul_f32 v[120:121], v[120:121], v[28:29]
v_pk_mul_f32 v[122:123], v[122:123], v[28:29]
v_pk_mul_f32 v[124:125], v[124:125], v[28:29]
v_pk_mul_f32 v[126:127], v[126:127], v[28:29]
v_cvt_pk_bf16_f32 v76, v120, v121
v_cvt_pk_bf16_f32 v77, v122, v123
v_cvt_pk_bf16_f32 v78, v124, v125
v_cvt_pk_bf16_f32 v79, v126, v127
v_accvgpr_read_b32 v96, a96
v_accvgpr_read_b32 v97, a97
v_accvgpr_read_b32 v98, a98
v_accvgpr_read_b32 v99, a99
v_accvgpr_read_b32 v100, a112
v_accvgpr_read_b32 v101, a113
v_accvgpr_read_b32 v102, a114
v_accvgpr_read_b32 v103, a115
v_pk_mul_f32 v[96:97], v[96:97], v[28:29]
v_pk_mul_f32 v[98:99], v[98:99], v[28:29]
v_pk_mul_f32 v[100:101], v[100:101], v[28:29]
v_pk_mul_f32 v[102:103], v[102:103], v[28:29]
v_cvt_pk_bf16_f32 v80, v96, v97
v_cvt_pk_bf16_f32 v81, v98, v99
v_cvt_pk_bf16_f32 v82, v100, v101
v_cvt_pk_bf16_f32 v83, v102, v103
v_accvgpr_read_b32 v104, a100
v_accvgpr_read_b32 v105, a101
v_accvgpr_read_b32 v106, a102
v_accvgpr_read_b32 v107, a103
v_accvgpr_read_b32 v108, a116
v_accvgpr_read_b32 v109, a117
v_accvgpr_read_b32 v110, a118
v_accvgpr_read_b32 v111, a119
v_pk_mul_f32 v[104:105], v[104:105], v[28:29]
v_pk_mul_f32 v[106:107], v[106:107], v[28:29]
v_pk_mul_f32 v[108:109], v[108:109], v[28:29]
v_pk_mul_f32 v[110:111], v[110:111], v[28:29]
v_cvt_pk_bf16_f32 v84, v104, v105
v_cvt_pk_bf16_f32 v85, v106, v107
v_cvt_pk_bf16_f32 v86, v108, v109
v_cvt_pk_bf16_f32 v87, v110, v111
v_accvgpr_read_b32 v112, a104
v_accvgpr_read_b32 v113, a105
v_accvgpr_read_b32 v114, a106
v_accvgpr_read_b32 v115, a107
v_accvgpr_read_b32 v116, a120
v_accvgpr_read_b32 v117, a121
v_accvgpr_read_b32 v118, a122
v_accvgpr_read_b32 v119, a123
v_pk_mul_f32 v[112:113], v[112:113], v[28:29]
v_pk_mul_f32 v[114:115], v[114:115], v[28:29]
v_pk_mul_f32 v[116:117], v[116:117], v[28:29]
v_pk_mul_f32 v[118:119], v[118:119], v[28:29]
v_cvt_pk_bf16_f32 v88, v112, v113
v_cvt_pk_bf16_f32 v89, v114, v115
v_cvt_pk_bf16_f32 v90, v116, v117
v_cvt_pk_bf16_f32 v91, v118, v119
v_accvgpr_read_b32 v120, a108
v_accvgpr_read_b32 v121, a109
v_accvgpr_read_b32 v122, a110
v_accvgpr_read_b32 v123, a111
v_accvgpr_read_b32 v124, a124
v_accvgpr_read_b32 v125, a125
v_accvgpr_read_b32 v126, a126
v_accvgpr_read_b32 v127, a127
v_pk_mul_f32 v[120:121], v[120:121], v[28:29]
v_pk_mul_f32 v[122:123], v[122:123], v[28:29]
v_pk_mul_f32 v[124:125], v[124:125], v[28:29]
v_pk_mul_f32 v[126:127], v[126:127], v[28:29]
v_cvt_pk_bf16_f32 v92, v120, v121
v_cvt_pk_bf16_f32 v93, v122, v123
v_cvt_pk_bf16_f32 v94, v124, v125
v_cvt_pk_bf16_f32 v95, v126, v127
KA:
s_add_u32 s51, s59, 0
s_and_b32 s51, s51, 3
s_lshl_b32 s51, s51, 10
v_add_u32 v236, s51, v2
s_add_u32 s51, s59, 1
s_and_b32 s51, s51, 3
s_lshl_b32 s51, s51, 10
v_add_u32 v237, s51, v2
s_add_u32 s51, s59, 2
s_and_b32 s51, s51, 3
s_lshl_b32 s51, s51, 10
v_add_u32 v238, s51, v2
s_add_u32 s51, s59, 3
s_and_b32 s51, s51, 3
s_lshl_b32 s51, s51, 10
v_add_u32 v239, s51, v2
v_add_u32 v232, 49152, v2
ds_read_b128 a[128:131], v232 offset:0
ds_read_b128 a[132:135], v232 offset:1024
ds_read_b128 a[136:139], v232 offset:2048
v_mov_b32 v208, 0
v_mov_b32 v209, 0
v_mov_b32 v210, 0
v_mov_b32 v211, 0
v_mov_b32 v212, 0
v_mov_b32 v213, 0
v_mov_b32 v214, 0
v_mov_b32 v215, 0
v_mov_b32 v240, 0x4e800000
s_waitcnt lgkmcnt(2)
v_mfma_f32_16x16x32_bf16 v[96:99], a[128:131], v[32:35], 0
ds_read_b128 a[140:143], v232 offset:3072
v_mfma_f32_16x16x32_bf16 v[100:103], a[128:131], v[36:39], 0
v_mfma_f32_16x16x32_bf16 v[104:107], a[128:131], v[40:43], 0
s_mov_b32 s36, s18
s_mov_b32 s37, s19
s_and_b32 s37, s37, 0xffff
v_mfma_f32_16x16x32_bf16 v[108:111], a[128:131], v[44:47], 0
s_waitcnt lgkmcnt(2)
s_mov_b32 s38, 7340032
s_mov_b32 s39, 0x20000
v_mfma_f32_16x16x32_bf16 v[108:111], a[132:135], v[60:63], v[108:111]
ds_read_b128 a[144:147], v232 offset:4096
s_lshl_b32 s100, s98, 2
v_mfma_f32_16x16x32_bf16 v[104:107], a[132:135], v[56:59], v[104:107]
s_add_u32 s100, s100, s43
s_lshl_b32 s100, s100, 13
v_lshlrev_b32 v4, 7, v1
v_mfma_f32_16x16x32_bf16 v[100:103], a[132:135], v[52:55], v[100:103]
v_cmp_gt_u32 vcc, 16, v1
v_mov_b32 v5, 0x7ffffff0
v_mfma_f32_16x16x32_bf16 v[96:99], a[132:135], v[48:51], v[96:99]
s_waitcnt lgkmcnt(2)
v_cndmask_b32 v4, v5, v4, vcc
v_mfma_f32_16x16x32_bf16 v[96:99], a[136:139], v[64:67], v[96:99]
ds_read_b128 a[148:151], v232 offset:5120
v_mfma_f32_16x16x32_bf16 v[100:103], a[136:139], v[68:71], v[100:103]
v_mfma_f32_16x16x32_bf16 v[104:107], a[136:139], v[72:75], v[104:107]
v_mfma_f32_16x16x32_bf16 v[108:111], a[136:139], v[76:79], v[108:111]
s_waitcnt lgkmcnt(2)
v_mfma_f32_16x16x32_bf16 v[108:111], a[140:143], v[92:95], v[108:111]
ds_read_b128 a[152:155], v232 offset:6144
v_mfma_f32_16x16x32_bf16 v[104:107], a[140:143], v[88:91], v[104:107]
v_mfma_f32_16x16x32_bf16 v[100:103], a[140:143], v[84:87], v[100:103]
v_mfma_f32_16x16x32_bf16 v[96:99], a[140:143], v[80:83], v[96:99]
s_waitcnt lgkmcnt(2)
s_nop 6
v_max3_f32 v216, v96, v97, v98
v_max3_f32 v217, v100, v101, v102
v_max3_f32 v218, v104, v105, v106
v_max3_f32 v219, v108, v109, v110
v_max_f32 v216, v216, v99
v_max_f32 v217, v217, v103
v_max_f32 v218, v218, v107
v_max_f32 v219, v219, v111
v_mov_b32 v220, v216
v_mov_b32 v221, v217
v_mov_b32 v222, v218
v_mov_b32 v223, v219
v_permlane16_swap_b32 v216, v220
v_permlane16_swap_b32 v217, v221
v_permlane16_swap_b32 v218, v222
v_permlane16_swap_b32 v219, v223
v_max_f32 v216, v216, v220
v_max_f32 v217, v217, v221
v_max_f32 v218, v218, v222
v_max_f32 v219, v219, v223
v_mov_b32 v220, v216
v_mov_b32 v221, v217
v_mov_b32 v222, v218
v_mov_b32 v223, v219
v_permlane32_swap_b32 v216, v220
v_permlane32_swap_b32 v217, v221
v_permlane32_swap_b32 v218, v222
v_permlane32_swap_b32 v219, v223
v_max_f32 v216, v216, v220
v_max_f32 v217, v217, v221
v_max_f32 v218, v218, v222
v_max_f32 v219, v219, v223
v_sub_f32 v192, 0, v216
v_sub_f32 v193, 0, v216
v_sub_f32 v194, 0, v216
v_sub_f32 v195, 0, v216
v_sub_f32 v196, 0, v217
v_sub_f32 v197, 0, v217
v_sub_f32 v198, 0, v217
v_sub_f32 v199, 0, v217
v_sub_f32 v200, 0, v218
v_sub_f32 v201, 0, v218
v_sub_f32 v202, 0, v218
v_sub_f32 v203, 0, v218
v_sub_f32 v204, 0, v219
v_sub_f32 v205, 0, v219
v_sub_f32 v206, 0, v219
v_sub_f32 v207, 0, v219
v_sub_f32 v96, v96, v216
v_sub_f32 v97, v97, v216
v_sub_f32 v98, v98, v216
v_sub_f32 v99, v99, v216
v_sub_f32 v100, v100, v217
v_sub_f32 v101, v101, v217
v_sub_f32 v102, v102, v217
v_sub_f32 v103, v103, v217
v_sub_f32 v104, v104, v218
v_sub_f32 v105, v105, v218
v_sub_f32 v106, v106, v218
v_sub_f32 v107, v107, v218
v_sub_f32 v108, v108, v219
v_sub_f32 v109, v109, v219
v_sub_f32 v110, v110, v219
v_sub_f32 v111, v111, v219
v_mfma_f32_16x16x32_bf16 v[112:115], a[144:147], v[32:35], v[192:195]
ds_read_b128 a[156:159], v232 offset:7168
v_mfma_f32_16x16x32_bf16 v[116:119], a[144:147], v[36:39], v[196:199]
v_mfma_f32_16x16x32_bf16 v[120:123], a[144:147], v[40:43], v[200:203]
v_exp_f32 v96, v96
v_mfma_f32_16x16x32_bf16 v[124:127], a[144:147], v[44:47], v[204:207]
s_waitcnt lgkmcnt(2)
v_exp_f32 v97, v97
v_mfma_f32_16x16x32_bf16 v[124:127], a[148:151], v[60:63], v[124:127]
ds_read_b128 a[128:131], v232 offset:8192
v_cvt_pk_bf16_f32 v160, v96, v97
v_mfma_f32_16x16x32_bf16 v[120:123], a[148:151], v[56:59], v[120:123]
v_exp_f32 v98, v98
v_mfma_f32_16x16x32_bf16 v[116:119], a[148:151], v[52:55], v[116:119]
v_exp_f32 v99, v99
v_mfma_f32_16x16x32_bf16 v[112:115], a[148:151], v[48:51], v[112:115]
s_waitcnt lgkmcnt(2)
v_exp_f32 v100, v100
v_mfma_f32_16x16x32_bf16 v[112:115], a[152:155], v[64:67], v[112:115]
ds_read_b128 a[132:135], v232 offset:9216
v_cvt_pk_bf16_f32 v161, v98, v99
v_mfma_f32_16x16x32_bf16 v[116:119], a[152:155], v[68:71], v[116:119]
v_exp_f32 v101, v101
v_mfma_f32_16x16x32_bf16 v[120:123], a[152:155], v[72:75], v[120:123]
v_exp_f32 v102, v102
v_mfma_f32_16x16x32_bf16 v[124:127], a[152:155], v[76:79], v[124:127]
s_waitcnt lgkmcnt(2)
v_exp_f32 v103, v103
v_mfma_f32_16x16x32_bf16 v[124:127], a[156:159], v[92:95], v[124:127]
ds_read_b128 a[136:139], v232 offset:10240
v_cvt_pk_bf16_f32 v164, v100, v101
v_mfma_f32_16x16x32_bf16 v[120:123], a[156:159], v[88:91], v[120:123]
s_add_u32 s69, s100, 0
v_exp_f32 v104, v104
v_mfma_f32_16x16x32_bf16 v[116:119], a[156:159], v[84:87], v[116:119]
v_exp_f32 v105, v105
v_mfma_f32_16x16x32_bf16 v[112:115], a[156:159], v[80:83], v[112:115]
s_waitcnt lgkmcnt(2)
v_exp_f32 v106, v106
v_mfma_f32_16x16x32_bf16 v[128:131], a[128:131], v[32:35], v[192:195]
ds_read_b128 a[140:143], v232 offset:11264
v_cvt_pk_bf16_f32 v165, v102, v103
v_mfma_f32_16x16x32_bf16 v[132:135], a[128:131], v[36:39], v[196:199]
v_exp_f32 v107, v107
v_mfma_f32_16x16x32_bf16 v[136:139], a[128:131], v[40:43], v[200:203]
v_exp_f32 v108, v108
v_mfma_f32_16x16x32_bf16 v[140:143], a[128:131], v[44:47], v[204:207]
s_waitcnt lgkmcnt(2)
v_exp_f32 v109, v109
v_mfma_f32_16x16x32_bf16 v[140:143], a[132:135], v[60:63], v[140:143]
ds_read_b128 a[144:147], v232 offset:12288
v_cvt_pk_bf16_f32 v168, v104, v105
v_mfma_f32_16x16x32_bf16 v[136:139], a[132:135], v[56:59], v[136:139]
s_add_u32 s54, s0, 2
s_and_b32 s54, s54, 7
s_mul_i32 s54, s54, 32768
v_mfma_f32_16x16x32_bf16 v[132:135], a[132:135], v[52:55], v[132:135]
s_add_u32 s54, s54, s71
s_add_u32 s54, s54, 16384
s_add_u32 m0, s70, 131072
v_mfma_f32_16x16x32_bf16 v[128:131], a[132:135], v[48:51], v[128:131]
s_waitcnt lgkmcnt(2)
v_exp_f32 v110, v110
v_mfma_f32_16x16x32_bf16 v[128:131], a[136:139], v[64:67], v[128:131]
ds_read_b128 a[148:151], v232 offset:13312
v_cvt_pk_bf16_f32 v169, v106, v107
v_mfma_f32_16x16x32_bf16 v[132:135], a[136:139], v[68:71], v[132:135]
buffer_load_dword v5, v4, s[36:39], s69 offen
v_mfma_f32_16x16x32_bf16 v[136:139], a[136:139], v[72:75], v[136:139]
v_exp_f32 v111, v111
v_mfma_f32_16x16x32_bf16 v[140:143], a[136:139], v[76:79], v[140:143]
s_waitcnt lgkmcnt(2)
v_exp_f32 v112, v112
v_mfma_f32_16x16x32_bf16 v[140:143], a[140:143], v[92:95], v[140:143]
ds_read_b128 a[152:155], v232 offset:14336
v_exp_f32 v113, v113
v_mfma_f32_16x16x32_bf16 v[136:139], a[140:143], v[88:91], v[136:139]
v_exp_f32 v114, v114
v_mfma_f32_16x16x32_bf16 v[132:135], a[140:143], v[84:87], v[132:135]
v_exp_f32 v115, v115
v_mfma_f32_16x16x32_bf16 v[128:131], a[140:143], v[80:83], v[128:131]
s_waitcnt lgkmcnt(2)
v_exp_f32 v116, v116
v_mfma_f32_16x16x32_bf16 v[144:147], a[144:147], v[32:35], v[192:195]
ds_read_b128 a[156:159], v232 offset:15360
v_exp_f32 v117, v117
v_mfma_f32_16x16x32_bf16 v[148:151], a[144:147], v[36:39], v[196:199]
s_add_u32 s69, s100, 2048
v_exp_f32 v118, v118
v_mfma_f32_16x16x32_bf16 v[152:155], a[144:147], v[40:43], v[200:203]
v_exp_f32 v119, v119
v_mfma_f32_16x16x32_bf16 v[156:159], a[144:147], v[44:47], v[204:207]
s_waitcnt lgkmcnt(2)
v_exp_f32 v120, v120
v_mfma_f32_16x16x32_bf16 v[156:159], a[148:151], v[60:63], v[156:159]
v_add_u32 v233, 65536, v2
v_cvt_pk_bf16_f32 v172, v108, v109
v_mfma_f32_16x16x32_bf16 v[152:155], a[148:151], v[56:59], v[152:155]
v_exp_f32 v121, v121
v_mfma_f32_16x16x32_bf16 v[148:151], a[148:151], v[52:55], v[148:151]
v_exp_f32 v122, v122
v_mfma_f32_16x16x32_bf16 v[144:147], a[148:151], v[48:51], v[144:147]
s_waitcnt lgkmcnt(1)
v_exp_f32 v123, v123
v_mfma_f32_16x16x32_bf16 v[144:147], a[152:155], v[64:67], v[144:147]
v_exp_f32 v124, v124
v_mfma_f32_16x16x32_bf16 v[148:151], a[152:155], v[68:71], v[148:151]
v_exp_f32 v125, v125
v_mfma_f32_16x16x32_bf16 v[152:155], a[152:155], v[72:75], v[152:155]
v_exp_f32 v126, v126
v_mfma_f32_16x16x32_bf16 v[156:159], a[152:155], v[76:79], v[156:159]
s_waitcnt lgkmcnt(0)
v_exp_f32 v127, v127
v_mfma_f32_16x16x32_bf16 v[156:159], a[156:159], v[92:95], v[156:159]
v_cvt_pk_bf16_f32 v173, v110, v111
v_add_f32 v208, v208, v96
v_mfma_f32_16x16x32_bf16 v[152:155], a[156:159], v[88:91], v[152:155]
buffer_load_dword v6, v4, s[36:39], s69 offen
v_mfma_f32_16x16x32_bf16 v[148:151], a[156:159], v[84:87], v[148:151]
v_add_f32 v209, v209, v97
v_add_f32 v208, v208, v98
v_mfma_f32_16x16x32_bf16 v[144:147], a[156:159], v[80:83], v[144:147]
v_add_f32 v209, v209, v99
v_add_f32 v210, v210, v100
v_add_f32 v211, v211, v101
v_add_f32 v210, v210, v102
v_add_f32 v211, v211, v103
v_add_f32 v212, v212, v104
v_add_f32 v213, v213, v105
v_add_f32 v212, v212, v106
v_add_f32 v213, v213, v107
v_add_f32 v214, v214, v108
v_add_f32 v215, v215, v109
v_add_f32 v214, v214, v110
v_add_f32 v215, v215, v111
v_cvt_pk_bf16_f32 v162, v112, v113
s_lshl_b32 s97, s40, 2
v_cvt_pk_bf16_f32 v163, v114, v115
v_cvt_pk_bf16_f32 v166, v116, v117
s_add_u32 s97, s97, s43
v_cvt_pk_bf16_f32 v167, v118, v119
v_cvt_pk_bf16_f32 v170, v120, v121
s_lshl_b32 s97, s97, 3
s_add_u32 s97, s97, 8192
v_cvt_pk_bf16_f32 v171, v122, v123
v_cvt_pk_bf16_f32 v174, v124, v125
v_cvt_pk_bf16_f32 v175, v126, v127
v_add_f32 v208, v208, v112
v_add_f32 v209, v209, v113
v_add_f32 v208, v208, v114
s_waitcnt vmcnt(10)
s_barrier
v_add_f32 v209, v209, v115
ds_read_b128 a[128:131], v233 offset:0
ds_read_b128 a[132:135], v233 offset:2048
ds_read_b128 a[136:139], v233 offset:4096
v_add_f32 v210, v210, v116
buffer_load_dwordx4 v2, s[72:75], s54 offen lds
v_add_f32 v211, v211, v117
v_add_f32 v210, v210, v118
v_add_f32 v211, v211, v119
v_add_f32 v212, v212, v120
v_add_f32 v213, v213, v121
v_add_f32 v212, v212, v122
v_add_f32 v213, v213, v123
v_add_f32 v214, v214, v124
v_add_f32 v215, v215, v125
v_add_f32 v214, v214, v126
v_add_f32 v215, v215, v127
v_add_u32 v235, 81920, v2
buffer_load_dwordx4 v2, s[72:75], s54 offen offset:1024 lds
v_mov_b32 v26, s97
v_mov_b32 v28, s44
v_mov_b32 v29, s45
v_exp_f32 v128, v128
v_exp_f32 v129, v129
v_exp_f32 v130, v130
v_exp_f32 v131, v131
v_exp_f32 v132, v132
buffer_load_dwordx4 v2, s[72:75], s54 offen offset:2048 lds
v_exp_f32 v133, v133
s_waitcnt lgkmcnt(2)
v_exp_f32 v134, v134
v_mfma_f32_16x16x32_bf16 a[0:3], a[128:131], v[160:163], 0
ds_read_b128 a[140:143], v233 offset:6144
v_exp_f32 v135, v135
v_mfma_f32_16x16x32_bf16 a[4:7], a[128:131], v[164:167], 0
v_exp_f32 v136, v136
v_mfma_f32_16x16x32_bf16 a[8:11], a[128:131], v[168:171], 0
buffer_load_dwordx4 v2, s[72:75], s54 offen offset:3072 lds
v_mfma_f32_16x16x32_bf16 a[12:15], a[128:131], v[172:175], 0
s_waitcnt lgkmcnt(2)
s_add_u32 s54, s0, 3
s_and_b32 s54, s54, 7
v_mfma_f32_16x16x32_bf16 a[28:31], a[132:135], v[172:175], 0
ds_read_b128 a[144:147], v233 offset:8192
s_mul_i32 s54, s54, 32768
s_add_u32 s54, s54, s71
v_mfma_f32_16x16x32_bf16 a[24:27], a[132:135], v[168:171], 0
s_add_u32 m0, s70, 147456
v_cvt_pk_bf16_f32 v176, v128, v129
v_mfma_f32_16x16x32_bf16 a[20:23], a[132:135], v[164:167], 0
global_store_dwordx2 v26, v[28:29], s[22:23] sc1
v_mfma_f32_16x16x32_bf16 a[16:19], a[132:135], v[160:163], 0
s_waitcnt lgkmcnt(2)
v_exp_f32 v137, v137
v_mfma_f32_16x16x32_bf16 a[32:35], a[136:139], v[160:163], 0
ds_read_b128 a[148:151], v233 offset:10240
v_exp_f32 v138, v138
v_mfma_f32_16x16x32_bf16 a[36:39], a[136:139], v[164:167], 0
buffer_load_dwordx4 v2, s[72:75], s54 offen lds
v_mfma_f32_16x16x32_bf16 a[40:43], a[136:139], v[168:171], 0
v_exp_f32 v139, v139
v_mfma_f32_16x16x32_bf16 a[44:47], a[136:139], v[172:175], 0
s_waitcnt lgkmcnt(2)
v_exp_f32 v140, v140
v_mfma_f32_16x16x32_bf16 a[60:63], a[140:143], v[172:175], 0
ds_read_b128 a[152:155], v233 offset:12288
v_exp_f32 v141, v141
v_mfma_f32_16x16x32_bf16 a[56:59], a[140:143], v[168:171], 0
v_exp_f32 v142, v142
v_mfma_f32_16x16x32_bf16 a[52:55], a[140:143], v[164:167], 0
buffer_load_dwordx4 v2, s[72:75], s54 offen offset:1024 lds
v_mfma_f32_16x16x32_bf16 a[48:51], a[140:143], v[160:163], 0
s_waitcnt lgkmcnt(2)
v_exp_f32 v143, v143
v_mfma_f32_16x16x32_bf16 a[64:67], a[144:147], v[160:163], 0
ds_read_b128 a[156:159], v233 offset:14336
v_exp_f32 v144, v144
v_mfma_f32_16x16x32_bf16 a[68:71], a[144:147], v[164:167], 0
v_exp_f32 v145, v145
v_mfma_f32_16x16x32_bf16 a[72:75], a[144:147], v[168:171], 0
v_exp_f32 v146, v146
v_mfma_f32_16x16x32_bf16 a[76:79], a[144:147], v[172:175], 0
s_waitcnt lgkmcnt(2)
v_exp_f32 v147, v147
v_mfma_f32_16x16x32_bf16 a[92:95], a[148:151], v[172:175], 0
ds_read_b128 a[128:131], v235 offset:0
v_exp_f32 v148, v148
v_mfma_f32_16x16x32_bf16 a[88:91], a[148:151], v[168:171], 0
v_exp_f32 v149, v149
v_mfma_f32_16x16x32_bf16 a[84:87], a[148:151], v[164:167], 0
buffer_load_dwordx4 v2, s[72:75], s54 offen offset:2048 lds
v_mfma_f32_16x16x32_bf16 a[80:83], a[148:151], v[160:163], 0
s_waitcnt lgkmcnt(2)
v_exp_f32 v150, v150
v_mfma_f32_16x16x32_bf16 a[96:99], a[152:155], v[160:163], 0
ds_read_b128 a[132:135], v235 offset:1024
v_exp_f32 v151, v151
v_mfma_f32_16x16x32_bf16 a[100:103], a[152:155], v[164:167], 0
v_cvt_pk_bf16_f32 v177, v130, v131
v_cvt_pk_bf16_f32 v180, v132, v133
v_mfma_f32_16x16x32_bf16 a[104:107], a[152:155], v[168:171], 0
v_cvt_pk_bf16_f32 v181, v134, v135
v_cvt_pk_bf16_f32 v184, v136, v137
v_mfma_f32_16x16x32_bf16 a[108:111], a[152:155], v[172:175], 0
s_waitcnt lgkmcnt(2)
v_cvt_pk_bf16_f32 v185, v138, v139
v_cvt_pk_bf16_f32 v188, v140, v141
v_mfma_f32_16x16x32_bf16 a[124:127], a[156:159], v[172:175], 0
ds_read_b128 a[136:139], v235 offset:2048
v_cvt_pk_bf16_f32 v189, v142, v143
v_add_f32 v208, v208, v128
v_mfma_f32_16x16x32_bf16 a[120:123], a[156:159], v[168:171], 0
buffer_load_dwordx4 v2, s[72:75], s54 offen offset:3072 lds
v_mfma_f32_16x16x32_bf16 a[116:119], a[156:159], v[164:167], 0
v_add_f32 v209, v209, v129
v_add_f32 v208, v208, v130
v_add_f32 v209, v209, v131
v_add_f32 v210, v210, v132
v_add_f32 v211, v211, v133
v_add_f32 v210, v210, v134
v_add_f32 v211, v211, v135
v_add_f32 v212, v212, v136
v_add_f32 v213, v213, v137
v_add_f32 v212, v212, v138
v_add_f32 v213, v213, v139
v_add_f32 v214, v214, v140
v_add_f32 v215, v215, v141
v_add_f32 v214, v214, v142
v_add_f32 v215, v215, v143
v_mfma_f32_16x16x32_bf16 a[112:115], a[156:159], v[160:163], 0
s_waitcnt lgkmcnt(2)
v_cvt_pk_bf16_f32 v178, v144, v145
v_cvt_pk_bf16_f32 v179, v146, v147
v_cvt_pk_bf16_f32 v182, v148, v149
v_cvt_pk_bf16_f32 v183, v150, v151
v_add_f32 v208, v208, v144
v_add_f32 v209, v209, v145
v_add_f32 v208, v208, v146
v_add_f32 v209, v209, v147
v_add_f32 v210, v210, v148
v_add_f32 v211, v211, v149
v_add_f32 v210, v210, v150
v_add_f32 v211, v211, v151
v_mfma_f32_16x16x32_bf16 v[96:99], a[128:131], v[32:35], v[192:195]
ds_read_b128 a[140:143], v235 offset:3072
v_mfma_f32_16x16x32_bf16 v[100:103], a[128:131], v[36:39], v[196:199]
v_exp_f32 v152, v152
v_mfma_f32_16x16x32_bf16 v[104:107], a[128:131], v[40:43], v[200:203]
v_exp_f32 v153, v153
v_mfma_f32_16x16x32_bf16 v[108:111], a[128:131], v[44:47], v[204:207]
s_waitcnt lgkmcnt(2)
v_exp_f32 v154, v154
v_mfma_f32_16x16x32_bf16 v[108:111], a[132:135], v[60:63], v[108:111]
ds_read_b128 a[144:147], v235 offset:4096
v_cvt_pk_bf16_f32 v186, v152, v153
v_mfma_f32_16x16x32_bf16 v[104:107], a[132:135], v[56:59], v[104:107]
v_exp_f32 v155, v155
v_mfma_f32_16x16x32_bf16 v[100:103], a[132:135], v[52:55], v[100:103]
v_exp_f32 v156, v156
v_mfma_f32_16x16x32_bf16 v[96:99], a[132:135], v[48:51], v[96:99]
s_waitcnt lgkmcnt(2)
v_exp_f32 v157, v157
v_mfma_f32_16x16x32_bf16 v[96:99], a[136:139], v[64:67], v[96:99]
ds_read_b128 a[148:151], v235 offset:5120
v_cvt_pk_bf16_f32 v187, v154, v155
v_mfma_f32_16x16x32_bf16 v[100:103], a[136:139], v[68:71], v[100:103]
s_add_u32 s69, s100, 4096
v_exp_f32 v158, v158
v_mfma_f32_16x16x32_bf16 v[104:107], a[136:139], v[72:75], v[104:107]
v_exp_f32 v159, v159
v_mfma_f32_16x16x32_bf16 v[108:111], a[136:139], v[76:79], v[108:111]
s_waitcnt lgkmcnt(2)
v_cvt_pk_bf16_f32 v190, v156, v157
v_cvt_pk_bf16_f32 v191, v158, v159
v_mfma_f32_16x16x32_bf16 v[108:111], a[140:143], v[92:95], v[108:111]
ds_read_b128 a[152:155], v235 offset:6144
v_add_f32 v212, v212, v152
v_mfma_f32_16x16x32_bf16 v[104:107], a[140:143], v[88:91], v[104:107]
v_add_f32 v213, v213, v153
v_add_f32 v212, v212, v154
v_mfma_f32_16x16x32_bf16 v[100:103], a[140:143], v[84:87], v[100:103]
v_add_f32 v213, v213, v155
v_add_f32 v214, v214, v156
v_mfma_f32_16x16x32_bf16 v[96:99], a[140:143], v[80:83], v[96:99]
s_waitcnt lgkmcnt(2)
v_add_f32 v215, v215, v157
v_add_f32 v214, v214, v158
v_mfma_f32_16x16x32_bf16 v[112:115], a[144:147], v[32:35], v[192:195]
ds_read_b128 a[156:159], v235 offset:7168
v_add_f32 v215, v215, v159
v_mfma_f32_16x16x32_bf16 v[116:119], a[144:147], v[36:39], v[196:199]
v_mfma_f32_16x16x32_bf16 v[120:123], a[144:147], v[40:43], v[200:203]
v_exp_f32 v96, v96
v_mfma_f32_16x16x32_bf16 v[124:127], a[144:147], v[44:47], v[204:207]
s_waitcnt lgkmcnt(2)
v_exp_f32 v97, v97
v_mfma_f32_16x16x32_bf16 v[124:127], a[148:151], v[60:63], v[124:127]
ds_read_b128 a[128:131], v235 offset:8192
v_cvt_pk_bf16_f32 v160, v96, v97
v_mfma_f32_16x16x32_bf16 v[120:123], a[148:151], v[56:59], v[120:123]
buffer_load_dword v7, v4, s[36:39], s69 offen
v_mfma_f32_16x16x32_bf16 v[116:119], a[148:151], v[52:55], v[116:119]
v_exp_f32 v98, v98
v_mfma_f32_16x16x32_bf16 v[112:115], a[148:151], v[48:51], v[112:115]
s_waitcnt lgkmcnt(2)
v_exp_f32 v99, v99
v_mfma_f32_16x16x32_bf16 v[112:115], a[152:155], v[64:67], v[112:115]
ds_read_b128 a[132:135], v235 offset:9216
v_cvt_pk_bf16_f32 v161, v98, v99
v_mfma_f32_16x16x32_bf16 v[116:119], a[152:155], v[68:71], v[116:119]
v_exp_f32 v100, v100
v_mfma_f32_16x16x32_bf16 v[120:123], a[152:155], v[72:75], v[120:123]
v_exp_f32 v101, v101
v_mfma_f32_16x16x32_bf16 v[124:127], a[152:155], v[76:79], v[124:127]
s_waitcnt lgkmcnt(2)
v_exp_f32 v102, v102
v_mfma_f32_16x16x32_bf16 v[124:127], a[156:159], v[92:95], v[124:127]
ds_read_b128 a[136:139], v235 offset:10240
v_cvt_pk_bf16_f32 v164, v100, v101
v_mfma_f32_16x16x32_bf16 v[120:123], a[156:159], v[88:91], v[120:123]
v_exp_f32 v103, v103
v_mfma_f32_16x16x32_bf16 v[116:119], a[156:159], v[84:87], v[116:119]
v_exp_f32 v104, v104
v_mfma_f32_16x16x32_bf16 v[112:115], a[156:159], v[80:83], v[112:115]
s_waitcnt lgkmcnt(2)
v_exp_f32 v105, v105
v_mfma_f32_16x16x32_bf16 v[128:131], a[128:131], v[32:35], v[192:195]
ds_read_b128 a[140:143], v235 offset:11264
v_cvt_pk_bf16_f32 v165, v102, v103
v_mfma_f32_16x16x32_bf16 v[132:135], a[128:131], v[36:39], v[196:199]
v_exp_f32 v106, v106
v_mfma_f32_16x16x32_bf16 v[136:139], a[128:131], v[40:43], v[200:203]
v_exp_f32 v107, v107
v_mfma_f32_16x16x32_bf16 v[140:143], a[128:131], v[44:47], v[204:207]
s_waitcnt lgkmcnt(2)
v_exp_f32 v108, v108
v_mfma_f32_16x16x32_bf16 v[140:143], a[132:135], v[60:63], v[140:143]
ds_read_b128 a[144:147], v235 offset:12288
v_cvt_pk_bf16_f32 v168, v104, v105
v_mfma_f32_16x16x32_bf16 v[136:139], a[132:135], v[56:59], v[136:139]
s_add_u32 s54, s0, 3
s_and_b32 s54, s54, 7
s_mul_i32 s54, s54, 32768
v_mfma_f32_16x16x32_bf16 v[132:135], a[132:135], v[52:55], v[132:135]
s_add_u32 s54, s54, s71
s_add_u32 s54, s54, 16384
s_add_u32 m0, s70, 0
v_mfma_f32_16x16x32_bf16 v[128:131], a[132:135], v[48:51], v[128:131]
s_waitcnt lgkmcnt(2)
v_exp_f32 v109, v109
v_mfma_f32_16x16x32_bf16 v[128:131], a[136:139], v[64:67], v[128:131]
ds_read_b128 a[148:151], v235 offset:13312
v_cvt_pk_bf16_f32 v169, v106, v107
v_mfma_f32_16x16x32_bf16 v[132:135], a[136:139], v[68:71], v[132:135]
v_exp_f32 v110, v110
v_mfma_f32_16x16x32_bf16 v[136:139], a[136:139], v[72:75], v[136:139]
v_exp_f32 v111, v111
v_mfma_f32_16x16x32_bf16 v[140:143], a[136:139], v[76:79], v[140:143]
s_waitcnt lgkmcnt(2)
v_exp_f32 v112, v112
v_mfma_f32_16x16x32_bf16 v[140:143], a[140:143], v[92:95], v[140:143]
ds_read_b128 a[152:155], v235 offset:14336
v_exp_f32 v113, v113
v_mfma_f32_16x16x32_bf16 v[136:139], a[140:143], v[88:91], v[136:139]
v_exp_f32 v114, v114
v_mfma_f32_16x16x32_bf16 v[132:135], a[140:143], v[84:87], v[132:135]
v_exp_f32 v115, v115
v_mfma_f32_16x16x32_bf16 v[128:131], a[140:143], v[80:83], v[128:131]
s_waitcnt lgkmcnt(2)
v_exp_f32 v116, v116
v_mfma_f32_16x16x32_bf16 v[144:147], a[144:147], v[32:35], v[192:195]
ds_read_b128 a[156:159], v235 offset:15360
v_exp_f32 v117, v117
v_mfma_f32_16x16x32_bf16 v[148:151], a[144:147], v[36:39], v[196:199]
s_add_u32 s69, s100, 6144
v_exp_f32 v118, v118
v_mfma_f32_16x16x32_bf16 v[152:155], a[144:147], v[40:43], v[200:203]
v_exp_f32 v119, v119
v_mfma_f32_16x16x32_bf16 v[156:159], a[144:147], v[44:47], v[204:207]
s_waitcnt lgkmcnt(2)
v_exp_f32 v120, v120
v_mfma_f32_16x16x32_bf16 v[156:159], a[148:151], v[60:63], v[156:159]
ds_read_b128 a[128:131], v233 offset:1024
v_add_u32 v234, 98304, v2
v_cvt_pk_bf16_f32 v172, v108, v109
v_mfma_f32_16x16x32_bf16 v[152:155], a[148:151], v[56:59], v[152:155]
v_exp_f32 v121, v121
v_mfma_f32_16x16x32_bf16 v[148:151], a[148:151], v[52:55], v[148:151]
v_exp_f32 v122, v122
v_mfma_f32_16x16x32_bf16 v[144:147], a[148:151], v[48:51], v[144:147]
s_waitcnt lgkmcnt(2)
v_exp_f32 v123, v123
v_mfma_f32_16x16x32_bf16 v[144:147], a[152:155], v[64:67], v[144:147]
ds_read_b128 a[132:135], v233 offset:3072
v_exp_f32 v124, v124
v_mfma_f32_16x16x32_bf16 v[148:151], a[152:155], v[68:71], v[148:151]
v_exp_f32 v125, v125
v_mfma_f32_16x16x32_bf16 v[152:155], a[152:155], v[72:75], v[152:155]
v_exp_f32 v126, v126
v_mfma_f32_16x16x32_bf16 v[156:159], a[152:155], v[76:79], v[156:159]
s_waitcnt lgkmcnt(2)
v_exp_f32 v127, v127
v_mfma_f32_16x16x32_bf16 v[156:159], a[156:159], v[92:95], v[156:159]
ds_read_b128 a[136:139], v233 offset:5120
v_cvt_pk_bf16_f32 v173, v110, v111
v_add_f32 v208, v208, v96
v_mfma_f32_16x16x32_bf16 v[152:155], a[156:159], v[88:91], v[152:155]
buffer_load_dword v8, v4, s[36:39], s69 offen
v_mfma_f32_16x16x32_bf16 v[148:151], a[156:159], v[84:87], v[148:151]
v_add_f32 v209, v209, v97
v_add_f32 v208, v208, v98
v_mfma_f32_16x16x32_bf16 v[144:147], a[156:159], v[80:83], v[144:147]
s_waitcnt lgkmcnt(2)
v_add_f32 v209, v209, v99
v_add_f32 v210, v210, v100
v_mfma_f32_16x16x32_bf16 a[0:3], a[128:131], v[176:179], a[0:3]
ds_read_b128 a[140:143], v233 offset:7168
v_add_f32 v211, v211, v101
v_add_f32 v210, v210, v102
v_mfma_f32_16x16x32_bf16 a[4:7], a[128:131], v[180:183], a[4:7]
v_add_f32 v211, v211, v103
v_add_f32 v212, v212, v104
v_mfma_f32_16x16x32_bf16 a[8:11], a[128:131], v[184:187], a[8:11]
v_add_f32 v213, v213, v105
v_add_f32 v212, v212, v106
v_mfma_f32_16x16x32_bf16 a[12:15], a[128:131], v[188:191], a[12:15]
s_waitcnt lgkmcnt(2)
v_add_f32 v213, v213, v107
v_add_f32 v214, v214, v108
v_mfma_f32_16x16x32_bf16 a[28:31], a[132:135], v[188:191], a[28:31]
ds_read_b128 a[144:147], v233 offset:9216
v_add_f32 v215, v215, v109
v_add_f32 v214, v214, v110
v_mfma_f32_16x16x32_bf16 a[24:27], a[132:135], v[184:187], a[24:27]
v_add_f32 v215, v215, v111
v_cvt_pk_bf16_f32 v162, v112, v113
v_mfma_f32_16x16x32_bf16 a[20:23], a[132:135], v[180:183], a[20:23]
v_cvt_pk_bf16_f32 v163, v114, v115
v_cvt_pk_bf16_f32 v166, v116, v117
v_mfma_f32_16x16x32_bf16 a[16:19], a[132:135], v[176:179], a[16:19]
s_waitcnt lgkmcnt(2)
v_cvt_pk_bf16_f32 v167, v118, v119
v_cvt_pk_bf16_f32 v170, v120, v121
v_mfma_f32_16x16x32_bf16 a[32:35], a[136:139], v[176:179], a[32:35]
ds_read_b128 a[148:151], v233 offset:11264
v_cvt_pk_bf16_f32 v171, v122, v123
v_cvt_pk_bf16_f32 v174, v124, v125
v_mfma_f32_16x16x32_bf16 a[36:39], a[136:139], v[180:183], a[36:39]
v_cvt_pk_bf16_f32 v175, v126, v127
v_add_f32 v208, v208, v112
v_mfma_f32_16x16x32_bf16 a[40:43], a[136:139], v[184:187], a[40:43]
v_add_f32 v209, v209, v113
v_add_f32 v208, v208, v114
v_mfma_f32_16x16x32_bf16 a[44:47], a[136:139], v[188:191], a[44:47]
s_waitcnt vmcnt(13)
s_barrier
s_waitcnt lgkmcnt(2)
v_mfma_f32_16x16x32_bf16 a[60:63], a[140:143], v[188:191], a[60:63]
ds_read_b128 a[152:155], v233 offset:13312
v_add_f32 v209, v209, v115
v_add_f32 v210, v210, v116
v_mfma_f32_16x16x32_bf16 a[56:59], a[140:143], v[184:187], a[56:59]
v_add_f32 v211, v211, v117
v_add_f32 v210, v210, v118
v_mfma_f32_16x16x32_bf16 a[52:55], a[140:143], v[180:183], a[52:55]
buffer_load_dwordx4 v2, s[72:75], s54 offen lds
v_mfma_f32_16x16x32_bf16 a[48:51], a[140:143], v[176:179], a[48:51]
s_waitcnt lgkmcnt(2)
v_add_f32 v211, v211, v119
v_add_f32 v212, v212, v120
v_mfma_f32_16x16x32_bf16 a[64:67], a[144:147], v[176:179], a[64:67]
ds_read_b128 a[156:159], v233 offset:15360
v_add_f32 v213, v213, v121
v_add_f32 v212, v212, v122
v_mfma_f32_16x16x32_bf16 a[68:71], a[144:147], v[180:183], a[68:71]
v_add_f32 v213, v213, v123
v_add_f32 v214, v214, v124
v_mfma_f32_16x16x32_bf16 a[72:75], a[144:147], v[184:187], a[72:75]
v_add_f32 v215, v215, v125
v_add_f32 v214, v214, v126
v_mfma_f32_16x16x32_bf16 a[76:79], a[144:147], v[188:191], a[76:79]
s_waitcnt lgkmcnt(2)
v_add_f32 v215, v215, v127
v_add_u32 v232, 114688, v2
v_mfma_f32_16x16x32_bf16 a[92:95], a[148:151], v[188:191], a[92:95]
ds_read_b128 a[128:131], v234 offset:0
v_exp_f32 v128, v128
v_mfma_f32_16x16x32_bf16 a[88:91], a[148:151], v[184:187], a[88:91]
buffer_load_dwordx4 v2, s[72:75], s54 offen offset:1024 lds
v_mfma_f32_16x16x32_bf16 a[84:87], a[148:151], v[180:183], a[84:87]
v_exp_f32 v129, v129
v_mfma_f32_16x16x32_bf16 a[80:83], a[148:151], v[176:179], a[80:83]
s_waitcnt lgkmcnt(2)
v_exp_f32 v130, v130
v_mfma_f32_16x16x32_bf16 a[96:99], a[152:155], v[176:179], a[96:99]
ds_read_b128 a[132:135], v234 offset:2048
v_exp_f32 v131, v131
v_mfma_f32_16x16x32_bf16 a[100:103], a[152:155], v[180:183], a[100:103]
v_exp_f32 v132, v132
v_mfma_f32_16x16x32_bf16 a[104:107], a[152:155], v[184:187], a[104:107]
v_exp_f32 v133, v133
v_mfma_f32_16x16x32_bf16 a[108:111], a[152:155], v[188:191], a[108:111]
s_waitcnt lgkmcnt(2)
v_exp_f32 v134, v134
v_mfma_f32_16x16x32_bf16 a[124:127], a[156:159], v[188:191], a[124:127]
ds_read_b128 a[136:139], v234 offset:4096
v_exp_f32 v135, v135
v_mfma_f32_16x16x32_bf16 a[120:123], a[156:159], v[184:187], a[120:123]
buffer_load_dwordx4 v2, s[72:75], s54 offen offset:2048 lds
v_mfma_f32_16x16x32_bf16 a[116:119], a[156:159], v[180:183], a[116:119]
v_exp_f32 v136, v136
v_mfma_f32_16x16x32_bf16 a[112:115], a[156:159], v[176:179], a[112:115]
s_waitcnt lgkmcnt(2)
v_exp_f32 v137, v137
v_mfma_f32_16x16x32_bf16 a[0:3], a[128:131], v[160:163], a[0:3]
ds_read_b128 a[140:143], v234 offset:6144
v_exp_f32 v138, v138
v_mfma_f32_16x16x32_bf16 a[4:7], a[128:131], v[164:167], a[4:7]
v_exp_f32 v139, v139
v_mfma_f32_16x16x32_bf16 a[8:11], a[128:131], v[168:171], a[8:11]
buffer_load_dwordx4 v2, s[72:75], s54 offen offset:3072 lds
v_mfma_f32_16x16x32_bf16 a[12:15], a[128:131], v[172:175], a[12:15]
s_waitcnt lgkmcnt(2)
s_add_u32 s54, s0, 4
s_and_b32 s54, s54, 7
v_mfma_f32_16x16x32_bf16 a[28:31], a[132:135], v[172:175], a[28:31]
ds_read_b128 a[144:147], v234 offset:8192
s_mul_i32 s54, s54, 32768
s_add_u32 s54, s54, s71
v_mfma_f32_16x16x32_bf16 a[24:27], a[132:135], v[168:171], a[24:27]
s_add_u32 m0, s70, 16384
v_exp_f32 v140, v140
v_mfma_f32_16x16x32_bf16 a[20:23], a[132:135], v[164:167], a[20:23]
v_exp_f32 v141, v141
v_mfma_f32_16x16x32_bf16 a[16:19], a[132:135], v[160:163], a[16:19]
s_waitcnt lgkmcnt(2)
v_exp_f32 v142, v142
v_mfma_f32_16x16x32_bf16 a[32:35], a[136:139], v[160:163], a[32:35]
ds_read_b128 a[148:151], v234 offset:10240
v_exp_f32 v143, v143
v_mfma_f32_16x16x32_bf16 a[36:39], a[136:139], v[164:167], a[36:39]
buffer_load_dwordx4 v2, s[72:75], s54 offen lds
v_mfma_f32_16x16x32_bf16 a[40:43], a[136:139], v[168:171], a[40:43]
v_exp_f32 v144, v144
v_mfma_f32_16x16x32_bf16 a[44:47], a[136:139], v[172:175], a[44:47]
s_waitcnt lgkmcnt(2)
v_exp_f32 v145, v145
v_mfma_f32_16x16x32_bf16 a[60:63], a[140:143], v[172:175], a[60:63]
ds_read_b128 a[152:155], v234 offset:12288
v_exp_f32 v146, v146
v_mfma_f32_16x16x32_bf16 a[56:59], a[140:143], v[168:171], a[56:59]
v_exp_f32 v147, v147
v_mfma_f32_16x16x32_bf16 a[52:55], a[140:143], v[164:167], a[52:55]
buffer_load_dwordx4 v2, s[72:75], s54 offen offset:1024 lds
v_mfma_f32_16x16x32_bf16 a[48:51], a[140:143], v[160:163], a[48:51]
s_waitcnt lgkmcnt(2)
v_exp_f32 v148, v148
v_mfma_f32_16x16x32_bf16 a[64:67], a[144:147], v[160:163], a[64:67]
ds_read_b128 a[156:159], v234 offset:14336
v_exp_f32 v149, v149
v_mfma_f32_16x16x32_bf16 a[68:71], a[144:147], v[164:167], a[68:71]
v_exp_f32 v150, v150
v_mfma_f32_16x16x32_bf16 a[72:75], a[144:147], v[168:171], a[72:75]
v_exp_f32 v151, v151
v_mfma_f32_16x16x32_bf16 a[76:79], a[144:147], v[172:175], a[76:79]
s_waitcnt lgkmcnt(2)
v_cvt_pk_bf16_f32 v176, v128, v129
v_cvt_pk_bf16_f32 v177, v130, v131
v_mfma_f32_16x16x32_bf16 a[92:95], a[148:151], v[172:175], a[92:95]
ds_read_b128 a[128:131], v232 offset:0
v_cvt_pk_bf16_f32 v180, v132, v133
v_cvt_pk_bf16_f32 v181, v134, v135
v_mfma_f32_16x16x32_bf16 a[88:91], a[148:151], v[168:171], a[88:91]
v_cvt_pk_bf16_f32 v184, v136, v137
v_cvt_pk_bf16_f32 v185, v138, v139
v_mfma_f32_16x16x32_bf16 a[84:87], a[148:151], v[164:167], a[84:87]
buffer_load_dwordx4 v2, s[72:75], s54 offen offset:2048 lds
v_mfma_f32_16x16x32_bf16 a[80:83], a[148:151], v[160:163], a[80:83]
s_waitcnt lgkmcnt(2)
v_cvt_pk_bf16_f32 v188, v140, v141
v_cvt_pk_bf16_f32 v189, v142, v143
v_mfma_f32_16x16x32_bf16 a[96:99], a[152:155], v[160:163], a[96:99]
ds_read_b128 a[132:135], v232 offset:1024
v_add_f32 v208, v208, v128
v_add_f32 v209, v209, v129
v_mfma_f32_16x16x32_bf16 a[100:103], a[152:155], v[164:167], a[100:103]
v_add_f32 v208, v208, v130
v_add_f32 v209, v209, v131
v_mfma_f32_16x16x32_bf16 a[104:107], a[152:155], v[168:171], a[104:107]
v_add_f32 v210, v210, v132
v_add_f32 v211, v211, v133
v_mfma_f32_16x16x32_bf16 a[108:111], a[152:155], v[172:175], a[108:111]
s_waitcnt lgkmcnt(2)
v_add_f32 v210, v210, v134
v_add_f32 v211, v211, v135
v_mfma_f32_16x16x32_bf16 a[124:127], a[156:159], v[172:175], a[124:127]
ds_read_b128 a[136:139], v232 offset:2048
v_add_f32 v212, v212, v136
v_add_f32 v213, v213, v137
v_mfma_f32_16x16x32_bf16 a[120:123], a[156:159], v[168:171], a[120:123]
buffer_load_dwordx4 v2, s[72:75], s54 offen offset:3072 lds
v_mfma_f32_16x16x32_bf16 a[116:119], a[156:159], v[164:167], a[116:119]
v_add_f32 v212, v212, v138
v_add_f32 v213, v213, v139
v_add_f32 v214, v214, v140
v_add_f32 v215, v215, v141
v_add_f32 v214, v214, v142
v_add_f32 v215, v215, v143
v_mfma_f32_16x16x32_bf16 a[112:115], a[156:159], v[160:163], a[112:115]
s_waitcnt lgkmcnt(2)
v_cvt_pk_bf16_f32 v178, v144, v145
v_cvt_pk_bf16_f32 v179, v146, v147
v_cvt_pk_bf16_f32 v182, v148, v149
v_cvt_pk_bf16_f32 v183, v150, v151
v_add_f32 v208, v208, v144
v_add_f32 v209, v209, v145
v_add_f32 v208, v208, v146
v_add_f32 v209, v209, v147
v_add_f32 v210, v210, v148
v_add_f32 v211, v211, v149
v_add_f32 v210, v210, v150
v_add_f32 v211, v211, v151
v_mfma_f32_16x16x32_bf16 v[96:99], a[128:131], v[32:35], v[192:195]
ds_read_b128 a[140:143], v232 offset:3072
v_mfma_f32_16x16x32_bf16 v[100:103], a[128:131], v[36:39], v[196:199]
v_exp_f32 v152, v152
v_mfma_f32_16x16x32_bf16 v[104:107], a[128:131], v[40:43], v[200:203]
s_mov_b32 s36, s12
s_mov_b32 s37, s13
s_and_b32 s37, s37, 0xffff
v_mfma_f32_16x16x32_bf16 v[108:111], a[128:131], v[44:47], v[204:207]
s_waitcnt lgkmcnt(2)
s_mov_b32 s38, 8388608
s_mov_b32 s39, 0x20000
v_mfma_f32_16x16x32_bf16 v[108:111], a[132:135], v[60:63], v[108:111]
ds_read_b128 a[144:147], v232 offset:4096
v_add_f32 v212, v212, v152
v_mfma_f32_16x16x32_bf16 v[104:107], a[132:135], v[56:59], v[104:107]
v_exp_f32 v153, v153
v_mfma_f32_16x16x32_bf16 v[100:103], a[132:135], v[52:55], v[100:103]
v_exp_f32 v154, v154
v_mfma_f32_16x16x32_bf16 v[96:99], a[132:135], v[48:51], v[96:99]
s_waitcnt lgkmcnt(2)
v_exp_f32 v155, v155
v_mfma_f32_16x16x32_bf16 v[96:99], a[136:139], v[64:67], v[96:99]
ds_read_b128 a[148:151], v232 offset:5120
v_cvt_pk_bf16_f32 v186, v152, v153
v_mfma_f32_16x16x32_bf16 v[100:103], a[136:139], v[68:71], v[100:103]
v_exp_f32 v156, v156
v_mfma_f32_16x16x32_bf16 v[104:107], a[136:139], v[72:75], v[104:107]
v_exp_f32 v157, v157
v_mfma_f32_16x16x32_bf16 v[108:111], a[136:139], v[76:79], v[108:111]
s_waitcnt lgkmcnt(2)
v_exp_f32 v158, v158
v_mfma_f32_16x16x32_bf16 v[108:111], a[140:143], v[92:95], v[108:111]
ds_read_b128 a[152:155], v232 offset:6144
v_cvt_pk_bf16_f32 v187, v154, v155
v_mfma_f32_16x16x32_bf16 v[104:107], a[140:143], v[88:91], v[104:107]
v_exp_f32 v159, v159
v_mfma_f32_16x16x32_bf16 v[100:103], a[140:143], v[84:87], v[100:103]
v_cvt_pk_bf16_f32 v190, v156, v157
v_cvt_pk_bf16_f32 v191, v158, v159
v_mfma_f32_16x16x32_bf16 v[96:99], a[140:143], v[80:83], v[96:99]
s_waitcnt lgkmcnt(2)
v_add_f32 v213, v213, v153
v_add_f32 v212, v212, v154
v_mfma_f32_16x16x32_bf16 v[112:115], a[144:147], v[32:35], v[192:195]
ds_read_b128 a[156:159], v232 offset:7168
v_add_f32 v213, v213, v155
v_mfma_f32_16x16x32_bf16 v[116:119], a[144:147], v[36:39], v[196:199]
v_add_f32 v214, v214, v156
v_add_f32 v215, v215, v157
v_mfma_f32_16x16x32_bf16 v[120:123], a[144:147], v[40:43], v[200:203]
v_add_f32 v214, v214, v158
v_add_f32 v215, v215, v159
v_mfma_f32_16x16x32_bf16 v[124:127], a[144:147], v[44:47], v[204:207]
s_waitcnt lgkmcnt(2)
v_exp_f32 v96, v96
v_mfma_f32_16x16x32_bf16 v[124:127], a[148:151], v[60:63], v[124:127]
ds_read_b128 a[128:131], v232 offset:8192
v_add_f32 v208, v208, v96
v_mfma_f32_16x16x32_bf16 v[120:123], a[148:151], v[56:59], v[120:123]
v_exp_f32 v97, v97
v_mfma_f32_16x16x32_bf16 v[116:119], a[148:151], v[52:55], v[116:119]
v_exp_f32 v98, v98
v_mfma_f32_16x16x32_bf16 v[112:115], a[148:151], v[48:51], v[112:115]
s_waitcnt lgkmcnt(2)
v_exp_f32 v99, v99
v_mfma_f32_16x16x32_bf16 v[112:115], a[152:155], v[64:67], v[112:115]
ds_read_b128 a[132:135], v232 offset:9216
v_cvt_pk_bf16_f32 v160, v96, v97
v_mfma_f32_16x16x32_bf16 v[116:119], a[152:155], v[68:71], v[116:119]
v_exp_f32 v100, v100
v_mfma_f32_16x16x32_bf16 v[120:123], a[152:155], v[72:75], v[120:123]
v_exp_f32 v101, v101
v_mfma_f32_16x16x32_bf16 v[124:127], a[152:155], v[76:79], v[124:127]
s_waitcnt lgkmcnt(2)
v_exp_f32 v102, v102
v_mfma_f32_16x16x32_bf16 v[124:127], a[156:159], v[92:95], v[124:127]
ds_read_b128 a[136:139], v232 offset:10240
v_cvt_pk_bf16_f32 v161, v98, v99
v_mfma_f32_16x16x32_bf16 v[120:123], a[156:159], v[88:91], v[120:123]
s_add_u32 s69, s100, 0
v_exp_f32 v103, v103
v_mfma_f32_16x16x32_bf16 v[116:119], a[156:159], v[84:87], v[116:119]
v_exp_f32 v104, v104
v_mfma_f32_16x16x32_bf16 v[112:115], a[156:159], v[80:83], v[112:115]
s_waitcnt lgkmcnt(2)
v_exp_f32 v105, v105
v_mfma_f32_16x16x32_bf16 v[128:131], a[128:131], v[32:35], v[192:195]
ds_read_b128 a[140:143], v232 offset:11264
v_cvt_pk_bf16_f32 v164, v100, v101
v_mfma_f32_16x16x32_bf16 v[132:135], a[128:131], v[36:39], v[196:199]
v_exp_f32 v106, v106
v_mfma_f32_16x16x32_bf16 v[136:139], a[128:131], v[40:43], v[200:203]
v_exp_f32 v107, v107
v_mfma_f32_16x16x32_bf16 v[140:143], a[128:131], v[44:47], v[204:207]
s_waitcnt lgkmcnt(2)
v_exp_f32 v108, v108
v_mfma_f32_16x16x32_bf16 v[140:143], a[132:135], v[60:63], v[140:143]
ds_read_b128 a[144:147], v232 offset:12288
v_cvt_pk_bf16_f32 v165, v102, v103
v_mfma_f32_16x16x32_bf16 v[136:139], a[132:135], v[56:59], v[136:139]
s_add_u32 s54, s0, 4
s_and_b32 s54, s54, 7
s_mul_i32 s54, s54, 32768
v_mfma_f32_16x16x32_bf16 v[132:135], a[132:135], v[52:55], v[132:135]
s_add_u32 s54, s54, s71
s_add_u32 s54, s54, 16384
s_add_u32 m0, s70, 32768
v_mfma_f32_16x16x32_bf16 v[128:131], a[132:135], v[48:51], v[128:131]
s_waitcnt lgkmcnt(2)
v_exp_f32 v109, v109
v_mfma_f32_16x16x32_bf16 v[128:131], a[136:139], v[64:67], v[128:131]
ds_read_b128 a[148:151], v232 offset:13312
v_cvt_pk_bf16_f32 v168, v104, v105
v_mfma_f32_16x16x32_bf16 v[132:135], a[136:139], v[68:71], v[132:135]
buffer_load_dword v9, v4, s[36:39], s69 offen
v_mfma_f32_16x16x32_bf16 v[136:139], a[136:139], v[72:75], v[136:139]
v_exp_f32 v110, v110
v_mfma_f32_16x16x32_bf16 v[140:143], a[136:139], v[76:79], v[140:143]
s_waitcnt lgkmcnt(2)
v_exp_f32 v111, v111
v_mfma_f32_16x16x32_bf16 v[140:143], a[140:143], v[92:95], v[140:143]
ds_read_b128 a[152:155], v232 offset:14336
v_exp_f32 v112, v112
v_mfma_f32_16x16x32_bf16 v[136:139], a[140:143], v[88:91], v[136:139]
v_exp_f32 v113, v113
v_mfma_f32_16x16x32_bf16 v[132:135], a[140:143], v[84:87], v[132:135]
v_exp_f32 v114, v114
v_mfma_f32_16x16x32_bf16 v[128:131], a[140:143], v[80:83], v[128:131]
s_waitcnt lgkmcnt(2)
v_exp_f32 v115, v115
v_mfma_f32_16x16x32_bf16 v[144:147], a[144:147], v[32:35], v[192:195]
ds_read_b128 a[156:159], v232 offset:15360
v_exp_f32 v116, v116
v_mfma_f32_16x16x32_bf16 v[148:151], a[144:147], v[36:39], v[196:199]
s_add_u32 s69, s100, 2048
v_exp_f32 v117, v117
v_mfma_f32_16x16x32_bf16 v[152:155], a[144:147], v[40:43], v[200:203]
v_exp_f32 v118, v118
v_mfma_f32_16x16x32_bf16 v[156:159], a[144:147], v[44:47], v[204:207]
s_waitcnt lgkmcnt(2)
v_exp_f32 v119, v119
v_mfma_f32_16x16x32_bf16 v[156:159], a[148:151], v[60:63], v[156:159]
ds_read_b128 a[128:131], v234 offset:1024
v_add_u32 v233, 131072, v2
v_cvt_pk_bf16_f32 v169, v106, v107
v_mfma_f32_16x16x32_bf16 v[152:155], a[148:151], v[56:59], v[152:155]
v_exp_f32 v120, v120
v_mfma_f32_16x16x32_bf16 v[148:151], a[148:151], v[52:55], v[148:151]
v_exp_f32 v121, v121
v_mfma_f32_16x16x32_bf16 v[144:147], a[148:151], v[48:51], v[144:147]
s_waitcnt lgkmcnt(2)
v_exp_f32 v122, v122
v_mfma_f32_16x16x32_bf16 v[144:147], a[152:155], v[64:67], v[144:147]
ds_read_b128 a[132:135], v234 offset:3072
v_exp_f32 v123, v123
v_mfma_f32_16x16x32_bf16 v[148:151], a[152:155], v[68:71], v[148:151]
v_exp_f32 v124, v124
v_mfma_f32_16x16x32_bf16 v[152:155], a[152:155], v[72:75], v[152:155]
v_exp_f32 v125, v125
v_mfma_f32_16x16x32_bf16 v[156:159], a[152:155], v[76:79], v[156:159]
s_waitcnt lgkmcnt(2)
v_exp_f32 v126, v126
v_mfma_f32_16x16x32_bf16 v[156:159], a[156:159], v[92:95], v[156:159]
ds_read_b128 a[136:139], v234 offset:5120
v_exp_f32 v127, v127
v_mfma_f32_16x16x32_bf16 v[152:155], a[156:159], v[88:91], v[152:155]
buffer_load_dword v10, v4, s[36:39], s69 offen
v_mfma_f32_16x16x32_bf16 v[148:151], a[156:159], v[84:87], v[148:151]
v_cvt_pk_bf16_f32 v172, v108, v109
v_cvt_pk_bf16_f32 v173, v110, v111
v_mfma_f32_16x16x32_bf16 v[144:147], a[156:159], v[80:83], v[144:147]
s_waitcnt lgkmcnt(2)
v_add_f32 v209, v209, v97
v_add_f32 v208, v208, v98
v_mfma_f32_16x16x32_bf16 a[0:3], a[128:131], v[176:179], a[0:3]
ds_read_b128 a[140:143], v234 offset:7168
v_add_f32 v209, v209, v99
v_add_f32 v210, v210, v100
v_mfma_f32_16x16x32_bf16 a[4:7], a[128:131], v[180:183], a[4:7]
v_add_f32 v211, v211, v101
v_add_f32 v210, v210, v102
v_mfma_f32_16x16x32_bf16 a[8:11], a[128:131], v[184:187], a[8:11]
v_add_f32 v211, v211, v103
v_add_f32 v212, v212, v104
v_mfma_f32_16x16x32_bf16 a[12:15], a[128:131], v[188:191], a[12:15]
s_waitcnt lgkmcnt(2)
v_add_f32 v213, v213, v105
v_add_f32 v212, v212, v106
v_mfma_f32_16x16x32_bf16 a[28:31], a[132:135], v[188:191], a[28:31]
ds_read_b128 a[144:147], v234 offset:9216
v_add_f32 v213, v213, v107
v_add_f32 v214, v214, v108
v_mfma_f32_16x16x32_bf16 a[24:27], a[132:135], v[184:187], a[24:27]
v_add_f32 v215, v215, v109
v_add_f32 v214, v214, v110
v_mfma_f32_16x16x32_bf16 a[20:23], a[132:135], v[180:183], a[20:23]
v_add_f32 v215, v215, v111
v_cvt_pk_bf16_f32 v162, v112, v113
v_mfma_f32_16x16x32_bf16 a[16:19], a[132:135], v[176:179], a[16:19]
s_waitcnt lgkmcnt(2)
v_cvt_pk_bf16_f32 v163, v114, v115
v_cvt_pk_bf16_f32 v166, v116, v117
v_mfma_f32_16x16x32_bf16 a[32:35], a[136:139], v[176:179], a[32:35]
ds_read_b128 a[148:151], v234 offset:11264
v_cvt_pk_bf16_f32 v167, v118, v119
v_cvt_pk_bf16_f32 v170, v120, v121
v_mfma_f32_16x16x32_bf16 a[36:39], a[136:139], v[180:183], a[36:39]
v_cvt_pk_bf16_f32 v171, v122, v123
v_cvt_pk_bf16_f32 v174, v124, v125
v_mfma_f32_16x16x32_bf16 a[40:43], a[136:139], v[184:187], a[40:43]
v_cvt_pk_bf16_f32 v175, v126, v127
v_add_f32 v208, v208, v112
v_mfma_f32_16x16x32_bf16 a[44:47], a[136:139], v[188:191], a[44:47]
s_waitcnt vmcnt(12)
s_barrier
s_waitcnt lgkmcnt(2)
v_mfma_f32_16x16x32_bf16 a[60:63], a[140:143], v[188:191], a[60:63]
ds_read_b128 a[152:155], v234 offset:13312
v_add_f32 v209, v209, v113
v_add_f32 v208, v208, v114
v_mfma_f32_16x16x32_bf16 a[56:59], a[140:143], v[184:187], a[56:59]
v_add_f32 v209, v209, v115
v_add_f32 v210, v210, v116
v_mfma_f32_16x16x32_bf16 a[52:55], a[140:143], v[180:183], a[52:55]
buffer_load_dwordx4 v2, s[72:75], s54 offen lds
v_mfma_f32_16x16x32_bf16 a[48:51], a[140:143], v[176:179], a[48:51]
s_waitcnt lgkmcnt(2)
v_add_f32 v211, v211, v117
v_add_f32 v210, v210, v118
v_mfma_f32_16x16x32_bf16 a[64:67], a[144:147], v[176:179], a[64:67]
ds_read_b128 a[156:159], v234 offset:15360
v_add_f32 v211, v211, v119
v_add_f32 v212, v212, v120
v_mfma_f32_16x16x32_bf16 a[68:71], a[144:147], v[180:183], a[68:71]
v_add_f32 v213, v213, v121
v_add_f32 v212, v212, v122
v_mfma_f32_16x16x32_bf16 a[72:75], a[144:147], v[184:187], a[72:75]
v_add_f32 v213, v213, v123
v_add_f32 v214, v214, v124
v_mfma_f32_16x16x32_bf16 a[76:79], a[144:147], v[188:191], a[76:79]
s_waitcnt lgkmcnt(2)
v_add_f32 v215, v215, v125
v_add_f32 v214, v214, v126
v_mfma_f32_16x16x32_bf16 a[92:95], a[148:151], v[188:191], a[92:95]
ds_read_b128 a[128:131], v233 offset:0
v_add_f32 v215, v215, v127
v_add_u32 v235, 147456, v2
v_mfma_f32_16x16x32_bf16 a[88:91], a[148:151], v[184:187], a[88:91]
buffer_load_dwordx4 v2, s[72:75], s54 offen offset:1024 lds
v_mfma_f32_16x16x32_bf16 a[84:87], a[148:151], v[180:183], a[84:87]
v_exp_f32 v128, v128
v_mfma_f32_16x16x32_bf16 a[80:83], a[148:151], v[176:179], a[80:83]
s_waitcnt lgkmcnt(2)
v_exp_f32 v129, v129
v_mfma_f32_16x16x32_bf16 a[96:99], a[152:155], v[176:179], a[96:99]
ds_read_b128 a[132:135], v233 offset:2048
v_exp_f32 v130, v130
v_mfma_f32_16x16x32_bf16 a[100:103], a[152:155], v[180:183], a[100:103]
v_exp_f32 v131, v131
v_mfma_f32_16x16x32_bf16 a[104:107], a[152:155], v[184:187], a[104:107]
v_exp_f32 v132, v132
v_mfma_f32_16x16x32_bf16 a[108:111], a[152:155], v[188:191], a[108:111]
s_waitcnt lgkmcnt(2)
v_exp_f32 v133, v133
v_mfma_f32_16x16x32_bf16 a[124:127], a[156:159], v[188:191], a[124:127]
ds_read_b128 a[136:139], v233 offset:4096
v_exp_f32 v134, v134
v_mfma_f32_16x16x32_bf16 a[120:123], a[156:159], v[184:187], a[120:123]
buffer_load_dwordx4 v2, s[72:75], s54 offen offset:2048 lds
v_mfma_f32_16x16x32_bf16 a[116:119], a[156:159], v[180:183], a[116:119]
v_exp_f32 v135, v135
v_mfma_f32_16x16x32_bf16 a[112:115], a[156:159], v[176:179], a[112:115]
s_waitcnt lgkmcnt(2)
v_exp_f32 v136, v136
v_mfma_f32_16x16x32_bf16 a[0:3], a[128:131], v[160:163], a[0:3]
ds_read_b128 a[140:143], v233 offset:6144
v_exp_f32 v137, v137
v_mfma_f32_16x16x32_bf16 a[4:7], a[128:131], v[164:167], a[4:7]
v_exp_f32 v138, v138
v_mfma_f32_16x16x32_bf16 a[8:11], a[128:131], v[168:171], a[8:11]
buffer_load_dwordx4 v2, s[72:75], s54 offen offset:3072 lds
v_mfma_f32_16x16x32_bf16 a[12:15], a[128:131], v[172:175], a[12:15]
s_waitcnt lgkmcnt(2)
s_add_u32 s54, s0, 5
s_and_b32 s54, s54, 7
v_mfma_f32_16x16x32_bf16 a[28:31], a[132:135], v[172:175], a[28:31]
ds_read_b128 a[144:147], v233 offset:8192
s_mul_i32 s54, s54, 32768
s_add_u32 s54, s54, s71
v_mfma_f32_16x16x32_bf16 a[24:27], a[132:135], v[168:171], a[24:27]
s_add_u32 m0, s70, 49152
v_exp_f32 v139, v139
v_mfma_f32_16x16x32_bf16 a[20:23], a[132:135], v[164:167], a[20:23]
v_exp_f32 v140, v140
v_mfma_f32_16x16x32_bf16 a[16:19], a[132:135], v[160:163], a[16:19]
s_waitcnt lgkmcnt(2)
v_exp_f32 v141, v141
v_mfma_f32_16x16x32_bf16 a[32:35], a[136:139], v[160:163], a[32:35]
ds_read_b128 a[148:151], v233 offset:10240
v_exp_f32 v142, v142
v_mfma_f32_16x16x32_bf16 a[36:39], a[136:139], v[164:167], a[36:39]
buffer_load_dwordx4 v2, s[72:75], s54 offen lds
v_mfma_f32_16x16x32_bf16 a[40:43], a[136:139], v[168:171], a[40:43]
v_exp_f32 v143, v143
v_mfma_f32_16x16x32_bf16 a[44:47], a[136:139], v[172:175], a[44:47]
s_waitcnt lgkmcnt(2)
v_exp_f32 v144, v144
v_mfma_f32_16x16x32_bf16 a[60:63], a[140:143], v[172:175], a[60:63]
ds_read_b128 a[152:155], v233 offset:12288
v_exp_f32 v145, v145
v_mfma_f32_16x16x32_bf16 a[56:59], a[140:143], v[168:171], a[56:59]
v_exp_f32 v146, v146
v_mfma_f32_16x16x32_bf16 a[52:55], a[140:143], v[164:167], a[52:55]
buffer_load_dwordx4 v2, s[72:75], s54 offen offset:1024 lds
v_mfma_f32_16x16x32_bf16 a[48:51], a[140:143], v[160:163], a[48:51]
s_waitcnt lgkmcnt(2)
v_exp_f32 v147, v147
v_mfma_f32_16x16x32_bf16 a[64:67], a[144:147], v[160:163], a[64:67]
ds_read_b128 a[156:159], v233 offset:14336
v_exp_f32 v148, v148
v_mfma_f32_16x16x32_bf16 a[68:71], a[144:147], v[164:167], a[68:71]
v_exp_f32 v149, v149
v_mfma_f32_16x16x32_bf16 a[72:75], a[144:147], v[168:171], a[72:75]
v_exp_f32 v150, v150
v_mfma_f32_16x16x32_bf16 a[76:79], a[144:147], v[172:175], a[76:79]
s_waitcnt lgkmcnt(2)
v_exp_f32 v151, v151
v_mfma_f32_16x16x32_bf16 a[92:95], a[148:151], v[172:175], a[92:95]
ds_read_b128 a[128:131], v235 offset:0
v_cvt_pk_bf16_f32 v176, v128, v129
v_cvt_pk_bf16_f32 v177, v130, v131
v_mfma_f32_16x16x32_bf16 a[88:91], a[148:151], v[168:171], a[88:91]
v_cvt_pk_bf16_f32 v180, v132, v133
v_cvt_pk_bf16_f32 v181, v134, v135
v_mfma_f32_16x16x32_bf16 a[84:87], a[148:151], v[164:167], a[84:87]
buffer_load_dwordx4 v2, s[72:75], s54 offen offset:2048 lds
v_mfma_f32_16x16x32_bf16 a[80:83], a[148:151], v[160:163], a[80:83]
s_waitcnt lgkmcnt(2)
v_cvt_pk_bf16_f32 v184, v136, v137
v_cvt_pk_bf16_f32 v185, v138, v139
v_mfma_f32_16x16x32_bf16 a[96:99], a[152:155], v[160:163], a[96:99]
ds_read_b128 a[132:135], v235 offset:1024
v_cvt_pk_bf16_f32 v188, v140, v141
v_cvt_pk_bf16_f32 v189, v142, v143
v_mfma_f32_16x16x32_bf16 a[100:103], a[152:155], v[164:167], a[100:103]
v_add_f32 v208, v208, v128
v_add_f32 v209, v209, v129
v_mfma_f32_16x16x32_bf16 a[104:107], a[152:155], v[168:171], a[104:107]
v_add_f32 v208, v208, v130
v_add_f32 v209, v209, v131
v_mfma_f32_16x16x32_bf16 a[108:111], a[152:155], v[172:175], a[108:111]
s_waitcnt lgkmcnt(2)
v_add_f32 v210, v210, v132
v_add_f32 v211, v211, v133
v_mfma_f32_16x16x32_bf16 a[124:127], a[156:159], v[172:175], a[124:127]
ds_read_b128 a[136:139], v235 offset:2048
v_add_f32 v210, v210, v134
v_add_f32 v211, v211, v135
v_mfma_f32_16x16x32_bf16 a[120:123], a[156:159], v[168:171], a[120:123]
buffer_load_dwordx4 v2, s[72:75], s54 offen offset:3072 lds
v_mfma_f32_16x16x32_bf16 a[116:119], a[156:159], v[164:167], a[116:119]
v_add_f32 v212, v212, v136
v_add_f32 v213, v213, v137
v_add_f32 v212, v212, v138
v_add_f32 v213, v213, v139
v_add_f32 v214, v214, v140
v_add_f32 v215, v215, v141
v_add_f32 v214, v214, v142
v_add_f32 v215, v215, v143
v_mfma_f32_16x16x32_bf16 a[112:115], a[156:159], v[160:163], a[112:115]
s_waitcnt lgkmcnt(2)
v_cvt_pk_bf16_f32 v178, v144, v145
v_cvt_pk_bf16_f32 v179, v146, v147
v_cvt_pk_bf16_f32 v182, v148, v149
v_cvt_pk_bf16_f32 v183, v150, v151
v_add_f32 v208, v208, v144
v_add_f32 v209, v209, v145
v_add_f32 v208, v208, v146
v_add_f32 v209, v209, v147
v_add_f32 v210, v210, v148
v_add_f32 v211, v211, v149
v_add_f32 v210, v210, v150
v_add_f32 v211, v211, v151
v_mfma_f32_16x16x32_bf16 v[96:99], a[128:131], v[32:35], v[192:195]
ds_read_b128 a[140:143], v235 offset:3072
v_mfma_f32_16x16x32_bf16 v[100:103], a[128:131], v[36:39], v[196:199]
v_exp_f32 v152, v152
v_mfma_f32_16x16x32_bf16 v[104:107], a[128:131], v[40:43], v[200:203]
v_exp_f32 v153, v153
v_mfma_f32_16x16x32_bf16 v[108:111], a[128:131], v[44:47], v[204:207]
s_waitcnt lgkmcnt(2)
v_exp_f32 v154, v154
v_mfma_f32_16x16x32_bf16 v[108:111], a[132:135], v[60:63], v[108:111]
ds_read_b128 a[144:147], v235 offset:4096
v_cvt_pk_bf16_f32 v186, v152, v153
v_mfma_f32_16x16x32_bf16 v[104:107], a[132:135], v[56:59], v[104:107]
v_exp_f32 v155, v155
v_mfma_f32_16x16x32_bf16 v[100:103], a[132:135], v[52:55], v[100:103]
v_exp_f32 v156, v156
v_mfma_f32_16x16x32_bf16 v[96:99], a[132:135], v[48:51], v[96:99]
s_waitcnt lgkmcnt(2)
v_exp_f32 v157, v157
v_mfma_f32_16x16x32_bf16 v[96:99], a[136:139], v[64:67], v[96:99]
ds_read_b128 a[148:151], v235 offset:5120
v_cvt_pk_bf16_f32 v187, v154, v155
v_mfma_f32_16x16x32_bf16 v[100:103], a[136:139], v[68:71], v[100:103]
s_add_u32 s69, s100, 4096
v_exp_f32 v158, v158
v_mfma_f32_16x16x32_bf16 v[104:107], a[136:139], v[72:75], v[104:107]
s_lshl_b32 s97, s41, 5
v_exp_f32 v159, v159
v_mfma_f32_16x16x32_bf16 v[108:111], a[136:139], v[76:79], v[108:111]
s_waitcnt lgkmcnt(2)
v_cvt_pk_bf16_f32 v190, v156, v157
v_cvt_pk_bf16_f32 v191, v158, v159
v_mfma_f32_16x16x32_bf16 v[108:111], a[140:143], v[92:95], v[108:111]
ds_read_b128 a[152:155], v235 offset:6144
v_add_f32 v212, v212, v152
v_mfma_f32_16x16x32_bf16 v[104:107], a[140:143], v[88:91], v[104:107]
s_lshl_b32 s56, s59, 3
v_add_f32 v213, v213, v153
v_add_f32 v212, v212, v154
v_mfma_f32_16x16x32_bf16 v[100:103], a[140:143], v[84:87], v[100:103]
s_add_u32 s97, s97, s56
v_add_f32 v213, v213, v155
v_add_f32 v214, v214, v156
v_mfma_f32_16x16x32_bf16 v[96:99], a[140:143], v[80:83], v[96:99]
s_waitcnt lgkmcnt(2)
v_add_f32 v215, v215, v157
v_add_f32 v214, v214, v158
v_mfma_f32_16x16x32_bf16 v[112:115], a[144:147], v[32:35], v[192:195]
ds_read_b128 a[156:159], v235 offset:7168
v_add_f32 v215, v215, v159
v_mfma_f32_16x16x32_bf16 v[116:119], a[144:147], v[36:39], v[196:199]
s_lshl_b32 s97, s97, 2
s_add_u32 s97, s97, s43
s_lshl_b32 s97, s97, 3
v_mfma_f32_16x16x32_bf16 v[120:123], a[144:147], v[40:43], v[200:203]
s_add_u32 s97, s97, 8192
v_and_b32 v26, 7, v1
v_lshlrev_b32 v26, 5, v26
v_mfma_f32_16x16x32_bf16 v[124:127], a[144:147], v[44:47], v[204:207]
s_waitcnt lgkmcnt(2)
v_add_u32 v26, s97, v26
v_mfma_f32_16x16x32_bf16 v[124:127], a[148:151], v[60:63], v[124:127]
ds_read_b128 a[128:131], v235 offset:8192
v_mfma_f32_16x16x32_bf16 v[120:123], a[148:151], v[56:59], v[120:123]
buffer_load_dword v11, v4, s[36:39], s69 offen
v_mfma_f32_16x16x32_bf16 v[116:119], a[148:151], v[52:55], v[116:119]
v_exp_f32 v96, v96
v_mfma_f32_16x16x32_bf16 v[112:115], a[148:151], v[48:51], v[112:115]
s_waitcnt lgkmcnt(2)
v_exp_f32 v97, v97
v_mfma_f32_16x16x32_bf16 v[112:115], a[152:155], v[64:67], v[112:115]
ds_read_b128 a[132:135], v235 offset:9216
v_cvt_pk_bf16_f32 v160, v96, v97
v_mfma_f32_16x16x32_bf16 v[116:119], a[152:155], v[68:71], v[116:119]
v_exp_f32 v98, v98
v_mfma_f32_16x16x32_bf16 v[120:123], a[152:155], v[72:75], v[120:123]
v_exp_f32 v99, v99
v_mfma_f32_16x16x32_bf16 v[124:127], a[152:155], v[76:79], v[124:127]
s_waitcnt lgkmcnt(2)
v_exp_f32 v100, v100
v_mfma_f32_16x16x32_bf16 v[124:127], a[156:159], v[92:95], v[124:127]
ds_read_b128 a[136:139], v235 offset:10240
v_cvt_pk_bf16_f32 v161, v98, v99
v_mfma_f32_16x16x32_bf16 v[120:123], a[156:159], v[88:91], v[120:123]
v_exp_f32 v101, v101
v_mfma_f32_16x16x32_bf16 v[116:119], a[156:159], v[84:87], v[116:119]
v_exp_f32 v102, v102
v_mfma_f32_16x16x32_bf16 v[112:115], a[156:159], v[80:83], v[112:115]
s_waitcnt lgkmcnt(2)
v_exp_f32 v103, v103
v_mfma_f32_16x16x32_bf16 v[128:131], a[128:131], v[32:35], v[192:195]
ds_read_b128 a[140:143], v235 offset:11264
v_cvt_pk_bf16_f32 v164, v100, v101
v_mfma_f32_16x16x32_bf16 v[132:135], a[128:131], v[36:39], v[196:199]
v_exp_f32 v104, v104
v_mfma_f32_16x16x32_bf16 v[136:139], a[128:131], v[40:43], v[200:203]
v_exp_f32 v105, v105
v_mfma_f32_16x16x32_bf16 v[140:143], a[128:131], v[44:47], v[204:207]
s_waitcnt lgkmcnt(2)
v_exp_f32 v106, v106
v_mfma_f32_16x16x32_bf16 v[140:143], a[132:135], v[60:63], v[140:143]
ds_read_b128 a[144:147], v235 offset:12288
v_cvt_pk_bf16_f32 v165, v102, v103
v_mfma_f32_16x16x32_bf16 v[136:139], a[132:135], v[56:59], v[136:139]
s_add_u32 s54, s0, 5
s_and_b32 s54, s54, 7
s_mul_i32 s54, s54, 32768
v_mfma_f32_16x16x32_bf16 v[132:135], a[132:135], v[52:55], v[132:135]
s_add_u32 s54, s54, s71
s_add_u32 s54, s54, 16384
s_add_u32 m0, s70, 65536
v_mfma_f32_16x16x32_bf16 v[128:131], a[132:135], v[48:51], v[128:131]
s_waitcnt lgkmcnt(2)
v_exp_f32 v107, v107
v_mfma_f32_16x16x32_bf16 v[128:131], a[136:139], v[64:67], v[128:131]
ds_read_b128 a[148:151], v235 offset:13312
v_cvt_pk_bf16_f32 v168, v104, v105
v_mfma_f32_16x16x32_bf16 v[132:135], a[136:139], v[68:71], v[132:135]
global_load_dwordx2 v[28:29], v26, s[22:23] sc1
v_mfma_f32_16x16x32_bf16 v[136:139], a[136:139], v[72:75], v[136:139]
v_exp_f32 v108, v108
v_mfma_f32_16x16x32_bf16 v[140:143], a[136:139], v[76:79], v[140:143]
s_waitcnt lgkmcnt(2)
v_exp_f32 v109, v109
v_mfma_f32_16x16x32_bf16 v[140:143], a[140:143], v[92:95], v[140:143]
ds_read_b128 a[152:155], v235 offset:14336
v_exp_f32 v110, v110
v_mfma_f32_16x16x32_bf16 v[136:139], a[140:143], v[88:91], v[136:139]
v_exp_f32 v111, v111
v_mfma_f32_16x16x32_bf16 v[132:135], a[140:143], v[84:87], v[132:135]
v_exp_f32 v112, v112
v_mfma_f32_16x16x32_bf16 v[128:131], a[140:143], v[80:83], v[128:131]
s_waitcnt lgkmcnt(2)
v_exp_f32 v113, v113
v_mfma_f32_16x16x32_bf16 v[144:147], a[144:147], v[32:35], v[192:195]
ds_read_b128 a[156:159], v235 offset:15360
v_exp_f32 v114, v114
v_mfma_f32_16x16x32_bf16 v[148:151], a[144:147], v[36:39], v[196:199]
s_add_u32 s69, s100, 6144
v_exp_f32 v115, v115
v_mfma_f32_16x16x32_bf16 v[152:155], a[144:147], v[40:43], v[200:203]
v_exp_f32 v116, v116
v_mfma_f32_16x16x32_bf16 v[156:159], a[144:147], v[44:47], v[204:207]
s_waitcnt lgkmcnt(2)
v_exp_f32 v117, v117
v_mfma_f32_16x16x32_bf16 v[156:159], a[148:151], v[60:63], v[156:159]
ds_read_b128 a[128:131], v233 offset:1024
v_add_u32 v234, 0, v2
v_cvt_pk_bf16_f32 v169, v106, v107
v_mfma_f32_16x16x32_bf16 v[152:155], a[148:151], v[56:59], v[152:155]
v_exp_f32 v118, v118
v_mfma_f32_16x16x32_bf16 v[148:151], a[148:151], v[52:55], v[148:151]
v_exp_f32 v119, v119
v_mfma_f32_16x16x32_bf16 v[144:147], a[148:151], v[48:51], v[144:147]
s_waitcnt lgkmcnt(2)
v_exp_f32 v120, v120
v_mfma_f32_16x16x32_bf16 v[144:147], a[152:155], v[64:67], v[144:147]
ds_read_b128 a[132:135], v233 offset:3072
v_exp_f32 v121, v121
v_mfma_f32_16x16x32_bf16 v[148:151], a[152:155], v[68:71], v[148:151]
v_exp_f32 v122, v122
v_mfma_f32_16x16x32_bf16 v[152:155], a[152:155], v[72:75], v[152:155]
v_exp_f32 v123, v123
v_mfma_f32_16x16x32_bf16 v[156:159], a[152:155], v[76:79], v[156:159]
s_waitcnt lgkmcnt(2)
v_exp_f32 v124, v124
v_mfma_f32_16x16x32_bf16 v[156:159], a[156:159], v[92:95], v[156:159]
ds_read_b128 a[136:139], v233 offset:5120
v_exp_f32 v125, v125
v_mfma_f32_16x16x32_bf16 v[152:155], a[156:159], v[88:91], v[152:155]
buffer_load_dword v12, v4, s[36:39], s69 offen
v_mfma_f32_16x16x32_bf16 v[148:151], a[156:159], v[84:87], v[148:151]
v_exp_f32 v126, v126
v_mfma_f32_16x16x32_bf16 v[144:147], a[156:159], v[80:83], v[144:147]
s_waitcnt lgkmcnt(2)
v_exp_f32 v127, v127
v_mfma_f32_16x16x32_bf16 a[0:3], a[128:131], v[176:179], a[0:3]
ds_read_b128 a[140:143], v233 offset:7168
v_cvt_pk_bf16_f32 v172, v108, v109
v_cvt_pk_bf16_f32 v173, v110, v111
v_mfma_f32_16x16x32_bf16 a[4:7], a[128:131], v[180:183], a[4:7]
v_add_f32 v208, v208, v96
v_add_f32 v209, v209, v97
v_mfma_f32_16x16x32_bf16 a[8:11], a[128:131], v[184:187], a[8:11]
v_add_f32 v208, v208, v98
v_add_f32 v209, v209, v99
v_mfma_f32_16x16x32_bf16 a[12:15], a[128:131], v[188:191], a[12:15]
s_waitcnt lgkmcnt(2)
v_add_f32 v210, v210, v100
v_add_f32 v211, v211, v101
v_mfma_f32_16x16x32_bf16 a[28:31], a[132:135], v[188:191], a[28:31]
ds_read_b128 a[144:147], v233 offset:9216
v_add_f32 v210, v210, v102
v_add_f32 v211, v211, v103
v_mfma_f32_16x16x32_bf16 a[24:27], a[132:135], v[184:187], a[24:27]
v_add_f32 v212, v212, v104
v_add_f32 v213, v213, v105
v_mfma_f32_16x16x32_bf16 a[20:23], a[132:135], v[180:183], a[20:23]
v_add_f32 v212, v212, v106
v_add_f32 v213, v213, v107
v_mfma_f32_16x16x32_bf16 a[16:19], a[132:135], v[176:179], a[16:19]
s_waitcnt lgkmcnt(2)
v_add_f32 v214, v214, v108
v_add_f32 v215, v215, v109
v_mfma_f32_16x16x32_bf16 a[32:35], a[136:139], v[176:179], a[32:35]
ds_read_b128 a[148:151], v233 offset:11264
v_add_f32 v214, v214, v110
v_add_f32 v215, v215, v111
v_mfma_f32_16x16x32_bf16 a[36:39], a[136:139], v[180:183], a[36:39]
v_cvt_pk_bf16_f32 v162, v112, v113
v_cvt_pk_bf16_f32 v163, v114, v115
v_mfma_f32_16x16x32_bf16 a[40:43], a[136:139], v[184:187], a[40:43]
v_cvt_pk_bf16_f32 v166, v116, v117
v_cvt_pk_bf16_f32 v167, v118, v119
v_mfma_f32_16x16x32_bf16 a[44:47], a[136:139], v[188:191], a[44:47]
s_waitcnt vmcnt(13)
s_barrier
s_waitcnt lgkmcnt(2)
v_mfma_f32_16x16x32_bf16 a[60:63], a[140:143], v[188:191], a[60:63]
ds_read_b128 a[152:155], v233 offset:13312
v_cvt_pk_bf16_f32 v170, v120, v121
v_cvt_pk_bf16_f32 v171, v122, v123
v_mfma_f32_16x16x32_bf16 a[56:59], a[140:143], v[184:187], a[56:59]
v_cvt_pk_bf16_f32 v174, v124, v125
v_cvt_pk_bf16_f32 v175, v126, v127
v_mfma_f32_16x16x32_bf16 a[52:55], a[140:143], v[180:183], a[52:55]
buffer_load_dwordx4 v2, s[72:75], s54 offen lds
v_mfma_f32_16x16x32_bf16 a[48:51], a[140:143], v[176:179], a[48:51]
s_waitcnt lgkmcnt(2)
v_add_f32 v208, v208, v112
v_add_f32 v209, v209, v113
v_mfma_f32_16x16x32_bf16 a[64:67], a[144:147], v[176:179], a[64:67]
ds_read_b128 a[156:159], v233 offset:15360
v_add_f32 v208, v208, v114
v_add_f32 v209, v209, v115
v_mfma_f32_16x16x32_bf16 a[68:71], a[144:147], v[180:183], a[68:71]
v_add_f32 v210, v210, v116
v_add_f32 v211, v211, v117
v_mfma_f32_16x16x32_bf16 a[72:75], a[144:147], v[184:187], a[72:75]
v_add_f32 v210, v210, v118
v_add_f32 v211, v211, v119
v_mfma_f32_16x16x32_bf16 a[76:79], a[144:147], v[188:191], a[76:79]
s_waitcnt lgkmcnt(2)
v_add_f32 v212, v212, v120
v_add_f32 v213, v213, v121
v_mfma_f32_16x16x32_bf16 a[92:95], a[148:151], v[188:191], a[92:95]
ds_read_b128 a[128:131], v234 offset:0
v_add_f32 v212, v212, v122
v_add_f32 v213, v213, v123
v_mfma_f32_16x16x32_bf16 a[88:91], a[148:151], v[184:187], a[88:91]
buffer_load_dwordx4 v2, s[72:75], s54 offen offset:1024 lds
v_mfma_f32_16x16x32_bf16 a[84:87], a[148:151], v[180:183], a[84:87]
v_add_f32 v214, v214, v124
v_add_f32 v215, v215, v125
v_mfma_f32_16x16x32_bf16 a[80:83], a[148:151], v[176:179], a[80:83]
s_waitcnt lgkmcnt(2)
v_add_f32 v214, v214, v126
v_add_f32 v215, v215, v127
v_mfma_f32_16x16x32_bf16 a[96:99], a[152:155], v[176:179], a[96:99]
ds_read_b128 a[132:135], v234 offset:2048
v_add_u32 v232, 16384, v2
v_mfma_f32_16x16x32_bf16 a[100:103], a[152:155], v[180:183], a[100:103]
v_exp_f32 v128, v128
v_mfma_f32_16x16x32_bf16 a[104:107], a[152:155], v[184:187], a[104:107]
v_exp_f32 v129, v129
v_mfma_f32_16x16x32_bf16 a[108:111], a[152:155], v[188:191], a[108:111]
s_waitcnt lgkmcnt(2)
v_exp_f32 v130, v130
v_mfma_f32_16x16x32_bf16 a[124:127], a[156:159], v[188:191], a[124:127]
ds_read_b128 a[136:139], v234 offset:4096
v_exp_f32 v131, v131
v_mfma_f32_16x16x32_bf16 a[120:123], a[156:159], v[184:187], a[120:123]
buffer_load_dwordx4 v2, s[72:75], s54 offen offset:2048 lds
v_mfma_f32_16x16x32_bf16 a[116:119], a[156:159], v[180:183], a[116:119]
v_exp_f32 v132, v132
v_mfma_f32_16x16x32_bf16 a[112:115], a[156:159], v[176:179], a[112:115]
s_waitcnt lgkmcnt(2)
v_exp_f32 v133, v133
v_mfma_f32_16x16x32_bf16 a[0:3], a[128:131], v[160:163], a[0:3]
ds_read_b128 a[140:143], v234 offset:6144
v_exp_f32 v134, v134
v_mfma_f32_16x16x32_bf16 a[4:7], a[128:131], v[164:167], a[4:7]
v_exp_f32 v135, v135
v_mfma_f32_16x16x32_bf16 a[8:11], a[128:131], v[168:171], a[8:11]
buffer_load_dwordx4 v2, s[72:75], s54 offen offset:3072 lds
v_mfma_f32_16x16x32_bf16 a[12:15], a[128:131], v[172:175], a[12:15]
s_waitcnt lgkmcnt(2)
s_add_u32 s54, s0, 6
s_and_b32 s54, s54, 7
v_mfma_f32_16x16x32_bf16 a[28:31], a[132:135], v[172:175], a[28:31]
ds_read_b128 a[144:147], v234 offset:8192
s_mul_i32 s54, s54, 32768
s_add_u32 s54, s54, s71
v_mfma_f32_16x16x32_bf16 a[24:27], a[132:135], v[168:171], a[24:27]
s_add_u32 m0, s70, 81920
v_exp_f32 v136, v136
v_mfma_f32_16x16x32_bf16 a[20:23], a[132:135], v[164:167], a[20:23]
v_exp_f32 v137, v137
v_mfma_f32_16x16x32_bf16 a[16:19], a[132:135], v[160:163], a[16:19]
s_waitcnt lgkmcnt(2)
v_exp_f32 v138, v138
v_mfma_f32_16x16x32_bf16 a[32:35], a[136:139], v[160:163], a[32:35]
ds_read_b128 a[148:151], v234 offset:10240
v_exp_f32 v139, v139
v_mfma_f32_16x16x32_bf16 a[36:39], a[136:139], v[164:167], a[36:39]
buffer_load_dwordx4 v2, s[72:75], s54 offen lds
v_mfma_f32_16x16x32_bf16 a[40:43], a[136:139], v[168:171], a[40:43]
v_exp_f32 v140, v140
v_mfma_f32_16x16x32_bf16 a[44:47], a[136:139], v[172:175], a[44:47]
s_waitcnt lgkmcnt(2)
v_exp_f32 v141, v141
v_mfma_f32_16x16x32_bf16 a[60:63], a[140:143], v[172:175], a[60:63]
ds_read_b128 a[152:155], v234 offset:12288
v_exp_f32 v142, v142
v_mfma_f32_16x16x32_bf16 a[56:59], a[140:143], v[168:171], a[56:59]
v_exp_f32 v143, v143
v_mfma_f32_16x16x32_bf16 a[52:55], a[140:143], v[164:167], a[52:55]
buffer_load_dwordx4 v2, s[72:75], s54 offen offset:1024 lds
v_mfma_f32_16x16x32_bf16 a[48:51], a[140:143], v[160:163], a[48:51]
s_waitcnt lgkmcnt(2)
v_exp_f32 v144, v144
v_mfma_f32_16x16x32_bf16 a[64:67], a[144:147], v[160:163], a[64:67]
ds_read_b128 a[156:159], v234 offset:14336
v_exp_f32 v145, v145
v_mfma_f32_16x16x32_bf16 a[68:71], a[144:147], v[164:167], a[68:71]
v_exp_f32 v146, v146
v_mfma_f32_16x16x32_bf16 a[72:75], a[144:147], v[168:171], a[72:75]
v_exp_f32 v147, v147
v_mfma_f32_16x16x32_bf16 a[76:79], a[144:147], v[172:175], a[76:79]
s_waitcnt lgkmcnt(2)
v_exp_f32 v148, v148
v_mfma_f32_16x16x32_bf16 a[92:95], a[148:151], v[172:175], a[92:95]
ds_read_b128 a[128:131], v232 offset:0
v_exp_f32 v149, v149
v_mfma_f32_16x16x32_bf16 a[88:91], a[148:151], v[168:171], a[88:91]
v_exp_f32 v150, v150
v_mfma_f32_16x16x32_bf16 a[84:87], a[148:151], v[164:167], a[84:87]
buffer_load_dwordx4 v2, s[72:75], s54 offen offset:2048 lds
v_mfma_f32_16x16x32_bf16 a[80:83], a[148:151], v[160:163], a[80:83]
s_waitcnt lgkmcnt(2)
s_waitcnt vmcnt(8)
v_cvt_pk_bf16_f32 v176, v128, v129
v_mfma_f32_16x16x32_bf16 a[96:99], a[152:155], v[160:163], a[96:99]
ds_read_b128 a[132:135], v232 offset:1024
v_exp_f32 v151, v151
v_mfma_f32_16x16x32_bf16 a[100:103], a[152:155], v[164:167], a[100:103]
v_cmp_eq_u64 vcc, s[44:45], v[28:29]
s_nop 4
s_cmp_eq_u64 vcc, -1
s_cbranch_scc0 DP_16_s
DP_16_b:
v_mfma_f32_16x16x32_bf16 a[104:107], a[152:155], v[168:171], a[104:107]
v_cvt_pk_bf16_f32 v177, v130, v131
v_cvt_pk_bf16_f32 v180, v132, v133
v_mfma_f32_16x16x32_bf16 a[108:111], a[152:155], v[172:175], a[108:111]
s_waitcnt lgkmcnt(2)
v_cvt_pk_bf16_f32 v181, v134, v135
v_cvt_pk_bf16_f32 v184, v136, v137
v_mfma_f32_16x16x32_bf16 a[124:127], a[156:159], v[172:175], a[124:127]
ds_read_b128 a[136:139], v232 offset:2048
v_cvt_pk_bf16_f32 v185, v138, v139
v_cvt_pk_bf16_f32 v188, v140, v141
v_mfma_f32_16x16x32_bf16 a[120:123], a[156:159], v[168:171], a[120:123]
buffer_load_dwordx4 v2, s[72:75], s54 offen offset:3072 lds
v_mfma_f32_16x16x32_bf16 a[116:119], a[156:159], v[164:167], a[116:119]
v_cvt_pk_bf16_f32 v189, v142, v143
v_add_f32 v208, v208, v128
v_add_f32 v209, v209, v129
v_add_f32 v208, v208, v130
v_add_f32 v209, v209, v131
v_add_f32 v210, v210, v132
v_add_f32 v211, v211, v133
v_add_f32 v210, v210, v134
v_add_f32 v211, v211, v135
v_add_f32 v212, v212, v136
v_add_f32 v213, v213, v137
v_add_f32 v212, v212, v138
v_add_f32 v213, v213, v139
v_add_f32 v214, v214, v140
v_add_f32 v215, v215, v141
v_add_f32 v214, v214, v142
v_add_f32 v215, v215, v143
v_mfma_f32_16x16x32_bf16 a[112:115], a[156:159], v[160:163], a[112:115]
s_waitcnt lgkmcnt(2)
v_cvt_pk_bf16_f32 v178, v144, v145
v_cvt_pk_bf16_f32 v179, v146, v147
v_cvt_pk_bf16_f32 v182, v148, v149
v_cvt_pk_bf16_f32 v183, v150, v151
v_add_f32 v208, v208, v144
v_add_f32 v209, v209, v145
v_add_f32 v208, v208, v146
v_add_f32 v209, v209, v147
v_add_f32 v210, v210, v148
v_add_f32 v211, v211, v149
v_add_f32 v210, v210, v150
v_add_f32 v211, v211, v151
v_mfma_f32_16x16x32_bf16 v[96:99], a[128:131], v[32:35], v[192:195]
ds_read_b128 a[140:143], v232 offset:3072
v_mfma_f32_16x16x32_bf16 v[100:103], a[128:131], v[36:39], v[196:199]
v_exp_f32 v152, v152
v_mfma_f32_16x16x32_bf16 v[104:107], a[128:131], v[40:43], v[200:203]
s_add_u32 s24, s22, 6291456
v_exp_f32 v153, v153
v_mfma_f32_16x16x32_bf16 v[108:111], a[128:131], v[44:47], v[204:207]
s_addc_u32 s25, s23, 0
s_waitcnt lgkmcnt(2)
v_cvt_pk_bf16_f32 v186, v152, v153
v_mfma_f32_16x16x32_bf16 v[108:111], a[132:135], v[60:63], v[108:111]
ds_read_b128 a[144:147], v232 offset:4096
v_add_f32 v212, v212, v152
v_mfma_f32_16x16x32_bf16 v[104:107], a[132:135], v[56:59], v[104:107]
s_and_b32 s25, s25, 0xffff
v_exp_f32 v154, v154
v_mfma_f32_16x16x32_bf16 v[100:103], a[132:135], v[52:55], v[100:103]
s_mov_b32 s26, 14680064
v_exp_f32 v155, v155
v_mfma_f32_16x16x32_bf16 v[96:99], a[132:135], v[48:51], v[96:99]
s_waitcnt lgkmcnt(2)
v_exp_f32 v156, v156
v_mfma_f32_16x16x32_bf16 v[96:99], a[136:139], v[64:67], v[96:99]
ds_read_b128 a[148:151], v232 offset:5120
v_cvt_pk_bf16_f32 v187, v154, v155
v_mfma_f32_16x16x32_bf16 v[100:103], a[136:139], v[68:71], v[100:103]
s_mov_b32 s27, 0x20000
v_exp_f32 v157, v157
v_mfma_f32_16x16x32_bf16 v[104:107], a[136:139], v[72:75], v[104:107]
s_lshl_b32 s58, s41, 2
v_exp_f32 v158, v158
v_mfma_f32_16x16x32_bf16 v[108:111], a[136:139], v[76:79], v[108:111]
s_waitcnt lgkmcnt(2)
v_exp_f32 v159, v159
v_mfma_f32_16x16x32_bf16 v[108:111], a[140:143], v[92:95], v[108:111]
ds_read_b128 a[152:155], v232 offset:6144
v_cvt_pk_bf16_f32 v190, v156, v157
v_mfma_f32_16x16x32_bf16 v[104:107], a[140:143], v[88:91], v[104:107]
s_add_u32 s58, s58, s43
v_cvt_pk_bf16_f32 v191, v158, v159
v_add_f32 v213, v213, v153
v_mfma_f32_16x16x32_bf16 v[100:103], a[140:143], v[84:87], v[100:103]
s_mul_i32 s58, s58, 458752
v_add_f32 v212, v212, v154
v_add_f32 v213, v213, v155
v_mfma_f32_16x16x32_bf16 v[96:99], a[140:143], v[80:83], v[96:99]
s_waitcnt lgkmcnt(2)
v_add_f32 v214, v214, v156
v_add_f32 v215, v215, v157
v_mfma_f32_16x16x32_bf16 v[112:115], a[144:147], v[32:35], v[192:195]
ds_read_b128 a[156:159], v232 offset:7168
v_add_f32 v214, v214, v158
v_mfma_f32_16x16x32_bf16 v[116:119], a[144:147], v[36:39], v[196:199]
s_mov_b32 s36, s49
s_mov_b32 s37, s50
v_add_f32 v215, v215, v159
v_mfma_f32_16x16x32_bf16 v[120:123], a[144:147], v[40:43], v[200:203]
s_mov_b32 s38, KTEXT1-KTEXT0
s_mov_b32 s39, 0x20000
s_lshr_b32 s69, s98, 3
v_mfma_f32_16x16x32_bf16 v[124:127], a[144:147], v[44:47], v[204:207]
s_waitcnt lgkmcnt(2)
s_lshl_b32 s69, s69, 2
s_add_u32 s69, s69, s43
v_mfma_f32_16x16x32_bf16 v[124:127], a[148:151], v[60:63], v[124:127]
ds_read_b128 a[128:131], v232 offset:8192
s_and_b32 s69, s69, 63
v_mfma_f32_16x16x32_bf16 v[120:123], a[148:151], v[56:59], v[120:123]
s_lshl_b32 s69, s69, 10
s_add_u32 s69, s69, KY-KTEXT0
s_mul_i32 s97, s59, 114688
v_mfma_f32_16x16x32_bf16 v[116:119], a[148:151], v[52:55], v[116:119]
s_add_u32 s58, s58, s97
v_exp_f32 v96, v96
v_mfma_f32_16x16x32_bf16 v[112:115], a[148:151], v[48:51], v[112:115]
s_waitcnt lgkmcnt(2)
v_exp_f32 v97, v97
v_mfma_f32_16x16x32_bf16 v[112:115], a[152:155], v[64:67], v[112:115]
ds_read_b128 a[132:135], v232 offset:9216
v_cvt_pk_bf16_f32 v160, v96, v97
v_mfma_f32_16x16x32_bf16 v[116:119], a[152:155], v[68:71], v[116:119]
v_exp_f32 v98, v98
v_mfma_f32_16x16x32_bf16 v[120:123], a[152:155], v[72:75], v[120:123]
v_exp_f32 v99, v99
v_mfma_f32_16x16x32_bf16 v[124:127], a[152:155], v[76:79], v[124:127]
s_waitcnt lgkmcnt(2)
v_exp_f32 v100, v100
v_mfma_f32_16x16x32_bf16 v[124:127], a[156:159], v[92:95], v[124:127]
ds_read_b128 a[136:139], v232 offset:10240
v_cvt_pk_bf16_f32 v161, v98, v99
v_mfma_f32_16x16x32_bf16 v[120:123], a[156:159], v[88:91], v[120:123]
v_exp_f32 v101, v101
v_mfma_f32_16x16x32_bf16 v[116:119], a[156:159], v[84:87], v[116:119]
v_exp_f32 v102, v102
v_mfma_f32_16x16x32_bf16 v[112:115], a[156:159], v[80:83], v[112:115]
s_waitcnt lgkmcnt(2)
v_exp_f32 v103, v103
v_mfma_f32_16x16x32_bf16 v[128:131], a[128:131], v[32:35], v[192:195]
ds_read_b128 a[140:143], v232 offset:11264
v_cvt_pk_bf16_f32 v164, v100, v101
v_mfma_f32_16x16x32_bf16 v[132:135], a[128:131], v[36:39], v[196:199]
v_exp_f32 v104, v104
v_mfma_f32_16x16x32_bf16 v[136:139], a[128:131], v[40:43], v[200:203]
v_exp_f32 v105, v105
v_mfma_f32_16x16x32_bf16 v[140:143], a[128:131], v[44:47], v[204:207]
s_waitcnt lgkmcnt(2)
v_exp_f32 v106, v106
v_mfma_f32_16x16x32_bf16 v[140:143], a[132:135], v[60:63], v[140:143]
ds_read_b128 a[144:147], v232 offset:12288
v_cvt_pk_bf16_f32 v165, v102, v103
v_mfma_f32_16x16x32_bf16 v[136:139], a[132:135], v[56:59], v[136:139]
s_add_u32 s54, s0, 6
s_and_b32 s54, s54, 7
s_mul_i32 s54, s54, 32768
v_mfma_f32_16x16x32_bf16 v[132:135], a[132:135], v[52:55], v[132:135]
s_add_u32 s54, s54, s71
s_add_u32 s54, s54, 16384
s_add_u32 m0, s70, 98304
v_mfma_f32_16x16x32_bf16 v[128:131], a[132:135], v[48:51], v[128:131]
s_waitcnt lgkmcnt(2)
v_exp_f32 v107, v107
v_mfma_f32_16x16x32_bf16 v[128:131], a[136:139], v[64:67], v[128:131]
ds_read_b128 a[148:151], v232 offset:13312
v_cvt_pk_bf16_f32 v168, v104, v105
v_mfma_f32_16x16x32_bf16 v[132:135], a[136:139], v[68:71], v[132:135]
v_exp_f32 v108, v108
v_mfma_f32_16x16x32_bf16 v[136:139], a[136:139], v[72:75], v[136:139]
v_exp_f32 v109, v109
v_mfma_f32_16x16x32_bf16 v[140:143], a[136:139], v[76:79], v[140:143]
s_waitcnt lgkmcnt(2)
v_exp_f32 v110, v110
v_mfma_f32_16x16x32_bf16 v[140:143], a[140:143], v[92:95], v[140:143]
ds_read_b128 a[152:155], v232 offset:14336
v_exp_f32 v111, v111
v_mfma_f32_16x16x32_bf16 v[136:139], a[140:143], v[88:91], v[136:139]
v_exp_f32 v112, v112
v_mfma_f32_16x16x32_bf16 v[132:135], a[140:143], v[84:87], v[132:135]
v_exp_f32 v113, v113
v_mfma_f32_16x16x32_bf16 v[128:131], a[140:143], v[80:83], v[128:131]
s_waitcnt lgkmcnt(2)
v_exp_f32 v114, v114
v_mfma_f32_16x16x32_bf16 v[144:147], a[144:147], v[32:35], v[192:195]
ds_read_b128 a[156:159], v232 offset:15360
v_exp_f32 v115, v115
v_mfma_f32_16x16x32_bf16 v[148:151], a[144:147], v[36:39], v[196:199]
v_exp_f32 v116, v116
v_mfma_f32_16x16x32_bf16 v[152:155], a[144:147], v[40:43], v[200:203]
v_exp_f32 v117, v117
v_mfma_f32_16x16x32_bf16 v[156:159], a[144:147], v[44:47], v[204:207]
s_waitcnt lgkmcnt(2)
v_exp_f32 v118, v118
v_mfma_f32_16x16x32_bf16 v[156:159], a[148:151], v[60:63], v[156:159]
ds_read_b128 a[128:131], v234 offset:1024
v_add_u32 v233, 32768, v2
v_cvt_pk_bf16_f32 v169, v106, v107
v_mfma_f32_16x16x32_bf16 v[152:155], a[148:151], v[56:59], v[152:155]
v_exp_f32 v119, v119
v_mfma_f32_16x16x32_bf16 v[148:151], a[148:151], v[52:55], v[148:151]
v_exp_f32 v120, v120
v_mfma_f32_16x16x32_bf16 v[144:147], a[148:151], v[48:51], v[144:147]
s_waitcnt lgkmcnt(2)
v_exp_f32 v121, v121
v_mfma_f32_16x16x32_bf16 v[144:147], a[152:155], v[64:67], v[144:147]
ds_read_b128 a[132:135], v234 offset:3072
v_exp_f32 v122, v122
v_mfma_f32_16x16x32_bf16 v[148:151], a[152:155], v[68:71], v[148:151]
buffer_load_dwordx4 v[244:247], v2, s[36:39], s69 offen
v_mfma_f32_16x16x32_bf16 v[152:155], a[152:155], v[72:75], v[152:155]
v_exp_f32 v123, v123
v_mfma_f32_16x16x32_bf16 v[156:159], a[152:155], v[76:79], v[156:159]
s_waitcnt lgkmcnt(2)
v_exp_f32 v124, v124
v_mfma_f32_16x16x32_bf16 v[156:159], a[156:159], v[92:95], v[156:159]
ds_read_b128 a[136:139], v234 offset:5120
v_exp_f32 v125, v125
v_mfma_f32_16x16x32_bf16 v[152:155], a[156:159], v[88:91], v[152:155]
v_exp_f32 v126, v126
v_mfma_f32_16x16x32_bf16 v[148:151], a[156:159], v[84:87], v[148:151]
v_exp_f32 v127, v127
v_mfma_f32_16x16x32_bf16 v[144:147], a[156:159], v[80:83], v[144:147]
s_waitcnt lgkmcnt(2)
v_cvt_pk_bf16_f32 v172, v108, v109
v_cvt_pk_bf16_f32 v173, v110, v111
v_mfma_f32_16x16x32_bf16 a[0:3], a[128:131], v[176:179], a[0:3]
ds_read_b128 a[140:143], v234 offset:7168
v_add_f32 v208, v208, v96
v_add_f32 v209, v209, v97
v_mfma_f32_16x16x32_bf16 a[4:7], a[128:131], v[180:183], a[4:7]
v_add_f32 v208, v208, v98
v_add_f32 v209, v209, v99
v_mfma_f32_16x16x32_bf16 a[8:11], a[128:131], v[184:187], a[8:11]
v_add_f32 v210, v210, v100
v_add_f32 v211, v211, v101
v_mfma_f32_16x16x32_bf16 a[12:15], a[128:131], v[188:191], a[12:15]
s_waitcnt lgkmcnt(2)
v_add_f32 v210, v210, v102
v_add_f32 v211, v211, v103
v_mfma_f32_16x16x32_bf16 a[28:31], a[132:135], v[188:191], a[28:31]
ds_read_b128 a[144:147], v234 offset:9216
v_add_f32 v212, v212, v104
v_add_f32 v213, v213, v105
v_mfma_f32_16x16x32_bf16 a[24:27], a[132:135], v[184:187], a[24:27]
v_add_f32 v212, v212, v106
v_add_f32 v213, v213, v107
v_mfma_f32_16x16x32_bf16 a[20:23], a[132:135], v[180:183], a[20:23]
v_add_f32 v214, v214, v108
v_add_f32 v215, v215, v109
v_mfma_f32_16x16x32_bf16 a[16:19], a[132:135], v[176:179], a[16:19]
s_waitcnt lgkmcnt(2)
v_add_f32 v214, v214, v110
v_add_f32 v215, v215, v111
v_mfma_f32_16x16x32_bf16 a[32:35], a[136:139], v[176:179], a[32:35]
ds_read_b128 a[148:151], v234 offset:11264
v_cvt_pk_bf16_f32 v162, v112, v113
v_cvt_pk_bf16_f32 v163, v114, v115
v_mfma_f32_16x16x32_bf16 a[36:39], a[136:139], v[180:183], a[36:39]
v_cvt_pk_bf16_f32 v166, v116, v117
v_cvt_pk_bf16_f32 v167, v118, v119
v_mfma_f32_16x16x32_bf16 a[40:43], a[136:139], v[184:187], a[40:43]
s_add_u32 s47, s0, 0
s_and_b32 s47, s47, 7
s_mul_i32 s47, s47, 8192
v_mfma_f32_16x16x32_bf16 a[44:47], a[136:139], v[188:191], a[44:47]
s_barrier
s_waitcnt lgkmcnt(2)
v_mfma_f32_16x16x32_bf16 a[60:63], a[140:143], v[188:191], a[60:63]
ds_read_b128 a[152:155], v234 offset:13312
s_add_u32 s47, s47, s58
v_cvt_pk_bf16_f32 v170, v120, v121
v_mfma_f32_16x16x32_bf16 a[56:59], a[140:143], v[184:187], a[56:59]
v_cvt_pk_bf16_f32 v171, v122, v123
v_cvt_pk_bf16_f32 v174, v124, v125
v_mfma_f32_16x16x32_bf16 a[52:55], a[140:143], v[180:183], a[52:55]
buffer_load_dwordx4 v2, s[72:75], s54 offen lds
v_mfma_f32_16x16x32_bf16 a[48:51], a[140:143], v[176:179], a[48:51]
s_waitcnt lgkmcnt(2)
v_cvt_pk_bf16_f32 v175, v126, v127
v_add_f32 v208, v208, v112
v_mfma_f32_16x16x32_bf16 a[64:67], a[144:147], v[176:179], a[64:67]
ds_read_b128 a[156:159], v234 offset:15360
v_add_f32 v209, v209, v113
v_add_f32 v208, v208, v114
v_mfma_f32_16x16x32_bf16 a[68:71], a[144:147], v[180:183], a[68:71]
buffer_load_dwordx4 a[160:163], v236, s[24:27], s47 offen sc1
v_mfma_f32_16x16x32_bf16 a[72:75], a[144:147], v[184:187], a[72:75]
v_add_f32 v209, v209, v115
v_add_f32 v210, v210, v116
v_mfma_f32_16x16x32_bf16 a[76:79], a[144:147], v[188:191], a[76:79]
s_waitcnt lgkmcnt(2)
v_add_f32 v211, v211, v117
v_add_f32 v210, v210, v118
v_mfma_f32_16x16x32_bf16 a[92:95], a[148:151], v[188:191], a[92:95]
ds_read_b128 a[128:131], v233 offset:0
v_add_f32 v211, v211, v119
v_add_f32 v212, v212, v120
v_mfma_f32_16x16x32_bf16 a[88:91], a[148:151], v[184:187], a[88:91]
buffer_load_dwordx4 v2, s[72:75], s54 offen offset:1024 lds
v_mfma_f32_16x16x32_bf16 a[84:87], a[148:151], v[180:183], a[84:87]
v_add_f32 v213, v213, v121
v_add_f32 v212, v212, v122
v_mfma_f32_16x16x32_bf16 a[80:83], a[148:151], v[176:179], a[80:83]
s_waitcnt lgkmcnt(2)
v_add_f32 v213, v213, v123
v_add_f32 v214, v214, v124
v_mfma_f32_16x16x32_bf16 a[96:99], a[152:155], v[176:179], a[96:99]
ds_read_b128 a[132:135], v233 offset:2048
v_add_f32 v215, v215, v125
v_add_f32 v214, v214, v126
v_mfma_f32_16x16x32_bf16 a[100:103], a[152:155], v[180:183], a[100:103]
v_add_f32 v215, v215, v127
v_add_u32 v235, 49152, v2
v_mfma_f32_16x16x32_bf16 a[104:107], a[152:155], v[184:187], a[104:107]
v_exp_f32 v128, v128
v_mfma_f32_16x16x32_bf16 a[108:111], a[152:155], v[188:191], a[108:111]
s_waitcnt lgkmcnt(2)
v_exp_f32 v129, v129
v_mfma_f32_16x16x32_bf16 a[124:127], a[156:159], v[188:191], a[124:127]
ds_read_b128 a[136:139], v233 offset:4096
v_exp_f32 v130, v130
v_mfma_f32_16x16x32_bf16 a[120:123], a[156:159], v[184:187], a[120:123]
buffer_load_dwordx4 v2, s[72:75], s54 offen offset:2048 lds
v_mfma_f32_16x16x32_bf16 a[116:119], a[156:159], v[180:183], a[116:119]
buffer_load_dwordx4 a[164:167], v237, s[24:27], s47 offen sc1
v_mfma_f32_16x16x32_bf16 a[112:115], a[156:159], v[176:179], a[112:115]
s_waitcnt lgkmcnt(2)
v_exp_f32 v131, v131
v_mfma_f32_16x16x32_bf16 a[0:3], a[128:131], v[160:163], a[0:3]
ds_read_b128 a[140:143], v233 offset:6144
v_exp_f32 v132, v132
v_mfma_f32_16x16x32_bf16 a[4:7], a[128:131], v[164:167], a[4:7]
v_exp_f32 v133, v133
v_mfma_f32_16x16x32_bf16 a[8:11], a[128:131], v[168:171], a[8:11]
buffer_load_dwordx4 v2, s[72:75], s54 offen offset:3072 lds
v_mfma_f32_16x16x32_bf16 a[12:15], a[128:131], v[172:175], a[12:15]
s_waitcnt lgkmcnt(2)
s_add_u32 s54, s0, 7
s_and_b32 s54, s54, 7
v_mfma_f32_16x16x32_bf16 a[28:31], a[132:135], v[172:175], a[28:31]
ds_read_b128 a[144:147], v233 offset:8192
s_mul_i32 s54, s54, 32768
s_add_u32 s54, s54, s71
v_mfma_f32_16x16x32_bf16 a[24:27], a[132:135], v[168:171], a[24:27]
s_add_u32 m0, s70, 114688
v_exp_f32 v134, v134
v_mfma_f32_16x16x32_bf16 a[20:23], a[132:135], v[164:167], a[20:23]
v_exp_f32 v135, v135
v_mfma_f32_16x16x32_bf16 a[16:19], a[132:135], v[160:163], a[16:19]
s_waitcnt lgkmcnt(2)
v_exp_f32 v136, v136
v_mfma_f32_16x16x32_bf16 a[32:35], a[136:139], v[160:163], a[32:35]
ds_read_b128 a[148:151], v233 offset:10240
v_exp_f32 v137, v137
v_mfma_f32_16x16x32_bf16 a[36:39], a[136:139], v[164:167], a[36:39]
buffer_load_dwordx4 v2, s[72:75], s54 offen lds
v_mfma_f32_16x16x32_bf16 a[40:43], a[136:139], v[168:171], a[40:43]
buffer_load_dwordx4 a[168:171], v238, s[24:27], s47 offen sc1
v_mfma_f32_16x16x32_bf16 a[44:47], a[136:139], v[172:175], a[44:47]
s_waitcnt lgkmcnt(2)
v_exp_f32 v138, v138
v_mfma_f32_16x16x32_bf16 a[60:63], a[140:143], v[172:175], a[60:63]
ds_read_b128 a[152:155], v233 offset:12288
v_exp_f32 v139, v139
v_mfma_f32_16x16x32_bf16 a[56:59], a[140:143], v[168:171], a[56:59]
v_exp_f32 v140, v140
v_mfma_f32_16x16x32_bf16 a[52:55], a[140:143], v[164:167], a[52:55]
buffer_load_dwordx4 v2, s[72:75], s54 offen offset:1024 lds
v_mfma_f32_16x16x32_bf16 a[48:51], a[140:143], v[160:163], a[48:51]
s_waitcnt lgkmcnt(2)
v_exp_f32 v141, v141
v_mfma_f32_16x16x32_bf16 a[64:67], a[144:147], v[160:163], a[64:67]
ds_read_b128 a[156:159], v233 offset:14336
v_exp_f32 v142, v142
v_mfma_f32_16x16x32_bf16 a[68:71], a[144:147], v[164:167], a[68:71]
v_exp_f32 v143, v143
v_mfma_f32_16x16x32_bf16 a[72:75], a[144:147], v[168:171], a[72:75]
v_exp_f32 v144, v144
v_mfma_f32_16x16x32_bf16 a[76:79], a[144:147], v[172:175], a[76:79]
s_waitcnt lgkmcnt(2)
v_exp_f32 v145, v145
v_mfma_f32_16x16x32_bf16 a[92:95], a[148:151], v[172:175], a[92:95]
ds_read_b128 a[128:131], v235 offset:0
v_exp_f32 v146, v146
v_mfma_f32_16x16x32_bf16 a[88:91], a[148:151], v[168:171], a[88:91]
v_exp_f32 v147, v147
v_mfma_f32_16x16x32_bf16 a[84:87], a[148:151], v[164:167], a[84:87]
buffer_load_dwordx4 v2, s[72:75], s54 offen offset:2048 lds
v_mfma_f32_16x16x32_bf16 a[80:83], a[148:151], v[160:163], a[80:83]
s_waitcnt lgkmcnt(2)
v_exp_f32 v148, v148
v_mfma_f32_16x16x32_bf16 a[96:99], a[152:155], v[160:163], a[96:99]
ds_read_b128 a[132:135], v235 offset:1024
v_exp_f32 v149, v149
v_mfma_f32_16x16x32_bf16 a[100:103], a[152:155], v[164:167], a[100:103]
buffer_load_dwordx4 a[172:175], v239, s[24:27], s47 offen sc1
v_mfma_f32_16x16x32_bf16 a[104:107], a[152:155], v[168:171], a[104:107]
s_add_u32 s47, s47, 4096
v_exp_f32 v150, v150
v_mfma_f32_16x16x32_bf16 a[108:111], a[152:155], v[172:175], a[108:111]
s_waitcnt lgkmcnt(2)
v_exp_f32 v151, v151
v_mfma_f32_16x16x32_bf16 a[124:127], a[156:159], v[172:175], a[124:127]
ds_read_b128 a[136:139], v235 offset:2048
v_cvt_pk_bf16_f32 v176, v128, v129
v_cvt_pk_bf16_f32 v177, v130, v131
v_mfma_f32_16x16x32_bf16 a[120:123], a[156:159], v[168:171], a[120:123]
buffer_load_dwordx4 v2, s[72:75], s54 offen offset:3072 lds
v_mfma_f32_16x16x32_bf16 a[116:119], a[156:159], v[164:167], a[116:119]
v_cvt_pk_bf16_f32 v180, v132, v133
v_cvt_pk_bf16_f32 v181, v134, v135
v_cvt_pk_bf16_f32 v184, v136, v137
v_cvt_pk_bf16_f32 v185, v138, v139
v_cvt_pk_bf16_f32 v188, v140, v141
v_cvt_pk_bf16_f32 v189, v142, v143
v_add_f32 v208, v208, v128
v_add_f32 v209, v209, v129
v_add_f32 v208, v208, v130
v_add_f32 v209, v209, v131
v_add_f32 v210, v210, v132
v_add_f32 v211, v211, v133
v_add_f32 v210, v210, v134
v_add_f32 v211, v211, v135
v_add_f32 v212, v212, v136
v_add_f32 v213, v213, v137
v_add_f32 v212, v212, v138
v_add_f32 v213, v213, v139
v_add_f32 v214, v214, v140
v_add_f32 v215, v215, v141
v_add_f32 v214, v214, v142
v_add_f32 v215, v215, v143
v_mfma_f32_16x16x32_bf16 a[112:115], a[156:159], v[160:163], a[112:115]
s_waitcnt lgkmcnt(2)
v_cvt_pk_bf16_f32 v178, v144, v145
v_cvt_pk_bf16_f32 v179, v146, v147
v_cvt_pk_bf16_f32 v182, v148, v149
v_cvt_pk_bf16_f32 v183, v150, v151
v_add_f32 v208, v208, v144
v_add_f32 v209, v209, v145
v_add_f32 v208, v208, v146
v_add_f32 v209, v209, v147
v_add_f32 v210, v210, v148
v_add_f32 v211, v211, v149
v_add_f32 v210, v210, v150
v_add_f32 v211, v211, v151
v_mfma_f32_16x16x32_bf16 v[96:99], a[128:131], v[32:35], v[192:195]
ds_read_b128 a[140:143], v235 offset:3072
v_mfma_f32_16x16x32_bf16 v[100:103], a[128:131], v[36:39], v[196:199]
v_exp_f32 v152, v152
v_mfma_f32_16x16x32_bf16 v[104:107], a[128:131], v[40:43], v[200:203]
v_exp_f32 v153, v153
v_mfma_f32_16x16x32_bf16 v[108:111], a[128:131], v[44:47], v[204:207]
s_waitcnt lgkmcnt(2)
v_exp_f32 v154, v154
v_mfma_f32_16x16x32_bf16 v[108:111], a[132:135], v[60:63], v[108:111]
ds_read_b128 a[144:147], v235 offset:4096
v_cvt_pk_bf16_f32 v186, v152, v153
v_mfma_f32_16x16x32_bf16 v[104:107], a[132:135], v[56:59], v[104:107]
v_exp_f32 v155, v155
v_mfma_f32_16x16x32_bf16 v[100:103], a[132:135], v[52:55], v[100:103]
v_exp_f32 v156, v156
v_mfma_f32_16x16x32_bf16 v[96:99], a[132:135], v[48:51], v[96:99]
s_waitcnt lgkmcnt(2)
v_exp_f32 v157, v157
v_mfma_f32_16x16x32_bf16 v[96:99], a[136:139], v[64:67], v[96:99]
ds_read_b128 a[148:151], v235 offset:5120
v_cvt_pk_bf16_f32 v187, v154, v155
v_mfma_f32_16x16x32_bf16 v[100:103], a[136:139], v[68:71], v[100:103]
v_exp_f32 v158, v158
v_mfma_f32_16x16x32_bf16 v[104:107], a[136:139], v[72:75], v[104:107]
v_exp_f32 v159, v159
v_mfma_f32_16x16x32_bf16 v[108:111], a[136:139], v[76:79], v[108:111]
s_waitcnt lgkmcnt(2)
v_cvt_pk_bf16_f32 v190, v156, v157
v_cvt_pk_bf16_f32 v191, v158, v159
v_mfma_f32_16x16x32_bf16 v[108:111], a[140:143], v[92:95], v[108:111]
ds_read_b128 a[152:155], v235 offset:6144
v_add_f32 v212, v212, v152
v_mfma_f32_16x16x32_bf16 v[104:107], a[140:143], v[88:91], v[104:107]
v_add_f32 v213, v213, v153
v_add_f32 v212, v212, v154
v_mfma_f32_16x16x32_bf16 v[100:103], a[140:143], v[84:87], v[100:103]
buffer_load_dwordx4 a[176:179], v236, s[24:27], s47 offen sc1
v_mfma_f32_16x16x32_bf16 v[96:99], a[140:143], v[80:83], v[96:99]
s_waitcnt lgkmcnt(2)
v_add_f32 v213, v213, v155
v_add_f32 v214, v214, v156
v_mfma_f32_16x16x32_bf16 v[112:115], a[144:147], v[32:35], v[192:195]
ds_read_b128 a[156:159], v235 offset:7168
v_add_f32 v215, v215, v157
v_mfma_f32_16x16x32_bf16 v[116:119], a[144:147], v[36:39], v[196:199]
v_add_f32 v214, v214, v158
v_add_f32 v215, v215, v159
v_mfma_f32_16x16x32_bf16 v[120:123], a[144:147], v[40:43], v[200:203]
v_exp_f32 v96, v96
v_mfma_f32_16x16x32_bf16 v[124:127], a[144:147], v[44:47], v[204:207]
s_waitcnt lgkmcnt(2)
v_exp_f32 v97, v97
v_mfma_f32_16x16x32_bf16 v[124:127], a[148:151], v[60:63], v[124:127]
ds_read_b128 a[128:131], v235 offset:8192
v_cvt_pk_bf16_f32 v160, v96, v97
v_mfma_f32_16x16x32_bf16 v[120:123], a[148:151], v[56:59], v[120:123]
v_exp_f32 v98, v98
v_mfma_f32_16x16x32_bf16 v[116:119], a[148:151], v[52:55], v[116:119]
v_exp_f32 v99, v99
v_mfma_f32_16x16x32_bf16 v[112:115], a[148:151], v[48:51], v[112:115]
s_waitcnt lgkmcnt(2)
v_exp_f32 v100, v100
v_mfma_f32_16x16x32_bf16 v[112:115], a[152:155], v[64:67], v[112:115]
ds_read_b128 a[132:135], v235 offset:9216
v_cvt_pk_bf16_f32 v161, v98, v99
v_mfma_f32_16x16x32_bf16 v[116:119], a[152:155], v[68:71], v[116:119]
v_exp_f32 v101, v101
v_mfma_f32_16x16x32_bf16 v[120:123], a[152:155], v[72:75], v[120:123]
v_exp_f32 v102, v102
v_mfma_f32_16x16x32_bf16 v[124:127], a[152:155], v[76:79], v[124:127]
s_waitcnt lgkmcnt(2)
v_exp_f32 v103, v103
v_mfma_f32_16x16x32_bf16 v[124:127], a[156:159], v[92:95], v[124:127]
ds_read_b128 a[136:139], v235 offset:10240
v_cvt_pk_bf16_f32 v164, v100, v101
v_mfma_f32_16x16x32_bf16 v[120:123], a[156:159], v[88:91], v[120:123]
v_exp_f32 v104, v104
v_mfma_f32_16x16x32_bf16 v[116:119], a[156:159], v[84:87], v[116:119]
buffer_load_dwordx4 a[180:183], v237, s[24:27], s47 offen sc1
v_mfma_f32_16x16x32_bf16 v[112:115], a[156:159], v[80:83], v[112:115]
s_waitcnt lgkmcnt(2)
v_exp_f32 v105, v105
v_mfma_f32_16x16x32_bf16 v[128:131], a[128:131], v[32:35], v[192:195]
ds_read_b128 a[140:143], v235 offset:11264
v_cvt_pk_bf16_f32 v165, v102, v103
v_mfma_f32_16x16x32_bf16 v[132:135], a[128:131], v[36:39], v[196:199]
v_exp_f32 v106, v106
v_mfma_f32_16x16x32_bf16 v[136:139], a[128:131], v[40:43], v[200:203]
v_exp_f32 v107, v107
v_mfma_f32_16x16x32_bf16 v[140:143], a[128:131], v[44:47], v[204:207]
s_waitcnt lgkmcnt(2)
v_exp_f32 v108, v108
v_mfma_f32_16x16x32_bf16 v[140:143], a[132:135], v[60:63], v[140:143]
ds_read_b128 a[144:147], v235 offset:12288
v_cvt_pk_bf16_f32 v168, v104, v105
v_mfma_f32_16x16x32_bf16 v[136:139], a[132:135], v[56:59], v[136:139]
s_add_u32 s54, s0, 7
s_and_b32 s54, s54, 7
s_mul_i32 s54, s54, 32768
v_mfma_f32_16x16x32_bf16 v[132:135], a[132:135], v[52:55], v[132:135]
s_add_u32 s54, s54, s71
s_add_u32 s54, s54, 16384
s_add_u32 m0, s70, 131072
v_mfma_f32_16x16x32_bf16 v[128:131], a[132:135], v[48:51], v[128:131]
s_waitcnt lgkmcnt(2)
v_exp_f32 v109, v109
v_mfma_f32_16x16x32_bf16 v[128:131], a[136:139], v[64:67], v[128:131]
ds_read_b128 a[148:151], v235 offset:13312
v_cvt_pk_bf16_f32 v169, v106, v107
v_mfma_f32_16x16x32_bf16 v[132:135], a[136:139], v[68:71], v[132:135]
v_exp_f32 v110, v110
v_mfma_f32_16x16x32_bf16 v[136:139], a[136:139], v[72:75], v[136:139]
v_exp_f32 v111, v111
v_mfma_f32_16x16x32_bf16 v[140:143], a[136:139], v[76:79], v[140:143]
s_waitcnt lgkmcnt(2)
v_exp_f32 v112, v112
v_mfma_f32_16x16x32_bf16 v[140:143], a[140:143], v[92:95], v[140:143]
ds_read_b128 a[152:155], v235 offset:14336
v_exp_f32 v113, v113
v_mfma_f32_16x16x32_bf16 v[136:139], a[140:143], v[88:91], v[136:139]
v_exp_f32 v114, v114
v_mfma_f32_16x16x32_bf16 v[132:135], a[140:143], v[84:87], v[132:135]
buffer_load_dwordx4 a[184:187], v238, s[24:27], s47 offen sc1
v_mfma_f32_16x16x32_bf16 v[128:131], a[140:143], v[80:83], v[128:131]
s_waitcnt lgkmcnt(2)
v_exp_f32 v115, v115
v_mfma_f32_16x16x32_bf16 v[144:147], a[144:147], v[32:35], v[192:195]
ds_read_b128 a[156:159], v235 offset:15360
v_exp_f32 v116, v116
v_mfma_f32_16x16x32_bf16 v[148:151], a[144:147], v[36:39], v[196:199]
v_exp_f32 v117, v117
v_mfma_f32_16x16x32_bf16 v[152:155], a[144:147], v[40:43], v[200:203]
v_exp_f32 v118, v118
v_mfma_f32_16x16x32_bf16 v[156:159], a[144:147], v[44:47], v[204:207]
s_waitcnt lgkmcnt(2)
v_exp_f32 v119, v119
v_mfma_f32_16x16x32_bf16 v[156:159], a[148:151], v[60:63], v[156:159]
ds_read_b128 a[128:131], v233 offset:1024
v_add_u32 v234, 65536, v2
v_cvt_pk_bf16_f32 v172, v108, v109
v_mfma_f32_16x16x32_bf16 v[152:155], a[148:151], v[56:59], v[152:155]
v_exp_f32 v120, v120
v_mfma_f32_16x16x32_bf16 v[148:151], a[148:151], v[52:55], v[148:151]
v_exp_f32 v121, v121
v_mfma_f32_16x16x32_bf16 v[144:147], a[148:151], v[48:51], v[144:147]
s_waitcnt lgkmcnt(2)
v_exp_f32 v122, v122
v_mfma_f32_16x16x32_bf16 v[144:147], a[152:155], v[64:67], v[144:147]
ds_read_b128 a[132:135], v233 offset:3072
v_exp_f32 v123, v123
v_mfma_f32_16x16x32_bf16 v[148:151], a[152:155], v[68:71], v[148:151]
v_exp_f32 v124, v124
v_mfma_f32_16x16x32_bf16 v[152:155], a[152:155], v[72:75], v[152:155]
v_exp_f32 v125, v125
v_mfma_f32_16x16x32_bf16 v[156:159], a[152:155], v[76:79], v[156:159]
s_waitcnt lgkmcnt(2)
v_exp_f32 v126, v126
v_mfma_f32_16x16x32_bf16 v[156:159], a[156:159], v[92:95], v[156:159]
ds_read_b128 a[136:139], v233 offset:5120
v_exp_f32 v127, v127
v_mfma_f32_16x16x32_bf16 v[152:155], a[156:159], v[88:91], v[152:155]
v_cvt_pk_bf16_f32 v173, v110, v111
v_add_f32 v208, v208, v96
v_mfma_f32_16x16x32_bf16 v[148:151], a[156:159], v[84:87], v[148:151]
buffer_load_dwordx4 a[188:191], v239, s[24:27], s47 offen sc1
v_mfma_f32_16x16x32_bf16 v[144:147], a[156:159], v[80:83], v[144:147]
s_waitcnt lgkmcnt(2)
v_add_f32 v209, v209, v97
v_add_f32 v208, v208, v98
v_mfma_f32_16x16x32_bf16 a[0:3], a[128:131], v[176:179], a[0:3]
ds_read_b128 a[140:143], v233 offset:7168
v_add_f32 v209, v209, v99
v_add_f32 v210, v210, v100
v_mfma_f32_16x16x32_bf16 a[4:7], a[128:131], v[180:183], a[4:7]
v_add_f32 v211, v211, v101
v_add_f32 v210, v210, v102
v_mfma_f32_16x16x32_bf16 a[8:11], a[128:131], v[184:187], a[8:11]
v_add_f32 v211, v211, v103
v_add_f32 v212, v212, v104
v_mfma_f32_16x16x32_bf16 a[12:15], a[128:131], v[188:191], a[12:15]
s_waitcnt lgkmcnt(2)
v_add_f32 v213, v213, v105
v_add_f32 v212, v212, v106
v_mfma_f32_16x16x32_bf16 a[28:31], a[132:135], v[188:191], a[28:31]
ds_read_b128 a[144:147], v233 offset:9216
v_add_f32 v213, v213, v107
v_add_f32 v214, v214, v108
v_mfma_f32_16x16x32_bf16 a[24:27], a[132:135], v[184:187], a[24:27]
v_add_f32 v215, v215, v109
v_add_f32 v214, v214, v110
v_mfma_f32_16x16x32_bf16 a[20:23], a[132:135], v[180:183], a[20:23]
v_add_f32 v215, v215, v111
v_cvt_pk_bf16_f32 v162, v112, v113
v_mfma_f32_16x16x32_bf16 a[16:19], a[132:135], v[176:179], a[16:19]
s_waitcnt lgkmcnt(2)
v_cvt_pk_bf16_f32 v163, v114, v115
v_cvt_pk_bf16_f32 v166, v116, v117
v_mfma_f32_16x16x32_bf16 a[32:35], a[136:139], v[176:179], a[32:35]
ds_read_b128 a[148:151], v233 offset:11264
v_cvt_pk_bf16_f32 v167, v118, v119
v_cvt_pk_bf16_f32 v170, v120, v121
v_mfma_f32_16x16x32_bf16 a[36:39], a[136:139], v[180:183], a[36:39]
v_cvt_pk_bf16_f32 v171, v122, v123
v_cvt_pk_bf16_f32 v174, v124, v125
v_mfma_f32_16x16x32_bf16 a[40:43], a[136:139], v[184:187], a[40:43]
v_cvt_pk_bf16_f32 v175, v126, v127
v_add_f32 v208, v208, v112
v_mfma_f32_16x16x32_bf16 a[44:47], a[136:139], v[188:191], a[44:47]
s_waitcnt vmcnt(17)
s_barrier
s_waitcnt lgkmcnt(2)
v_mfma_f32_16x16x32_bf16 a[60:63], a[140:143], v[188:191], a[60:63]
ds_read_b128 a[152:155], v233 offset:13312
v_add_f32 v209, v209, v113
v_add_f32 v208, v208, v114
v_mfma_f32_16x16x32_bf16 a[56:59], a[140:143], v[184:187], a[56:59]
v_add_f32 v209, v209, v115
v_add_f32 v210, v210, v116
v_mfma_f32_16x16x32_bf16 a[52:55], a[140:143], v[180:183], a[52:55]
buffer_load_dwordx4 v2, s[72:75], s54 offen lds
v_mfma_f32_16x16x32_bf16 a[48:51], a[140:143], v[176:179], a[48:51]
s_waitcnt lgkmcnt(2)
v_add_f32 v211, v211, v117
v_add_f32 v210, v210, v118
v_mfma_f32_16x16x32_bf16 a[64:67], a[144:147], v[176:179], a[64:67]
ds_read_b128 a[156:159], v233 offset:15360
v_add_f32 v211, v211, v119
v_add_f32 v212, v212, v120
v_mfma_f32_16x16x32_bf16 a[68:71], a[144:147], v[180:183], a[68:71]
v_add_f32 v213, v213, v121
v_add_f32 v212, v212, v122
v_mfma_f32_16x16x32_bf16 a[72:75], a[144:147], v[184:187], a[72:75]
v_add_f32 v213, v213, v123
v_add_f32 v214, v214, v124
v_mfma_f32_16x16x32_bf16 a[76:79], a[144:147], v[188:191], a[76:79]
s_waitcnt lgkmcnt(2)
v_add_f32 v215, v215, v125
v_add_f32 v214, v214, v126
v_mfma_f32_16x16x32_bf16 a[92:95], a[148:151], v[188:191], a[92:95]
ds_read_b128 a[128:131], v234 offset:0
v_add_f32 v215, v215, v127
v_add_u32 v232, 81920, v2
v_mfma_f32_16x16x32_bf16 a[88:91], a[148:151], v[184:187], a[88:91]
buffer_load_dwordx4 v2, s[72:75], s54 offen offset:1024 lds
v_mfma_f32_16x16x32_bf16 a[84:87], a[148:151], v[180:183], a[84:87]
v_exp_f32 v128, v128
v_mfma_f32_16x16x32_bf16 a[80:83], a[148:151], v[176:179], a[80:83]
s_waitcnt lgkmcnt(2)
v_exp_f32 v129, v129
v_mfma_f32_16x16x32_bf16 a[96:99], a[152:155], v[176:179], a[96:99]
ds_read_b128 a[132:135], v234 offset:2048
v_exp_f32 v130, v130
v_mfma_f32_16x16x32_bf16 a[100:103], a[152:155], v[180:183], a[100:103]
v_exp_f32 v131, v131
v_mfma_f32_16x16x32_bf16 a[104:107], a[152:155], v[184:187], a[104:107]
v_exp_f32 v132, v132
v_mfma_f32_16x16x32_bf16 a[108:111], a[152:155], v[188:191], a[108:111]
s_waitcnt lgkmcnt(2)
v_exp_f32 v133, v133
v_mfma_f32_16x16x32_bf16 a[124:127], a[156:159], v[188:191], a[124:127]
ds_read_b128 a[136:139], v234 offset:4096
v_exp_f32 v134, v134
v_mfma_f32_16x16x32_bf16 a[120:123], a[156:159], v[184:187], a[120:123]
buffer_load_dwordx4 v2, s[72:75], s54 offen offset:2048 lds
v_mfma_f32_16x16x32_bf16 a[116:119], a[156:159], v[180:183], a[116:119]
v_exp_f32 v135, v135
v_mfma_f32_16x16x32_bf16 a[112:115], a[156:159], v[176:179], a[112:115]
s_waitcnt lgkmcnt(2)
v_exp_f32 v136, v136
v_mfma_f32_16x16x32_bf16 a[0:3], a[128:131], v[160:163], a[0:3]
ds_read_b128 a[140:143], v234 offset:6144
v_exp_f32 v137, v137
v_mfma_f32_16x16x32_bf16 a[4:7], a[128:131], v[164:167], a[4:7]
v_exp_f32 v138, v138
v_mfma_f32_16x16x32_bf16 a[8:11], a[128:131], v[168:171], a[8:11]
buffer_load_dwordx4 v2, s[72:75], s54 offen offset:3072 lds
v_mfma_f32_16x16x32_bf16 a[12:15], a[128:131], v[172:175], a[12:15]
s_waitcnt lgkmcnt(2)
s_add_u32 s54, s0, 0
s_and_b32 s54, s54, 7
v_mfma_f32_16x16x32_bf16 a[28:31], a[132:135], v[172:175], a[28:31]
ds_read_b128 a[144:147], v234 offset:8192
s_mul_i32 s54, s54, 131072
s_add_u32 s54, s54, s85
v_mfma_f32_16x16x32_bf16 a[24:27], a[132:135], v[168:171], a[24:27]
s_add_u32 m0, s70, 147456
v_exp_f32 v139, v139
v_mfma_f32_16x16x32_bf16 a[20:23], a[132:135], v[164:167], a[20:23]
v_exp_f32 v140, v140
v_mfma_f32_16x16x32_bf16 a[16:19], a[132:135], v[160:163], a[16:19]
s_waitcnt lgkmcnt(2)
v_exp_f32 v141, v141
v_mfma_f32_16x16x32_bf16 a[32:35], a[136:139], v[160:163], a[32:35]
ds_read_b128 a[148:151], v234 offset:10240
v_exp_f32 v142, v142
v_mfma_f32_16x16x32_bf16 a[36:39], a[136:139], v[164:167], a[36:39]
buffer_load_dwordx4 v13, s[88:91], s54 offen lds
v_mfma_f32_16x16x32_bf16 a[40:43], a[136:139], v[168:171], a[40:43]
s_add_u32 m0, s70, 148352
v_exp_f32 v143, v143
v_mfma_f32_16x16x32_bf16 a[44:47], a[136:139], v[172:175], a[44:47]
s_waitcnt lgkmcnt(2)
v_exp_f32 v144, v144
v_mfma_f32_16x16x32_bf16 a[60:63], a[140:143], v[172:175], a[60:63]
ds_read_b128 a[152:155], v234 offset:12288
v_exp_f32 v145, v145
v_mfma_f32_16x16x32_bf16 a[56:59], a[140:143], v[168:171], a[56:59]
v_exp_f32 v146, v146
v_mfma_f32_16x16x32_bf16 a[52:55], a[140:143], v[164:167], a[52:55]
buffer_load_dwordx4 v13, s[88:91], s54 offen offset:128 lds
v_mfma_f32_16x16x32_bf16 a[48:51], a[140:143], v[160:163], a[48:51]
s_waitcnt lgkmcnt(2)
s_add_u32 s54, s54, 16384
s_add_u32 m0, s70, 149504
v_mfma_f32_16x16x32_bf16 a[64:67], a[144:147], v[160:163], a[64:67]
ds_read_b128 a[156:159], v234 offset:14336
v_exp_f32 v147, v147
v_mfma_f32_16x16x32_bf16 a[68:71], a[144:147], v[164:167], a[68:71]
v_exp_f32 v148, v148
v_mfma_f32_16x16x32_bf16 a[72:75], a[144:147], v[168:171], a[72:75]
v_exp_f32 v149, v149
v_mfma_f32_16x16x32_bf16 a[76:79], a[144:147], v[172:175], a[76:79]
s_waitcnt lgkmcnt(2)
v_exp_f32 v150, v150
v_mfma_f32_16x16x32_bf16 a[92:95], a[148:151], v[172:175], a[92:95]
ds_read_b128 a[128:131], v232 offset:0
v_exp_f32 v151, v151
v_mfma_f32_16x16x32_bf16 a[88:91], a[148:151], v[168:171], a[88:91]
v_cvt_pk_bf16_f32 v176, v128, v129
v_cvt_pk_bf16_f32 v177, v130, v131
v_mfma_f32_16x16x32_bf16 a[84:87], a[148:151], v[164:167], a[84:87]
buffer_load_dwordx4 v13, s[88:91], s54 offen lds
v_mfma_f32_16x16x32_bf16 a[80:83], a[148:151], v[160:163], a[80:83]
s_waitcnt lgkmcnt(2)
s_add_u32 m0, s70, 150400
v_cvt_pk_bf16_f32 v180, v132, v133
v_mfma_f32_16x16x32_bf16 a[96:99], a[152:155], v[160:163], a[96:99]
ds_read_b128 a[132:135], v232 offset:1024
v_cvt_pk_bf16_f32 v181, v134, v135
v_cvt_pk_bf16_f32 v184, v136, v137
v_mfma_f32_16x16x32_bf16 a[100:103], a[152:155], v[164:167], a[100:103]
v_cvt_pk_bf16_f32 v185, v138, v139
v_cvt_pk_bf16_f32 v188, v140, v141
v_mfma_f32_16x16x32_bf16 a[104:107], a[152:155], v[168:171], a[104:107]
v_cvt_pk_bf16_f32 v189, v142, v143
v_add_f32 v208, v208, v128
v_mfma_f32_16x16x32_bf16 a[108:111], a[152:155], v[172:175], a[108:111]
s_waitcnt lgkmcnt(2)
v_add_f32 v209, v209, v129
v_add_f32 v208, v208, v130
v_mfma_f32_16x16x32_bf16 a[124:127], a[156:159], v[172:175], a[124:127]
ds_read_b128 a[136:139], v232 offset:2048
v_add_f32 v209, v209, v131
v_add_f32 v210, v210, v132
v_mfma_f32_16x16x32_bf16 a[120:123], a[156:159], v[168:171], a[120:123]
buffer_load_dwordx4 v13, s[88:91], s54 offen offset:128 lds
v_mfma_f32_16x16x32_bf16 a[116:119], a[156:159], v[164:167], a[116:119]
v_add_f32 v211, v211, v133
v_add_f32 v210, v210, v134
v_add_f32 v211, v211, v135
v_add_f32 v212, v212, v136
v_add_f32 v213, v213, v137
v_add_f32 v212, v212, v138
v_add_f32 v213, v213, v139
v_add_f32 v214, v214, v140
v_add_f32 v215, v215, v141
v_add_f32 v214, v214, v142
v_add_f32 v215, v215, v143
v_mfma_f32_16x16x32_bf16 a[112:115], a[156:159], v[160:163], a[112:115]
s_waitcnt lgkmcnt(2)
v_cvt_pk_bf16_f32 v178, v144, v145
v_cvt_pk_bf16_f32 v179, v146, v147
v_cvt_pk_bf16_f32 v182, v148, v149
v_cvt_pk_bf16_f32 v183, v150, v151
v_add_f32 v208, v208, v144
v_add_f32 v209, v209, v145
v_add_f32 v208, v208, v146
v_add_f32 v209, v209, v147
v_add_f32 v210, v210, v148
v_add_f32 v211, v211, v149
v_add_f32 v210, v210, v150
v_add_f32 v211, v211, v151
v_mfma_f32_16x16x32_bf16 v[96:99], a[128:131], v[32:35], v[192:195]
ds_read_b128 a[140:143], v232 offset:3072
s_add_u32 s47, s0, 1
v_mfma_f32_16x16x32_bf16 v[100:103], a[128:131], v[36:39], v[196:199]
s_and_b32 s47, s47, 7
s_mul_i32 s47, s47, 8192
s_add_u32 s47, s47, s58
v_mfma_f32_16x16x32_bf16 v[104:107], a[128:131], v[40:43], v[200:203]
v_exp_f32 v152, v152
v_mfma_f32_16x16x32_bf16 v[108:111], a[128:131], v[44:47], v[204:207]
s_waitcnt lgkmcnt(2)
v_exp_f32 v153, v153
v_mfma_f32_16x16x32_bf16 v[108:111], a[132:135], v[60:63], v[108:111]
ds_read_b128 a[144:147], v232 offset:4096
v_cvt_pk_bf16_f32 v186, v152, v153
v_mfma_f32_16x16x32_bf16 v[104:107], a[132:135], v[56:59], v[104:107]
v_exp_f32 v154, v154
v_mfma_f32_16x16x32_bf16 v[100:103], a[132:135], v[52:55], v[100:103]
v_exp_f32 v155, v155
v_mfma_f32_16x16x32_bf16 v[96:99], a[132:135], v[48:51], v[96:99]
s_waitcnt lgkmcnt(2)
v_exp_f32 v156, v156
v_mfma_f32_16x16x32_bf16 v[96:99], a[136:139], v[64:67], v[96:99]
ds_read_b128 a[148:151], v232 offset:5120
v_cvt_pk_bf16_f32 v187, v154, v155
v_mfma_f32_16x16x32_bf16 v[100:103], a[136:139], v[68:71], v[100:103]
v_exp_f32 v157, v157
v_mfma_f32_16x16x32_bf16 v[104:107], a[136:139], v[72:75], v[104:107]
v_exp_f32 v158, v158
v_mfma_f32_16x16x32_bf16 v[108:111], a[136:139], v[76:79], v[108:111]
s_waitcnt lgkmcnt(2)
v_exp_f32 v159, v159
v_mfma_f32_16x16x32_bf16 v[108:111], a[140:143], v[92:95], v[108:111]
ds_read_b128 a[152:155], v232 offset:6144
v_cvt_pk_bf16_f32 v190, v156, v157
v_mfma_f32_16x16x32_bf16 v[104:107], a[140:143], v[88:91], v[104:107]
v_cvt_pk_bf16_f32 v191, v158, v159
v_add_f32 v212, v212, v152
v_mfma_f32_16x16x32_bf16 v[100:103], a[140:143], v[84:87], v[100:103]
buffer_load_dwordx4 a[192:195], v236, s[24:27], s47 offen sc1
v_mfma_f32_16x16x32_bf16 v[96:99], a[140:143], v[80:83], v[96:99]
s_waitcnt lgkmcnt(2)
v_add_f32 v213, v213, v153
v_add_f32 v212, v212, v154
v_mfma_f32_16x16x32_bf16 v[112:115], a[144:147], v[32:35], v[192:195]
ds_read_b128 a[156:159], v232 offset:7168
v_add_f32 v213, v213, v155
v_mfma_f32_16x16x32_bf16 v[116:119], a[144:147], v[36:39], v[196:199]
v_add_f32 v214, v214, v156
v_add_f32 v215, v215, v157
v_mfma_f32_16x16x32_bf16 v[120:123], a[144:147], v[40:43], v[200:203]
v_add_f32 v214, v214, v158
v_add_f32 v215, v215, v159
v_mfma_f32_16x16x32_bf16 v[124:127], a[144:147], v[44:47], v[204:207]
s_waitcnt lgkmcnt(2)
v_exp_f32 v96, v96
v_mfma_f32_16x16x32_bf16 v[124:127], a[148:151], v[60:63], v[124:127]
ds_read_b128 a[128:131], v232 offset:8192
v_add_f32 v208, v208, v96
v_mfma_f32_16x16x32_bf16 v[120:123], a[148:151], v[56:59], v[120:123]
v_exp_f32 v97, v97
v_mfma_f32_16x16x32_bf16 v[116:119], a[148:151], v[52:55], v[116:119]
v_exp_f32 v98, v98
v_mfma_f32_16x16x32_bf16 v[112:115], a[148:151], v[48:51], v[112:115]
s_waitcnt lgkmcnt(2)
v_exp_f32 v99, v99
v_mfma_f32_16x16x32_bf16 v[112:115], a[152:155], v[64:67], v[112:115]
ds_read_b128 a[132:135], v232 offset:9216
v_cvt_pk_bf16_f32 v160, v96, v97
v_mfma_f32_16x16x32_bf16 v[116:119], a[152:155], v[68:71], v[116:119]
v_exp_f32 v100, v100
v_mfma_f32_16x16x32_bf16 v[120:123], a[152:155], v[72:75], v[120:123]
v_exp_f32 v101, v101
v_mfma_f32_16x16x32_bf16 v[124:127], a[152:155], v[76:79], v[124:127]
s_waitcnt lgkmcnt(2)
v_exp_f32 v102, v102
v_mfma_f32_16x16x32_bf16 v[124:127], a[156:159], v[92:95], v[124:127]
ds_read_b128 a[136:139], v232 offset:10240
v_cvt_pk_bf16_f32 v161, v98, v99
v_mfma_f32_16x16x32_bf16 v[120:123], a[156:159], v[88:91], v[120:123]
v_exp_f32 v103, v103
v_mfma_f32_16x16x32_bf16 v[116:119], a[156:159], v[84:87], v[116:119]
buffer_load_dwordx4 a[196:199], v237, s[24:27], s47 offen sc1
v_mfma_f32_16x16x32_bf16 v[112:115], a[156:159], v[80:83], v[112:115]
s_waitcnt lgkmcnt(2)
v_exp_f32 v104, v104
v_mfma_f32_16x16x32_bf16 v[128:131], a[128:131], v[32:35], v[192:195]
ds_read_b128 a[140:143], v232 offset:11264
v_cvt_pk_bf16_f32 v164, v100, v101
v_mfma_f32_16x16x32_bf16 v[132:135], a[128:131], v[36:39], v[196:199]
v_exp_f32 v105, v105
v_mfma_f32_16x16x32_bf16 v[136:139], a[128:131], v[40:43], v[200:203]
v_exp_f32 v106, v106
v_mfma_f32_16x16x32_bf16 v[140:143], a[128:131], v[44:47], v[204:207]
s_waitcnt lgkmcnt(2)
v_exp_f32 v107, v107
v_mfma_f32_16x16x32_bf16 v[140:143], a[132:135], v[60:63], v[140:143]
ds_read_b128 a[144:147], v232 offset:12288
v_cvt_pk_bf16_f32 v165, v102, v103
v_mfma_f32_16x16x32_bf16 v[136:139], a[132:135], v[56:59], v[136:139]
s_add_u32 s54, s0, 1
s_and_b32 s54, s54, 7
s_mul_i32 s54, s54, 131072
v_mfma_f32_16x16x32_bf16 v[132:135], a[132:135], v[52:55], v[132:135]
s_add_u32 s54, s54, s85
s_add_u32 m0, s70, 0
v_cvt_pk_bf16_f32 v168, v104, v105
v_mfma_f32_16x16x32_bf16 v[128:131], a[132:135], v[48:51], v[128:131]
s_waitcnt lgkmcnt(2)
v_exp_f32 v108, v108
v_mfma_f32_16x16x32_bf16 v[128:131], a[136:139], v[64:67], v[128:131]
ds_read_b128 a[148:151], v232 offset:13312
v_cvt_pk_bf16_f32 v169, v106, v107
v_mfma_f32_16x16x32_bf16 v[132:135], a[136:139], v[68:71], v[132:135]
v_exp_f32 v109, v109
v_mfma_f32_16x16x32_bf16 v[136:139], a[136:139], v[72:75], v[136:139]
v_exp_f32 v110, v110
v_mfma_f32_16x16x32_bf16 v[140:143], a[136:139], v[76:79], v[140:143]
s_waitcnt lgkmcnt(2)
v_exp_f32 v111, v111
v_mfma_f32_16x16x32_bf16 v[140:143], a[140:143], v[92:95], v[140:143]
ds_read_b128 a[152:155], v232 offset:14336
v_exp_f32 v112, v112
v_mfma_f32_16x16x32_bf16 v[136:139], a[140:143], v[88:91], v[136:139]
v_exp_f32 v113, v113
v_mfma_f32_16x16x32_bf16 v[132:135], a[140:143], v[84:87], v[132:135]
buffer_load_dwordx4 a[200:203], v238, s[24:27], s47 offen sc1
v_mfma_f32_16x16x32_bf16 v[128:131], a[140:143], v[80:83], v[128:131]
s_waitcnt lgkmcnt(2)
v_exp_f32 v114, v114
v_mfma_f32_16x16x32_bf16 v[144:147], a[144:147], v[32:35], v[192:195]
ds_read_b128 a[156:159], v232 offset:15360
v_exp_f32 v115, v115
v_mfma_f32_16x16x32_bf16 v[148:151], a[144:147], v[36:39], v[196:199]
v_exp_f32 v116, v116
v_mfma_f32_16x16x32_bf16 v[152:155], a[144:147], v[40:43], v[200:203]
v_exp_f32 v117, v117
v_mfma_f32_16x16x32_bf16 v[156:159], a[144:147], v[44:47], v[204:207]
s_waitcnt lgkmcnt(2)
v_exp_f32 v118, v118
v_mfma_f32_16x16x32_bf16 v[156:159], a[148:151], v[60:63], v[156:159]
ds_read_b128 a[128:131], v234 offset:1024
v_add_u32 v233, 98304, v2
v_cvt_pk_bf16_f32 v172, v108, v109
v_mfma_f32_16x16x32_bf16 v[152:155], a[148:151], v[56:59], v[152:155]
v_exp_f32 v119, v119
v_mfma_f32_16x16x32_bf16 v[148:151], a[148:151], v[52:55], v[148:151]
v_exp_f32 v120, v120
v_mfma_f32_16x16x32_bf16 v[144:147], a[148:151], v[48:51], v[144:147]
s_waitcnt lgkmcnt(2)
v_exp_f32 v121, v121
v_mfma_f32_16x16x32_bf16 v[144:147], a[152:155], v[64:67], v[144:147]
ds_read_b128 a[132:135], v234 offset:3072
v_exp_f32 v122, v122
v_mfma_f32_16x16x32_bf16 v[148:151], a[152:155], v[68:71], v[148:151]
v_exp_f32 v123, v123
v_mfma_f32_16x16x32_bf16 v[152:155], a[152:155], v[72:75], v[152:155]
v_exp_f32 v124, v124
v_mfma_f32_16x16x32_bf16 v[156:159], a[152:155], v[76:79], v[156:159]
s_waitcnt lgkmcnt(2)
v_exp_f32 v125, v125
v_mfma_f32_16x16x32_bf16 v[156:159], a[156:159], v[92:95], v[156:159]
ds_read_b128 a[136:139], v234 offset:5120
v_exp_f32 v126, v126
v_mfma_f32_16x16x32_bf16 v[152:155], a[156:159], v[88:91], v[152:155]
v_exp_f32 v127, v127
v_mfma_f32_16x16x32_bf16 v[148:151], a[156:159], v[84:87], v[148:151]
buffer_load_dwordx4 a[204:207], v239, s[24:27], s47 offen sc1
v_mfma_f32_16x16x32_bf16 v[144:147], a[156:159], v[80:83], v[144:147]
s_waitcnt lgkmcnt(2)
s_add_u32 s47, s47, 4096
v_cvt_pk_bf16_f32 v173, v110, v111
v_mfma_f32_16x16x32_bf16 a[0:3], a[128:131], v[176:179], a[0:3]
ds_read_b128 a[140:143], v234 offset:7168
v_add_f32 v209, v209, v97
v_add_f32 v208, v208, v98
v_mfma_f32_16x16x32_bf16 a[4:7], a[128:131], v[180:183], a[4:7]
v_add_f32 v209, v209, v99
v_add_f32 v210, v210, v100
v_mfma_f32_16x16x32_bf16 a[8:11], a[128:131], v[184:187], a[8:11]
v_add_f32 v211, v211, v101
v_add_f32 v210, v210, v102
v_mfma_f32_16x16x32_bf16 a[12:15], a[128:131], v[188:191], a[12:15]
s_waitcnt lgkmcnt(2)
v_add_f32 v211, v211, v103
v_add_f32 v212, v212, v104
v_mfma_f32_16x16x32_bf16 a[28:31], a[132:135], v[188:191], a[28:31]
ds_read_b128 a[144:147], v234 offset:9216
v_add_f32 v213, v213, v105
v_add_f32 v212, v212, v106
v_mfma_f32_16x16x32_bf16 a[24:27], a[132:135], v[184:187], a[24:27]
v_add_f32 v213, v213, v107
v_add_f32 v214, v214, v108
v_mfma_f32_16x16x32_bf16 a[20:23], a[132:135], v[180:183], a[20:23]
v_add_f32 v215, v215, v109
v_add_f32 v214, v214, v110
v_mfma_f32_16x16x32_bf16 a[16:19], a[132:135], v[176:179], a[16:19]
s_waitcnt lgkmcnt(2)
v_add_f32 v215, v215, v111
v_cvt_pk_bf16_f32 v162, v112, v113
v_mfma_f32_16x16x32_bf16 a[32:35], a[136:139], v[176:179], a[32:35]
ds_read_b128 a[148:151], v234 offset:11264
v_cvt_pk_bf16_f32 v163, v114, v115
v_cvt_pk_bf16_f32 v166, v116, v117
v_mfma_f32_16x16x32_bf16 a[36:39], a[136:139], v[180:183], a[36:39]
v_cvt_pk_bf16_f32 v167, v118, v119
v_cvt_pk_bf16_f32 v170, v120, v121
v_mfma_f32_16x16x32_bf16 a[40:43], a[136:139], v[184:187], a[40:43]
v_cvt_pk_bf16_f32 v171, v122, v123
v_cvt_pk_bf16_f32 v174, v124, v125
v_mfma_f32_16x16x32_bf16 a[44:47], a[136:139], v[188:191], a[44:47]
s_waitcnt vmcnt(16)
s_barrier
s_waitcnt lgkmcnt(2)
v_mfma_f32_16x16x32_bf16 a[60:63], a[140:143], v[188:191], a[60:63]
ds_read_b128 a[152:155], v234 offset:13312
v_cvt_pk_bf16_f32 v175, v126, v127
v_add_f32 v208, v208, v112
v_mfma_f32_16x16x32_bf16 a[56:59], a[140:143], v[184:187], a[56:59]
v_add_f32 v209, v209, v113
v_add_f32 v208, v208, v114
v_mfma_f32_16x16x32_bf16 a[52:55], a[140:143], v[180:183], a[52:55]
buffer_load_dwordx4 v13, s[88:91], s54 offen lds
v_mfma_f32_16x16x32_bf16 a[48:51], a[140:143], v[176:179], a[48:51]
s_waitcnt lgkmcnt(2)
s_add_u32 m0, s70, 896
v_add_f32 v209, v209, v115
v_mfma_f32_16x16x32_bf16 a[64:67], a[144:147], v[176:179], a[64:67]
ds_read_b128 a[156:159], v234 offset:15360
v_add_f32 v210, v210, v116
v_add_f32 v211, v211, v117
v_mfma_f32_16x16x32_bf16 a[68:71], a[144:147], v[180:183], a[68:71]
v_add_f32 v210, v210, v118
v_add_f32 v211, v211, v119
v_mfma_f32_16x16x32_bf16 a[72:75], a[144:147], v[184:187], a[72:75]
v_add_f32 v212, v212, v120
v_add_f32 v213, v213, v121
v_mfma_f32_16x16x32_bf16 a[76:79], a[144:147], v[188:191], a[76:79]
s_waitcnt lgkmcnt(2)
v_add_f32 v212, v212, v122
v_add_f32 v213, v213, v123
v_mfma_f32_16x16x32_bf16 a[92:95], a[148:151], v[188:191], a[92:95]
ds_read_b128 a[128:131], v233 offset:0
v_add_f32 v214, v214, v124
v_add_f32 v215, v215, v125
v_mfma_f32_16x16x32_bf16 a[88:91], a[148:151], v[184:187], a[88:91]
buffer_load_dwordx4 v13, s[88:91], s54 offen offset:128 lds
v_mfma_f32_16x16x32_bf16 a[84:87], a[148:151], v[180:183], a[84:87]
s_add_u32 s54, s54, 16384
s_add_u32 m0, s70, 2048
v_add_f32 v214, v214, v126
v_mfma_f32_16x16x32_bf16 a[80:83], a[148:151], v[176:179], a[80:83]
s_waitcnt lgkmcnt(2)
v_add_f32 v215, v215, v127
v_add_u32 v235, 114688, v2
v_mfma_f32_16x16x32_bf16 a[96:99], a[152:155], v[176:179], a[96:99]
ds_read_b128 a[132:135], v233 offset:2048
v_exp_f32 v128, v128
v_mfma_f32_16x16x32_bf16 a[100:103], a[152:155], v[180:183], a[100:103]
v_exp_f32 v129, v129
v_mfma_f32_16x16x32_bf16 a[104:107], a[152:155], v[184:187], a[104:107]
v_exp_f32 v130, v130
v_mfma_f32_16x16x32_bf16 a[108:111], a[152:155], v[188:191], a[108:111]
s_waitcnt lgkmcnt(2)
v_exp_f32 v131, v131
v_mfma_f32_16x16x32_bf16 a[124:127], a[156:159], v[188:191], a[124:127]
ds_read_b128 a[136:139], v233 offset:4096
v_exp_f32 v132, v132
v_mfma_f32_16x16x32_bf16 a[120:123], a[156:159], v[184:187], a[120:123]
buffer_load_dwordx4 v13, s[88:91], s54 offen lds
v_mfma_f32_16x16x32_bf16 a[116:119], a[156:159], v[180:183], a[116:119]
s_add_u32 m0, s70, 2944
v_exp_f32 v133, v133
v_mfma_f32_16x16x32_bf16 a[112:115], a[156:159], v[176:179], a[112:115]
s_waitcnt lgkmcnt(2)
v_exp_f32 v134, v134
v_mfma_f32_16x16x32_bf16 a[0:3], a[128:131], v[160:163], a[0:3]
ds_read_b128 a[140:143], v233 offset:6144
v_exp_f32 v135, v135
v_mfma_f32_16x16x32_bf16 a[4:7], a[128:131], v[164:167], a[4:7]
v_exp_f32 v136, v136
v_mfma_f32_16x16x32_bf16 a[8:11], a[128:131], v[168:171], a[8:11]
buffer_load_dwordx4 v13, s[88:91], s54 offen offset:128 lds
v_mfma_f32_16x16x32_bf16 a[12:15], a[128:131], v[172:175], a[12:15]
s_waitcnt lgkmcnt(2)
s_add_u32 s54, s0, 2
s_and_b32 s54, s54, 7
v_mfma_f32_16x16x32_bf16 a[28:31], a[132:135], v[172:175], a[28:31]
ds_read_b128 a[144:147], v233 offset:8192
s_mul_i32 s54, s54, 131072
s_add_u32 s54, s54, s85
v_mfma_f32_16x16x32_bf16 a[24:27], a[132:135], v[168:171], a[24:27]
s_add_u32 m0, s70, 16384
v_exp_f32 v137, v137
v_mfma_f32_16x16x32_bf16 a[20:23], a[132:135], v[164:167], a[20:23]
v_exp_f32 v138, v138
v_mfma_f32_16x16x32_bf16 a[16:19], a[132:135], v[160:163], a[16:19]
s_waitcnt lgkmcnt(2)
v_exp_f32 v139, v139
v_mfma_f32_16x16x32_bf16 a[32:35], a[136:139], v[160:163], a[32:35]
ds_read_b128 a[148:151], v233 offset:10240
v_exp_f32 v140, v140
v_mfma_f32_16x16x32_bf16 a[36:39], a[136:139], v[164:167], a[36:39]
buffer_load_dwordx4 v13, s[88:91], s54 offen lds
v_mfma_f32_16x16x32_bf16 a[40:43], a[136:139], v[168:171], a[40:43]
s_add_u32 m0, s70, 17280
v_exp_f32 v141, v141
v_mfma_f32_16x16x32_bf16 a[44:47], a[136:139], v[172:175], a[44:47]
s_waitcnt lgkmcnt(2)
v_exp_f32 v142, v142
v_mfma_f32_16x16x32_bf16 a[60:63], a[140:143], v[172:175], a[60:63]
ds_read_b128 a[152:155], v233 offset:12288
v_exp_f32 v143, v143
v_mfma_f32_16x16x32_bf16 a[56:59], a[140:143], v[168:171], a[56:59]
v_exp_f32 v144, v144
v_mfma_f32_16x16x32_bf16 a[52:55], a[140:143], v[164:167], a[52:55]
buffer_load_dwordx4 v13, s[88:91], s54 offen offset:128 lds
v_mfma_f32_16x16x32_bf16 a[48:51], a[140:143], v[160:163], a[48:51]
s_waitcnt lgkmcnt(2)
s_add_u32 s54, s54, 16384
s_add_u32 m0, s70, 18432
v_mfma_f32_16x16x32_bf16 a[64:67], a[144:147], v[160:163], a[64:67]
ds_read_b128 a[156:159], v233 offset:14336
v_exp_f32 v145, v145
v_mfma_f32_16x16x32_bf16 a[68:71], a[144:147], v[164:167], a[68:71]
v_exp_f32 v146, v146
v_mfma_f32_16x16x32_bf16 a[72:75], a[144:147], v[168:171], a[72:75]
v_exp_f32 v147, v147
v_mfma_f32_16x16x32_bf16 a[76:79], a[144:147], v[172:175], a[76:79]
s_waitcnt lgkmcnt(2)
v_exp_f32 v148, v148
v_mfma_f32_16x16x32_bf16 a[92:95], a[148:151], v[172:175], a[92:95]
ds_read_b128 a[128:131], v235 offset:0
v_exp_f32 v149, v149
v_mfma_f32_16x16x32_bf16 a[88:91], a[148:151], v[168:171], a[88:91]
v_exp_f32 v150, v150
v_mfma_f32_16x16x32_bf16 a[84:87], a[148:151], v[164:167], a[84:87]
buffer_load_dwordx4 v13, s[88:91], s54 offen lds
v_mfma_f32_16x16x32_bf16 a[80:83], a[148:151], v[160:163], a[80:83]
s_waitcnt lgkmcnt(2)
s_add_u32 m0, s70, 19328
v_cvt_pk_bf16_f32 v176, v128, v129
v_mfma_f32_16x16x32_bf16 a[96:99], a[152:155], v[160:163], a[96:99]
ds_read_b128 a[132:135], v235 offset:1024
v_exp_f32 v151, v151
v_mfma_f32_16x16x32_bf16 a[100:103], a[152:155], v[164:167], a[100:103]
v_cvt_pk_bf16_f32 v177, v130, v131
v_cvt_pk_bf16_f32 v180, v132, v133
v_mfma_f32_16x16x32_bf16 a[104:107], a[152:155], v[168:171], a[104:107]
v_cvt_pk_bf16_f32 v181, v134, v135
v_cvt_pk_bf16_f32 v184, v136, v137
v_mfma_f32_16x16x32_bf16 a[108:111], a[152:155], v[172:175], a[108:111]
s_waitcnt lgkmcnt(2)
v_cvt_pk_bf16_f32 v185, v138, v139
v_cvt_pk_bf16_f32 v188, v140, v141
v_mfma_f32_16x16x32_bf16 a[124:127], a[156:159], v[172:175], a[124:127]
ds_read_b128 a[136:139], v235 offset:2048
v_cvt_pk_bf16_f32 v189, v142, v143
v_add_f32 v208, v208, v128
v_mfma_f32_16x16x32_bf16 a[120:123], a[156:159], v[168:171], a[120:123]
buffer_load_dwordx4 v13, s[88:91], s54 offen offset:128 lds
v_mfma_f32_16x16x32_bf16 a[116:119], a[156:159], v[164:167], a[116:119]
v_add_f32 v209, v209, v129
v_add_f32 v208, v208, v130
v_add_f32 v209, v209, v131
v_add_f32 v210, v210, v132
v_add_f32 v211, v211, v133
v_add_f32 v210, v210, v134
v_add_f32 v211, v211, v135
v_add_f32 v212, v212, v136
v_add_f32 v213, v213, v137
v_add_f32 v212, v212, v138
v_add_f32 v213, v213, v139
v_add_f32 v214, v214, v140
v_add_f32 v215, v215, v141
v_add_f32 v214, v214, v142
v_add_f32 v215, v215, v143
v_mfma_f32_16x16x32_bf16 a[112:115], a[156:159], v[160:163], a[112:115]
s_waitcnt lgkmcnt(2)
v_cvt_pk_bf16_f32 v178, v144, v145
v_cvt_pk_bf16_f32 v179, v146, v147
v_cvt_pk_bf16_f32 v182, v148, v149
v_cvt_pk_bf16_f32 v183, v150, v151
v_add_f32 v208, v208, v144
v_add_f32 v209, v209, v145
v_add_f32 v208, v208, v146
v_add_f32 v209, v209, v147
v_add_f32 v210, v210, v148
v_add_f32 v211, v211, v149
v_add_f32 v210, v210, v150
v_add_f32 v211, v211, v151
v_mfma_f32_16x16x32_bf16 v[96:99], a[128:131], v[32:35], v[192:195]
ds_read_b128 a[140:143], v235 offset:3072
v_mfma_f32_16x16x32_bf16 v[100:103], a[128:131], v[36:39], v[196:199]
v_exp_f32 v152, v152
v_mfma_f32_16x16x32_bf16 v[104:107], a[128:131], v[40:43], v[200:203]
v_exp_f32 v153, v153
v_mfma_f32_16x16x32_bf16 v[108:111], a[128:131], v[44:47], v[204:207]
s_waitcnt lgkmcnt(2)
v_exp_f32 v154, v154
v_mfma_f32_16x16x32_bf16 v[108:111], a[132:135], v[60:63], v[108:111]
ds_read_b128 a[144:147], v235 offset:4096
v_cvt_pk_bf16_f32 v186, v152, v153
v_mfma_f32_16x16x32_bf16 v[104:107], a[132:135], v[56:59], v[104:107]
v_exp_f32 v155, v155
v_mfma_f32_16x16x32_bf16 v[100:103], a[132:135], v[52:55], v[100:103]
v_exp_f32 v156, v156
v_mfma_f32_16x16x32_bf16 v[96:99], a[132:135], v[48:51], v[96:99]
s_waitcnt lgkmcnt(2)
v_exp_f32 v157, v157
v_mfma_f32_16x16x32_bf16 v[96:99], a[136:139], v[64:67], v[96:99]
ds_read_b128 a[148:151], v235 offset:5120
v_cvt_pk_bf16_f32 v187, v154, v155
v_mfma_f32_16x16x32_bf16 v[100:103], a[136:139], v[68:71], v[100:103]
buffer_load_dwordx4 a[208:211], v236, s[24:27], s47 offen sc1
v_mfma_f32_16x16x32_bf16 v[104:107], a[136:139], v[72:75], v[104:107]
v_exp_f32 v158, v158
v_mfma_f32_16x16x32_bf16 v[108:111], a[136:139], v[76:79], v[108:111]
s_waitcnt lgkmcnt(2)
v_exp_f32 v159, v159
v_mfma_f32_16x16x32_bf16 v[108:111], a[140:143], v[92:95], v[108:111]
ds_read_b128 a[152:155], v235 offset:6144
v_cvt_pk_bf16_f32 v190, v156, v157
v_mfma_f32_16x16x32_bf16 v[104:107], a[140:143], v[88:91], v[104:107]
v_cvt_pk_bf16_f32 v191, v158, v159
v_add_f32 v212, v212, v152
v_mfma_f32_16x16x32_bf16 v[100:103], a[140:143], v[84:87], v[100:103]
v_add_f32 v213, v213, v153
v_add_f32 v212, v212, v154
v_mfma_f32_16x16x32_bf16 v[96:99], a[140:143], v[80:83], v[96:99]
s_waitcnt lgkmcnt(2)
v_add_f32 v213, v213, v155
v_add_f32 v214, v214, v156
v_mfma_f32_16x16x32_bf16 v[112:115], a[144:147], v[32:35], v[192:195]
ds_read_b128 a[156:159], v235 offset:7168
v_add_f32 v215, v215, v157
v_mfma_f32_16x16x32_bf16 v[116:119], a[144:147], v[36:39], v[196:199]
buffer_load_dwordx4 a[212:215], v237, s[24:27], s47 offen sc1
v_mfma_f32_16x16x32_bf16 v[120:123], a[144:147], v[40:43], v[200:203]
v_add_f32 v214, v214, v158
v_add_f32 v215, v215, v159
v_mfma_f32_16x16x32_bf16 v[124:127], a[144:147], v[44:47], v[204:207]
s_waitcnt lgkmcnt(2)
v_exp_f32 v96, v96
v_mfma_f32_16x16x32_bf16 v[124:127], a[148:151], v[60:63], v[124:127]
ds_read_b128 a[128:131], v235 offset:8192
v_add_f32 v208, v208, v96
v_mfma_f32_16x16x32_bf16 v[120:123], a[148:151], v[56:59], v[120:123]
v_exp_f32 v97, v97
v_mfma_f32_16x16x32_bf16 v[116:119], a[148:151], v[52:55], v[116:119]
v_exp_f32 v98, v98
v_mfma_f32_16x16x32_bf16 v[112:115], a[148:151], v[48:51], v[112:115]
s_waitcnt lgkmcnt(2)
v_exp_f32 v99, v99
v_mfma_f32_16x16x32_bf16 v[112:115], a[152:155], v[64:67], v[112:115]
ds_read_b128 a[132:135], v235 offset:9216
v_cvt_pk_bf16_f32 v160, v96, v97
v_mfma_f32_16x16x32_bf16 v[116:119], a[152:155], v[68:71], v[116:119]
buffer_load_dwordx4 a[216:219], v238, s[24:27], s47 offen sc1
v_mfma_f32_16x16x32_bf16 v[120:123], a[152:155], v[72:75], v[120:123]
v_exp_f32 v100, v100
v_mfma_f32_16x16x32_bf16 v[124:127], a[152:155], v[76:79], v[124:127]
s_waitcnt lgkmcnt(2)
v_exp_f32 v101, v101
v_mfma_f32_16x16x32_bf16 v[124:127], a[156:159], v[92:95], v[124:127]
ds_read_b128 a[136:139], v235 offset:10240
v_cvt_pk_bf16_f32 v161, v98, v99
v_mfma_f32_16x16x32_bf16 v[120:123], a[156:159], v[88:91], v[120:123]
v_exp_f32 v102, v102
v_mfma_f32_16x16x32_bf16 v[116:119], a[156:159], v[84:87], v[116:119]
v_exp_f32 v103, v103
v_mfma_f32_16x16x32_bf16 v[112:115], a[156:159], v[80:83], v[112:115]
s_waitcnt lgkmcnt(2)
v_exp_f32 v104, v104
v_mfma_f32_16x16x32_bf16 v[128:131], a[128:131], v[32:35], v[192:195]
ds_read_b128 a[140:143], v235 offset:11264
v_cvt_pk_bf16_f32 v164, v100, v101
v_mfma_f32_16x16x32_bf16 v[132:135], a[128:131], v[36:39], v[196:199]
buffer_load_dwordx4 a[220:223], v239, s[24:27], s47 offen sc1
v_mfma_f32_16x16x32_bf16 v[136:139], a[128:131], v[40:43], v[200:203]
s_add_u32 s47, s0, 2
s_and_b32 s47, s47, 7
s_mul_i32 s47, s47, 8192
v_mfma_f32_16x16x32_bf16 v[140:143], a[128:131], v[44:47], v[204:207]
s_waitcnt lgkmcnt(2)
s_add_u32 s47, s47, s58
v_cvt_pk_bf16_f32 v165, v102, v103
v_mfma_f32_16x16x32_bf16 v[140:143], a[132:135], v[60:63], v[140:143]
ds_read_b128 a[144:147], v235 offset:12288
v_add_f32 v209, v209, v97
v_mfma_f32_16x16x32_bf16 v[136:139], a[132:135], v[56:59], v[136:139]
s_add_u32 s54, s0, 3
s_and_b32 s54, s54, 7
s_mul_i32 s54, s54, 131072
v_mfma_f32_16x16x32_bf16 v[132:135], a[132:135], v[52:55], v[132:135]
s_add_u32 s54, s54, s85
s_add_u32 m0, s70, 32768
v_add_f32 v208, v208, v98
v_mfma_f32_16x16x32_bf16 v[128:131], a[132:135], v[48:51], v[128:131]
s_waitcnt lgkmcnt(2)
v_exp_f32 v105, v105
v_mfma_f32_16x16x32_bf16 v[128:131], a[136:139], v[64:67], v[128:131]
ds_read_b128 a[148:151], v235 offset:13312
v_cvt_pk_bf16_f32 v168, v104, v105
v_mfma_f32_16x16x32_bf16 v[132:135], a[136:139], v[68:71], v[132:135]
v_exp_f32 v106, v106
v_mfma_f32_16x16x32_bf16 v[136:139], a[136:139], v[72:75], v[136:139]
buffer_load_dwordx4 a[224:227], v236, s[24:27], s47 offen sc1
v_mfma_f32_16x16x32_bf16 v[140:143], a[136:139], v[76:79], v[140:143]
s_waitcnt lgkmcnt(2)
v_exp_f32 v107, v107
v_mfma_f32_16x16x32_bf16 v[140:143], a[140:143], v[92:95], v[140:143]
ds_read_b128 a[152:155], v235 offset:14336
v_exp_f32 v108, v108
v_mfma_f32_16x16x32_bf16 v[136:139], a[140:143], v[88:91], v[136:139]
v_exp_f32 v109, v109
v_mfma_f32_16x16x32_bf16 v[132:135], a[140:143], v[84:87], v[132:135]
v_exp_f32 v110, v110
v_mfma_f32_16x16x32_bf16 v[128:131], a[140:143], v[80:83], v[128:131]
s_waitcnt lgkmcnt(2)
v_exp_f32 v111, v111
v_mfma_f32_16x16x32_bf16 v[144:147], a[144:147], v[32:35], v[192:195]
ds_read_b128 a[156:159], v235 offset:15360
v_exp_f32 v112, v112
v_mfma_f32_16x16x32_bf16 v[148:151], a[144:147], v[36:39], v[196:199]
v_exp_f32 v113, v113
v_mfma_f32_16x16x32_bf16 v[152:155], a[144:147], v[40:43], v[200:203]
buffer_load_dwordx4 a[228:231], v237, s[24:27], s47 offen sc1
v_mfma_f32_16x16x32_bf16 v[156:159], a[144:147], v[44:47], v[204:207]
s_waitcnt lgkmcnt(2)
v_exp_f32 v114, v114
v_mfma_f32_16x16x32_bf16 v[156:159], a[148:151], v[60:63], v[156:159]
ds_read_b128 a[128:131], v233 offset:1024
v_add_u32 v234, 131072, v2
v_cvt_pk_bf16_f32 v169, v106, v107
v_mfma_f32_16x16x32_bf16 v[152:155], a[148:151], v[56:59], v[152:155]
v_exp_f32 v115, v115
v_mfma_f32_16x16x32_bf16 v[148:151], a[148:151], v[52:55], v[148:151]
v_exp_f32 v116, v116
v_mfma_f32_16x16x32_bf16 v[144:147], a[148:151], v[48:51], v[144:147]
s_waitcnt lgkmcnt(2)
v_exp_f32 v117, v117
v_mfma_f32_16x16x32_bf16 v[144:147], a[152:155], v[64:67], v[144:147]
ds_read_b128 a[132:135], v233 offset:3072
v_exp_f32 v118, v118
v_mfma_f32_16x16x32_bf16 v[148:151], a[152:155], v[68:71], v[148:151]
v_exp_f32 v119, v119
v_mfma_f32_16x16x32_bf16 v[152:155], a[152:155], v[72:75], v[152:155]
buffer_load_dwordx4 a[232:235], v238, s[24:27], s47 offen sc1
v_mfma_f32_16x16x32_bf16 v[156:159], a[152:155], v[76:79], v[156:159]
s_waitcnt lgkmcnt(2)
v_exp_f32 v120, v120
v_mfma_f32_16x16x32_bf16 v[156:159], a[156:159], v[92:95], v[156:159]
ds_read_b128 a[136:139], v233 offset:5120
v_exp_f32 v121, v121
v_mfma_f32_16x16x32_bf16 v[152:155], a[156:159], v[88:91], v[152:155]
v_exp_f32 v122, v122
v_mfma_f32_16x16x32_bf16 v[148:151], a[156:159], v[84:87], v[148:151]
v_exp_f32 v123, v123
v_mfma_f32_16x16x32_bf16 v[144:147], a[156:159], v[80:83], v[144:147]
s_waitcnt lgkmcnt(2)
v_exp_f32 v124, v124
v_mfma_f32_16x16x32_bf16 a[0:3], a[128:131], v[176:179], a[0:3]
ds_read_b128 a[140:143], v233 offset:7168
v_exp_f32 v125, v125
v_mfma_f32_16x16x32_bf16 a[4:7], a[128:131], v[180:183], a[4:7]
v_exp_f32 v126, v126
v_mfma_f32_16x16x32_bf16 a[8:11], a[128:131], v[184:187], a[8:11]
buffer_load_dwordx4 a[236:239], v239, s[24:27], s47 offen sc1
v_mfma_f32_16x16x32_bf16 a[12:15], a[128:131], v[188:191], a[12:15]
s_waitcnt lgkmcnt(2)
s_add_u32 s47, s47, 4096
v_cvt_pk_bf16_f32 v172, v108, v109
v_mfma_f32_16x16x32_bf16 a[28:31], a[132:135], v[188:191], a[28:31]
ds_read_b128 a[144:147], v233 offset:9216
v_exp_f32 v127, v127
v_mfma_f32_16x16x32_bf16 a[24:27], a[132:135], v[184:187], a[24:27]
v_cvt_pk_bf16_f32 v173, v110, v111
v_add_f32 v209, v209, v99
v_mfma_f32_16x16x32_bf16 a[20:23], a[132:135], v[180:183], a[20:23]
v_add_f32 v210, v210, v100
v_add_f32 v211, v211, v101
v_mfma_f32_16x16x32_bf16 a[16:19], a[132:135], v[176:179], a[16:19]
s_waitcnt lgkmcnt(2)
v_add_f32 v210, v210, v102
v_add_f32 v211, v211, v103
v_mfma_f32_16x16x32_bf16 a[32:35], a[136:139], v[176:179], a[32:35]
ds_read_b128 a[148:151], v233 offset:11264
v_add_f32 v212, v212, v104
v_add_f32 v213, v213, v105
v_mfma_f32_16x16x32_bf16 a[36:39], a[136:139], v[180:183], a[36:39]
v_add_f32 v212, v212, v106
v_add_f32 v213, v213, v107
v_mfma_f32_16x16x32_bf16 a[40:43], a[136:139], v[184:187], a[40:43]
v_add_f32 v214, v214, v108
v_add_f32 v215, v215, v109
v_mfma_f32_16x16x32_bf16 a[44:47], a[136:139], v[188:191], a[44:47]
s_waitcnt vmcnt(20)
s_barrier
s_waitcnt lgkmcnt(2)
v_mfma_f32_16x16x32_bf16 a[60:63], a[140:143], v[188:191], a[60:63]
ds_read_b128 a[152:155], v233 offset:13312
v_add_f32 v214, v214, v110
v_add_f32 v215, v215, v111
v_mfma_f32_16x16x32_bf16 a[56:59], a[140:143], v[184:187], a[56:59]
v_cvt_pk_bf16_f32 v162, v112, v113
v_cvt_pk_bf16_f32 v163, v114, v115
v_mfma_f32_16x16x32_bf16 a[52:55], a[140:143], v[180:183], a[52:55]
buffer_load_dwordx4 v13, s[88:91], s54 offen lds
v_mfma_f32_16x16x32_bf16 a[48:51], a[140:143], v[176:179], a[48:51]
s_waitcnt lgkmcnt(2)
s_add_u32 m0, s70, 33664
v_cvt_pk_bf16_f32 v166, v116, v117
v_mfma_f32_16x16x32_bf16 a[64:67], a[144:147], v[176:179], a[64:67]
ds_read_b128 a[156:159], v233 offset:15360
v_cvt_pk_bf16_f32 v167, v118, v119
v_cvt_pk_bf16_f32 v170, v120, v121
v_mfma_f32_16x16x32_bf16 a[68:71], a[144:147], v[180:183], a[68:71]
v_cvt_pk_bf16_f32 v171, v122, v123
v_cvt_pk_bf16_f32 v174, v124, v125
v_mfma_f32_16x16x32_bf16 a[72:75], a[144:147], v[184:187], a[72:75]
v_cvt_pk_bf16_f32 v175, v126, v127
v_add_f32 v208, v208, v112
v_mfma_f32_16x16x32_bf16 a[76:79], a[144:147], v[188:191], a[76:79]
s_waitcnt lgkmcnt(2)
v_add_f32 v209, v209, v113
v_add_f32 v208, v208, v114
v_mfma_f32_16x16x32_bf16 a[92:95], a[148:151], v[188:191], a[92:95]
ds_read_b128 a[128:131], v234 offset:0
v_add_f32 v209, v209, v115
v_add_f32 v210, v210, v116
v_mfma_f32_16x16x32_bf16 a[88:91], a[148:151], v[184:187], a[88:91]
buffer_load_dwordx4 v13, s[88:91], s54 offen offset:128 lds
v_mfma_f32_16x16x32_bf16 a[84:87], a[148:151], v[180:183], a[84:87]
s_add_u32 s54, s54, 16384
s_add_u32 m0, s70, 34816
v_add_f32 v211, v211, v117
v_mfma_f32_16x16x32_bf16 a[80:83], a[148:151], v[176:179], a[80:83]
s_waitcnt lgkmcnt(2)
v_add_f32 v210, v210, v118
v_add_f32 v211, v211, v119
v_mfma_f32_16x16x32_bf16 a[96:99], a[152:155], v[176:179], a[96:99]
ds_read_b128 a[132:135], v234 offset:2048
v_add_f32 v212, v212, v120
v_add_f32 v213, v213, v121
v_mfma_f32_16x16x32_bf16 a[100:103], a[152:155], v[180:183], a[100:103]
v_add_f32 v212, v212, v122
v_add_f32 v213, v213, v123
v_mfma_f32_16x16x32_bf16 a[104:107], a[152:155], v[184:187], a[104:107]
v_add_f32 v214, v214, v124
v_add_f32 v215, v215, v125
v_mfma_f32_16x16x32_bf16 a[108:111], a[152:155], v[188:191], a[108:111]
s_waitcnt lgkmcnt(2)
v_add_f32 v214, v214, v126
v_add_f32 v215, v215, v127
v_mfma_f32_16x16x32_bf16 a[124:127], a[156:159], v[188:191], a[124:127]
ds_read_b128 a[136:139], v234 offset:4096
v_add_u32 v18, 147456, v14
v_add_u32 v19, 147456, v15
v_mfma_f32_16x16x32_bf16 a[120:123], a[156:159], v[184:187], a[120:123]
buffer_load_dwordx4 v13, s[88:91], s54 offen lds
v_mfma_f32_16x16x32_bf16 a[116:119], a[156:159], v[180:183], a[116:119]
s_add_u32 m0, s70, 35712
v_add_u32 v20, 147456, v16
v_add_u32 v21, 147456, v17
v_mfma_f32_16x16x32_bf16 a[112:115], a[156:159], v[176:179], a[112:115]
s_waitcnt lgkmcnt(2)
v_exp_f32 v128, v128
v_mfma_f32_16x16x32_bf16 a[0:3], a[128:131], v[160:163], a[0:3]
ds_read_b128 a[140:143], v234 offset:6144
v_exp_f32 v129, v129
v_mfma_f32_16x16x32_bf16 a[4:7], a[128:131], v[164:167], a[4:7]
v_exp_f32 v130, v130
v_mfma_f32_16x16x32_bf16 a[8:11], a[128:131], v[168:171], a[8:11]
buffer_load_dwordx4 v13, s[88:91], s54 offen offset:128 lds
v_mfma_f32_16x16x32_bf16 a[12:15], a[128:131], v[172:175], a[12:15]
s_waitcnt lgkmcnt(2)
s_add_u32 s54, s0, 4
s_and_b32 s54, s54, 7
v_mfma_f32_16x16x32_bf16 a[28:31], a[132:135], v[172:175], a[28:31]
ds_read_b128 a[144:147], v234 offset:8192
s_mul_i32 s54, s54, 131072
s_add_u32 s54, s54, s85
v_mfma_f32_16x16x32_bf16 a[24:27], a[132:135], v[168:171], a[24:27]
s_add_u32 m0, s70, 49152
v_exp_f32 v131, v131
v_mfma_f32_16x16x32_bf16 a[20:23], a[132:135], v[164:167], a[20:23]
v_exp_f32 v132, v132
v_mfma_f32_16x16x32_bf16 a[16:19], a[132:135], v[160:163], a[16:19]
s_waitcnt lgkmcnt(2)
v_exp_f32 v133, v133
v_mfma_f32_16x16x32_bf16 a[32:35], a[136:139], v[160:163], a[32:35]
ds_read_b128 a[148:151], v234 offset:10240
v_exp_f32 v134, v134
v_mfma_f32_16x16x32_bf16 a[36:39], a[136:139], v[164:167], a[36:39]
buffer_load_dwordx4 v13, s[88:91], s54 offen lds
v_mfma_f32_16x16x32_bf16 a[40:43], a[136:139], v[168:171], a[40:43]
s_add_u32 m0, s70, 50048
v_exp_f32 v135, v135
v_mfma_f32_16x16x32_bf16 a[44:47], a[136:139], v[172:175], a[44:47]
s_waitcnt lgkmcnt(2)
v_exp_f32 v136, v136
v_mfma_f32_16x16x32_bf16 a[60:63], a[140:143], v[172:175], a[60:63]
ds_read_b128 a[152:155], v234 offset:12288
v_exp_f32 v137, v137
v_mfma_f32_16x16x32_bf16 a[56:59], a[140:143], v[168:171], a[56:59]
v_exp_f32 v138, v138
v_mfma_f32_16x16x32_bf16 a[52:55], a[140:143], v[164:167], a[52:55]
buffer_load_dwordx4 v13, s[88:91], s54 offen offset:128 lds
v_mfma_f32_16x16x32_bf16 a[48:51], a[140:143], v[160:163], a[48:51]
s_waitcnt lgkmcnt(2)
s_add_u32 s54, s54, 16384
s_add_u32 m0, s70, 51200
v_mfma_f32_16x16x32_bf16 a[64:67], a[144:147], v[160:163], a[64:67]
ds_read_b128 a[156:159], v234 offset:14336
v_exp_f32 v139, v139
v_mfma_f32_16x16x32_bf16 a[68:71], a[144:147], v[164:167], a[68:71]
v_exp_f32 v140, v140
v_mfma_f32_16x16x32_bf16 a[72:75], a[144:147], v[168:171], a[72:75]
v_exp_f32 v141, v141
v_mfma_f32_16x16x32_bf16 a[76:79], a[144:147], v[172:175], a[76:79]
s_waitcnt lgkmcnt(2)
v_exp_f32 v142, v142
v_mfma_f32_16x16x32_bf16 a[92:95], a[148:151], v[172:175], a[92:95]
v_exp_f32 v143, v143
v_mfma_f32_16x16x32_bf16 a[88:91], a[148:151], v[168:171], a[88:91]
v_exp_f32 v144, v144
v_mfma_f32_16x16x32_bf16 a[84:87], a[148:151], v[164:167], a[84:87]
buffer_load_dwordx4 v13, s[88:91], s54 offen lds
v_mfma_f32_16x16x32_bf16 a[80:83], a[148:151], v[160:163], a[80:83]
s_waitcnt lgkmcnt(1)
s_add_u32 m0, s70, 52096
v_cvt_pk_bf16_f32 v176, v128, v129
v_mfma_f32_16x16x32_bf16 a[96:99], a[152:155], v[160:163], a[96:99]
v_exp_f32 v145, v145
v_mfma_f32_16x16x32_bf16 a[100:103], a[152:155], v[164:167], a[100:103]
v_exp_f32 v146, v146
v_mfma_f32_16x16x32_bf16 a[104:107], a[152:155], v[168:171], a[104:107]
v_exp_f32 v147, v147
v_mfma_f32_16x16x32_bf16 a[108:111], a[152:155], v[172:175], a[108:111]
s_waitcnt lgkmcnt(0)
v_exp_f32 v148, v148
v_mfma_f32_16x16x32_bf16 a[124:127], a[156:159], v[172:175], a[124:127]
v_exp_f32 v149, v149
v_exp_f32 v150, v150
v_exp_f32 v151, v151
v_mfma_f32_16x16x32_bf16 a[120:123], a[156:159], v[168:171], a[120:123]
buffer_load_dwordx4 v13, s[88:91], s54 offen offset:128 lds
v_mfma_f32_16x16x32_bf16 a[116:119], a[156:159], v[164:167], a[116:119]
v_cvt_pk_bf16_f32 v177, v130, v131
v_cvt_pk_bf16_f32 v180, v132, v133
v_cvt_pk_bf16_f32 v181, v134, v135
v_cvt_pk_bf16_f32 v184, v136, v137
v_cvt_pk_bf16_f32 v185, v138, v139
v_cvt_pk_bf16_f32 v188, v140, v141
v_cvt_pk_bf16_f32 v189, v142, v143
v_add_f32 v208, v208, v128
v_add_f32 v209, v209, v129
v_add_f32 v208, v208, v130
v_add_f32 v209, v209, v131
v_add_f32 v210, v210, v132
v_add_f32 v211, v211, v133
v_add_f32 v210, v210, v134
v_add_f32 v211, v211, v135
v_add_f32 v212, v212, v136
v_add_f32 v213, v213, v137
v_add_f32 v212, v212, v138
v_add_f32 v213, v213, v139
v_add_f32 v214, v214, v140
v_add_f32 v215, v215, v141
v_add_f32 v214, v214, v142
v_add_f32 v215, v215, v143
v_mfma_f32_16x16x32_bf16 a[112:115], a[156:159], v[160:163], a[112:115]
v_cvt_pk_bf16_f32 v178, v144, v145
v_cvt_pk_bf16_f32 v179, v146, v147
v_cvt_pk_bf16_f32 v182, v148, v149
v_cvt_pk_bf16_f32 v183, v150, v151
v_add_f32 v208, v208, v144
v_add_f32 v209, v209, v145
v_add_f32 v208, v208, v146
v_add_f32 v209, v209, v147
v_add_f32 v210, v210, v148
v_add_f32 v211, v211, v149
v_add_f32 v210, v210, v150
v_add_f32 v211, v211, v151
ds_read_b128 a[128:131], v234 offset:1024
ds_read_b128 a[132:135], v234 offset:3072
ds_read_b128 a[136:139], v234 offset:5120
s_waitcnt lgkmcnt(2)
v_mfma_f32_16x16x32_bf16 a[0:3], a[128:131], v[176:179], a[0:3]
v_mfma_f32_16x16x32_bf16 a[4:7], a[128:131], v[180:183], a[4:7]
ds_read_b128 a[140:143], v234 offset:7168
s_waitcnt lgkmcnt(2)
v_mfma_f32_16x16x32_bf16 a[20:23], a[132:135], v[180:183], a[20:23]
ds_read_b128 a[144:147], v234 offset:9216
v_mfma_f32_16x16x32_bf16 a[16:19], a[132:135], v[176:179], a[16:19]
ds_read_b128 a[148:151], v234 offset:11264
s_waitcnt lgkmcnt(3)
v_mfma_f32_16x16x32_bf16 a[32:35], a[136:139], v[176:179], a[32:35]
ds_read_b128 a[152:155], v234 offset:13312
v_mfma_f32_16x16x32_bf16 a[36:39], a[136:139], v[180:183], a[36:39]
v_exp_f32 v152, v152
s_waitcnt lgkmcnt(3)
v_mfma_f32_16x16x32_bf16 a[52:55], a[140:143], v[180:183], a[52:55]
v_exp_f32 v153, v153
v_mfma_f32_16x16x32_bf16 a[48:51], a[140:143], v[176:179], a[48:51]
v_exp_f32 v154, v154
s_waitcnt lgkmcnt(2)
v_mfma_f32_16x16x32_bf16 a[64:67], a[144:147], v[176:179], a[64:67]
v_exp_f32 v155, v155
v_mfma_f32_16x16x32_bf16 a[68:71], a[144:147], v[180:183], a[68:71]
v_exp_f32 v156, v156
v_exp_f32 v157, v157
v_exp_f32 v158, v158
v_exp_f32 v159, v159
s_waitcnt lgkmcnt(1)
v_mfma_f32_16x16x32_bf16 a[84:87], a[148:151], v[180:183], a[84:87]
ds_read_b128 a[156:159], v234 offset:15360
v_cvt_pk_bf16_f32 v186, v152, v153
v_mfma_f32_16x16x32_bf16 a[80:83], a[148:151], v[176:179], a[80:83]
v_cvt_pk_bf16_f32 v187, v154, v155
v_cvt_pk_bf16_f32 v190, v156, v157
s_waitcnt lgkmcnt(1)
v_mfma_f32_16x16x32_bf16 a[96:99], a[152:155], v[176:179], a[96:99]
v_cvt_pk_bf16_f32 v191, v158, v159
v_add_f32 v212, v212, v152
v_mfma_f32_16x16x32_bf16 a[100:103], a[152:155], v[180:183], a[100:103]
v_add_f32 v213, v213, v153
v_add_f32 v212, v212, v154
s_waitcnt lgkmcnt(0)
v_mfma_f32_16x16x32_bf16 a[116:119], a[156:159], v[180:183], a[116:119]
v_add_f32 v213, v213, v155
v_add_f32 v214, v214, v156
v_mfma_f32_16x16x32_bf16 a[112:115], a[156:159], v[176:179], a[112:115]
v_add_f32 v215, v215, v157
v_add_f32 v214, v214, v158
v_mfma_f32_16x16x32_bf16 a[8:11], a[128:131], v[184:187], a[8:11]
ds_read_b64_tr_b16 a[240:241], v18 offset:0
v_add_f32 v215, v215, v159
v_mfma_f32_16x16x32_bf16 a[12:15], a[128:131], v[188:191], a[12:15]
ds_read_b64_tr_b16 a[242:243], v18 offset:4096
v_mfma_f32_16x16x32_bf16 a[28:31], a[132:135], v[188:191], a[28:31]
v_mfma_f32_16x16x32_bf16 a[24:27], a[132:135], v[184:187], a[24:27]
v_mfma_f32_16x16x32_bf16 a[40:43], a[136:139], v[184:187], a[40:43]
ds_read_b64_tr_b16 a[244:245], v19 offset:0
v_mfma_f32_16x16x32_bf16 a[44:47], a[136:139], v[188:191], a[44:47]
ds_read_b64_tr_b16 a[246:247], v19 offset:4096
v_mfma_f32_16x16x32_bf16 a[60:63], a[140:143], v[188:191], a[60:63]
v_mfma_f32_16x16x32_bf16 a[56:59], a[140:143], v[184:187], a[56:59]
v_mfma_f32_16x16x32_bf16 a[72:75], a[144:147], v[184:187], a[72:75]
ds_read_b64_tr_b16 a[248:249], v20 offset:0
v_mfma_f32_16x16x32_bf16 a[76:79], a[144:147], v[188:191], a[76:79]
ds_read_b64_tr_b16 a[250:251], v20 offset:4096
v_mfma_f32_16x16x32_bf16 a[92:95], a[148:151], v[188:191], a[92:95]
v_mfma_f32_16x16x32_bf16 a[88:91], a[148:151], v[184:187], a[88:91]
v_mfma_f32_16x16x32_bf16 a[104:107], a[152:155], v[184:187], a[104:107]
v_mfma_f32_16x16x32_bf16 a[108:111], a[152:155], v[188:191], a[108:111]
v_mfma_f32_16x16x32_bf16 a[124:127], a[156:159], v[188:191], a[124:127]
v_mfma_f32_16x16x32_bf16 a[120:123], a[156:159], v[184:187], a[120:123]
s_waitcnt lgkmcnt(4)
v_add_f32 v216, v208, v209
v_add_f32 v217, v210, v211
v_add_f32 v218, v212, v213
v_add_f32 v219, v214, v215
v_add_f32 v220, v216, v217
v_add_f32 v221, v218, v219
v_add_f32 v220, v220, v221
v_cmp_nle_f32 vcc, v220, v240
s_nop 4
s_cbranch_vccnz SMF_18
SMB_17:
KY:
v_mfma_f32_16x16x32_bf16 v[32:35], a[240:243], a[160:163], 0
s_nop 0
ds_read_b64_tr_b16 a[252:253], v21 offset:0
v_mfma_f32_16x16x32_bf16 v[36:39], a[240:243], a[164:167], 0
ds_read_b64_tr_b16 a[254:255], v21 offset:4096
v_mov_b32 v220, v216
v_mfma_f32_16x16x32_bf16 v[40:43], a[240:243], a[168:171], 0
buffer_load_dwordx4 a[128:131], v236, s[24:27], s47 offen sc1
v_mfma_f32_16x16x32_bf16 v[44:47], a[240:243], a[172:175], 0
s_waitcnt lgkmcnt(4)
v_mov_b32 v221, v217
v_mov_b32 v222, v218
v_mfma_f32_16x16x32_bf16 v[60:63], a[244:247], a[172:175], 0
ds_read_b64_tr_b16 a[240:241], v18 offset:1024
v_mov_b32 v223, v219
v_mfma_f32_16x16x32_bf16 v[56:59], a[244:247], a[168:171], 0
ds_read_b64_tr_b16 a[242:243], v18 offset:5120
v_permlane16_swap_b32 v216, v220
v_mfma_f32_16x16x32_bf16 v[52:55], a[244:247], a[164:167], 0
buffer_load_dwordx4 a[132:135], v237, s[24:27], s47 offen sc1
v_mfma_f32_16x16x32_bf16 v[48:51], a[244:247], a[160:163], 0
s_waitcnt lgkmcnt(4)
v_permlane16_swap_b32 v217, v221
v_permlane16_swap_b32 v218, v222
v_mfma_f32_16x16x32_bf16 v[64:67], a[248:251], a[160:163], 0
ds_read_b64_tr_b16 a[244:245], v19 offset:1024
v_permlane16_swap_b32 v219, v223
v_mfma_f32_16x16x32_bf16 v[68:71], a[248:251], a[164:167], 0
ds_read_b64_tr_b16 a[246:247], v19 offset:5120
v_add_f32 v216, v216, v220
v_mfma_f32_16x16x32_bf16 v[72:75], a[248:251], a[168:171], 0
buffer_load_dwordx4 a[136:139], v238, s[24:27], s47 offen sc1
v_mfma_f32_16x16x32_bf16 v[76:79], a[248:251], a[172:175], 0
s_waitcnt lgkmcnt(4)
v_add_f32 v217, v217, v221
v_add_f32 v218, v218, v222
v_mfma_f32_16x16x32_bf16 v[92:95], a[252:255], a[172:175], 0
ds_read_b64_tr_b16 a[248:249], v20 offset:1024
v_add_f32 v219, v219, v223
v_mfma_f32_16x16x32_bf16 v[88:91], a[252:255], a[168:171], 0
ds_read_b64_tr_b16 a[250:251], v20 offset:5120
v_mov_b32 v220, v216
v_mfma_f32_16x16x32_bf16 v[84:87], a[252:255], a[164:167], 0
buffer_load_dwordx4 a[140:143], v239, s[24:27], s47 offen sc1
v_mfma_f32_16x16x32_bf16 v[80:83], a[252:255], a[160:163], 0
s_waitcnt lgkmcnt(4)
s_add_u32 s47, s0, 3
s_and_b32 s47, s47, 7
v_mfma_f32_16x16x32_bf16 v[96:99], a[240:243], a[160:163], 0
s_mul_i32 s47, s47, 8192
s_add_u32 s47, s47, s58
v_mov_b32 v221, v217
v_mfma_f32_16x16x32_bf16 v[100:103], a[240:243], a[164:167], 0
ds_read_b64_tr_b16 a[252:253], v21 offset:1024
v_mov_b32 v222, v218
v_mfma_f32_16x16x32_bf16 v[104:107], a[240:243], a[168:171], 0
buffer_load_dwordx4 a[144:147], v236, s[24:27], s47 offen sc1
ds_read_b64_tr_b16 a[254:255], v21 offset:5120
v_mfma_f32_16x16x32_bf16 v[108:111], a[240:243], a[172:175], 0
s_waitcnt lgkmcnt(4)
v_mov_b32 v223, v219
v_permlane32_swap_b32 v216, v220
v_mfma_f32_16x16x32_bf16 v[124:127], a[244:247], a[172:175], 0
ds_read_b64_tr_b16 a[240:241], v18 offset:8192
v_permlane32_swap_b32 v217, v221
v_mfma_f32_16x16x32_bf16 v[120:123], a[244:247], a[168:171], 0
ds_read_b64_tr_b16 a[242:243], v18 offset:12288
v_permlane32_swap_b32 v218, v222
v_mfma_f32_16x16x32_bf16 v[116:119], a[244:247], a[164:167], 0
buffer_load_dwordx4 a[148:151], v237, s[24:27], s47 offen sc1
v_mfma_f32_16x16x32_bf16 v[112:115], a[244:247], a[160:163], 0
s_waitcnt lgkmcnt(4)
v_permlane32_swap_b32 v219, v223
v_add_f32 v216, v216, v220
v_mfma_f32_16x16x32_bf16 v[128:131], a[248:251], a[160:163], 0
ds_read_b64_tr_b16 a[244:245], v19 offset:8192
v_add_f32 v217, v217, v221
v_mfma_f32_16x16x32_bf16 v[132:135], a[248:251], a[164:167], 0
ds_read_b64_tr_b16 a[246:247], v19 offset:12288
v_add_f32 v218, v218, v222
v_mfma_f32_16x16x32_bf16 v[136:139], a[248:251], a[168:171], 0
buffer_load_dwordx4 a[152:155], v238, s[24:27], s47 offen sc1
v_mfma_f32_16x16x32_bf16 v[140:143], a[248:251], a[172:175], 0
s_waitcnt lgkmcnt(4)
s_add_u32 s54, s0, 5
v_add_f32 v219, v219, v223
v_mfma_f32_16x16x32_bf16 v[156:159], a[252:255], a[172:175], 0
ds_read_b64_tr_b16 a[248:249], v20 offset:8192
s_and_b32 s54, s54, 7
v_mfma_f32_16x16x32_bf16 v[152:155], a[252:255], a[168:171], 0
ds_read_b64_tr_b16 a[250:251], v20 offset:12288
s_mul_i32 s54, s54, 131072
v_mfma_f32_16x16x32_bf16 v[148:151], a[252:255], a[164:167], 0
buffer_load_dwordx4 a[156:159], v239, s[24:27], s47 offen sc1
v_mfma_f32_16x16x32_bf16 v[144:147], a[252:255], a[160:163], 0
s_waitcnt lgkmcnt(4)
s_add_u32 s47, s47, 4096
v_mfma_f32_16x16x32_bf16 v[32:35], a[240:243], a[176:179], v[32:35]
ds_read_b64_tr_b16 a[252:253], v21 offset:8192
s_add_u32 s54, s54, s85
v_mfma_f32_16x16x32_bf16 v[36:39], a[240:243], a[180:183], v[36:39]
ds_read_b64_tr_b16 a[254:255], v21 offset:12288
s_add_u32 m0, s70, 65536
v_mfma_f32_16x16x32_bf16 v[40:43], a[240:243], a[184:187], v[40:43]
buffer_load_dwordx4 a[160:163], v236, s[24:27], s47 offen sc1
v_mfma_f32_16x16x32_bf16 v[44:47], a[240:243], a[188:191], v[44:47]
s_waitcnt vmcnt(29)
s_barrier
s_waitcnt lgkmcnt(4)
v_mfma_f32_16x16x32_bf16 v[60:63], a[244:247], a[188:191], v[60:63]
ds_read_b64_tr_b16 a[240:241], v18 offset:9216
v_add_u32 v22, 0, v14
v_mfma_f32_16x16x32_bf16 v[56:59], a[244:247], a[184:187], v[56:59]
ds_read_b64_tr_b16 a[242:243], v18 offset:13312
v_add_u32 v23, 0, v15
v_mfma_f32_16x16x32_bf16 v[52:55], a[244:247], a[180:183], v[52:55]
buffer_load_dwordx4 a[164:167], v237, s[24:27], s47 offen sc1
v_mfma_f32_16x16x32_bf16 v[48:51], a[244:247], a[176:179], v[48:51]
s_waitcnt lgkmcnt(4)
v_rcp_f32 v224, v216
v_mfma_f32_16x16x32_bf16 v[64:67], a[248:251], a[176:179], v[64:67]
ds_read_b64_tr_b16 a[244:245], v19 offset:9216
v_add_u32 v24, 0, v16
v_mfma_f32_16x16x32_bf16 v[68:71], a[248:251], a[180:183], v[68:71]
ds_read_b64_tr_b16 a[246:247], v19 offset:13312
s_add_u32 s32, s22, 20971520
v_mfma_f32_16x16x32_bf16 v[72:75], a[248:251], a[184:187], v[72:75]
s_addc_u32 s33, s23, 0
buffer_load_dwordx4 a[168:171], v238, s[24:27], s47 offen sc1
v_rcp_f32 v225, v217
v_mfma_f32_16x16x32_bf16 v[76:79], a[248:251], a[188:191], v[76:79]
s_waitcnt lgkmcnt(4)
v_rcp_f32 v226, v218
v_mfma_f32_16x16x32_bf16 v[92:95], a[252:255], a[188:191], v[92:95]
ds_read_b64_tr_b16 a[248:249], v20 offset:9216
s_and_b32 s33, s33, 0xffff
v_mfma_f32_16x16x32_bf16 v[88:91], a[252:255], a[184:187], v[88:91]
ds_read_b64_tr_b16 a[250:251], v20 offset:13312
s_mov_b32 s34, 16777216
v_mfma_f32_16x16x32_bf16 v[84:87], a[252:255], a[180:183], v[84:87]
buffer_load_dwordx4 a[172:175], v239, s[24:27], s47 offen sc1
v_mfma_f32_16x16x32_bf16 v[80:83], a[252:255], a[176:179], v[80:83]
s_waitcnt lgkmcnt(4)
v_rcp_f32 v227, v219
v_mfma_f32_16x16x32_bf16 v[96:99], a[240:243], a[176:179], v[96:99]
ds_read_b64_tr_b16 a[252:253], v21 offset:9216
s_mov_b32 s35, 0x20000
v_mfma_f32_16x16x32_bf16 v[100:103], a[240:243], a[180:183], v[100:103]
ds_read_b64_tr_b16 a[254:255], v21 offset:13312
s_lshl_b32 s86, s41, 2
v_mfma_f32_16x16x32_bf16 v[104:107], a[240:243], a[184:187], v[104:107]
buffer_load_dwordx4 v13, s[88:91], s54 offen lds
v_mfma_f32_16x16x32_bf16 v[108:111], a[240:243], a[188:191], v[108:111]
s_waitcnt lgkmcnt(4)
s_add_u32 m0, s70, 66432
s_add_u32 s86, s86, s43
v_mfma_f32_16x16x32_bf16 v[124:127], a[244:247], a[188:191], v[124:127]
ds_read_b64_tr_b16 a[240:241], v22 offset:0
s_lshl_b32 s86, s86, 19
v_mfma_f32_16x16x32_bf16 v[120:123], a[244:247], a[184:187], v[120:123]
ds_read_b64_tr_b16 a[242:243], v22 offset:4096
s_lshl_b32 s97, s42, 14
v_mfma_f32_16x16x32_bf16 v[116:119], a[244:247], a[180:183], v[116:119]
buffer_load_dwordx4 v13, s[88:91], s54 offen offset:128 lds
v_mfma_f32_16x16x32_bf16 v[112:115], a[244:247], a[176:179], v[112:115]
s_waitcnt lgkmcnt(4)
s_add_u32 s54, s54, 16384
s_add_u32 m0, s70, 67584
v_mfma_f32_16x16x32_bf16 v[128:131], a[248:251], a[176:179], v[128:131]
ds_read_b64_tr_b16 a[244:245], v23 offset:0
s_add_u32 s86, s86, s97
v_mfma_f32_16x16x32_bf16 v[132:135], a[248:251], a[180:183], v[132:135]
ds_read_b64_tr_b16 a[246:247], v23 offset:4096
v_add_u32 v25, 0, v17
v_mfma_f32_16x16x32_bf16 v[136:139], a[248:251], a[184:187], v[136:139]
buffer_load_dwordx4 v13, s[88:91], s54 offen lds
v_mfma_f32_16x16x32_bf16 v[140:143], a[248:251], a[188:191], v[140:143]
s_waitcnt lgkmcnt(4)
s_add_u32 m0, s70, 68480
s_add_u32 s47, s0, 4
v_mfma_f32_16x16x32_bf16 v[156:159], a[252:255], a[188:191], v[156:159]
ds_read_b64_tr_b16 a[248:249], v24 offset:0
s_and_b32 s47, s47, 7
v_mfma_f32_16x16x32_bf16 v[152:155], a[252:255], a[184:187], v[152:155]
ds_read_b64_tr_b16 a[250:251], v24 offset:4096
s_mul_i32 s47, s47, 8192
v_mfma_f32_16x16x32_bf16 v[148:151], a[252:255], a[180:183], v[148:151]
buffer_load_dwordx4 v13, s[88:91], s54 offen offset:128 lds
v_mfma_f32_16x16x32_bf16 v[144:147], a[252:255], a[176:179], v[144:147]
s_waitcnt lgkmcnt(4)
s_add_u32 s47, s47, s58
v_mfma_f32_16x16x32_bf16 v[32:35], a[240:243], a[192:195], v[32:35]
ds_read_b64_tr_b16 a[252:253], v25 offset:0
v_accvgpr_read_b32 v160, a0
v_mfma_f32_16x16x32_bf16 v[36:39], a[240:243], a[196:199], v[36:39]
ds_read_b64_tr_b16 a[254:255], v25 offset:4096
v_mul_f32 v160, v160, v224
v_mfma_f32_16x16x32_bf16 v[40:43], a[240:243], a[200:203], v[40:43]
s_add_u32 s92, s22, 20971520
v_accvgpr_read_b32 v161, a1
v_mul_f32 v161, v161, v224
v_mfma_f32_16x16x32_bf16 v[44:47], a[240:243], a[204:207], v[44:47]
s_addc_u32 s93, s23, 0
s_waitcnt lgkmcnt(4)
v_cvt_pk_bf16_f32 v168, v160, v161
v_mfma_f32_16x16x32_bf16 v[60:63], a[244:247], a[204:207], v[60:63]
ds_read_b64_tr_b16 a[240:241], v22 offset:1024
v_accvgpr_read_b32 v162, a2
v_mfma_f32_16x16x32_bf16 v[56:59], a[244:247], a[200:203], v[56:59]
ds_read_b64_tr_b16 a[242:243], v22 offset:5120
v_mul_f32 v162, v162, v224
v_mfma_f32_16x16x32_bf16 v[52:55], a[244:247], a[196:199], v[52:55]
buffer_load_dwordx4 a[176:179], v236, s[24:27], s47 offen sc1
v_mfma_f32_16x16x32_bf16 v[48:51], a[244:247], a[192:195], v[48:51]
s_waitcnt lgkmcnt(4)
v_accvgpr_read_b32 v163, a3
v_mul_f32 v163, v163, v224
v_mfma_f32_16x16x32_bf16 v[64:67], a[248:251], a[192:195], v[64:67]
ds_read_b64_tr_b16 a[244:245], v23 offset:1024
v_cvt_pk_bf16_f32 v169, v162, v163
v_mfma_f32_16x16x32_bf16 v[68:71], a[248:251], a[196:199], v[68:71]
ds_read_b64_tr_b16 a[246:247], v23 offset:5120
v_accvgpr_read_b32 v164, a16
v_mfma_f32_16x16x32_bf16 v[72:75], a[248:251], a[200:203], v[72:75]
s_and_b32 s93, s93, 0xffff
v_mul_f32 v164, v164, v224
v_accvgpr_read_b32 v165, a17
v_mfma_f32_16x16x32_bf16 v[76:79], a[248:251], a[204:207], v[76:79]
s_waitcnt lgkmcnt(4)
v_mul_f32 v165, v165, v224
v_cvt_pk_bf16_f32 v170, v164, v165
v_mfma_f32_16x16x32_bf16 v[92:95], a[252:255], a[204:207], v[92:95]
s_mov_b32 s94, 16777216
v_accvgpr_read_b32 v166, a18
v_mul_f32 v166, v166, v224
v_accvgpr_read_b32 v167, a19
v_mul_f32 v167, v167, v224
v_cvt_pk_bf16_f32 v171, v166, v167
v_mfma_f32_16x16x32_bf16 v[88:91], a[252:255], a[200:203], v[88:91]
ds_read_b64_tr_b16 a[248:249], v24 offset:1024
v_accvgpr_read_b32 v172, a4
v_mfma_f32_16x16x32_bf16 v[84:87], a[252:255], a[196:199], v[84:87]
buffer_load_dwordx4 a[180:183], v237, s[24:27], s47 offen sc1
ds_read_b64_tr_b16 a[250:251], v24 offset:5120
v_mfma_f32_16x16x32_bf16 v[80:83], a[252:255], a[192:195], v[80:83]
s_waitcnt lgkmcnt(4)
v_mul_f32 v172, v172, v225
v_accvgpr_read_b32 v173, a5
v_mfma_f32_16x16x32_bf16 v[96:99], a[240:243], a[192:195], v[96:99]
ds_read_b64_tr_b16 a[252:253], v25 offset:1024
v_mul_f32 v173, v173, v225
v_mfma_f32_16x16x32_bf16 v[100:103], a[240:243], a[196:199], v[100:103]
ds_read_b64_tr_b16 a[254:255], v25 offset:5120
v_cvt_pk_bf16_f32 v180, v172, v173
v_mfma_f32_16x16x32_bf16 v[104:107], a[240:243], a[200:203], v[104:107]
buffer_store_dwordx4 v[168:171], v2, s[32:35], s86 offen offset:0 sc1
v_mfma_f32_16x16x32_bf16 v[108:111], a[240:243], a[204:207], v[108:111]
s_waitcnt lgkmcnt(4)
v_accvgpr_read_b32 v174, a6
v_mul_f32 v174, v174, v225
v_mfma_f32_16x16x32_bf16 v[124:127], a[244:247], a[204:207], v[124:127]
ds_read_b64_tr_b16 a[240:241], v22 offset:8192
v_accvgpr_read_b32 v175, a7
v_mfma_f32_16x16x32_bf16 v[120:123], a[244:247], a[200:203], v[120:123]
ds_read_b64_tr_b16 a[242:243], v22 offset:12288
v_mul_f32 v175, v175, v225
v_mfma_f32_16x16x32_bf16 v[116:119], a[244:247], a[196:199], v[116:119]
buffer_load_dwordx4 a[184:187], v238, s[24:27], s47 offen sc1
v_mfma_f32_16x16x32_bf16 v[112:115], a[244:247], a[192:195], v[112:115]
s_waitcnt lgkmcnt(4)
v_cvt_pk_bf16_f32 v181, v174, v175
v_accvgpr_read_b32 v176, a20
v_mfma_f32_16x16x32_bf16 v[128:131], a[248:251], a[192:195], v[128:131]
ds_read_b64_tr_b16 a[244:245], v23 offset:8192
v_mul_f32 v176, v176, v225
v_mfma_f32_16x16x32_bf16 v[132:135], a[248:251], a[196:199], v[132:135]
ds_read_b64_tr_b16 a[246:247], v23 offset:12288
v_accvgpr_read_b32 v177, a21
v_mfma_f32_16x16x32_bf16 v[136:139], a[248:251], a[200:203], v[136:139]
s_add_u32 s54, s0, 6
v_mul_f32 v177, v177, v225
v_cvt_pk_bf16_f32 v182, v176, v177
v_mfma_f32_16x16x32_bf16 v[140:143], a[248:251], a[204:207], v[140:143]
s_waitcnt lgkmcnt(4)
v_accvgpr_read_b32 v178, a22
v_mul_f32 v178, v178, v225
v_mfma_f32_16x16x32_bf16 v[156:159], a[252:255], a[204:207], v[156:159]
ds_read_b64_tr_b16 a[248:249], v24 offset:8192
v_accvgpr_read_b32 v179, a23
v_mfma_f32_16x16x32_bf16 v[152:155], a[252:255], a[200:203], v[152:155]
ds_read_b64_tr_b16 a[250:251], v24 offset:12288
v_mul_f32 v179, v179, v225
v_mfma_f32_16x16x32_bf16 v[148:151], a[252:255], a[196:199], v[148:151]
buffer_load_dwordx4 a[188:191], v239, s[24:27], s47 offen sc1
v_mfma_f32_16x16x32_bf16 v[144:147], a[252:255], a[192:195], v[144:147]
s_waitcnt lgkmcnt(4)
s_waitcnt vmcnt(33)
s_add_u32 s47, s47, 4096
v_cvt_pk_bf16_f32 v183, v178, v179
v_mfma_f32_16x16x32_bf16 v[32:35], a[240:243], a[208:211], v[32:35]
ds_read_b64_tr_b16 a[252:253], v25 offset:8192
s_and_b32 s54, s54, 7
v_mfma_f32_16x16x32_bf16 v[36:39], a[240:243], a[212:215], v[36:39]
ds_read_b64_tr_b16 a[254:255], v25 offset:12288
s_mul_i32 s54, s54, 131072
v_mfma_f32_16x16x32_bf16 v[40:43], a[240:243], a[216:219], v[40:43]
buffer_load_dwordx4 a[192:195], v236, s[24:27], s47 offen sc1
v_mfma_f32_16x16x32_bf16 v[44:47], a[240:243], a[220:223], v[44:47]
s_barrier
s_waitcnt lgkmcnt(4)
v_mfma_f32_16x16x32_bf16 v[60:63], a[244:247], a[220:223], v[60:63]
ds_read_b64_tr_b16 a[240:241], v22 offset:9216
s_add_u32 s54, s54, s85
v_mfma_f32_16x16x32_bf16 v[56:59], a[244:247], a[216:219], v[56:59]
ds_read_b64_tr_b16 a[242:243], v22 offset:13312
s_add_u32 m0, s70, 81920
v_mfma_f32_16x16x32_bf16 v[52:55], a[244:247], a[212:215], v[52:55]
buffer_load_dwordx4 a[196:199], v237, s[24:27], s47 offen sc1
v_mfma_f32_16x16x32_bf16 v[48:51], a[244:247], a[208:211], v[48:51]
s_waitcnt lgkmcnt(4)
v_accvgpr_read_b32 v184, a8
v_mul_f32 v184, v184, v226
v_mfma_f32_16x16x32_bf16 v[64:67], a[248:251], a[208:211], v[64:67]
ds_read_b64_tr_b16 a[244:245], v23 offset:9216
v_accvgpr_read_b32 v185, a9
v_mfma_f32_16x16x32_bf16 v[68:71], a[248:251], a[212:215], v[68:71]
ds_read_b64_tr_b16 a[246:247], v23 offset:13312
v_mul_f32 v185, v185, v226
v_mfma_f32_16x16x32_bf16 v[72:75], a[248:251], a[216:219], v[72:75]
buffer_load_dwordx4 a[200:203], v238, s[24:27], s47 offen sc1
v_mfma_f32_16x16x32_bf16 v[76:79], a[248:251], a[220:223], v[76:79]
s_waitcnt lgkmcnt(4)
buffer_store_dwordx4 v[180:183], v2, s[32:35], s86 offen offset:1024 sc1
v_cvt_pk_bf16_f32 v192, v184, v185
v_accvgpr_read_b32 v186, a10
v_mfma_f32_16x16x32_bf16 v[92:95], a[252:255], a[220:223], v[92:95]
ds_read_b64_tr_b16 a[248:249], v24 offset:9216
v_mul_f32 v186, v186, v226
v_mfma_f32_16x16x32_bf16 v[88:91], a[252:255], a[216:219], v[88:91]
ds_read_b64_tr_b16 a[250:251], v24 offset:13312
v_accvgpr_read_b32 v187, a11
v_mfma_f32_16x16x32_bf16 v[84:87], a[252:255], a[212:215], v[84:87]
buffer_load_dwordx4 a[204:207], v239, s[24:27], s47 offen sc1
v_mfma_f32_16x16x32_bf16 v[80:83], a[252:255], a[208:211], v[80:83]
s_waitcnt lgkmcnt(4)
v_mul_f32 v187, v187, v226
v_cvt_pk_bf16_f32 v193, v186, v187
v_mfma_f32_16x16x32_bf16 v[96:99], a[240:243], a[208:211], v[96:99]
s_add_u32 s47, s0, 5
v_accvgpr_read_b32 v188, a24
v_mul_f32 v188, v188, v226
v_mfma_f32_16x16x32_bf16 v[100:103], a[240:243], a[212:215], v[100:103]
s_and_b32 s47, s47, 7
v_accvgpr_read_b32 v189, a25
v_mul_f32 v189, v189, v226
v_cvt_pk_bf16_f32 v194, v188, v189
v_accvgpr_read_b32 v190, a26
v_mul_f32 v190, v190, v226
v_accvgpr_read_b32 v191, a27
v_mul_f32 v191, v191, v226
v_cvt_pk_bf16_f32 v195, v190, v191
v_mfma_f32_16x16x32_bf16 v[104:107], a[240:243], a[216:219], v[104:107]
buffer_load_dwordx4 v13, s[88:91], s54 offen lds
ds_read_b64_tr_b16 a[252:253], v25 offset:9216
ds_read_b64_tr_b16 a[254:255], v25 offset:13312
v_mfma_f32_16x16x32_bf16 v[108:111], a[240:243], a[220:223], v[108:111]
s_waitcnt lgkmcnt(4)
s_add_u32 m0, s70, 82816
v_add_u32 v18, 16384, v14
v_mfma_f32_16x16x32_bf16 v[124:127], a[244:247], a[220:223], v[124:127]
ds_read_b64_tr_b16 a[240:241], v18 offset:0
v_add_u32 v19, 16384, v15
v_mfma_f32_16x16x32_bf16 v[120:123], a[244:247], a[216:219], v[120:123]
ds_read_b64_tr_b16 a[242:243], v18 offset:4096
v_add_u32 v20, 16384, v16
v_mfma_f32_16x16x32_bf16 v[116:119], a[244:247], a[212:215], v[116:119]
buffer_load_dwordx4 v13, s[88:91], s54 offen offset:128 lds
v_mfma_f32_16x16x32_bf16 v[112:115], a[244:247], a[208:211], v[112:115]
s_waitcnt lgkmcnt(4)
s_add_u32 s54, s54, 16384
s_add_u32 m0, s70, 83968
buffer_store_dwordx4 v[192:195], v2, s[32:35], s86 offen offset:2048 sc1
v_mfma_f32_16x16x32_bf16 v[128:131], a[248:251], a[208:211], v[128:131]
ds_read_b64_tr_b16 a[244:245], v19 offset:0
v_add_u32 v21, 16384, v17
v_mfma_f32_16x16x32_bf16 v[132:135], a[248:251], a[212:215], v[132:135]
ds_read_b64_tr_b16 a[246:247], v19 offset:4096
v_accvgpr_read_b32 v196, a12
v_mfma_f32_16x16x32_bf16 v[136:139], a[248:251], a[216:219], v[136:139]
buffer_load_dwordx4 v13, s[88:91], s54 offen lds
v_mfma_f32_16x16x32_bf16 v[140:143], a[248:251], a[220:223], v[140:143]
s_waitcnt lgkmcnt(4)
s_add_u32 m0, s70, 84864
v_mul_f32 v196, v196, v227
v_mfma_f32_16x16x32_bf16 v[156:159], a[252:255], a[220:223], v[156:159]
ds_read_b64_tr_b16 a[248:249], v20 offset:0
v_accvgpr_read_b32 v197, a13
v_mfma_f32_16x16x32_bf16 v[152:155], a[252:255], a[216:219], v[152:155]
ds_read_b64_tr_b16 a[250:251], v20 offset:4096
v_mul_f32 v197, v197, v227
v_mfma_f32_16x16x32_bf16 v[148:151], a[252:255], a[212:215], v[148:151]
buffer_load_dwordx4 v13, s[88:91], s54 offen offset:128 lds
v_mfma_f32_16x16x32_bf16 v[144:147], a[252:255], a[208:211], v[144:147]
s_waitcnt lgkmcnt(4)
s_waitcnt vmcnt(39)
v_cvt_pk_bf16_f32 v204, v196, v197
v_mfma_f32_16x16x32_bf16 v[32:35], a[240:243], a[224:227], v[32:35]
ds_read_b64_tr_b16 a[252:253], v21 offset:0
v_accvgpr_read_b32 v198, a14
v_mfma_f32_16x16x32_bf16 v[36:39], a[240:243], a[228:231], v[36:39]
ds_read_b64_tr_b16 a[254:255], v21 offset:4096
v_mul_f32 v198, v198, v227
v_mfma_f32_16x16x32_bf16 v[40:43], a[240:243], a[232:235], v[40:43]
s_mul_i32 s47, s47, 8192
v_accvgpr_read_b32 v199, a15
v_mul_f32 v199, v199, v227
v_mfma_f32_16x16x32_bf16 v[44:47], a[240:243], a[236:239], v[44:47]
s_waitcnt lgkmcnt(4)
v_cvt_pk_bf16_f32 v205, v198, v199
v_accvgpr_read_b32 v200, a28
v_mfma_f32_16x16x32_bf16 v[60:63], a[244:247], a[236:239], v[60:63]
s_add_u32 s47, s47, s58
v_mul_f32 v200, v200, v227
v_accvgpr_read_b32 v201, a29
v_mul_f32 v201, v201, v227
v_cvt_pk_bf16_f32 v206, v200, v201
v_accvgpr_read_b32 v202, a30
v_mul_f32 v202, v202, v227
v_accvgpr_read_b32 v203, a31
v_mul_f32 v203, v203, v227
v_cvt_pk_bf16_f32 v207, v202, v203
v_mfma_f32_16x16x32_bf16 v[56:59], a[244:247], a[232:235], v[56:59]
ds_read_b64_tr_b16 a[240:241], v18 offset:1024
v_accvgpr_read_b32 v160, a32
v_mfma_f32_16x16x32_bf16 v[52:55], a[244:247], a[228:231], v[52:55]
buffer_load_dwordx4 a[208:211], v236, s[24:27], s47 offen sc1
ds_read_b64_tr_b16 a[242:243], v18 offset:5120
v_mfma_f32_16x16x32_bf16 v[48:51], a[244:247], a[224:227], v[48:51]
s_waitcnt lgkmcnt(4)
v_mul_f32 v160, v160, v224
v_accvgpr_read_b32 v161, a33
v_mfma_f32_16x16x32_bf16 v[64:67], a[248:251], a[224:227], v[64:67]
ds_read_b64_tr_b16 a[244:245], v19 offset:1024
v_mul_f32 v161, v161, v224
v_mfma_f32_16x16x32_bf16 v[68:71], a[248:251], a[228:231], v[68:71]
ds_read_b64_tr_b16 a[246:247], v19 offset:5120
v_cvt_pk_bf16_f32 v168, v160, v161
v_mfma_f32_16x16x32_bf16 v[72:75], a[248:251], a[232:235], v[72:75]
buffer_store_dwordx4 v[204:207], v2, s[32:35], s86 offen offset:3072 sc1
v_mfma_f32_16x16x32_bf16 v[76:79], a[248:251], a[236:239], v[76:79]
s_waitcnt lgkmcnt(4)
s_add_u32 s86, s86, 4096
v_accvgpr_read_b32 v162, a34
v_mfma_f32_16x16x32_bf16 v[92:95], a[252:255], a[236:239], v[92:95]
ds_read_b64_tr_b16 a[248:249], v20 offset:1024
v_mul_f32 v162, v162, v224
v_mfma_f32_16x16x32_bf16 v[88:91], a[252:255], a[232:235], v[88:91]
ds_read_b64_tr_b16 a[250:251], v20 offset:5120
v_accvgpr_read_b32 v163, a35
v_mfma_f32_16x16x32_bf16 v[84:87], a[252:255], a[228:231], v[84:87]
buffer_load_dwordx4 a[212:215], v237, s[24:27], s47 offen sc1
v_mfma_f32_16x16x32_bf16 v[80:83], a[252:255], a[224:227], v[80:83]
s_waitcnt lgkmcnt(4)
v_mul_f32 v163, v163, v224
v_cvt_pk_bf16_f32 v169, v162, v163
v_mfma_f32_16x16x32_bf16 v[96:99], a[240:243], a[224:227], v[96:99]
ds_read_b64_tr_b16 a[252:253], v21 offset:1024
v_accvgpr_read_b32 v164, a48
v_mfma_f32_16x16x32_bf16 v[100:103], a[240:243], a[228:231], v[100:103]
ds_read_b64_tr_b16 a[254:255], v21 offset:5120
v_mul_f32 v164, v164, v224
v_mfma_f32_16x16x32_bf16 v[104:107], a[240:243], a[232:235], v[104:107]
s_mov_b32 s95, 0x20000
v_accvgpr_read_b32 v165, a49
v_mul_f32 v165, v165, v224
v_mfma_f32_16x16x32_bf16 v[108:111], a[240:243], a[236:239], v[108:111]
s_waitcnt lgkmcnt(4)
v_cvt_pk_bf16_f32 v170, v164, v165
v_accvgpr_read_b32 v166, a50
v_mfma_f32_16x16x32_bf16 v[124:127], a[244:247], a[236:239], v[124:127]
ds_read_b64_tr_b16 a[240:241], v18 offset:8192
v_mul_f32 v166, v166, v224
v_mfma_f32_16x16x32_bf16 v[120:123], a[244:247], a[232:235], v[120:123]
ds_read_b64_tr_b16 a[242:243], v18 offset:12288
v_accvgpr_read_b32 v167, a51
v_mfma_f32_16x16x32_bf16 v[116:119], a[244:247], a[228:231], v[116:119]
buffer_load_dwordx4 a[216:219], v238, s[24:27], s47 offen sc1
v_mfma_f32_16x16x32_bf16 v[112:115], a[244:247], a[224:227], v[112:115]
s_waitcnt lgkmcnt(4)
v_mul_f32 v167, v167, v224
v_cvt_pk_bf16_f32 v171, v166, v167
v_mfma_f32_16x16x32_bf16 v[128:131], a[248:251], a[224:227], v[128:131]
ds_read_b64_tr_b16 a[244:245], v19 offset:8192
v_accvgpr_read_b32 v172, a36
v_mfma_f32_16x16x32_bf16 v[132:135], a[248:251], a[228:231], v[132:135]
ds_read_b64_tr_b16 a[246:247], v19 offset:12288
v_mul_f32 v172, v172, v225
v_mfma_f32_16x16x32_bf16 v[136:139], a[248:251], a[232:235], v[136:139]
buffer_store_dwordx4 v[168:171], v2, s[32:35], s86 offen offset:0 sc1
v_mfma_f32_16x16x32_bf16 v[140:143], a[248:251], a[236:239], v[140:143]
s_waitcnt lgkmcnt(4)
v_accvgpr_read_b32 v173, a37
v_mul_f32 v173, v173, v225
v_mfma_f32_16x16x32_bf16 v[156:159], a[252:255], a[236:239], v[156:159]
ds_read_b64_tr_b16 a[248:249], v20 offset:8192
v_cvt_pk_bf16_f32 v180, v172, v173
v_mfma_f32_16x16x32_bf16 v[152:155], a[252:255], a[232:235], v[152:155]
ds_read_b64_tr_b16 a[250:251], v20 offset:12288
v_accvgpr_read_b32 v174, a38
v_mfma_f32_16x16x32_bf16 v[148:151], a[252:255], a[228:231], v[148:151]
buffer_load_dwordx4 a[220:223], v239, s[24:27], s47 offen sc1
v_mfma_f32_16x16x32_bf16 v[144:147], a[252:255], a[224:227], v[144:147]
s_waitcnt lgkmcnt(4)
s_waitcnt vmcnt(33)
s_add_u32 s47, s47, 4096
v_mfma_f32_16x16x32_bf16 v[32:35], a[240:243], a[128:131], v[32:35]
ds_read_b64_tr_b16 a[252:253], v21 offset:8192
v_mul_f32 v174, v174, v225
v_mfma_f32_16x16x32_bf16 v[36:39], a[240:243], a[132:135], v[36:39]
ds_read_b64_tr_b16 a[254:255], v21 offset:12288
v_accvgpr_read_b32 v175, a39
v_mfma_f32_16x16x32_bf16 v[40:43], a[240:243], a[136:139], v[40:43]
buffer_load_dwordx4 a[0:3], v236, s[24:27], s47 offen sc1
v_mfma_f32_16x16x32_bf16 v[44:47], a[240:243], a[140:143], v[44:47]
s_barrier
s_waitcnt lgkmcnt(4)
v_mfma_f32_16x16x32_bf16 v[60:63], a[244:247], a[140:143], v[60:63]
ds_read_b64_tr_b16 a[240:241], v18 offset:9216
v_mul_f32 v175, v175, v225
v_mfma_f32_16x16x32_bf16 v[56:59], a[244:247], a[136:139], v[56:59]
ds_read_b64_tr_b16 a[242:243], v18 offset:13312
v_cvt_pk_bf16_f32 v181, v174, v175
v_mfma_f32_16x16x32_bf16 v[52:55], a[244:247], a[132:135], v[52:55]
buffer_load_dwordx4 a[4:7], v237, s[24:27], s47 offen sc1
v_mfma_f32_16x16x32_bf16 v[48:51], a[244:247], a[128:131], v[48:51]
s_waitcnt lgkmcnt(4)
v_accvgpr_read_b32 v176, a52
v_mul_f32 v176, v176, v225
v_mfma_f32_16x16x32_bf16 v[64:67], a[248:251], a[128:131], v[64:67]
ds_read_b64_tr_b16 a[244:245], v19 offset:9216
v_accvgpr_read_b32 v177, a53
v_mfma_f32_16x16x32_bf16 v[68:71], a[248:251], a[132:135], v[68:71]
ds_read_b64_tr_b16 a[246:247], v19 offset:13312
v_mul_f32 v177, v177, v225
v_mfma_f32_16x16x32_bf16 v[72:75], a[248:251], a[136:139], v[72:75]
buffer_load_dwordx4 a[8:11], v238, s[24:27], s47 offen sc1
v_cvt_pk_bf16_f32 v182, v176, v177
v_accvgpr_read_b32 v178, a54
v_mul_f32 v178, v178, v225
v_accvgpr_read_b32 v179, a55
v_mul_f32 v179, v179, v225
v_cvt_pk_bf16_f32 v183, v178, v179
v_mfma_f32_16x16x32_bf16 v[76:79], a[248:251], a[140:143], v[76:79]
s_waitcnt lgkmcnt(4)
s_add_u32 s54, s0, 7
s_and_b32 s54, s54, 7
buffer_store_dwordx4 v[180:183], v2, s[32:35], s86 offen offset:1024 sc1
v_mfma_f32_16x16x32_bf16 v[92:95], a[252:255], a[140:143], v[92:95]
ds_read_b64_tr_b16 a[248:249], v20 offset:9216
s_mul_i32 s54, s54, 131072
v_mfma_f32_16x16x32_bf16 v[88:91], a[252:255], a[136:139], v[88:91]
ds_read_b64_tr_b16 a[250:251], v20 offset:13312
s_add_u32 s54, s54, s85
v_mfma_f32_16x16x32_bf16 v[84:87], a[252:255], a[132:135], v[84:87]
buffer_load_dwordx4 a[12:15], v239, s[24:27], s47 offen sc1
v_mfma_f32_16x16x32_bf16 v[80:83], a[252:255], a[128:131], v[80:83]
s_waitcnt lgkmcnt(4)
s_add_u32 m0, s70, 98304
v_add_u32 v22, 32768, v14
v_mfma_f32_16x16x32_bf16 v[96:99], a[240:243], a[128:131], v[96:99]
ds_read_b64_tr_b16 a[252:253], v21 offset:9216
v_add_u32 v23, 32768, v15
v_mfma_f32_16x16x32_bf16 v[100:103], a[240:243], a[132:135], v[100:103]
ds_read_b64_tr_b16 a[254:255], v21 offset:13312
v_add_u32 v24, 32768, v16
v_mfma_f32_16x16x32_bf16 v[104:107], a[240:243], a[136:139], v[104:107]
buffer_load_dwordx4 v13, s[88:91], s54 offen lds
v_mfma_f32_16x16x32_bf16 v[108:111], a[240:243], a[140:143], v[108:111]
s_waitcnt lgkmcnt(4)
s_add_u32 m0, s70, 99200
v_accvgpr_read_b32 v184, a40
v_mfma_f32_16x16x32_bf16 v[124:127], a[244:247], a[140:143], v[124:127]
ds_read_b64_tr_b16 a[240:241], v22 offset:0
v_mul_f32 v184, v184, v226
v_mfma_f32_16x16x32_bf16 v[120:123], a[244:247], a[136:139], v[120:123]
ds_read_b64_tr_b16 a[242:243], v22 offset:4096
v_accvgpr_read_b32 v185, a41
v_mfma_f32_16x16x32_bf16 v[116:119], a[244:247], a[132:135], v[116:119]
buffer_load_dwordx4 v13, s[88:91], s54 offen offset:128 lds
v_mfma_f32_16x16x32_bf16 v[112:115], a[244:247], a[128:131], v[112:115]
s_waitcnt lgkmcnt(4)
s_add_u32 s54, s54, 16384
s_add_u32 m0, s70, 100352
v_mfma_f32_16x16x32_bf16 v[128:131], a[248:251], a[128:131], v[128:131]
ds_read_b64_tr_b16 a[244:245], v23 offset:0
v_mul_f32 v185, v185, v226
v_mfma_f32_16x16x32_bf16 v[132:135], a[248:251], a[132:135], v[132:135]
ds_read_b64_tr_b16 a[246:247], v23 offset:4096
v_cvt_pk_bf16_f32 v192, v184, v185
v_mfma_f32_16x16x32_bf16 v[136:139], a[248:251], a[136:139], v[136:139]
buffer_load_dwordx4 v13, s[88:91], s54 offen lds
v_mfma_f32_16x16x32_bf16 v[140:143], a[248:251], a[140:143], v[140:143]
s_waitcnt lgkmcnt(4)
v_accvgpr_read_b32 v186, a42
v_mul_f32 v186, v186, v226
v_mfma_f32_16x16x32_bf16 v[156:159], a[252:255], a[140:143], v[156:159]
s_add_u32 m0, s70, 101248
v_accvgpr_read_b32 v187, a43
v_mul_f32 v187, v187, v226
v_cvt_pk_bf16_f32 v193, v186, v187
v_accvgpr_read_b32 v188, a56
v_mul_f32 v188, v188, v226
v_accvgpr_read_b32 v189, a57
v_mul_f32 v189, v189, v226
v_cvt_pk_bf16_f32 v194, v188, v189
v_accvgpr_read_b32 v190, a58
v_mul_f32 v190, v190, v226
v_accvgpr_read_b32 v191, a59
v_mul_f32 v191, v191, v226
v_cvt_pk_bf16_f32 v195, v190, v191
v_mfma_f32_16x16x32_bf16 v[152:155], a[252:255], a[136:139], v[152:155]
ds_read_b64_tr_b16 a[248:249], v24 offset:0
v_add_u32 v25, 32768, v17
v_mfma_f32_16x16x32_bf16 v[148:151], a[252:255], a[132:135], v[148:151]
buffer_load_dwordx4 v13, s[88:91], s54 offen offset:128 lds
ds_read_b64_tr_b16 a[250:251], v24 offset:4096
v_mfma_f32_16x16x32_bf16 v[144:147], a[252:255], a[128:131], v[144:147]
s_waitcnt lgkmcnt(4)
s_waitcnt vmcnt(38)
s_add_u32 s47, s0, 6
v_mfma_f32_16x16x32_bf16 v[32:35], a[240:243], a[144:147], v[32:35]
ds_read_b64_tr_b16 a[252:253], v25 offset:0
s_and_b32 s47, s47, 7
v_mfma_f32_16x16x32_bf16 v[36:39], a[240:243], a[148:151], v[36:39]
ds_read_b64_tr_b16 a[254:255], v25 offset:4096
s_mul_i32 s47, s47, 8192
v_mfma_f32_16x16x32_bf16 v[40:43], a[240:243], a[152:155], v[40:43]
buffer_store_dwordx4 v[192:195], v2, s[32:35], s86 offen offset:2048 sc1
v_mfma_f32_16x16x32_bf16 v[44:47], a[240:243], a[156:159], v[44:47]
s_waitcnt lgkmcnt(4)
s_add_u32 s47, s47, s58
v_accvgpr_read_b32 v196, a44
v_mfma_f32_16x16x32_bf16 v[60:63], a[244:247], a[156:159], v[60:63]
ds_read_b64_tr_b16 a[240:241], v22 offset:1024
v_mul_f32 v196, v196, v227
v_mfma_f32_16x16x32_bf16 v[56:59], a[244:247], a[152:155], v[56:59]
ds_read_b64_tr_b16 a[242:243], v22 offset:5120
v_accvgpr_read_b32 v197, a45
v_mfma_f32_16x16x32_bf16 v[52:55], a[244:247], a[148:151], v[52:55]
buffer_load_dwordx4 a[16:19], v236, s[24:27], s47 offen sc1
v_mfma_f32_16x16x32_bf16 v[48:51], a[244:247], a[144:147], v[48:51]
s_waitcnt lgkmcnt(4)
v_mul_f32 v197, v197, v227
v_cvt_pk_bf16_f32 v204, v196, v197
v_mfma_f32_16x16x32_bf16 v[64:67], a[248:251], a[144:147], v[64:67]
ds_read_b64_tr_b16 a[244:245], v23 offset:1024
v_accvgpr_read_b32 v198, a46
v_mfma_f32_16x16x32_bf16 v[68:71], a[248:251], a[148:151], v[68:71]
ds_read_b64_tr_b16 a[246:247], v23 offset:5120
v_mul_f32 v198, v198, v227
v_mfma_f32_16x16x32_bf16 v[72:75], a[248:251], a[152:155], v[72:75]
s_lshl_b32 s3, s41, 2
v_accvgpr_read_b32 v199, a47
v_mul_f32 v199, v199, v227
v_mfma_f32_16x16x32_bf16 v[76:79], a[248:251], a[156:159], v[76:79]
s_waitcnt lgkmcnt(4)
v_cvt_pk_bf16_f32 v205, v198, v199
v_accvgpr_read_b32 v200, a60
v_mfma_f32_16x16x32_bf16 v[92:95], a[252:255], a[156:159], v[92:95]
ds_read_b64_tr_b16 a[248:249], v24 offset:1024
v_mul_f32 v200, v200, v227
v_mfma_f32_16x16x32_bf16 v[88:91], a[252:255], a[152:155], v[88:91]
ds_read_b64_tr_b16 a[250:251], v24 offset:5120
v_accvgpr_read_b32 v201, a61
v_mfma_f32_16x16x32_bf16 v[84:87], a[252:255], a[148:151], v[84:87]
buffer_load_dwordx4 a[20:23], v237, s[24:27], s47 offen sc1
v_mfma_f32_16x16x32_bf16 v[80:83], a[252:255], a[144:147], v[80:83]
s_waitcnt lgkmcnt(4)
v_mul_f32 v201, v201, v227
v_cvt_pk_bf16_f32 v206, v200, v201
v_accvgpr_read_b32 v202, a62
v_mul_f32 v202, v202, v227
v_accvgpr_read_b32 v203, a63
v_mul_f32 v203, v203, v227
v_cvt_pk_bf16_f32 v207, v202, v203
v_mfma_f32_16x16x32_bf16 v[96:99], a[240:243], a[144:147], v[96:99]
ds_read_b64_tr_b16 a[252:253], v25 offset:1024
v_accvgpr_read_b32 v160, a64
v_mfma_f32_16x16x32_bf16 v[100:103], a[240:243], a[148:151], v[100:103]
ds_read_b64_tr_b16 a[254:255], v25 offset:5120
v_mul_f32 v160, v160, v224
v_mfma_f32_16x16x32_bf16 v[104:107], a[240:243], a[152:155], v[104:107]
buffer_store_dwordx4 v[204:207], v2, s[32:35], s86 offen offset:3072 sc1
v_mfma_f32_16x16x32_bf16 v[108:111], a[240:243], a[156:159], v[108:111]
s_waitcnt lgkmcnt(4)
s_add_u32 s86, s86, 4096
v_accvgpr_read_b32 v161, a65
v_mfma_f32_16x16x32_bf16 v[124:127], a[244:247], a[156:159], v[124:127]
ds_read_b64_tr_b16 a[240:241], v22 offset:8192
v_mul_f32 v161, v161, v224
v_mfma_f32_16x16x32_bf16 v[120:123], a[244:247], a[152:155], v[120:123]
ds_read_b64_tr_b16 a[242:243], v22 offset:12288
v_cvt_pk_bf16_f32 v168, v160, v161
v_mfma_f32_16x16x32_bf16 v[116:119], a[244:247], a[148:151], v[116:119]
buffer_load_dwordx4 a[24:27], v238, s[24:27], s47 offen sc1
v_mfma_f32_16x16x32_bf16 v[112:115], a[244:247], a[144:147], v[112:115]
s_waitcnt lgkmcnt(4)
v_accvgpr_read_b32 v162, a66
v_mul_f32 v162, v162, v224
v_mfma_f32_16x16x32_bf16 v[128:131], a[248:251], a[144:147], v[128:131]
ds_read_b64_tr_b16 a[244:245], v23 offset:8192
v_accvgpr_read_b32 v163, a67
v_mfma_f32_16x16x32_bf16 v[132:135], a[248:251], a[148:151], v[132:135]
ds_read_b64_tr_b16 a[246:247], v23 offset:12288
v_mul_f32 v163, v163, v224
v_mfma_f32_16x16x32_bf16 v[136:139], a[248:251], a[152:155], v[136:139]
s_add_u32 s54, s0, 0
v_cvt_pk_bf16_f32 v169, v162, v163
v_accvgpr_read_b32 v164, a80
v_mfma_f32_16x16x32_bf16 v[140:143], a[248:251], a[156:159], v[140:143]
s_waitcnt lgkmcnt(4)
v_mul_f32 v164, v164, v224
v_accvgpr_read_b32 v165, a81
v_mfma_f32_16x16x32_bf16 v[156:159], a[252:255], a[156:159], v[156:159]
ds_read_b64_tr_b16 a[248:249], v24 offset:8192
v_mul_f32 v165, v165, v224
v_mfma_f32_16x16x32_bf16 v[152:155], a[252:255], a[152:155], v[152:155]
ds_read_b64_tr_b16 a[250:251], v24 offset:12288
v_cvt_pk_bf16_f32 v170, v164, v165
v_mfma_f32_16x16x32_bf16 v[148:151], a[252:255], a[148:151], v[148:151]
buffer_load_dwordx4 a[28:31], v239, s[24:27], s47 offen sc1
v_mfma_f32_16x16x32_bf16 v[144:147], a[252:255], a[144:147], v[144:147]
s_waitcnt lgkmcnt(4)
s_waitcnt vmcnt(40)
s_add_u32 s47, s47, 4096
v_mfma_f32_16x16x32_bf16 v[32:35], a[240:243], a[160:163], v[32:35]
ds_read_b64_tr_b16 a[252:253], v25 offset:8192
v_accvgpr_read_b32 v166, a82
v_mfma_f32_16x16x32_bf16 v[36:39], a[240:243], a[164:167], v[36:39]
ds_read_b64_tr_b16 a[254:255], v25 offset:12288
v_mul_f32 v166, v166, v224
v_mfma_f32_16x16x32_bf16 v[40:43], a[240:243], a[168:171], v[40:43]
buffer_load_dwordx4 a[32:35], v236, s[24:27], s47 offen sc1
v_accvgpr_read_b32 v167, a83
v_mul_f32 v167, v167, v224
v_cvt_pk_bf16_f32 v171, v166, v167
v_mfma_f32_16x16x32_bf16 v[44:47], a[240:243], a[172:175], v[44:47]
s_barrier
s_waitcnt lgkmcnt(4)
v_mfma_f32_16x16x32_bf16 v[60:63], a[244:247], a[172:175], v[60:63]
ds_read_b64_tr_b16 a[240:241], v22 offset:9216
s_and_b32 s54, s54, 3
v_mfma_f32_16x16x32_bf16 v[56:59], a[244:247], a[168:171], v[56:59]
ds_read_b64_tr_b16 a[242:243], v22 offset:13312
s_mul_i32 s54, s54, 131072
v_mfma_f32_16x16x32_bf16 v[52:55], a[244:247], a[164:167], v[52:55]
buffer_load_dwordx4 a[36:39], v237, s[24:27], s47 offen sc1
v_mfma_f32_16x16x32_bf16 v[48:51], a[244:247], a[160:163], v[48:51]
s_waitcnt lgkmcnt(4)
s_add_u32 s54, s54, s85
s_add_u32 s54, s54, 1048576
v_mfma_f32_16x16x32_bf16 v[64:67], a[248:251], a[160:163], v[64:67]
ds_read_b64_tr_b16 a[244:245], v23 offset:9216
s_add_u32 m0, s70, 114688
v_mfma_f32_16x16x32_bf16 v[68:71], a[248:251], a[164:167], v[68:71]
ds_read_b64_tr_b16 a[246:247], v23 offset:13312
v_add_u32 v18, 49152, v14
v_mfma_f32_16x16x32_bf16 v[72:75], a[248:251], a[168:171], v[72:75]
buffer_load_dwordx4 a[40:43], v238, s[24:27], s47 offen sc1
v_mfma_f32_16x16x32_bf16 v[76:79], a[248:251], a[172:175], v[76:79]
s_waitcnt lgkmcnt(4)
buffer_store_dwordx4 v[168:171], v2, s[32:35], s86 offen offset:0 sc1
v_accvgpr_read_b32 v172, a68
v_mul_f32 v172, v172, v225
v_mfma_f32_16x16x32_bf16 v[92:95], a[252:255], a[172:175], v[92:95]
ds_read_b64_tr_b16 a[248:249], v24 offset:9216
v_accvgpr_read_b32 v173, a69
v_mfma_f32_16x16x32_bf16 v[88:91], a[252:255], a[168:171], v[88:91]
ds_read_b64_tr_b16 a[250:251], v24 offset:13312
v_mul_f32 v173, v173, v225
v_mfma_f32_16x16x32_bf16 v[84:87], a[252:255], a[164:167], v[84:87]
buffer_load_dwordx4 a[44:47], v239, s[24:27], s47 offen sc1
v_mfma_f32_16x16x32_bf16 v[80:83], a[252:255], a[160:163], v[80:83]
s_waitcnt lgkmcnt(4)
v_cvt_pk_bf16_f32 v180, v172, v173
v_accvgpr_read_b32 v174, a70
v_mfma_f32_16x16x32_bf16 v[96:99], a[240:243], a[160:163], v[96:99]
ds_read_b64_tr_b16 a[252:253], v25 offset:9216
v_mul_f32 v174, v174, v225
v_mfma_f32_16x16x32_bf16 v[100:103], a[240:243], a[164:167], v[100:103]
ds_read_b64_tr_b16 a[254:255], v25 offset:13312
v_accvgpr_read_b32 v175, a71
v_mfma_f32_16x16x32_bf16 v[104:107], a[240:243], a[168:171], v[104:107]
buffer_load_dwordx4 v13, s[88:91], s54 offen lds
v_mfma_f32_16x16x32_bf16 v[108:111], a[240:243], a[172:175], v[108:111]
s_waitcnt lgkmcnt(4)
s_add_u32 m0, s70, 115584
v_mul_f32 v175, v175, v225
v_mfma_f32_16x16x32_bf16 v[124:127], a[244:247], a[172:175], v[124:127]
s_add_u32 s47, s0, 7
v_cvt_pk_bf16_f32 v181, v174, v175
v_accvgpr_read_b32 v176, a84
v_mfma_f32_16x16x32_bf16 v[120:123], a[244:247], a[168:171], v[120:123]
s_and_b32 s47, s47, 7
v_mul_f32 v176, v176, v225
v_accvgpr_read_b32 v177, a85
v_mul_f32 v177, v177, v225
v_cvt_pk_bf16_f32 v182, v176, v177
v_accvgpr_read_b32 v178, a86
v_mul_f32 v178, v178, v225
v_accvgpr_read_b32 v179, a87
v_mul_f32 v179, v179, v225
v_cvt_pk_bf16_f32 v183, v178, v179
v_mfma_f32_16x16x32_bf16 v[116:119], a[244:247], a[164:167], v[116:119]
buffer_load_dwordx4 v13, s[88:91], s54 offen offset:128 lds
ds_read_b64_tr_b16 a[240:241], v18 offset:0
ds_read_b64_tr_b16 a[242:243], v18 offset:4096
v_mfma_f32_16x16x32_bf16 v[112:115], a[244:247], a[160:163], v[112:115]
s_waitcnt lgkmcnt(4)
s_add_u32 s54, s54, 16384
buffer_store_dwordx4 v[180:183], v2, s[32:35], s86 offen offset:1024 sc1
v_add_u32 v19, 49152, v15
v_mfma_f32_16x16x32_bf16 v[128:131], a[248:251], a[160:163], v[128:131]
s_add_u32 m0, s70, 116736
ds_read_b64_tr_b16 a[244:245], v19 offset:0
v_mfma_f32_16x16x32_bf16 v[132:135], a[248:251], a[164:167], v[132:135]
ds_read_b64_tr_b16 a[246:247], v19 offset:4096
v_add_u32 v20, 49152, v16
v_mfma_f32_16x16x32_bf16 v[136:139], a[248:251], a[168:171], v[136:139]
buffer_load_dwordx4 v13, s[88:91], s54 offen lds
v_mfma_f32_16x16x32_bf16 v[140:143], a[248:251], a[172:175], v[140:143]
s_waitcnt lgkmcnt(4)
s_add_u32 m0, s70, 117632
v_add_u32 v21, 49152, v17
v_mfma_f32_16x16x32_bf16 v[156:159], a[252:255], a[172:175], v[156:159]
ds_read_b64_tr_b16 a[248:249], v20 offset:0
s_mul_i32 s47, s47, 8192
v_mfma_f32_16x16x32_bf16 v[152:155], a[252:255], a[168:171], v[152:155]
ds_read_b64_tr_b16 a[250:251], v20 offset:4096
s_add_u32 s47, s47, s58
v_mfma_f32_16x16x32_bf16 v[148:151], a[252:255], a[164:167], v[148:151]
buffer_load_dwordx4 v13, s[88:91], s54 offen offset:128 lds
v_mfma_f32_16x16x32_bf16 v[144:147], a[252:255], a[160:163], v[144:147]
s_waitcnt lgkmcnt(4)
s_waitcnt vmcnt(41)
v_accvgpr_read_b32 v184, a72
v_mfma_f32_16x16x32_bf16 v[32:35], a[240:243], a[176:179], v[32:35]
ds_read_b64_tr_b16 a[252:253], v21 offset:0
v_mul_f32 v184, v184, v226
v_mfma_f32_16x16x32_bf16 v[36:39], a[240:243], a[180:183], v[36:39]
ds_read_b64_tr_b16 a[254:255], v21 offset:4096
v_accvgpr_read_b32 v185, a73
v_mfma_f32_16x16x32_bf16 v[40:43], a[240:243], a[184:187], v[40:43]
s_add_u32 s3, s3, s43
v_mul_f32 v185, v185, v226
v_cvt_pk_bf16_f32 v192, v184, v185
v_mfma_f32_16x16x32_bf16 v[44:47], a[240:243], a[188:191], v[44:47]
s_waitcnt lgkmcnt(4)
v_accvgpr_read_b32 v186, a74
v_mul_f32 v186, v186, v226
v_mfma_f32_16x16x32_bf16 v[60:63], a[244:247], a[188:191], v[60:63]
ds_read_b64_tr_b16 a[240:241], v18 offset:1024
v_accvgpr_read_b32 v187, a75
v_mfma_f32_16x16x32_bf16 v[56:59], a[244:247], a[184:187], v[56:59]
ds_read_b64_tr_b16 a[242:243], v18 offset:5120
v_mul_f32 v187, v187, v226
v_mfma_f32_16x16x32_bf16 v[52:55], a[244:247], a[180:183], v[52:55]
buffer_load_dwordx4 a[48:51], v236, s[24:27], s47 offen sc1
v_mfma_f32_16x16x32_bf16 v[48:51], a[244:247], a[176:179], v[48:51]
s_waitcnt lgkmcnt(4)
v_cvt_pk_bf16_f32 v193, v186, v187
v_accvgpr_read_b32 v188, a88
v_mul_f32 v188, v188, v226
v_accvgpr_read_b32 v189, a89
v_mul_f32 v189, v189, v226
v_cvt_pk_bf16_f32 v194, v188, v189
v_accvgpr_read_b32 v190, a90
v_mul_f32 v190, v190, v226
v_accvgpr_read_b32 v191, a91
v_mul_f32 v191, v191, v226
v_cvt_pk_bf16_f32 v195, v190, v191
v_mfma_f32_16x16x32_bf16 v[64:67], a[248:251], a[176:179], v[64:67]
ds_read_b64_tr_b16 a[244:245], v19 offset:1024
v_accvgpr_read_b32 v196, a76
v_mfma_f32_16x16x32_bf16 v[68:71], a[248:251], a[180:183], v[68:71]
ds_read_b64_tr_b16 a[246:247], v19 offset:5120
v_mul_f32 v196, v196, v227
v_mfma_f32_16x16x32_bf16 v[72:75], a[248:251], a[184:187], v[72:75]
buffer_store_dwordx4 v[192:195], v2, s[32:35], s86 offen offset:2048 sc1
v_mfma_f32_16x16x32_bf16 v[76:79], a[248:251], a[188:191], v[76:79]
s_waitcnt lgkmcnt(4)
v_accvgpr_read_b32 v197, a77
v_mul_f32 v197, v197, v227
v_mfma_f32_16x16x32_bf16 v[92:95], a[252:255], a[188:191], v[92:95]
ds_read_b64_tr_b16 a[248:249], v20 offset:1024
v_cvt_pk_bf16_f32 v204, v196, v197
v_mfma_f32_16x16x32_bf16 v[88:91], a[252:255], a[184:187], v[88:91]
ds_read_b64_tr_b16 a[250:251], v20 offset:5120
v_accvgpr_read_b32 v198, a78
v_mfma_f32_16x16x32_bf16 v[84:87], a[252:255], a[180:183], v[84:87]
buffer_load_dwordx4 a[52:55], v237, s[24:27], s47 offen sc1
v_mfma_f32_16x16x32_bf16 v[80:83], a[252:255], a[176:179], v[80:83]
s_waitcnt lgkmcnt(4)
v_mul_f32 v198, v198, v227
v_accvgpr_read_b32 v199, a79
v_mfma_f32_16x16x32_bf16 v[96:99], a[240:243], a[176:179], v[96:99]
ds_read_b64_tr_b16 a[252:253], v21 offset:1024
v_mul_f32 v199, v199, v227
v_mfma_f32_16x16x32_bf16 v[100:103], a[240:243], a[180:183], v[100:103]
ds_read_b64_tr_b16 a[254:255], v21 offset:5120
v_cvt_pk_bf16_f32 v205, v198, v199
v_mfma_f32_16x16x32_bf16 v[104:107], a[240:243], a[184:187], v[104:107]
s_lshl_b32 s3, s3, 19
v_accvgpr_read_b32 v200, a92
v_mul_f32 v200, v200, v227
v_mfma_f32_16x16x32_bf16 v[108:111], a[240:243], a[188:191], v[108:111]
s_waitcnt lgkmcnt(4)
v_accvgpr_read_b32 v201, a93
v_mul_f32 v201, v201, v227
v_mfma_f32_16x16x32_bf16 v[124:127], a[244:247], a[188:191], v[124:127]
ds_read_b64_tr_b16 a[240:241], v18 offset:8192
v_cvt_pk_bf16_f32 v206, v200, v201
v_mfma_f32_16x16x32_bf16 v[120:123], a[244:247], a[184:187], v[120:123]
ds_read_b64_tr_b16 a[242:243], v18 offset:12288
v_accvgpr_read_b32 v202, a94
v_mfma_f32_16x16x32_bf16 v[116:119], a[244:247], a[180:183], v[116:119]
buffer_load_dwordx4 a[56:59], v238, s[24:27], s47 offen sc1
v_mfma_f32_16x16x32_bf16 v[112:115], a[244:247], a[176:179], v[112:115]
s_waitcnt lgkmcnt(4)
v_mul_f32 v202, v202, v227
v_accvgpr_read_b32 v203, a95
v_mfma_f32_16x16x32_bf16 v[128:131], a[248:251], a[176:179], v[128:131]
ds_read_b64_tr_b16 a[244:245], v19 offset:8192
v_mul_f32 v203, v203, v227
v_mfma_f32_16x16x32_bf16 v[132:135], a[248:251], a[180:183], v[132:135]
ds_read_b64_tr_b16 a[246:247], v19 offset:12288
v_cvt_pk_bf16_f32 v207, v202, v203
v_mfma_f32_16x16x32_bf16 v[136:139], a[248:251], a[184:187], v[136:139]
s_add_u32 s54, s0, 1
v_accvgpr_read_b32 v160, a96
v_mul_f32 v160, v160, v224
v_mfma_f32_16x16x32_bf16 v[140:143], a[248:251], a[188:191], v[140:143]
s_waitcnt lgkmcnt(4)
v_accvgpr_read_b32 v161, a97
v_mul_f32 v161, v161, v224
v_mfma_f32_16x16x32_bf16 v[156:159], a[252:255], a[188:191], v[156:159]
ds_read_b64_tr_b16 a[248:249], v20 offset:8192
v_cvt_pk_bf16_f32 v168, v160, v161
v_mfma_f32_16x16x32_bf16 v[152:155], a[252:255], a[184:187], v[152:155]
ds_read_b64_tr_b16 a[250:251], v20 offset:12288
v_accvgpr_read_b32 v162, a98
v_mfma_f32_16x16x32_bf16 v[148:151], a[252:255], a[180:183], v[148:151]
buffer_load_dwordx4 a[60:63], v239, s[24:27], s47 offen sc1
v_mfma_f32_16x16x32_bf16 v[144:147], a[252:255], a[176:179], v[144:147]
s_waitcnt lgkmcnt(4)
s_waitcnt vmcnt(41)
s_add_u32 s47, s47, 4096
v_mfma_f32_16x16x32_bf16 v[32:35], a[240:243], a[192:195], v[32:35]
ds_read_b64_tr_b16 a[252:253], v21 offset:8192
v_mul_f32 v162, v162, v224
v_mfma_f32_16x16x32_bf16 v[36:39], a[240:243], a[196:199], v[36:39]
ds_read_b64_tr_b16 a[254:255], v21 offset:12288
v_accvgpr_read_b32 v163, a99
v_mfma_f32_16x16x32_bf16 v[40:43], a[240:243], a[200:203], v[40:43]
buffer_load_dwordx4 a[128:131], v236, s[24:27], s47 offen sc1
v_mfma_f32_16x16x32_bf16 v[44:47], a[240:243], a[204:207], v[44:47]
s_barrier
s_waitcnt lgkmcnt(4)
v_mfma_f32_16x16x32_bf16 v[60:63], a[244:247], a[204:207], v[60:63]
ds_read_b64_tr_b16 a[240:241], v18 offset:9216
v_mul_f32 v163, v163, v224
v_mfma_f32_16x16x32_bf16 v[56:59], a[244:247], a[200:203], v[56:59]
ds_read_b64_tr_b16 a[242:243], v18 offset:13312
v_cvt_pk_bf16_f32 v169, v162, v163
v_mfma_f32_16x16x32_bf16 v[52:55], a[244:247], a[196:199], v[52:55]
buffer_load_dwordx4 a[132:135], v237, s[24:27], s47 offen sc1
v_mfma_f32_16x16x32_bf16 v[48:51], a[244:247], a[192:195], v[48:51]
s_waitcnt lgkmcnt(4)
v_accvgpr_read_b32 v164, a112
v_mul_f32 v164, v164, v224
v_mfma_f32_16x16x32_bf16 v[64:67], a[248:251], a[192:195], v[64:67]
ds_read_b64_tr_b16 a[244:245], v19 offset:9216
v_accvgpr_read_b32 v165, a113
v_mfma_f32_16x16x32_bf16 v[68:71], a[248:251], a[196:199], v[68:71]
ds_read_b64_tr_b16 a[246:247], v19 offset:13312
v_mul_f32 v165, v165, v224
v_mfma_f32_16x16x32_bf16 v[72:75], a[248:251], a[200:203], v[72:75]
buffer_load_dwordx4 a[136:139], v238, s[24:27], s47 offen sc1
v_mfma_f32_16x16x32_bf16 v[76:79], a[248:251], a[204:207], v[76:79]
s_waitcnt lgkmcnt(4)
buffer_store_dwordx4 v[204:207], v2, s[32:35], s86 offen offset:3072 sc1
v_cvt_pk_bf16_f32 v170, v164, v165
v_accvgpr_read_b32 v166, a114
v_mfma_f32_16x16x32_bf16 v[92:95], a[252:255], a[204:207], v[92:95]
s_and_b32 s54, s54, 3
v_mul_f32 v166, v166, v224
v_accvgpr_read_b32 v167, a115
v_mfma_f32_16x16x32_bf16 v[88:91], a[252:255], a[200:203], v[88:91]
s_mul_i32 s54, s54, 131072
v_mul_f32 v167, v167, v224
v_cvt_pk_bf16_f32 v171, v166, v167
v_mfma_f32_16x16x32_bf16 v[84:87], a[252:255], a[196:199], v[84:87]
buffer_load_dwordx4 a[140:143], v239, s[24:27], s47 offen sc1
ds_read_b64_tr_b16 a[248:249], v20 offset:9216
ds_read_b64_tr_b16 a[250:251], v20 offset:13312
v_mfma_f32_16x16x32_bf16 v[80:83], a[252:255], a[192:195], v[80:83]
s_waitcnt lgkmcnt(4)
s_add_u32 s54, s54, s85
s_add_u32 s54, s54, 1048576
v_mfma_f32_16x16x32_bf16 v[96:99], a[240:243], a[192:195], v[96:99]
s_add_u32 m0, s70, 131072
s_add_u32 s86, s86, 4096
v_add_u32 v22, 65536, v14
v_mfma_f32_16x16x32_bf16 v[100:103], a[240:243], a[196:199], v[100:103]
ds_read_b64_tr_b16 a[252:253], v21 offset:9216
v_add_u32 v23, 65536, v15
v_mfma_f32_16x16x32_bf16 v[104:107], a[240:243], a[200:203], v[104:107]
buffer_load_dwordx4 v13, s[88:91], s54 offen lds
ds_read_b64_tr_b16 a[254:255], v21 offset:13312
v_mfma_f32_16x16x32_bf16 v[108:111], a[240:243], a[204:207], v[108:111]
s_waitcnt lgkmcnt(4)
s_add_u32 m0, s70, 131968
v_add_u32 v24, 65536, v16
v_mfma_f32_16x16x32_bf16 v[124:127], a[244:247], a[204:207], v[124:127]
ds_read_b64_tr_b16 a[240:241], v22 offset:0
v_add_u32 v25, 65536, v17
v_mfma_f32_16x16x32_bf16 v[120:123], a[244:247], a[200:203], v[120:123]
ds_read_b64_tr_b16 a[242:243], v22 offset:4096
v_accvgpr_read_b32 v172, a100
v_mfma_f32_16x16x32_bf16 v[116:119], a[244:247], a[196:199], v[116:119]
buffer_load_dwordx4 v13, s[88:91], s54 offen offset:128 lds
v_mfma_f32_16x16x32_bf16 v[112:115], a[244:247], a[192:195], v[112:115]
s_waitcnt lgkmcnt(4)
s_add_u32 s54, s54, 16384
s_add_u32 m0, s70, 133120
buffer_store_dwordx4 v[168:171], v2, s[32:35], s86 offen offset:0 sc1
v_mfma_f32_16x16x32_bf16 v[128:131], a[248:251], a[192:195], v[128:131]
ds_read_b64_tr_b16 a[244:245], v23 offset:0
v_mul_f32 v172, v172, v225
v_mfma_f32_16x16x32_bf16 v[132:135], a[248:251], a[196:199], v[132:135]
ds_read_b64_tr_b16 a[246:247], v23 offset:4096
v_accvgpr_read_b32 v173, a101
v_mfma_f32_16x16x32_bf16 v[136:139], a[248:251], a[200:203], v[136:139]
buffer_load_dwordx4 v13, s[88:91], s54 offen lds
v_mfma_f32_16x16x32_bf16 v[140:143], a[248:251], a[204:207], v[140:143]
s_waitcnt lgkmcnt(4)
s_add_u32 m0, s70, 134016
v_mul_f32 v173, v173, v225
v_mfma_f32_16x16x32_bf16 v[156:159], a[252:255], a[204:207], v[156:159]
ds_read_b64_tr_b16 a[248:249], v24 offset:0
v_cvt_pk_bf16_f32 v180, v172, v173
v_mfma_f32_16x16x32_bf16 v[152:155], a[252:255], a[200:203], v[152:155]
ds_read_b64_tr_b16 a[250:251], v24 offset:4096
v_accvgpr_read_b32 v174, a102
v_mfma_f32_16x16x32_bf16 v[148:151], a[252:255], a[196:199], v[148:151]
buffer_load_dwordx4 v13, s[88:91], s54 offen offset:128 lds
v_mfma_f32_16x16x32_bf16 v[144:147], a[252:255], a[192:195], v[144:147]
s_waitcnt lgkmcnt(4)
s_waitcnt vmcnt(40)
v_mul_f32 v174, v174, v225
v_mfma_f32_16x16x32_bf16 v[32:35], a[240:243], a[208:211], v[32:35]
s_add_u32 s47, s0, 0
v_accvgpr_read_b32 v175, a103
v_mul_f32 v175, v175, v225
v_cvt_pk_bf16_f32 v181, v174, v175
v_accvgpr_read_b32 v176, a116
v_mul_f32 v176, v176, v225
v_accvgpr_read_b32 v177, a117
v_mul_f32 v177, v177, v225
v_cvt_pk_bf16_f32 v182, v176, v177
v_accvgpr_read_b32 v178, a118
v_mul_f32 v178, v178, v225
v_accvgpr_read_b32 v179, a119
v_mul_f32 v179, v179, v225
v_cvt_pk_bf16_f32 v183, v178, v179
v_mfma_f32_16x16x32_bf16 v[36:39], a[240:243], a[212:215], v[36:39]
ds_read_b64_tr_b16 a[252:253], v25 offset:0
s_and_b32 s47, s47, 3
v_mfma_f32_16x16x32_bf16 v[40:43], a[240:243], a[216:219], v[40:43]
ds_read_b64_tr_b16 a[254:255], v25 offset:4096
s_mul_i32 s47, s47, 8192
buffer_store_dwordx4 v[180:183], v2, s[32:35], s86 offen offset:1024 sc1
v_mfma_f32_16x16x32_bf16 v[44:47], a[240:243], a[220:223], v[44:47]
s_waitcnt lgkmcnt(4)
s_add_u32 s47, s47, s58
s_add_u32 s47, s47, 65536
v_mfma_f32_16x16x32_bf16 v[60:63], a[244:247], a[220:223], v[60:63]
ds_read_b64_tr_b16 a[240:241], v22 offset:1024
v_accvgpr_read_b32 v184, a104
v_mfma_f32_16x16x32_bf16 v[56:59], a[244:247], a[216:219], v[56:59]
ds_read_b64_tr_b16 a[242:243], v22 offset:5120
v_mul_f32 v184, v184, v226
v_mfma_f32_16x16x32_bf16 v[52:55], a[244:247], a[212:215], v[52:55]
buffer_load_dwordx4 a[64:67], v236, s[24:27], s47 offen sc1
v_mfma_f32_16x16x32_bf16 v[48:51], a[244:247], a[208:211], v[48:51]
s_waitcnt lgkmcnt(4)
v_accvgpr_read_b32 v185, a105
v_mul_f32 v185, v185, v226
v_mfma_f32_16x16x32_bf16 v[64:67], a[248:251], a[208:211], v[64:67]
ds_read_b64_tr_b16 a[244:245], v23 offset:1024
v_cvt_pk_bf16_f32 v192, v184, v185
v_mfma_f32_16x16x32_bf16 v[68:71], a[248:251], a[212:215], v[68:71]
ds_read_b64_tr_b16 a[246:247], v23 offset:5120
v_accvgpr_read_b32 v186, a106
v_mfma_f32_16x16x32_bf16 v[72:75], a[248:251], a[216:219], v[72:75]
s_lshl_b32 s51, s59, 17
v_mul_f32 v186, v186, v226
v_accvgpr_read_b32 v187, a107
v_mfma_f32_16x16x32_bf16 v[76:79], a[248:251], a[220:223], v[76:79]
s_waitcnt lgkmcnt(4)
v_mul_f32 v187, v187, v226
v_cvt_pk_bf16_f32 v193, v186, v187
v_mfma_f32_16x16x32_bf16 v[92:95], a[252:255], a[220:223], v[92:95]
ds_read_b64_tr_b16 a[248:249], v24 offset:1024
v_accvgpr_read_b32 v188, a120
v_mfma_f32_16x16x32_bf16 v[88:91], a[252:255], a[216:219], v[88:91]
ds_read_b64_tr_b16 a[250:251], v24 offset:5120
v_mul_f32 v188, v188, v226
v_mfma_f32_16x16x32_bf16 v[84:87], a[252:255], a[212:215], v[84:87]
buffer_load_dwordx4 a[68:71], v237, s[24:27], s47 offen sc1
v_mfma_f32_16x16x32_bf16 v[80:83], a[252:255], a[208:211], v[80:83]
s_waitcnt lgkmcnt(4)
v_accvgpr_read_b32 v189, a121
v_mul_f32 v189, v189, v226
v_mfma_f32_16x16x32_bf16 v[96:99], a[240:243], a[208:211], v[96:99]
ds_read_b64_tr_b16 a[252:253], v25 offset:1024
v_cvt_pk_bf16_f32 v194, v188, v189
v_mfma_f32_16x16x32_bf16 v[100:103], a[240:243], a[212:215], v[100:103]
ds_read_b64_tr_b16 a[254:255], v25 offset:5120
v_accvgpr_read_b32 v190, a122
v_mfma_f32_16x16x32_bf16 v[104:107], a[240:243], a[216:219], v[104:107]
s_add_u32 s3, s3, s51
v_mul_f32 v190, v190, v226
v_accvgpr_read_b32 v191, a123
v_mul_f32 v191, v191, v226
v_cvt_pk_bf16_f32 v195, v190, v191
v_mfma_f32_16x16x32_bf16 v[108:111], a[240:243], a[220:223], v[108:111]
s_waitcnt lgkmcnt(4)
v_accvgpr_read_b32 v196, a108
v_mul_f32 v196, v196, v227
v_mfma_f32_16x16x32_bf16 v[124:127], a[244:247], a[220:223], v[124:127]
ds_read_b64_tr_b16 a[240:241], v22 offset:8192
v_accvgpr_read_b32 v197, a109
v_mfma_f32_16x16x32_bf16 v[120:123], a[244:247], a[216:219], v[120:123]
ds_read_b64_tr_b16 a[242:243], v22 offset:12288
v_mul_f32 v197, v197, v227
v_mfma_f32_16x16x32_bf16 v[116:119], a[244:247], a[212:215], v[116:119]
buffer_load_dwordx4 a[72:75], v238, s[24:27], s47 offen sc1
v_mfma_f32_16x16x32_bf16 v[112:115], a[244:247], a[208:211], v[112:115]
s_waitcnt lgkmcnt(4)
v_cvt_pk_bf16_f32 v204, v196, v197
v_accvgpr_read_b32 v198, a110
v_mfma_f32_16x16x32_bf16 v[128:131], a[248:251], a[208:211], v[128:131]
ds_read_b64_tr_b16 a[244:245], v23 offset:8192
v_mul_f32 v198, v198, v227
v_mfma_f32_16x16x32_bf16 v[132:135], a[248:251], a[212:215], v[132:135]
ds_read_b64_tr_b16 a[246:247], v23 offset:12288
v_accvgpr_read_b32 v199, a111
v_mfma_f32_16x16x32_bf16 v[136:139], a[248:251], a[216:219], v[136:139]
buffer_store_dwordx4 v[192:195], v2, s[32:35], s86 offen offset:2048 sc1
v_mfma_f32_16x16x32_bf16 v[140:143], a[248:251], a[220:223], v[140:143]
s_waitcnt lgkmcnt(4)
v_mul_f32 v199, v199, v227
v_cvt_pk_bf16_f32 v205, v198, v199
v_mfma_f32_16x16x32_bf16 v[156:159], a[252:255], a[220:223], v[156:159]
ds_read_b64_tr_b16 a[248:249], v24 offset:8192
v_accvgpr_read_b32 v200, a124
v_mfma_f32_16x16x32_bf16 v[152:155], a[252:255], a[216:219], v[152:155]
ds_read_b64_tr_b16 a[250:251], v24 offset:12288
v_mul_f32 v200, v200, v227
v_mfma_f32_16x16x32_bf16 v[148:151], a[252:255], a[212:215], v[148:151]
buffer_load_dwordx4 a[76:79], v239, s[24:27], s47 offen sc1
v_mfma_f32_16x16x32_bf16 v[144:147], a[252:255], a[208:211], v[144:147]
s_waitcnt lgkmcnt(4)
s_waitcnt vmcnt(41)
s_add_u32 s47, s47, 4096
v_mfma_f32_16x16x32_bf16 v[32:35], a[240:243], a[0:3], v[32:35]
ds_read_b64_tr_b16 a[252:253], v25 offset:8192
v_accvgpr_read_b32 v201, a125
v_mfma_f32_16x16x32_bf16 v[36:39], a[240:243], a[4:7], v[36:39]
ds_read_b64_tr_b16 a[254:255], v25 offset:12288
v_mul_f32 v201, v201, v227
v_mfma_f32_16x16x32_bf16 v[40:43], a[240:243], a[8:11], v[40:43]
buffer_load_dwordx4 a[80:83], v236, s[24:27], s47 offen sc1
v_mfma_f32_16x16x32_bf16 v[44:47], a[240:243], a[12:15], v[44:47]
s_barrier
s_waitcnt lgkmcnt(4)
v_mfma_f32_16x16x32_bf16 v[60:63], a[244:247], a[12:15], v[60:63]
s_add_u32 s54, s0, 2
v_cvt_pk_bf16_f32 v206, v200, v201
v_accvgpr_read_b32 v202, a126
v_mfma_f32_16x16x32_bf16 v[56:59], a[244:247], a[8:11], v[56:59]
s_and_b32 s54, s54, 3
v_mul_f32 v202, v202, v227
v_accvgpr_read_b32 v203, a127
v_mul_f32 v203, v203, v227
v_cvt_pk_bf16_f32 v207, v202, v203
v_mfma_f32_16x16x32_bf16 v[52:55], a[244:247], a[4:7], v[52:55]
buffer_load_dwordx4 a[84:87], v237, s[24:27], s47 offen sc1
ds_read_b64_tr_b16 a[240:241], v22 offset:9216
ds_read_b64_tr_b16 a[242:243], v22 offset:13312
v_mfma_f32_16x16x32_bf16 v[48:51], a[244:247], a[0:3], v[48:51]
s_waitcnt lgkmcnt(4)
s_mul_i32 s54, s54, 131072
s_add_u32 s54, s54, s85
v_mfma_f32_16x16x32_bf16 v[64:67], a[248:251], a[0:3], v[64:67]
ds_read_b64_tr_b16 a[244:245], v23 offset:9216
s_add_u32 s54, s54, 1048576
v_mfma_f32_16x16x32_bf16 v[68:71], a[248:251], a[4:7], v[68:71]
ds_read_b64_tr_b16 a[246:247], v23 offset:13312
s_add_u32 m0, s70, 147456
v_mfma_f32_16x16x32_bf16 v[72:75], a[248:251], a[8:11], v[72:75]
buffer_load_dwordx4 a[88:91], v238, s[24:27], s47 offen sc1
v_mfma_f32_16x16x32_bf16 v[76:79], a[248:251], a[12:15], v[76:79]
s_waitcnt lgkmcnt(4)
buffer_store_dwordx4 v[204:207], v2, s[32:35], s86 offen offset:3072 sc1
v_add_u32 v18, 81920, v14
v_add_u32 v19, 81920, v15
v_mfma_f32_16x16x32_bf16 v[92:95], a[252:255], a[12:15], v[92:95]
ds_read_b64_tr_b16 a[248:249], v24 offset:9216
s_add_u32 s86, s86, 4096
v_mfma_f32_16x16x32_bf16 v[88:91], a[252:255], a[8:11], v[88:91]
ds_read_b64_tr_b16 a[250:251], v24 offset:13312
v_add_u32 v20, 81920, v16
v_mfma_f32_16x16x32_bf16 v[84:87], a[252:255], a[4:7], v[84:87]
buffer_load_dwordx4 a[92:95], v239, s[24:27], s47 offen sc1
v_mfma_f32_16x16x32_bf16 v[80:83], a[252:255], a[0:3], v[80:83]
s_waitcnt lgkmcnt(4)
s_add_u32 s47, s0, 1
v_add_u32 v21, 81920, v17
v_mfma_f32_16x16x32_bf16 v[96:99], a[240:243], a[0:3], v[96:99]
ds_read_b64_tr_b16 a[252:253], v25 offset:9216
s_and_b32 s47, s47, 3
v_mfma_f32_16x16x32_bf16 v[100:103], a[240:243], a[4:7], v[100:103]
ds_read_b64_tr_b16 a[254:255], v25 offset:13312
s_mul_i32 s47, s47, 8192
v_mfma_f32_16x16x32_bf16 v[104:107], a[240:243], a[8:11], v[104:107]
buffer_load_dwordx4 v13, s[88:91], s54 offen lds
v_mfma_f32_16x16x32_bf16 v[108:111], a[240:243], a[12:15], v[108:111]
s_waitcnt lgkmcnt(4)
s_add_u32 m0, s70, 148352
s_add_u32 s47, s47, s58
v_mfma_f32_16x16x32_bf16 v[124:127], a[244:247], a[12:15], v[124:127]
ds_read_b64_tr_b16 a[240:241], v18 offset:0
s_add_u32 s47, s47, 65536
v_mfma_f32_16x16x32_bf16 v[120:123], a[244:247], a[8:11], v[120:123]
ds_read_b64_tr_b16 a[242:243], v18 offset:4096
v_mfma_f32_16x16x32_bf16 v[116:119], a[244:247], a[4:7], v[116:119]
buffer_load_dwordx4 v13, s[88:91], s54 offen offset:128 lds
v_mfma_f32_16x16x32_bf16 v[112:115], a[244:247], a[0:3], v[112:115]
s_waitcnt lgkmcnt(4)
s_add_u32 s54, s54, 16384
s_add_u32 m0, s70, 149504
v_mfma_f32_16x16x32_bf16 v[128:131], a[248:251], a[0:3], v[128:131]
ds_read_b64_tr_b16 a[244:245], v19 offset:0
v_mfma_f32_16x16x32_bf16 v[132:135], a[248:251], a[4:7], v[132:135]
ds_read_b64_tr_b16 a[246:247], v19 offset:4096
v_mfma_f32_16x16x32_bf16 v[136:139], a[248:251], a[8:11], v[136:139]
buffer_load_dwordx4 v13, s[88:91], s54 offen lds
v_mfma_f32_16x16x32_bf16 v[140:143], a[248:251], a[12:15], v[140:143]
s_waitcnt lgkmcnt(4)
s_add_u32 m0, s70, 150400
v_mfma_f32_16x16x32_bf16 v[156:159], a[252:255], a[12:15], v[156:159]
ds_read_b64_tr_b16 a[248:249], v20 offset:0
v_mfma_f32_16x16x32_bf16 v[152:155], a[252:255], a[8:11], v[152:155]
ds_read_b64_tr_b16 a[250:251], v20 offset:4096
v_mfma_f32_16x16x32_bf16 v[148:151], a[252:255], a[4:7], v[148:151]
buffer_load_dwordx4 v13, s[88:91], s54 offen offset:128 lds
v_mfma_f32_16x16x32_bf16 v[144:147], a[252:255], a[0:3], v[144:147]
s_waitcnt lgkmcnt(4)
s_waitcnt vmcnt(40)
v_mfma_f32_16x16x32_bf16 v[32:35], a[240:243], a[16:19], v[32:35]
ds_read_b64_tr_b16 a[252:253], v21 offset:0
v_mfma_f32_16x16x32_bf16 v[36:39], a[240:243], a[20:23], v[36:39]
ds_read_b64_tr_b16 a[254:255], v21 offset:4096
v_mfma_f32_16x16x32_bf16 v[40:43], a[240:243], a[24:27], v[40:43]
v_mfma_f32_16x16x32_bf16 v[44:47], a[240:243], a[28:31], v[44:47]
s_waitcnt lgkmcnt(4)
v_mfma_f32_16x16x32_bf16 v[60:63], a[244:247], a[28:31], v[60:63]
ds_read_b64_tr_b16 a[240:241], v18 offset:1024
v_mfma_f32_16x16x32_bf16 v[56:59], a[244:247], a[24:27], v[56:59]
ds_read_b64_tr_b16 a[242:243], v18 offset:5120
v_mfma_f32_16x16x32_bf16 v[52:55], a[244:247], a[20:23], v[52:55]
buffer_load_dwordx4 a[0:3], v236, s[24:27], s47 offen sc1
v_mfma_f32_16x16x32_bf16 v[48:51], a[244:247], a[16:19], v[48:51]
s_waitcnt lgkmcnt(4)
v_mfma_f32_16x16x32_bf16 v[64:67], a[248:251], a[16:19], v[64:67]
ds_read_b64_tr_b16 a[244:245], v19 offset:1024
v_mfma_f32_16x16x32_bf16 v[68:71], a[248:251], a[20:23], v[68:71]
ds_read_b64_tr_b16 a[246:247], v19 offset:5120
v_mfma_f32_16x16x32_bf16 v[72:75], a[248:251], a[24:27], v[72:75]
v_mfma_f32_16x16x32_bf16 v[76:79], a[248:251], a[28:31], v[76:79]
s_waitcnt lgkmcnt(4)
v_mfma_f32_16x16x32_bf16 v[92:95], a[252:255], a[28:31], v[92:95]
ds_read_b64_tr_b16 a[248:249], v20 offset:1024
v_mfma_f32_16x16x32_bf16 v[88:91], a[252:255], a[24:27], v[88:91]
ds_read_b64_tr_b16 a[250:251], v20 offset:5120
v_add_u32 v22, 98304, v14
v_mfma_f32_16x16x32_bf16 v[84:87], a[252:255], a[20:23], v[84:87]
buffer_load_dwordx4 a[4:7], v237, s[24:27], s47 offen sc1
v_mfma_f32_16x16x32_bf16 v[80:83], a[252:255], a[16:19], v[80:83]
s_waitcnt lgkmcnt(4)
v_add_u32 v23, 98304, v15
v_add_u32 v24, 98304, v16
v_mfma_f32_16x16x32_bf16 v[96:99], a[240:243], a[16:19], v[96:99]
ds_read_b64_tr_b16 a[252:253], v21 offset:1024
v_add_u32 v25, 98304, v17
v_mfma_f32_16x16x32_bf16 v[100:103], a[240:243], a[20:23], v[100:103]
ds_read_b64_tr_b16 a[254:255], v21 offset:5120
v_mfma_f32_16x16x32_bf16 v[104:107], a[240:243], a[24:27], v[104:107]
v_mfma_f32_16x16x32_bf16 v[108:111], a[240:243], a[28:31], v[108:111]
s_waitcnt lgkmcnt(4)
s_add_u32 s54, s0, 3
s_and_b32 s54, s54, 3
v_mfma_f32_16x16x32_bf16 v[124:127], a[244:247], a[28:31], v[124:127]
ds_read_b64_tr_b16 a[240:241], v18 offset:8192
s_mul_i32 s54, s54, 131072
v_mfma_f32_16x16x32_bf16 v[120:123], a[244:247], a[24:27], v[120:123]
ds_read_b64_tr_b16 a[242:243], v18 offset:12288
s_add_u32 s54, s54, s85
v_mfma_f32_16x16x32_bf16 v[116:119], a[244:247], a[20:23], v[116:119]
buffer_load_dwordx4 a[8:11], v238, s[24:27], s47 offen sc1
v_mfma_f32_16x16x32_bf16 v[112:115], a[244:247], a[16:19], v[112:115]
s_waitcnt lgkmcnt(4)
s_add_u32 s54, s54, 1048576
s_add_u32 m0, s70, 0
v_mfma_f32_16x16x32_bf16 v[128:131], a[248:251], a[16:19], v[128:131]
ds_read_b64_tr_b16 a[244:245], v19 offset:8192
v_mfma_f32_16x16x32_bf16 v[132:135], a[248:251], a[20:23], v[132:135]
ds_read_b64_tr_b16 a[246:247], v19 offset:12288
v_mfma_f32_16x16x32_bf16 v[136:139], a[248:251], a[24:27], v[136:139]
s_mov_b32 s36, s49
s_mov_b32 s37, s50
s_mov_b32 s38, KTEXT1-KTEXT0
v_mfma_f32_16x16x32_bf16 v[140:143], a[248:251], a[28:31], v[140:143]
s_waitcnt lgkmcnt(4)
s_mov_b32 s39, 0x20000
s_lshr_b32 s69, s98, 3
v_mfma_f32_16x16x32_bf16 v[156:159], a[252:255], a[28:31], v[156:159]
ds_read_b64_tr_b16 a[248:249], v20 offset:8192
s_lshl_b32 s69, s69, 2
v_mfma_f32_16x16x32_bf16 v[152:155], a[252:255], a[24:27], v[152:155]
ds_read_b64_tr_b16 a[250:251], v20 offset:12288
s_add_u32 s69, s69, s43
v_mfma_f32_16x16x32_bf16 v[148:151], a[252:255], a[20:23], v[148:151]
buffer_load_dwordx4 a[12:15], v239, s[24:27], s47 offen sc1
v_mfma_f32_16x16x32_bf16 v[144:147], a[252:255], a[16:19], v[144:147]
s_waitcnt lgkmcnt(4)
s_waitcnt vmcnt(39)
s_add_u32 s47, s47, 4096
v_mfma_f32_16x16x32_bf16 v[32:35], a[240:243], a[32:35], v[32:35]
ds_read_b64_tr_b16 a[252:253], v21 offset:8192
s_and_b32 s69, s69, 63
v_mfma_f32_16x16x32_bf16 v[36:39], a[240:243], a[36:39], v[36:39]
ds_read_b64_tr_b16 a[254:255], v21 offset:12288
s_lshl_b32 s69, s69, 10
v_mfma_f32_16x16x32_bf16 v[40:43], a[240:243], a[40:43], v[40:43]
buffer_load_dwordx4 a[16:19], v236, s[24:27], s47 offen sc1
v_mfma_f32_16x16x32_bf16 v[44:47], a[240:243], a[44:47], v[44:47]
s_barrier
s_waitcnt lgkmcnt(4)
v_mfma_f32_16x16x32_bf16 v[60:63], a[244:247], a[44:47], v[60:63]
ds_read_b64_tr_b16 a[240:241], v18 offset:9216
s_add_u32 s69, s69, KS-KTEXT0
v_mfma_f32_16x16x32_bf16 v[56:59], a[244:247], a[40:43], v[56:59]
ds_read_b64_tr_b16 a[242:243], v18 offset:13312
v_mfma_f32_16x16x32_bf16 v[52:55], a[244:247], a[36:39], v[52:55]
buffer_load_dwordx4 a[20:23], v237, s[24:27], s47 offen sc1
v_mfma_f32_16x16x32_bf16 v[48:51], a[244:247], a[32:35], v[48:51]
s_waitcnt lgkmcnt(4)
v_mfma_f32_16x16x32_bf16 v[64:67], a[248:251], a[32:35], v[64:67]
ds_read_b64_tr_b16 a[244:245], v19 offset:9216
v_mfma_f32_16x16x32_bf16 v[68:71], a[248:251], a[36:39], v[68:71]
ds_read_b64_tr_b16 a[246:247], v19 offset:13312
v_mfma_f32_16x16x32_bf16 v[72:75], a[248:251], a[40:43], v[72:75]
buffer_load_dwordx4 a[24:27], v238, s[24:27], s47 offen sc1
v_mfma_f32_16x16x32_bf16 v[76:79], a[248:251], a[44:47], v[76:79]
s_waitcnt lgkmcnt(4)
v_mfma_f32_16x16x32_bf16 v[92:95], a[252:255], a[44:47], v[92:95]
ds_read_b64_tr_b16 a[248:249], v20 offset:9216
v_mfma_f32_16x16x32_bf16 v[88:91], a[252:255], a[40:43], v[88:91]
ds_read_b64_tr_b16 a[250:251], v20 offset:13312
v_mfma_f32_16x16x32_bf16 v[84:87], a[252:255], a[36:39], v[84:87]
buffer_load_dwordx4 a[28:31], v239, s[24:27], s47 offen sc1
v_mfma_f32_16x16x32_bf16 v[80:83], a[252:255], a[32:35], v[80:83]
s_waitcnt lgkmcnt(4)
s_add_u32 s47, s0, 2
s_and_b32 s47, s47, 3
v_mfma_f32_16x16x32_bf16 v[96:99], a[240:243], a[32:35], v[96:99]
ds_read_b64_tr_b16 a[252:253], v21 offset:9216
s_mul_i32 s47, s47, 8192
v_mfma_f32_16x16x32_bf16 v[100:103], a[240:243], a[36:39], v[100:103]
ds_read_b64_tr_b16 a[254:255], v21 offset:13312
s_add_u32 s47, s47, s58
v_mfma_f32_16x16x32_bf16 v[104:107], a[240:243], a[40:43], v[104:107]
buffer_load_dwordx4 v13, s[88:91], s54 offen lds
v_mfma_f32_16x16x32_bf16 v[108:111], a[240:243], a[44:47], v[108:111]
s_waitcnt lgkmcnt(4)
s_add_u32 m0, s70, 896
s_add_u32 s47, s47, 65536
v_mfma_f32_16x16x32_bf16 v[124:127], a[244:247], a[44:47], v[124:127]
ds_read_b64_tr_b16 a[240:241], v22 offset:0
v_mfma_f32_16x16x32_bf16 v[120:123], a[244:247], a[40:43], v[120:123]
ds_read_b64_tr_b16 a[242:243], v22 offset:4096
v_mfma_f32_16x16x32_bf16 v[116:119], a[244:247], a[36:39], v[116:119]
buffer_load_dwordx4 v13, s[88:91], s54 offen offset:128 lds
v_mfma_f32_16x16x32_bf16 v[112:115], a[244:247], a[32:35], v[112:115]
s_waitcnt lgkmcnt(4)
s_add_u32 s54, s54, 16384
s_add_u32 m0, s70, 2048
v_mfma_f32_16x16x32_bf16 v[128:131], a[248:251], a[32:35], v[128:131]
ds_read_b64_tr_b16 a[244:245], v23 offset:0
v_mfma_f32_16x16x32_bf16 v[132:135], a[248:251], a[36:39], v[132:135]
ds_read_b64_tr_b16 a[246:247], v23 offset:4096
v_mfma_f32_16x16x32_bf16 v[136:139], a[248:251], a[40:43], v[136:139]
buffer_load_dwordx4 v13, s[88:91], s54 offen lds
v_mfma_f32_16x16x32_bf16 v[140:143], a[248:251], a[44:47], v[140:143]
s_waitcnt lgkmcnt(4)
s_add_u32 m0, s70, 2944
v_mfma_f32_16x16x32_bf16 v[156:159], a[252:255], a[44:47], v[156:159]
ds_read_b64_tr_b16 a[248:249], v24 offset:0
v_mfma_f32_16x16x32_bf16 v[152:155], a[252:255], a[40:43], v[152:155]
ds_read_b64_tr_b16 a[250:251], v24 offset:4096
v_mfma_f32_16x16x32_bf16 v[148:151], a[252:255], a[36:39], v[148:151]
buffer_load_dwordx4 v13, s[88:91], s54 offen offset:128 lds
v_mfma_f32_16x16x32_bf16 v[144:147], a[252:255], a[32:35], v[144:147]
s_waitcnt lgkmcnt(4)
s_waitcnt vmcnt(37)
v_mfma_f32_16x16x32_bf16 v[32:35], a[240:243], a[48:51], v[32:35]
ds_read_b64_tr_b16 a[252:253], v25 offset:0
v_mfma_f32_16x16x32_bf16 v[36:39], a[240:243], a[52:55], v[36:39]
ds_read_b64_tr_b16 a[254:255], v25 offset:4096
v_mfma_f32_16x16x32_bf16 v[40:43], a[240:243], a[56:59], v[40:43]
buffer_load_dwordx4 v[244:247], v2, s[36:39], s69 offen
v_mfma_f32_16x16x32_bf16 v[44:47], a[240:243], a[60:63], v[44:47]
s_waitcnt lgkmcnt(4)
v_mfma_f32_16x16x32_bf16 v[60:63], a[244:247], a[60:63], v[60:63]
ds_read_b64_tr_b16 a[240:241], v22 offset:1024
v_mfma_f32_16x16x32_bf16 v[56:59], a[244:247], a[56:59], v[56:59]
ds_read_b64_tr_b16 a[242:243], v22 offset:5120
v_mfma_f32_16x16x32_bf16 v[52:55], a[244:247], a[52:55], v[52:55]
buffer_load_dwordx4 a[32:35], v236, s[24:27], s47 offen sc1
v_mfma_f32_16x16x32_bf16 v[48:51], a[244:247], a[48:51], v[48:51]
s_waitcnt lgkmcnt(4)
v_mfma_f32_16x16x32_bf16 v[64:67], a[248:251], a[48:51], v[64:67]
ds_read_b64_tr_b16 a[244:245], v23 offset:1024
v_mfma_f32_16x16x32_bf16 v[68:71], a[248:251], a[52:55], v[68:71]
ds_read_b64_tr_b16 a[246:247], v23 offset:5120
v_mfma_f32_16x16x32_bf16 v[72:75], a[248:251], a[56:59], v[72:75]
v_mfma_f32_16x16x32_bf16 v[76:79], a[248:251], a[60:63], v[76:79]
s_waitcnt lgkmcnt(4)
v_mfma_f32_16x16x32_bf16 v[92:95], a[252:255], a[60:63], v[92:95]
ds_read_b64_tr_b16 a[248:249], v24 offset:1024
v_mfma_f32_16x16x32_bf16 v[88:91], a[252:255], a[56:59], v[88:91]
ds_read_b64_tr_b16 a[250:251], v24 offset:5120
v_add_u32 v18, 114688, v14
v_mfma_f32_16x16x32_bf16 v[84:87], a[252:255], a[52:55], v[84:87]
buffer_load_dwordx4 a[36:39], v237, s[24:27], s47 offen sc1
v_mfma_f32_16x16x32_bf16 v[80:83], a[252:255], a[48:51], v[80:83]
s_waitcnt lgkmcnt(4)
v_add_u32 v19, 114688, v15
v_add_u32 v20, 114688, v16
v_mfma_f32_16x16x32_bf16 v[96:99], a[240:243], a[48:51], v[96:99]
ds_read_b64_tr_b16 a[252:253], v25 offset:1024
v_add_u32 v21, 114688, v17
v_mfma_f32_16x16x32_bf16 v[100:103], a[240:243], a[52:55], v[100:103]
ds_read_b64_tr_b16 a[254:255], v25 offset:5120
v_mfma_f32_16x16x32_bf16 v[104:107], a[240:243], a[56:59], v[104:107]
v_mfma_f32_16x16x32_bf16 v[108:111], a[240:243], a[60:63], v[108:111]
s_waitcnt lgkmcnt(4)
s_add_u32 s54, s0, 0
s_and_b32 s54, s54, 1
v_mfma_f32_16x16x32_bf16 v[124:127], a[244:247], a[60:63], v[124:127]
ds_read_b64_tr_b16 a[240:241], v22 offset:8192
s_mul_i32 s54, s54, 131072
v_mfma_f32_16x16x32_bf16 v[120:123], a[244:247], a[56:59], v[120:123]
ds_read_b64_tr_b16 a[242:243], v22 offset:12288
s_add_u32 s54, s54, s85
v_mfma_f32_16x16x32_bf16 v[116:119], a[244:247], a[52:55], v[116:119]
buffer_load_dwordx4 a[40:43], v238, s[24:27], s47 offen sc1
v_mfma_f32_16x16x32_bf16 v[112:115], a[244:247], a[48:51], v[112:115]
s_waitcnt lgkmcnt(4)
s_add_u32 s54, s54, 1572864
s_add_u32 m0, s70, 16384
v_mfma_f32_16x16x32_bf16 v[128:131], a[248:251], a[48:51], v[128:131]
ds_read_b64_tr_b16 a[244:245], v23 offset:8192
v_mfma_f32_16x16x32_bf16 v[132:135], a[248:251], a[52:55], v[132:135]
ds_read_b64_tr_b16 a[246:247], v23 offset:12288
v_mfma_f32_16x16x32_bf16 v[136:139], a[248:251], a[56:59], v[136:139]
v_mfma_f32_16x16x32_bf16 v[140:143], a[248:251], a[60:63], v[140:143]
s_waitcnt lgkmcnt(4)
v_mfma_f32_16x16x32_bf16 v[156:159], a[252:255], a[60:63], v[156:159]
ds_read_b64_tr_b16 a[248:249], v24 offset:8192
v_mfma_f32_16x16x32_bf16 v[152:155], a[252:255], a[56:59], v[152:155]
ds_read_b64_tr_b16 a[250:251], v24 offset:12288
v_mfma_f32_16x16x32_bf16 v[148:151], a[252:255], a[52:55], v[148:151]
buffer_load_dwordx4 a[44:47], v239, s[24:27], s47 offen sc1
v_mfma_f32_16x16x32_bf16 v[144:147], a[252:255], a[48:51], v[144:147]
s_waitcnt lgkmcnt(4)
s_waitcnt vmcnt(37)
s_add_u32 s47, s47, 4096
v_mfma_f32_16x16x32_bf16 v[32:35], a[240:243], a[128:131], v[32:35]
ds_read_b64_tr_b16 a[252:253], v25 offset:8192
v_mfma_f32_16x16x32_bf16 v[36:39], a[240:243], a[132:135], v[36:39]
ds_read_b64_tr_b16 a[254:255], v25 offset:12288
v_mfma_f32_16x16x32_bf16 v[40:43], a[240:243], a[136:139], v[40:43]
buffer_load_dwordx4 a[48:51], v236, s[24:27], s47 offen sc1
v_mfma_f32_16x16x32_bf16 v[44:47], a[240:243], a[140:143], v[44:47]
s_barrier
s_waitcnt lgkmcnt(4)
v_mfma_f32_16x16x32_bf16 v[60:63], a[244:247], a[140:143], v[60:63]
ds_read_b64_tr_b16 a[240:241], v22 offset:9216
v_mfma_f32_16x16x32_bf16 v[56:59], a[244:247], a[136:139], v[56:59]
ds_read_b64_tr_b16 a[242:243], v22 offset:13312
v_mfma_f32_16x16x32_bf16 v[52:55], a[244:247], a[132:135], v[52:55]
buffer_load_dwordx4 a[52:55], v237, s[24:27], s47 offen sc1
v_mfma_f32_16x16x32_bf16 v[48:51], a[244:247], a[128:131], v[48:51]
s_waitcnt lgkmcnt(4)
v_mfma_f32_16x16x32_bf16 v[64:67], a[248:251], a[128:131], v[64:67]
ds_read_b64_tr_b16 a[244:245], v23 offset:9216
v_mfma_f32_16x16x32_bf16 v[68:71], a[248:251], a[132:135], v[68:71]
ds_read_b64_tr_b16 a[246:247], v23 offset:13312
v_mfma_f32_16x16x32_bf16 v[72:75], a[248:251], a[136:139], v[72:75]
buffer_load_dwordx4 a[56:59], v238, s[24:27], s47 offen sc1
v_mfma_f32_16x16x32_bf16 v[76:79], a[248:251], a[140:143], v[76:79]
s_waitcnt lgkmcnt(4)
v_mfma_f32_16x16x32_bf16 v[92:95], a[252:255], a[140:143], v[92:95]
ds_read_b64_tr_b16 a[248:249], v24 offset:9216
v_mfma_f32_16x16x32_bf16 v[88:91], a[252:255], a[136:139], v[88:91]
ds_read_b64_tr_b16 a[250:251], v24 offset:13312
v_mfma_f32_16x16x32_bf16 v[84:87], a[252:255], a[132:135], v[84:87]
buffer_load_dwordx4 a[60:63], v239, s[24:27], s47 offen sc1
v_mfma_f32_16x16x32_bf16 v[80:83], a[252:255], a[128:131], v[80:83]
s_waitcnt lgkmcnt(4)
s_add_u32 s47, s0, 3
s_and_b32 s47, s47, 3
v_mfma_f32_16x16x32_bf16 v[96:99], a[240:243], a[128:131], v[96:99]
ds_read_b64_tr_b16 a[252:253], v25 offset:9216
s_mul_i32 s47, s47, 8192
v_mfma_f32_16x16x32_bf16 v[100:103], a[240:243], a[132:135], v[100:103]
ds_read_b64_tr_b16 a[254:255], v25 offset:13312
s_add_u32 s47, s47, s58
v_mfma_f32_16x16x32_bf16 v[104:107], a[240:243], a[136:139], v[104:107]
buffer_load_dwordx4 v13, s[88:91], s54 offen lds
v_mfma_f32_16x16x32_bf16 v[108:111], a[240:243], a[140:143], v[108:111]
s_waitcnt lgkmcnt(4)
s_add_u32 m0, s70, 17280
s_add_u32 s47, s47, 65536
v_mfma_f32_16x16x32_bf16 v[124:127], a[244:247], a[140:143], v[124:127]
ds_read_b64_tr_b16 a[240:241], v18 offset:0
v_mfma_f32_16x16x32_bf16 v[120:123], a[244:247], a[136:139], v[120:123]
ds_read_b64_tr_b16 a[242:243], v18 offset:4096
v_mfma_f32_16x16x32_bf16 v[116:119], a[244:247], a[132:135], v[116:119]
buffer_load_dwordx4 v13, s[88:91], s54 offen offset:128 lds
v_mfma_f32_16x16x32_bf16 v[112:115], a[244:247], a[128:131], v[112:115]
s_waitcnt lgkmcnt(4)
s_add_u32 s54, s54, 16384
s_add_u32 m0, s70, 18432
v_mfma_f32_16x16x32_bf16 v[128:131], a[248:251], a[128:131], v[128:131]
ds_read_b64_tr_b16 a[244:245], v19 offset:0
v_mfma_f32_16x16x32_bf16 v[132:135], a[248:251], a[132:135], v[132:135]
ds_read_b64_tr_b16 a[246:247], v19 offset:4096
v_mfma_f32_16x16x32_bf16 v[136:139], a[248:251], a[136:139], v[136:139]
buffer_load_dwordx4 v13, s[88:91], s54 offen lds
v_mfma_f32_16x16x32_bf16 v[140:143], a[248:251], a[140:143], v[140:143]
s_waitcnt lgkmcnt(4)
s_add_u32 m0, s70, 19328
v_mfma_f32_16x16x32_bf16 v[156:159], a[252:255], a[140:143], v[156:159]
ds_read_b64_tr_b16 a[248:249], v20 offset:0
v_mfma_f32_16x16x32_bf16 v[152:155], a[252:255], a[136:139], v[152:155]
ds_read_b64_tr_b16 a[250:251], v20 offset:4096
v_mfma_f32_16x16x32_bf16 v[148:151], a[252:255], a[132:135], v[148:151]
buffer_load_dwordx4 v13, s[88:91], s54 offen offset:128 lds
v_mfma_f32_16x16x32_bf16 v[144:147], a[252:255], a[128:131], v[144:147]
s_waitcnt lgkmcnt(4)
s_waitcnt vmcnt(34)
v_mfma_f32_16x16x32_bf16 v[32:35], a[240:243], a[64:67], v[32:35]
ds_read_b64_tr_b16 a[252:253], v21 offset:0
v_mfma_f32_16x16x32_bf16 v[36:39], a[240:243], a[68:71], v[36:39]
ds_read_b64_tr_b16 a[254:255], v21 offset:4096
v_mfma_f32_16x16x32_bf16 v[40:43], a[240:243], a[72:75], v[40:43]
v_mfma_f32_16x16x32_bf16 v[44:47], a[240:243], a[76:79], v[44:47]
s_waitcnt lgkmcnt(4)
v_mfma_f32_16x16x32_bf16 v[60:63], a[244:247], a[76:79], v[60:63]
ds_read_b64_tr_b16 a[240:241], v18 offset:1024
v_mfma_f32_16x16x32_bf16 v[56:59], a[244:247], a[72:75], v[56:59]
ds_read_b64_tr_b16 a[242:243], v18 offset:5120
v_mfma_f32_16x16x32_bf16 v[52:55], a[244:247], a[68:71], v[52:55]
buffer_load_dwordx4 a[96:99], v236, s[24:27], s47 offen sc1
v_mfma_f32_16x16x32_bf16 v[48:51], a[244:247], a[64:67], v[48:51]
s_waitcnt lgkmcnt(4)
v_mfma_f32_16x16x32_bf16 v[64:67], a[248:251], a[64:67], v[64:67]
ds_read_b64_tr_b16 a[244:245], v19 offset:1024
v_mfma_f32_16x16x32_bf16 v[68:71], a[248:251], a[68:71], v[68:71]
ds_read_b64_tr_b16 a[246:247], v19 offset:5120
v_mfma_f32_16x16x32_bf16 v[72:75], a[248:251], a[72:75], v[72:75]
v_mfma_f32_16x16x32_bf16 v[76:79], a[248:251], a[76:79], v[76:79]
s_waitcnt lgkmcnt(4)
v_mfma_f32_16x16x32_bf16 v[92:95], a[252:255], a[76:79], v[92:95]
ds_read_b64_tr_b16 a[248:249], v20 offset:1024
v_mfma_f32_16x16x32_bf16 v[88:91], a[252:255], a[72:75], v[88:91]
ds_read_b64_tr_b16 a[250:251], v20 offset:5120
v_add_u32 v22, 131072, v14
v_mfma_f32_16x16x32_bf16 v[84:87], a[252:255], a[68:71], v[84:87]
buffer_load_dwordx4 a[100:103], v237, s[24:27], s47 offen sc1
v_mfma_f32_16x16x32_bf16 v[80:83], a[252:255], a[64:67], v[80:83]
s_waitcnt lgkmcnt(4)
v_add_u32 v23, 131072, v15
v_add_u32 v24, 131072, v16
v_mfma_f32_16x16x32_bf16 v[96:99], a[240:243], a[64:67], v[96:99]
ds_read_b64_tr_b16 a[252:253], v21 offset:1024
v_add_u32 v25, 131072, v17
v_mfma_f32_16x16x32_bf16 v[100:103], a[240:243], a[68:71], v[100:103]
ds_read_b64_tr_b16 a[254:255], v21 offset:5120
v_mfma_f32_16x16x32_bf16 v[104:107], a[240:243], a[72:75], v[104:107]
v_mfma_f32_16x16x32_bf16 v[108:111], a[240:243], a[76:79], v[108:111]
s_waitcnt lgkmcnt(4)
s_add_u32 s54, s0, 1
s_and_b32 s54, s54, 1
v_mfma_f32_16x16x32_bf16 v[124:127], a[244:247], a[76:79], v[124:127]
ds_read_b64_tr_b16 a[240:241], v18 offset:8192
s_mul_i32 s54, s54, 131072
v_mfma_f32_16x16x32_bf16 v[120:123], a[244:247], a[72:75], v[120:123]
ds_read_b64_tr_b16 a[242:243], v18 offset:12288
s_add_u32 s54, s54, s85
v_mfma_f32_16x16x32_bf16 v[116:119], a[244:247], a[68:71], v[116:119]
buffer_load_dwordx4 a[104:107], v238, s[24:27], s47 offen sc1
v_mfma_f32_16x16x32_bf16 v[112:115], a[244:247], a[64:67], v[112:115]
s_waitcnt lgkmcnt(4)
s_add_u32 s54, s54, 1572864
s_add_u32 m0, s70, 32768
v_mfma_f32_16x16x32_bf16 v[128:131], a[248:251], a[64:67], v[128:131]
ds_read_b64_tr_b16 a[244:245], v19 offset:8192
v_mfma_f32_16x16x32_bf16 v[132:135], a[248:251], a[68:71], v[132:135]
ds_read_b64_tr_b16 a[246:247], v19 offset:12288
v_mfma_f32_16x16x32_bf16 v[136:139], a[248:251], a[72:75], v[136:139]
v_mfma_f32_16x16x32_bf16 v[140:143], a[248:251], a[76:79], v[140:143]
s_waitcnt lgkmcnt(4)
v_mfma_f32_16x16x32_bf16 v[156:159], a[252:255], a[76:79], v[156:159]
ds_read_b64_tr_b16 a[248:249], v20 offset:8192
v_mfma_f32_16x16x32_bf16 v[152:155], a[252:255], a[72:75], v[152:155]
ds_read_b64_tr_b16 a[250:251], v20 offset:12288
v_mfma_f32_16x16x32_bf16 v[148:151], a[252:255], a[68:71], v[148:151]
buffer_load_dwordx4 a[108:111], v239, s[24:27], s47 offen sc1
v_mfma_f32_16x16x32_bf16 v[144:147], a[252:255], a[64:67], v[144:147]
s_waitcnt lgkmcnt(4)
s_waitcnt vmcnt(33)
s_add_u32 s47, s47, 4096
v_mfma_f32_16x16x32_bf16 v[32:35], a[240:243], a[80:83], v[32:35]
ds_read_b64_tr_b16 a[252:253], v21 offset:8192
v_mfma_f32_16x16x32_bf16 v[36:39], a[240:243], a[84:87], v[36:39]
ds_read_b64_tr_b16 a[254:255], v21 offset:12288
v_mfma_f32_16x16x32_bf16 v[40:43], a[240:243], a[88:91], v[40:43]
buffer_load_dwordx4 a[64:67], v236, s[24:27], s47 offen sc1
v_mfma_f32_16x16x32_bf16 v[44:47], a[240:243], a[92:95], v[44:47]
s_barrier
s_waitcnt lgkmcnt(4)
v_mfma_f32_16x16x32_bf16 v[60:63], a[244:247], a[92:95], v[60:63]
ds_read_b64_tr_b16 a[240:241], v18 offset:9216
v_mfma_f32_16x16x32_bf16 v[56:59], a[244:247], a[88:91], v[56:59]
ds_read_b64_tr_b16 a[242:243], v18 offset:13312
v_mfma_f32_16x16x32_bf16 v[52:55], a[244:247], a[84:87], v[52:55]
buffer_load_dwordx4 a[68:71], v237, s[24:27], s47 offen sc1
v_mfma_f32_16x16x32_bf16 v[48:51], a[244:247], a[80:83], v[48:51]
s_waitcnt lgkmcnt(4)
s_lshl_b32 s97, s40, 2
s_add_u32 s97, s97, s43
v_mfma_f32_16x16x32_bf16 v[64:67], a[248:251], a[80:83], v[64:67]
ds_read_b64_tr_b16 a[244:245], v19 offset:9216
s_lshl_b32 s97, s97, 3
v_mfma_f32_16x16x32_bf16 v[68:71], a[248:251], a[84:87], v[68:71]
ds_read_b64_tr_b16 a[246:247], v19 offset:13312
s_add_u32 s97, s97, 16384
v_mfma_f32_16x16x32_bf16 v[72:75], a[248:251], a[88:91], v[72:75]
buffer_load_dwordx4 a[72:75], v238, s[24:27], s47 offen sc1
v_mfma_f32_16x16x32_bf16 v[76:79], a[248:251], a[92:95], v[76:79]
s_waitcnt lgkmcnt(4)
v_mov_b32 v26, s97
v_mov_b32 v28, s44
v_mfma_f32_16x16x32_bf16 v[92:95], a[252:255], a[92:95], v[92:95]
ds_read_b64_tr_b16 a[248:249], v20 offset:9216
v_mov_b32 v29, s45
v_mfma_f32_16x16x32_bf16 v[88:91], a[252:255], a[88:91], v[88:91]
ds_read_b64_tr_b16 a[250:251], v20 offset:13312
v_mfma_f32_16x16x32_bf16 v[84:87], a[252:255], a[84:87], v[84:87]
buffer_load_dwordx4 a[76:79], v239, s[24:27], s47 offen sc1
v_mfma_f32_16x16x32_bf16 v[80:83], a[252:255], a[80:83], v[80:83]
s_waitcnt lgkmcnt(4)
s_add_u32 s47, s0, 0
s_and_b32 s47, s47, 1
v_mfma_f32_16x16x32_bf16 v[96:99], a[240:243], a[80:83], v[96:99]
ds_read_b64_tr_b16 a[252:253], v21 offset:9216
s_mul_i32 s47, s47, 8192
v_mfma_f32_16x16x32_bf16 v[100:103], a[240:243], a[84:87], v[100:103]
ds_read_b64_tr_b16 a[254:255], v21 offset:13312
s_add_u32 s47, s47, s58
v_mfma_f32_16x16x32_bf16 v[104:107], a[240:243], a[88:91], v[104:107]
buffer_load_dwordx4 v13, s[88:91], s54 offen lds
v_mfma_f32_16x16x32_bf16 v[108:111], a[240:243], a[92:95], v[108:111]
s_waitcnt lgkmcnt(4)
s_add_u32 m0, s70, 33664
s_add_u32 s47, s47, 98304
v_mfma_f32_16x16x32_bf16 v[124:127], a[244:247], a[92:95], v[124:127]
ds_read_b64_tr_b16 a[240:241], v22 offset:0
v_mfma_f32_16x16x32_bf16 v[120:123], a[244:247], a[88:91], v[120:123]
ds_read_b64_tr_b16 a[242:243], v22 offset:4096
v_mfma_f32_16x16x32_bf16 v[116:119], a[244:247], a[84:87], v[116:119]
buffer_load_dwordx4 v13, s[88:91], s54 offen offset:128 lds
v_mfma_f32_16x16x32_bf16 v[112:115], a[244:247], a[80:83], v[112:115]
s_waitcnt lgkmcnt(4)
s_add_u32 s54, s54, 16384
s_add_u32 m0, s70, 34816
v_mfma_f32_16x16x32_bf16 v[128:131], a[248:251], a[80:83], v[128:131]
ds_read_b64_tr_b16 a[244:245], v23 offset:0
v_mfma_f32_16x16x32_bf16 v[132:135], a[248:251], a[84:87], v[132:135]
ds_read_b64_tr_b16 a[246:247], v23 offset:4096
v_mfma_f32_16x16x32_bf16 v[136:139], a[248:251], a[88:91], v[136:139]
buffer_load_dwordx4 v13, s[88:91], s54 offen lds
v_mfma_f32_16x16x32_bf16 v[140:143], a[248:251], a[92:95], v[140:143]
s_waitcnt lgkmcnt(4)
s_add_u32 m0, s70, 35712
v_mfma_f32_16x16x32_bf16 v[156:159], a[252:255], a[92:95], v[156:159]
ds_read_b64_tr_b16 a[248:249], v24 offset:0
v_mfma_f32_16x16x32_bf16 v[152:155], a[252:255], a[88:91], v[152:155]
ds_read_b64_tr_b16 a[250:251], v24 offset:4096
v_mfma_f32_16x16x32_bf16 v[148:151], a[252:255], a[84:87], v[148:151]
buffer_load_dwordx4 v13, s[88:91], s54 offen offset:128 lds
v_mfma_f32_16x16x32_bf16 v[144:147], a[252:255], a[80:83], v[144:147]
s_waitcnt lgkmcnt(4)
s_waitcnt vmcnt(33)
v_mfma_f32_16x16x32_bf16 v[32:35], a[240:243], a[0:3], v[32:35]
ds_read_b64_tr_b16 a[252:253], v25 offset:0
v_mfma_f32_16x16x32_bf16 v[36:39], a[240:243], a[4:7], v[36:39]
ds_read_b64_tr_b16 a[254:255], v25 offset:4096
v_mfma_f32_16x16x32_bf16 v[40:43], a[240:243], a[8:11], v[40:43]
v_mfma_f32_16x16x32_bf16 v[44:47], a[240:243], a[12:15], v[44:47]
s_waitcnt lgkmcnt(4)
v_mfma_f32_16x16x32_bf16 v[60:63], a[244:247], a[12:15], v[60:63]
ds_read_b64_tr_b16 a[240:241], v22 offset:1024
v_mfma_f32_16x16x32_bf16 v[56:59], a[244:247], a[8:11], v[56:59]
ds_read_b64_tr_b16 a[242:243], v22 offset:5120
v_mfma_f32_16x16x32_bf16 v[52:55], a[244:247], a[4:7], v[52:55]
buffer_load_dwordx4 a[80:83], v236, s[24:27], s47 offen sc1
v_mfma_f32_16x16x32_bf16 v[48:51], a[244:247], a[0:3], v[48:51]
s_waitcnt lgkmcnt(4)
v_mfma_f32_16x16x32_bf16 v[64:67], a[248:251], a[0:3], v[64:67]
ds_read_b64_tr_b16 a[244:245], v23 offset:1024
v_mfma_f32_16x16x32_bf16 v[68:71], a[248:251], a[4:7], v[68:71]
ds_read_b64_tr_b16 a[246:247], v23 offset:5120
v_mfma_f32_16x16x32_bf16 v[72:75], a[248:251], a[8:11], v[72:75]
global_store_dwordx2 v26, v[28:29], s[22:23] sc1
v_mfma_f32_16x16x32_bf16 v[76:79], a[248:251], a[12:15], v[76:79]
s_waitcnt lgkmcnt(4)
v_mfma_f32_16x16x32_bf16 v[92:95], a[252:255], a[12:15], v[92:95]
ds_read_b64_tr_b16 a[248:249], v24 offset:1024
v_mfma_f32_16x16x32_bf16 v[88:91], a[252:255], a[8:11], v[88:91]
ds_read_b64_tr_b16 a[250:251], v24 offset:5120
v_add_u32 v18, 147456, v14
v_mfma_f32_16x16x32_bf16 v[84:87], a[252:255], a[4:7], v[84:87]
buffer_load_dwordx4 a[84:87], v237, s[24:27], s47 offen sc1
v_mfma_f32_16x16x32_bf16 v[80:83], a[252:255], a[0:3], v[80:83]
s_waitcnt lgkmcnt(4)
v_add_u32 v19, 147456, v15
v_add_u32 v20, 147456, v16
v_mfma_f32_16x16x32_bf16 v[96:99], a[240:243], a[0:3], v[96:99]
ds_read_b64_tr_b16 a[252:253], v25 offset:1024
v_add_u32 v21, 147456, v17
v_mfma_f32_16x16x32_bf16 v[100:103], a[240:243], a[4:7], v[100:103]
ds_read_b64_tr_b16 a[254:255], v25 offset:5120
v_mfma_f32_16x16x32_bf16 v[104:107], a[240:243], a[8:11], v[104:107]
v_mfma_f32_16x16x32_bf16 v[108:111], a[240:243], a[12:15], v[108:111]
s_waitcnt lgkmcnt(4)
s_add_u32 s54, s0, 0
s_and_b32 s54, s54, 7
v_mfma_f32_16x16x32_bf16 v[124:127], a[244:247], a[12:15], v[124:127]
ds_read_b64_tr_b16 a[240:241], v22 offset:8192
s_mul_i32 s54, s54, 131072
v_mfma_f32_16x16x32_bf16 v[120:123], a[244:247], a[8:11], v[120:123]
ds_read_b64_tr_b16 a[242:243], v22 offset:12288
s_add_u32 s54, s54, s80
v_mfma_f32_16x16x32_bf16 v[116:119], a[244:247], a[4:7], v[116:119]
buffer_load_dwordx4 a[88:91], v238, s[24:27], s47 offen sc1
v_mfma_f32_16x16x32_bf16 v[112:115], a[244:247], a[0:3], v[112:115]
s_waitcnt lgkmcnt(4)
s_add_u32 m0, s70, 49152
v_mfma_f32_16x16x32_bf16 v[128:131], a[248:251], a[0:3], v[128:131]
ds_read_b64_tr_b16 a[244:245], v23 offset:8192
v_mfma_f32_16x16x32_bf16 v[132:135], a[248:251], a[4:7], v[132:135]
ds_read_b64_tr_b16 a[246:247], v23 offset:12288
v_mfma_f32_16x16x32_bf16 v[136:139], a[248:251], a[8:11], v[136:139]
v_mfma_f32_16x16x32_bf16 v[140:143], a[248:251], a[12:15], v[140:143]
s_waitcnt lgkmcnt(4)
v_mfma_f32_16x16x32_bf16 v[156:159], a[252:255], a[12:15], v[156:159]
ds_read_b64_tr_b16 a[248:249], v24 offset:8192
v_mfma_f32_16x16x32_bf16 v[152:155], a[252:255], a[8:11], v[152:155]
ds_read_b64_tr_b16 a[250:251], v24 offset:12288
v_mfma_f32_16x16x32_bf16 v[148:151], a[252:255], a[4:7], v[148:151]
buffer_load_dwordx4 a[92:95], v239, s[24:27], s47 offen sc1
v_mfma_f32_16x16x32_bf16 v[144:147], a[252:255], a[0:3], v[144:147]
s_waitcnt lgkmcnt(4)
s_waitcnt vmcnt(34)
s_add_u32 s47, s47, 4096
v_mfma_f32_16x16x32_bf16 v[32:35], a[240:243], a[16:19], v[32:35]
ds_read_b64_tr_b16 a[252:253], v25 offset:8192
v_mfma_f32_16x16x32_bf16 v[36:39], a[240:243], a[20:23], v[36:39]
ds_read_b64_tr_b16 a[254:255], v25 offset:12288
v_mfma_f32_16x16x32_bf16 v[40:43], a[240:243], a[24:27], v[40:43]
buffer_load_dwordx4 a[0:3], v236, s[24:27], s47 offen sc1
v_mfma_f32_16x16x32_bf16 v[44:47], a[240:243], a[28:31], v[44:47]
s_barrier
s_waitcnt lgkmcnt(4)
v_mfma_f32_16x16x32_bf16 v[60:63], a[244:247], a[28:31], v[60:63]
ds_read_b64_tr_b16 a[240:241], v22 offset:9216
v_mfma_f32_16x16x32_bf16 v[56:59], a[244:247], a[24:27], v[56:59]
ds_read_b64_tr_b16 a[242:243], v22 offset:13312
v_mfma_f32_16x16x32_bf16 v[52:55], a[244:247], a[20:23], v[52:55]
buffer_load_dwordx4 a[4:7], v237, s[24:27], s47 offen sc1
v_mfma_f32_16x16x32_bf16 v[48:51], a[244:247], a[16:19], v[48:51]
s_waitcnt lgkmcnt(4)
v_mfma_f32_16x16x32_bf16 v[64:67], a[248:251], a[16:19], v[64:67]
ds_read_b64_tr_b16 a[244:245], v23 offset:9216
v_mfma_f32_16x16x32_bf16 v[68:71], a[248:251], a[20:23], v[68:71]
ds_read_b64_tr_b16 a[246:247], v23 offset:13312
v_mfma_f32_16x16x32_bf16 v[72:75], a[248:251], a[24:27], v[72:75]
buffer_load_dwordx4 a[8:11], v238, s[24:27], s47 offen sc1
v_mfma_f32_16x16x32_bf16 v[76:79], a[248:251], a[28:31], v[76:79]
s_waitcnt lgkmcnt(4)
v_mfma_f32_16x16x32_bf16 v[92:95], a[252:255], a[28:31], v[92:95]
ds_read_b64_tr_b16 a[248:249], v24 offset:9216
v_mfma_f32_16x16x32_bf16 v[88:91], a[252:255], a[24:27], v[88:91]
ds_read_b64_tr_b16 a[250:251], v24 offset:13312
v_mfma_f32_16x16x32_bf16 v[84:87], a[252:255], a[20:23], v[84:87]
buffer_load_dwordx4 a[12:15], v239, s[24:27], s47 offen sc1
v_mfma_f32_16x16x32_bf16 v[80:83], a[252:255], a[16:19], v[80:83]
s_waitcnt lgkmcnt(4)
s_add_u32 s47, s0, 1
s_and_b32 s47, s47, 1
v_mfma_f32_16x16x32_bf16 v[96:99], a[240:243], a[16:19], v[96:99]
ds_read_b64_tr_b16 a[252:253], v25 offset:9216
s_mul_i32 s47, s47, 8192
v_mfma_f32_16x16x32_bf16 v[100:103], a[240:243], a[20:23], v[100:103]
ds_read_b64_tr_b16 a[254:255], v25 offset:13312
s_add_u32 s47, s47, s58
v_mfma_f32_16x16x32_bf16 v[104:107], a[240:243], a[24:27], v[104:107]
buffer_load_dwordx4 v13, s[76:79], s54 offen lds
v_mfma_f32_16x16x32_bf16 v[108:111], a[240:243], a[28:31], v[108:111]
s_waitcnt lgkmcnt(4)
s_add_u32 m0, s70, 50048
s_add_u32 s47, s47, 98304
v_mfma_f32_16x16x32_bf16 v[124:127], a[244:247], a[28:31], v[124:127]
ds_read_b64_tr_b16 a[240:241], v18 offset:0
v_mfma_f32_16x16x32_bf16 v[120:123], a[244:247], a[24:27], v[120:123]
ds_read_b64_tr_b16 a[242:243], v18 offset:4096
v_mfma_f32_16x16x32_bf16 v[116:119], a[244:247], a[20:23], v[116:119]
buffer_load_dwordx4 v13, s[76:79], s54 offen offset:128 lds
v_mfma_f32_16x16x32_bf16 v[112:115], a[244:247], a[16:19], v[112:115]
s_waitcnt lgkmcnt(4)
s_add_u32 s54, s54, 16384
s_add_u32 m0, s70, 51200
v_mfma_f32_16x16x32_bf16 v[128:131], a[248:251], a[16:19], v[128:131]
ds_read_b64_tr_b16 a[244:245], v19 offset:0
v_mfma_f32_16x16x32_bf16 v[132:135], a[248:251], a[20:23], v[132:135]
ds_read_b64_tr_b16 a[246:247], v19 offset:4096
v_mfma_f32_16x16x32_bf16 v[136:139], a[248:251], a[24:27], v[136:139]
buffer_load_dwordx4 v13, s[76:79], s54 offen lds
v_mfma_f32_16x16x32_bf16 v[140:143], a[248:251], a[28:31], v[140:143]
s_waitcnt lgkmcnt(4)
s_add_u32 m0, s70, 52096
v_mfma_f32_16x16x32_bf16 v[156:159], a[252:255], a[28:31], v[156:159]
ds_read_b64_tr_b16 a[248:249], v20 offset:0
v_mfma_f32_16x16x32_bf16 v[152:155], a[252:255], a[24:27], v[152:155]
ds_read_b64_tr_b16 a[250:251], v20 offset:4096
v_mfma_f32_16x16x32_bf16 v[148:151], a[252:255], a[20:23], v[148:151]
buffer_load_dwordx4 v13, s[76:79], s54 offen offset:128 lds
v_mfma_f32_16x16x32_bf16 v[144:147], a[252:255], a[16:19], v[144:147]
s_waitcnt lgkmcnt(4)
s_waitcnt vmcnt(33)
v_mfma_f32_16x16x32_bf16 v[32:35], a[240:243], a[32:35], v[32:35]
ds_read_b64_tr_b16 a[252:253], v21 offset:0
v_mfma_f32_16x16x32_bf16 v[36:39], a[240:243], a[36:39], v[36:39]
ds_read_b64_tr_b16 a[254:255], v21 offset:4096
v_mfma_f32_16x16x32_bf16 v[40:43], a[240:243], a[40:43], v[40:43]
v_mfma_f32_16x16x32_bf16 v[44:47], a[240:243], a[44:47], v[44:47]
s_waitcnt lgkmcnt(4)
v_mfma_f32_16x16x32_bf16 v[60:63], a[244:247], a[44:47], v[60:63]
ds_read_b64_tr_b16 a[240:241], v18 offset:1024
v_mfma_f32_16x16x32_bf16 v[56:59], a[244:247], a[40:43], v[56:59]
ds_read_b64_tr_b16 a[242:243], v18 offset:5120
v_mfma_f32_16x16x32_bf16 v[52:55], a[244:247], a[36:39], v[52:55]
buffer_load_dwordx4 a[16:19], v236, s[24:27], s47 offen sc1
v_mfma_f32_16x16x32_bf16 v[48:51], a[244:247], a[32:35], v[48:51]
s_waitcnt lgkmcnt(4)
v_mfma_f32_16x16x32_bf16 v[64:67], a[248:251], a[32:35], v[64:67]
ds_read_b64_tr_b16 a[244:245], v19 offset:1024
v_mfma_f32_16x16x32_bf16 v[68:71], a[248:251], a[36:39], v[68:71]
ds_read_b64_tr_b16 a[246:247], v19 offset:5120
v_mfma_f32_16x16x32_bf16 v[72:75], a[248:251], a[40:43], v[72:75]
v_mfma_f32_16x16x32_bf16 v[76:79], a[248:251], a[44:47], v[76:79]
s_waitcnt lgkmcnt(4)
v_mfma_f32_16x16x32_bf16 v[92:95], a[252:255], a[44:47], v[92:95]
ds_read_b64_tr_b16 a[248:249], v20 offset:1024
v_mfma_f32_16x16x32_bf16 v[88:91], a[252:255], a[40:43], v[88:91]
ds_read_b64_tr_b16 a[250:251], v20 offset:5120
v_add_u32 v22, 0, v14
v_mfma_f32_16x16x32_bf16 v[84:87], a[252:255], a[36:39], v[84:87]
buffer_load_dwordx4 a[20:23], v237, s[24:27], s47 offen sc1
v_mfma_f32_16x16x32_bf16 v[80:83], a[252:255], a[32:35], v[80:83]
s_waitcnt lgkmcnt(4)
v_add_u32 v23, 0, v15
v_add_u32 v24, 0, v16
v_mfma_f32_16x16x32_bf16 v[96:99], a[240:243], a[32:35], v[96:99]
ds_read_b64_tr_b16 a[252:253], v21 offset:1024
v_add_u32 v25, 0, v17
v_mfma_f32_16x16x32_bf16 v[100:103], a[240:243], a[36:39], v[100:103]
ds_read_b64_tr_b16 a[254:255], v21 offset:5120
v_mfma_f32_16x16x32_bf16 v[104:107], a[240:243], a[40:43], v[104:107]
v_mfma_f32_16x16x32_bf16 v[108:111], a[240:243], a[44:47], v[108:111]
s_waitcnt lgkmcnt(4)
s_add_u32 s54, s0, 1
s_and_b32 s54, s54, 7
v_mfma_f32_16x16x32_bf16 v[124:127], a[244:247], a[44:47], v[124:127]
ds_read_b64_tr_b16 a[240:241], v18 offset:8192
s_mul_i32 s54, s54, 131072
v_mfma_f32_16x16x32_bf16 v[120:123], a[244:247], a[40:43], v[120:123]
ds_read_b64_tr_b16 a[242:243], v18 offset:12288
s_add_u32 s54, s54, s80
v_mfma_f32_16x16x32_bf16 v[116:119], a[244:247], a[36:39], v[116:119]
buffer_load_dwordx4 a[24:27], v238, s[24:27], s47 offen sc1
v_mfma_f32_16x16x32_bf16 v[112:115], a[244:247], a[32:35], v[112:115]
s_waitcnt lgkmcnt(4)
s_add_u32 m0, s70, 65536
s_lshl_b32 s97, s41, 5
v_mfma_f32_16x16x32_bf16 v[128:131], a[248:251], a[32:35], v[128:131]
ds_read_b64_tr_b16 a[244:245], v19 offset:8192
s_lshl_b32 s56, s59, 3
v_mfma_f32_16x16x32_bf16 v[132:135], a[248:251], a[36:39], v[132:135]
ds_read_b64_tr_b16 a[246:247], v19 offset:12288
s_add_u32 s97, s97, s56
v_mfma_f32_16x16x32_bf16 v[136:139], a[248:251], a[40:43], v[136:139]
s_lshl_b32 s97, s97, 2
s_add_u32 s97, s97, s43
s_lshl_b32 s97, s97, 3
v_mfma_f32_16x16x32_bf16 v[140:143], a[248:251], a[44:47], v[140:143]
s_waitcnt lgkmcnt(4)
s_add_u32 s97, s97, 16384
v_and_b32 v26, 7, v1
v_mfma_f32_16x16x32_bf16 v[156:159], a[252:255], a[44:47], v[156:159]
ds_read_b64_tr_b16 a[248:249], v20 offset:8192
v_lshlrev_b32 v26, 5, v26
v_mfma_f32_16x16x32_bf16 v[152:155], a[252:255], a[40:43], v[152:155]
ds_read_b64_tr_b16 a[250:251], v20 offset:12288
v_add_u32 v26, s97, v26
v_mfma_f32_16x16x32_bf16 v[148:151], a[252:255], a[36:39], v[148:151]
buffer_load_dwordx4 a[28:31], v239, s[24:27], s47 offen sc1
v_mfma_f32_16x16x32_bf16 v[144:147], a[252:255], a[32:35], v[144:147]
s_waitcnt lgkmcnt(4)
s_waitcnt vmcnt(33)
s_add_u32 s47, s47, 4096
v_mfma_f32_16x16x32_bf16 v[32:35], a[240:243], a[48:51], v[32:35]
ds_read_b64_tr_b16 a[252:253], v21 offset:8192
v_mfma_f32_16x16x32_bf16 v[36:39], a[240:243], a[52:55], v[36:39]
ds_read_b64_tr_b16 a[254:255], v21 offset:12288
v_mfma_f32_16x16x32_bf16 v[40:43], a[240:243], a[56:59], v[40:43]
buffer_load_dwordx4 a[32:35], v236, s[24:27], s47 offen sc1
v_mfma_f32_16x16x32_bf16 v[44:47], a[240:243], a[60:63], v[44:47]
s_barrier
s_waitcnt lgkmcnt(4)
v_mfma_f32_16x16x32_bf16 v[60:63], a[244:247], a[60:63], v[60:63]
ds_read_b64_tr_b16 a[240:241], v18 offset:9216
v_mfma_f32_16x16x32_bf16 v[56:59], a[244:247], a[56:59], v[56:59]
ds_read_b64_tr_b16 a[242:243], v18 offset:13312
v_mfma_f32_16x16x32_bf16 v[52:55], a[244:247], a[52:55], v[52:55]
buffer_load_dwordx4 a[36:39], v237, s[24:27], s47 offen sc1
v_mfma_f32_16x16x32_bf16 v[48:51], a[244:247], a[48:51], v[48:51]
s_waitcnt lgkmcnt(4)
v_mfma_f32_16x16x32_bf16 v[64:67], a[248:251], a[48:51], v[64:67]
ds_read_b64_tr_b16 a[244:245], v19 offset:9216
v_mfma_f32_16x16x32_bf16 v[68:71], a[248:251], a[52:55], v[68:71]
ds_read_b64_tr_b16 a[246:247], v19 offset:13312
v_mfma_f32_16x16x32_bf16 v[72:75], a[248:251], a[56:59], v[72:75]
buffer_load_dwordx4 a[40:43], v238, s[24:27], s47 offen sc1
v_mfma_f32_16x16x32_bf16 v[76:79], a[248:251], a[60:63], v[76:79]
s_waitcnt lgkmcnt(4)
v_mfma_f32_16x16x32_bf16 v[92:95], a[252:255], a[60:63], v[92:95]
ds_read_b64_tr_b16 a[248:249], v20 offset:9216
v_mfma_f32_16x16x32_bf16 v[88:91], a[252:255], a[56:59], v[88:91]
ds_read_b64_tr_b16 a[250:251], v20 offset:13312
v_mfma_f32_16x16x32_bf16 v[84:87], a[252:255], a[52:55], v[84:87]
buffer_load_dwordx4 a[44:47], v239, s[24:27], s47 offen sc1
v_mfma_f32_16x16x32_bf16 v[80:83], a[252:255], a[48:51], v[80:83]
s_waitcnt lgkmcnt(4)
v_mfma_f32_16x16x32_bf16 v[96:99], a[240:243], a[48:51], v[96:99]
ds_read_b64_tr_b16 a[252:253], v21 offset:9216
v_mfma_f32_16x16x32_bf16 v[100:103], a[240:243], a[52:55], v[100:103]
ds_read_b64_tr_b16 a[254:255], v21 offset:13312
v_mfma_f32_16x16x32_bf16 v[104:107], a[240:243], a[56:59], v[104:107]
buffer_load_dwordx4 v13, s[76:79], s54 offen lds
v_mfma_f32_16x16x32_bf16 v[108:111], a[240:243], a[60:63], v[108:111]
s_waitcnt lgkmcnt(4)
s_add_u32 m0, s70, 66432
v_mfma_f32_16x16x32_bf16 v[124:127], a[244:247], a[60:63], v[124:127]
ds_read_b64_tr_b16 a[240:241], v22 offset:0
v_mfma_f32_16x16x32_bf16 v[120:123], a[244:247], a[56:59], v[120:123]
ds_read_b64_tr_b16 a[242:243], v22 offset:4096
v_mfma_f32_16x16x32_bf16 v[116:119], a[244:247], a[52:55], v[116:119]
buffer_load_dwordx4 v13, s[76:79], s54 offen offset:128 lds
v_mfma_f32_16x16x32_bf16 v[112:115], a[244:247], a[48:51], v[112:115]
s_waitcnt lgkmcnt(4)
s_add_u32 s54, s54, 16384
s_add_u32 m0, s70, 67584
v_mfma_f32_16x16x32_bf16 v[128:131], a[248:251], a[48:51], v[128:131]
ds_read_b64_tr_b16 a[244:245], v23 offset:0
v_mfma_f32_16x16x32_bf16 v[132:135], a[248:251], a[52:55], v[132:135]
ds_read_b64_tr_b16 a[246:247], v23 offset:4096
v_mfma_f32_16x16x32_bf16 v[136:139], a[248:251], a[56:59], v[136:139]
buffer_load_dwordx4 v13, s[76:79], s54 offen lds
v_mfma_f32_16x16x32_bf16 v[140:143], a[248:251], a[60:63], v[140:143]
s_waitcnt lgkmcnt(4)
s_add_u32 m0, s70, 68480
v_mfma_f32_16x16x32_bf16 v[156:159], a[252:255], a[60:63], v[156:159]
ds_read_b64_tr_b16 a[248:249], v24 offset:0
v_mfma_f32_16x16x32_bf16 v[152:155], a[252:255], a[56:59], v[152:155]
ds_read_b64_tr_b16 a[250:251], v24 offset:4096
v_mfma_f32_16x16x32_bf16 v[148:151], a[252:255], a[52:55], v[148:151]
buffer_load_dwordx4 v13, s[76:79], s54 offen offset:128 lds
v_mfma_f32_16x16x32_bf16 v[144:147], a[252:255], a[48:51], v[144:147]
s_waitcnt lgkmcnt(4)
s_waitcnt vmcnt(33)
v_mfma_f32_16x16x32_bf16 v[32:35], a[240:243], a[96:99], v[32:35]
ds_read_b64_tr_b16 a[252:253], v25 offset:0
v_mfma_f32_16x16x32_bf16 v[36:39], a[240:243], a[100:103], v[36:39]
ds_read_b64_tr_b16 a[254:255], v25 offset:4096
v_mfma_f32_16x16x32_bf16 v[40:43], a[240:243], a[104:107], v[40:43]
global_load_dwordx2 v[28:29], v26, s[22:23] sc1
v_mfma_f32_16x16x32_bf16 v[44:47], a[240:243], a[108:111], v[44:47]
s_waitcnt lgkmcnt(4)
v_mfma_f32_16x16x32_bf16 v[60:63], a[244:247], a[108:111], v[60:63]
ds_read_b64_tr_b16 a[240:241], v22 offset:1024
v_mfma_f32_16x16x32_bf16 v[56:59], a[244:247], a[104:107], v[56:59]
ds_read_b64_tr_b16 a[242:243], v22 offset:5120
v_mfma_f32_16x16x32_bf16 v[52:55], a[244:247], a[100:103], v[52:55]
v_mfma_f32_16x16x32_bf16 v[48:51], a[244:247], a[96:99], v[48:51]
s_waitcnt lgkmcnt(4)
v_mfma_f32_16x16x32_bf16 v[64:67], a[248:251], a[96:99], v[64:67]
ds_read_b64_tr_b16 a[244:245], v23 offset:1024
v_mfma_f32_16x16x32_bf16 v[68:71], a[248:251], a[100:103], v[68:71]
ds_read_b64_tr_b16 a[246:247], v23 offset:5120
v_mfma_f32_16x16x32_bf16 v[72:75], a[248:251], a[104:107], v[72:75]
v_mfma_f32_16x16x32_bf16 v[76:79], a[248:251], a[108:111], v[76:79]
s_waitcnt lgkmcnt(4)
v_mfma_f32_16x16x32_bf16 v[92:95], a[252:255], a[108:111], v[92:95]
ds_read_b64_tr_b16 a[248:249], v24 offset:1024
v_mfma_f32_16x16x32_bf16 v[88:91], a[252:255], a[104:107], v[88:91]
ds_read_b64_tr_b16 a[250:251], v24 offset:5120
v_add_u32 v18, 16384, v14
v_mfma_f32_16x16x32_bf16 v[84:87], a[252:255], a[100:103], v[84:87]
v_add_u32 v19, 16384, v15
v_add_u32 v20, 16384, v16
v_mfma_f32_16x16x32_bf16 v[80:83], a[252:255], a[96:99], v[80:83]
s_waitcnt lgkmcnt(4)
v_add_u32 v21, 16384, v17
v_mfma_f32_16x16x32_bf16 v[96:99], a[240:243], a[96:99], v[96:99]
ds_read_b64_tr_b16 a[252:253], v25 offset:1024
v_mfma_f32_16x16x32_bf16 v[100:103], a[240:243], a[100:103], v[100:103]
ds_read_b64_tr_b16 a[254:255], v25 offset:5120
v_mfma_f32_16x16x32_bf16 v[104:107], a[240:243], a[104:107], v[104:107]
v_mfma_f32_16x16x32_bf16 v[108:111], a[240:243], a[108:111], v[108:111]
s_waitcnt lgkmcnt(4)
s_add_u32 s54, s0, 2
s_and_b32 s54, s54, 7
v_mfma_f32_16x16x32_bf16 v[124:127], a[244:247], a[108:111], v[124:127]
ds_read_b64_tr_b16 a[240:241], v22 offset:8192
s_mul_i32 s54, s54, 131072
v_mfma_f32_16x16x32_bf16 v[120:123], a[244:247], a[104:107], v[120:123]
ds_read_b64_tr_b16 a[242:243], v22 offset:12288
s_add_u32 s54, s54, s80
v_mfma_f32_16x16x32_bf16 v[116:119], a[244:247], a[100:103], v[116:119]
s_add_u32 m0, s70, 81920
v_mfma_f32_16x16x32_bf16 v[112:115], a[244:247], a[96:99], v[112:115]
s_waitcnt lgkmcnt(4)
v_mfma_f32_16x16x32_bf16 v[128:131], a[248:251], a[96:99], v[128:131]
ds_read_b64_tr_b16 a[244:245], v23 offset:8192
v_mfma_f32_16x16x32_bf16 v[132:135], a[248:251], a[100:103], v[132:135]
ds_read_b64_tr_b16 a[246:247], v23 offset:12288
v_mfma_f32_16x16x32_bf16 v[136:139], a[248:251], a[104:107], v[136:139]
v_mfma_f32_16x16x32_bf16 v[140:143], a[248:251], a[108:111], v[140:143]
s_waitcnt lgkmcnt(4)
v_mfma_f32_16x16x32_bf16 v[156:159], a[252:255], a[108:111], v[156:159]
ds_read_b64_tr_b16 a[248:249], v24 offset:8192
v_mfma_f32_16x16x32_bf16 v[152:155], a[252:255], a[104:107], v[152:155]
ds_read_b64_tr_b16 a[250:251], v24 offset:12288
v_mfma_f32_16x16x32_bf16 v[148:151], a[252:255], a[100:103], v[148:151]
v_mfma_f32_16x16x32_bf16 v[144:147], a[252:255], a[96:99], v[144:147]
s_waitcnt lgkmcnt(4)
s_waitcnt vmcnt(30)
v_mfma_f32_16x16x32_bf16 v[32:35], a[240:243], a[64:67], v[32:35]
ds_read_b64_tr_b16 a[252:253], v25 offset:8192
v_mfma_f32_16x16x32_bf16 v[36:39], a[240:243], a[68:71], v[36:39]
ds_read_b64_tr_b16 a[254:255], v25 offset:12288
v_mfma_f32_16x16x32_bf16 v[40:43], a[240:243], a[72:75], v[40:43]
v_mfma_f32_16x16x32_bf16 v[44:47], a[240:243], a[76:79], v[44:47]
s_barrier
s_waitcnt lgkmcnt(4)
v_mfma_f32_16x16x32_bf16 v[60:63], a[244:247], a[76:79], v[60:63]
ds_read_b64_tr_b16 a[240:241], v22 offset:9216
v_mfma_f32_16x16x32_bf16 v[56:59], a[244:247], a[72:75], v[56:59]
ds_read_b64_tr_b16 a[242:243], v22 offset:13312
v_mfma_f32_16x16x32_bf16 v[52:55], a[244:247], a[68:71], v[52:55]
v_mfma_f32_16x16x32_bf16 v[48:51], a[244:247], a[64:67], v[48:51]
s_waitcnt lgkmcnt(4)
v_mfma_f32_16x16x32_bf16 v[64:67], a[248:251], a[64:67], v[64:67]
ds_read_b64_tr_b16 a[244:245], v23 offset:9216
v_mfma_f32_16x16x32_bf16 v[68:71], a[248:251], a[68:71], v[68:71]
ds_read_b64_tr_b16 a[246:247], v23 offset:13312
v_mfma_f32_16x16x32_bf16 v[72:75], a[248:251], a[72:75], v[72:75]
v_mfma_f32_16x16x32_bf16 v[76:79], a[248:251], a[76:79], v[76:79]
s_waitcnt lgkmcnt(4)
v_mfma_f32_16x16x32_bf16 v[92:95], a[252:255], a[76:79], v[92:95]
ds_read_b64_tr_b16 a[248:249], v24 offset:9216
v_mfma_f32_16x16x32_bf16 v[88:91], a[252:255], a[72:75], v[88:91]
ds_read_b64_tr_b16 a[250:251], v24 offset:13312
s_add_u32 s96, s0, 0
v_mfma_f32_16x16x32_bf16 v[84:87], a[252:255], a[68:71], v[84:87]
s_and_b32 s96, s96, 7
s_mul_i32 s96, s96, 8192
s_add_u32 s96, s96, s3
v_mfma_f32_16x16x32_bf16 v[80:83], a[252:255], a[64:67], v[80:83]
s_waitcnt lgkmcnt(4)
v_mfma_f32_16x16x32_bf16 v[96:99], a[240:243], a[64:67], v[96:99]
ds_read_b64_tr_b16 a[252:253], v25 offset:9216
v_mfma_f32_16x16x32_bf16 v[100:103], a[240:243], a[68:71], v[100:103]
ds_read_b64_tr_b16 a[254:255], v25 offset:13312
v_mfma_f32_16x16x32_bf16 v[104:107], a[240:243], a[72:75], v[104:107]
buffer_load_dwordx4 v13, s[76:79], s54 offen lds
v_mfma_f32_16x16x32_bf16 v[108:111], a[240:243], a[76:79], v[108:111]
s_waitcnt lgkmcnt(4)
s_add_u32 m0, s70, 82816
v_mfma_f32_16x16x32_bf16 v[124:127], a[244:247], a[76:79], v[124:127]
ds_read_b64_tr_b16 a[240:241], v18 offset:0
v_mfma_f32_16x16x32_bf16 v[120:123], a[244:247], a[72:75], v[120:123]
ds_read_b64_tr_b16 a[242:243], v18 offset:4096
v_mfma_f32_16x16x32_bf16 v[116:119], a[244:247], a[68:71], v[116:119]
buffer_load_dwordx4 v13, s[76:79], s54 offen offset:128 lds
v_mfma_f32_16x16x32_bf16 v[112:115], a[244:247], a[64:67], v[112:115]
s_waitcnt lgkmcnt(4)
s_add_u32 s54, s54, 16384
s_add_u32 m0, s70, 83968
v_mfma_f32_16x16x32_bf16 v[128:131], a[248:251], a[64:67], v[128:131]
ds_read_b64_tr_b16 a[244:245], v19 offset:0
s_waitcnt vmcnt(2)
v_mfma_f32_16x16x32_bf16 v[132:135], a[248:251], a[68:71], v[132:135]
ds_read_b64_tr_b16 a[246:247], v19 offset:4096
v_mfma_f32_16x16x32_bf16 v[136:139], a[248:251], a[72:75], v[136:139]
buffer_load_dwordx4 v13, s[76:79], s54 offen lds
v_mfma_f32_16x16x32_bf16 v[140:143], a[248:251], a[76:79], v[140:143]
s_waitcnt lgkmcnt(4)
s_add_u32 m0, s70, 84864
v_mfma_f32_16x16x32_bf16 v[156:159], a[252:255], a[76:79], v[156:159]
ds_read_b64_tr_b16 a[248:249], v20 offset:0
v_mfma_f32_16x16x32_bf16 v[152:155], a[252:255], a[72:75], v[152:155]
ds_read_b64_tr_b16 a[250:251], v20 offset:4096
v_mfma_f32_16x16x32_bf16 v[148:151], a[252:255], a[68:71], v[148:151]
buffer_load_dwordx4 v13, s[76:79], s54 offen offset:128 lds
v_mfma_f32_16x16x32_bf16 v[144:147], a[252:255], a[64:67], v[144:147]
s_waitcnt lgkmcnt(4)
v_cmp_eq_u64 vcc, s[44:45], v[28:29]
s_nop 4
s_cmp_eq_u64 vcc, -1
s_cbranch_scc0 DP_19_s
DP_19_b:
v_mfma_f32_16x16x32_bf16 v[32:35], a[240:243], a[80:83], v[32:35]
ds_read_b64_tr_b16 a[252:253], v21 offset:0
v_mfma_f32_16x16x32_bf16 v[36:39], a[240:243], a[84:87], v[36:39]
ds_read_b64_tr_b16 a[254:255], v21 offset:4096
v_mfma_f32_16x16x32_bf16 v[40:43], a[240:243], a[88:91], v[40:43]
buffer_load_dwordx4 a[128:131], v236, s[92:95], s96 offen sc1
v_mfma_f32_16x16x32_bf16 v[44:47], a[240:243], a[92:95], v[44:47]
s_waitcnt lgkmcnt(4)
v_mfma_f32_16x16x32_bf16 v[60:63], a[244:247], a[92:95], v[60:63]
ds_read_b64_tr_b16 a[240:241], v18 offset:1024
v_mfma_f32_16x16x32_bf16 v[56:59], a[244:247], a[88:91], v[56:59]
ds_read_b64_tr_b16 a[242:243], v18 offset:5120
v_mfma_f32_16x16x32_bf16 v[52:55], a[244:247], a[84:87], v[52:55]
buffer_load_dwordx4 a[132:135], v237, s[92:95], s96 offen sc1
v_mfma_f32_16x16x32_bf16 v[48:51], a[244:247], a[80:83], v[48:51]
s_waitcnt lgkmcnt(4)
v_mfma_f32_16x16x32_bf16 v[64:67], a[248:251], a[80:83], v[64:67]
ds_read_b64_tr_b16 a[244:245], v19 offset:1024
v_mfma_f32_16x16x32_bf16 v[68:71], a[248:251], a[84:87], v[68:71]
ds_read_b64_tr_b16 a[246:247], v19 offset:5120
v_mfma_f32_16x16x32_bf16 v[72:75], a[248:251], a[88:91], v[72:75]
buffer_load_dwordx4 a[136:139], v238, s[92:95], s96 offen sc1
v_mfma_f32_16x16x32_bf16 v[76:79], a[248:251], a[92:95], v[76:79]
s_waitcnt lgkmcnt(4)
v_mfma_f32_16x16x32_bf16 v[92:95], a[252:255], a[92:95], v[92:95]
ds_read_b64_tr_b16 a[248:249], v20 offset:1024
v_mfma_f32_16x16x32_bf16 v[88:91], a[252:255], a[88:91], v[88:91]
ds_read_b64_tr_b16 a[250:251], v20 offset:5120
v_add_u32 v22, 32768, v14
v_mfma_f32_16x16x32_bf16 v[84:87], a[252:255], a[84:87], v[84:87]
buffer_load_dwordx4 a[140:143], v239, s[92:95], s96 offen sc1
v_mfma_f32_16x16x32_bf16 v[80:83], a[252:255], a[80:83], v[80:83]
s_waitcnt lgkmcnt(4)
s_add_u32 s96, s96, 4096
v_add_u32 v23, 32768, v15
v_mfma_f32_16x16x32_bf16 v[96:99], a[240:243], a[80:83], v[96:99]
ds_read_b64_tr_b16 a[252:253], v21 offset:1024
v_add_u32 v24, 32768, v16
v_mfma_f32_16x16x32_bf16 v[100:103], a[240:243], a[84:87], v[100:103]
ds_read_b64_tr_b16 a[254:255], v21 offset:5120
v_add_u32 v25, 32768, v17
v_mfma_f32_16x16x32_bf16 v[104:107], a[240:243], a[88:91], v[104:107]
buffer_load_dwordx4 a[144:147], v236, s[92:95], s96 offen sc1
v_mfma_f32_16x16x32_bf16 v[108:111], a[240:243], a[92:95], v[108:111]
s_waitcnt lgkmcnt(4)
s_add_u32 s54, s0, 3
s_and_b32 s54, s54, 7
v_mfma_f32_16x16x32_bf16 v[124:127], a[244:247], a[92:95], v[124:127]
ds_read_b64_tr_b16 a[240:241], v18 offset:8192
s_mul_i32 s54, s54, 131072
v_mfma_f32_16x16x32_bf16 v[120:123], a[244:247], a[88:91], v[120:123]
ds_read_b64_tr_b16 a[242:243], v18 offset:12288
s_add_u32 s54, s54, s80
v_mfma_f32_16x16x32_bf16 v[116:119], a[244:247], a[84:87], v[116:119]
buffer_load_dwordx4 a[148:151], v237, s[92:95], s96 offen sc1
v_mfma_f32_16x16x32_bf16 v[112:115], a[244:247], a[80:83], v[112:115]
s_waitcnt lgkmcnt(4)
s_add_u32 m0, s70, 98304
v_mfma_f32_16x16x32_bf16 v[128:131], a[248:251], a[80:83], v[128:131]
ds_read_b64_tr_b16 a[244:245], v19 offset:8192
v_mfma_f32_16x16x32_bf16 v[132:135], a[248:251], a[84:87], v[132:135]
ds_read_b64_tr_b16 a[246:247], v19 offset:12288
v_mfma_f32_16x16x32_bf16 v[136:139], a[248:251], a[88:91], v[136:139]
buffer_load_dwordx4 a[152:155], v238, s[92:95], s96 offen sc1
v_mfma_f32_16x16x32_bf16 v[140:143], a[248:251], a[92:95], v[140:143]
s_waitcnt lgkmcnt(4)
v_mfma_f32_16x16x32_bf16 v[156:159], a[252:255], a[92:95], v[156:159]
ds_read_b64_tr_b16 a[248:249], v20 offset:8192
v_mfma_f32_16x16x32_bf16 v[152:155], a[252:255], a[88:91], v[152:155]
ds_read_b64_tr_b16 a[250:251], v20 offset:12288
v_mfma_f32_16x16x32_bf16 v[148:151], a[252:255], a[84:87], v[148:151]
buffer_load_dwordx4 a[156:159], v239, s[92:95], s96 offen sc1
v_mfma_f32_16x16x32_bf16 v[144:147], a[252:255], a[80:83], v[144:147]
s_waitcnt lgkmcnt(4)
s_add_u32 s96, s0, 1
v_mfma_f32_16x16x32_bf16 v[32:35], a[240:243], a[0:3], v[32:35]
s_and_b32 s96, s96, 7
s_mul_i32 s96, s96, 8192
s_add_u32 s96, s96, s3
v_mfma_f32_16x16x32_bf16 v[36:39], a[240:243], a[4:7], v[36:39]
ds_read_b64_tr_b16 a[252:253], v21 offset:8192
v_mfma_f32_16x16x32_bf16 v[40:43], a[240:243], a[8:11], v[40:43]
buffer_load_dwordx4 a[160:163], v236, s[92:95], s96 offen sc1
ds_read_b64_tr_b16 a[254:255], v21 offset:12288
v_mfma_f32_16x16x32_bf16 v[44:47], a[240:243], a[12:15], v[44:47]
s_barrier
s_waitcnt lgkmcnt(4)
v_mfma_f32_16x16x32_bf16 v[60:63], a[244:247], a[12:15], v[60:63]
ds_read_b64_tr_b16 a[240:241], v18 offset:9216
v_mfma_f32_16x16x32_bf16 v[56:59], a[244:247], a[8:11], v[56:59]
ds_read_b64_tr_b16 a[242:243], v18 offset:13312
v_mfma_f32_16x16x32_bf16 v[52:55], a[244:247], a[4:7], v[52:55]
buffer_load_dwordx4 a[164:167], v237, s[92:95], s96 offen sc1
v_mfma_f32_16x16x32_bf16 v[48:51], a[244:247], a[0:3], v[48:51]
s_waitcnt lgkmcnt(4)
v_mfma_f32_16x16x32_bf16 v[64:67], a[248:251], a[0:3], v[64:67]
ds_read_b64_tr_b16 a[244:245], v19 offset:9216
v_mfma_f32_16x16x32_bf16 v[68:71], a[248:251], a[4:7], v[68:71]
ds_read_b64_tr_b16 a[246:247], v19 offset:13312
v_mfma_f32_16x16x32_bf16 v[72:75], a[248:251], a[8:11], v[72:75]
buffer_load_dwordx4 a[168:171], v238, s[92:95], s96 offen sc1
v_mfma_f32_16x16x32_bf16 v[76:79], a[248:251], a[12:15], v[76:79]
s_waitcnt lgkmcnt(4)
v_mfma_f32_16x16x32_bf16 v[92:95], a[252:255], a[12:15], v[92:95]
ds_read_b64_tr_b16 a[248:249], v20 offset:9216
v_mfma_f32_16x16x32_bf16 v[88:91], a[252:255], a[8:11], v[88:91]
ds_read_b64_tr_b16 a[250:251], v20 offset:13312
v_mfma_f32_16x16x32_bf16 v[84:87], a[252:255], a[4:7], v[84:87]
buffer_load_dwordx4 a[172:175], v239, s[92:95], s96 offen sc1
v_mfma_f32_16x16x32_bf16 v[80:83], a[252:255], a[0:3], v[80:83]
s_waitcnt lgkmcnt(4)
s_add_u32 s96, s96, 4096
v_mfma_f32_16x16x32_bf16 v[96:99], a[240:243], a[0:3], v[96:99]
ds_read_b64_tr_b16 a[252:253], v21 offset:9216
v_mfma_f32_16x16x32_bf16 v[100:103], a[240:243], a[4:7], v[100:103]
ds_read_b64_tr_b16 a[254:255], v21 offset:13312
v_mfma_f32_16x16x32_bf16 v[104:107], a[240:243], a[8:11], v[104:107]
buffer_load_dwordx4 v13, s[76:79], s54 offen lds
v_mfma_f32_16x16x32_bf16 v[108:111], a[240:243], a[12:15], v[108:111]
s_waitcnt lgkmcnt(4)
s_add_u32 m0, s70, 99200
v_mfma_f32_16x16x32_bf16 v[124:127], a[244:247], a[12:15], v[124:127]
ds_read_b64_tr_b16 a[240:241], v22 offset:0
v_mfma_f32_16x16x32_bf16 v[120:123], a[244:247], a[8:11], v[120:123]
ds_read_b64_tr_b16 a[242:243], v22 offset:4096
v_mfma_f32_16x16x32_bf16 v[116:119], a[244:247], a[4:7], v[116:119]
buffer_load_dwordx4 v13, s[76:79], s54 offen offset:128 lds
v_mfma_f32_16x16x32_bf16 v[112:115], a[244:247], a[0:3], v[112:115]
s_waitcnt lgkmcnt(4)
s_add_u32 s54, s54, 16384
s_add_u32 m0, s70, 100352
v_mfma_f32_16x16x32_bf16 v[128:131], a[248:251], a[0:3], v[128:131]
ds_read_b64_tr_b16 a[244:245], v23 offset:0
v_mfma_f32_16x16x32_bf16 v[132:135], a[248:251], a[4:7], v[132:135]
ds_read_b64_tr_b16 a[246:247], v23 offset:4096
v_mfma_f32_16x16x32_bf16 v[136:139], a[248:251], a[8:11], v[136:139]
buffer_load_dwordx4 v13, s[76:79], s54 offen lds
v_mfma_f32_16x16x32_bf16 v[140:143], a[248:251], a[12:15], v[140:143]
s_waitcnt lgkmcnt(4)
s_add_u32 m0, s70, 101248
v_mfma_f32_16x16x32_bf16 v[156:159], a[252:255], a[12:15], v[156:159]
ds_read_b64_tr_b16 a[248:249], v24 offset:0
v_mfma_f32_16x16x32_bf16 v[152:155], a[252:255], a[8:11], v[152:155]
ds_read_b64_tr_b16 a[250:251], v24 offset:4096
v_mfma_f32_16x16x32_bf16 v[148:151], a[252:255], a[4:7], v[148:151]
buffer_load_dwordx4 v13, s[76:79], s54 offen offset:128 lds
v_mfma_f32_16x16x32_bf16 v[144:147], a[252:255], a[0:3], v[144:147]
s_waitcnt lgkmcnt(4)
v_mfma_f32_16x16x32_bf16 v[32:35], a[240:243], a[16:19], v[32:35]
ds_read_b64_tr_b16 a[252:253], v25 offset:0
v_mfma_f32_16x16x32_bf16 v[36:39], a[240:243], a[20:23], v[36:39]
ds_read_b64_tr_b16 a[254:255], v25 offset:4096
v_mfma_f32_16x16x32_bf16 v[40:43], a[240:243], a[24:27], v[40:43]
buffer_load_dwordx4 a[176:179], v236, s[92:95], s96 offen sc1
v_mfma_f32_16x16x32_bf16 v[44:47], a[240:243], a[28:31], v[44:47]
s_waitcnt lgkmcnt(4)
v_mfma_f32_16x16x32_bf16 v[60:63], a[244:247], a[28:31], v[60:63]
ds_read_b64_tr_b16 a[240:241], v22 offset:1024
v_mfma_f32_16x16x32_bf16 v[56:59], a[244:247], a[24:27], v[56:59]
ds_read_b64_tr_b16 a[242:243], v22 offset:5120
v_mfma_f32_16x16x32_bf16 v[52:55], a[244:247], a[20:23], v[52:55]
buffer_load_dwordx4 a[180:183], v237, s[92:95], s96 offen sc1
v_mfma_f32_16x16x32_bf16 v[48:51], a[244:247], a[16:19], v[48:51]
s_waitcnt lgkmcnt(4)
v_mfma_f32_16x16x32_bf16 v[64:67], a[248:251], a[16:19], v[64:67]
ds_read_b64_tr_b16 a[244:245], v23 offset:1024
v_mfma_f32_16x16x32_bf16 v[68:71], a[248:251], a[20:23], v[68:71]
ds_read_b64_tr_b16 a[246:247], v23 offset:5120
v_mfma_f32_16x16x32_bf16 v[72:75], a[248:251], a[24:27], v[72:75]
buffer_load_dwordx4 a[184:187], v238, s[92:95], s96 offen sc1
v_mfma_f32_16x16x32_bf16 v[76:79], a[248:251], a[28:31], v[76:79]
s_waitcnt lgkmcnt(4)
v_mfma_f32_16x16x32_bf16 v[92:95], a[252:255], a[28:31], v[92:95]
ds_read_b64_tr_b16 a[248:249], v24 offset:1024
v_mfma_f32_16x16x32_bf16 v[88:91], a[252:255], a[24:27], v[88:91]
ds_read_b64_tr_b16 a[250:251], v24 offset:5120
v_add_u32 v18, 49152, v14
v_mfma_f32_16x16x32_bf16 v[84:87], a[252:255], a[20:23], v[84:87]
buffer_load_dwordx4 a[188:191], v239, s[92:95], s96 offen sc1
v_mfma_f32_16x16x32_bf16 v[80:83], a[252:255], a[16:19], v[80:83]
s_waitcnt lgkmcnt(4)
s_add_u32 s96, s0, 2
s_and_b32 s96, s96, 7
v_mfma_f32_16x16x32_bf16 v[96:99], a[240:243], a[16:19], v[96:99]
s_mul_i32 s96, s96, 8192
s_add_u32 s96, s96, s3
v_add_u32 v19, 49152, v15
v_mfma_f32_16x16x32_bf16 v[100:103], a[240:243], a[20:23], v[100:103]
ds_read_b64_tr_b16 a[252:253], v25 offset:1024
v_add_u32 v20, 49152, v16
v_mfma_f32_16x16x32_bf16 v[104:107], a[240:243], a[24:27], v[104:107]
buffer_load_dwordx4 a[192:195], v236, s[92:95], s96 offen sc1
ds_read_b64_tr_b16 a[254:255], v25 offset:5120
v_mfma_f32_16x16x32_bf16 v[108:111], a[240:243], a[28:31], v[108:111]
s_waitcnt lgkmcnt(4)
s_add_u32 s54, s0, 4
s_and_b32 s54, s54, 7
v_mfma_f32_16x16x32_bf16 v[124:127], a[244:247], a[28:31], v[124:127]
ds_read_b64_tr_b16 a[240:241], v22 offset:8192
s_mul_i32 s54, s54, 131072
v_mfma_f32_16x16x32_bf16 v[120:123], a[244:247], a[24:27], v[120:123]
ds_read_b64_tr_b16 a[242:243], v22 offset:12288
s_add_u32 s54, s54, s80
v_mfma_f32_16x16x32_bf16 v[116:119], a[244:247], a[20:23], v[116:119]
buffer_load_dwordx4 a[196:199], v237, s[92:95], s96 offen sc1
v_mfma_f32_16x16x32_bf16 v[112:115], a[244:247], a[16:19], v[112:115]
s_waitcnt lgkmcnt(4)
s_add_u32 m0, s70, 114688
v_add_u32 v21, 49152, v17
v_mfma_f32_16x16x32_bf16 v[128:131], a[248:251], a[16:19], v[128:131]
ds_read_b64_tr_b16 a[244:245], v23 offset:8192
v_mfma_f32_16x16x32_bf16 v[132:135], a[248:251], a[20:23], v[132:135]
ds_read_b64_tr_b16 a[246:247], v23 offset:12288
v_mfma_f32_16x16x32_bf16 v[136:139], a[248:251], a[24:27], v[136:139]
buffer_load_dwordx4 a[200:203], v238, s[92:95], s96 offen sc1
v_mfma_f32_16x16x32_bf16 v[140:143], a[248:251], a[28:31], v[140:143]
s_waitcnt lgkmcnt(4)
v_mfma_f32_16x16x32_bf16 v[156:159], a[252:255], a[28:31], v[156:159]
ds_read_b64_tr_b16 a[248:249], v24 offset:8192
v_mfma_f32_16x16x32_bf16 v[152:155], a[252:255], a[24:27], v[152:155]
ds_read_b64_tr_b16 a[250:251], v24 offset:12288
v_mfma_f32_16x16x32_bf16 v[148:151], a[252:255], a[20:23], v[148:151]
buffer_load_dwordx4 a[204:207], v239, s[92:95], s96 offen sc1
v_mfma_f32_16x16x32_bf16 v[144:147], a[252:255], a[16:19], v[144:147]
s_waitcnt lgkmcnt(4)
s_add_u32 s96, s96, 4096
v_mfma_f32_16x16x32_bf16 v[32:35], a[240:243], a[32:35], v[32:35]
ds_read_b64_tr_b16 a[252:253], v25 offset:8192
v_mfma_f32_16x16x32_bf16 v[36:39], a[240:243], a[36:39], v[36:39]
ds_read_b64_tr_b16 a[254:255], v25 offset:12288
v_mfma_f32_16x16x32_bf16 v[40:43], a[240:243], a[40:43], v[40:43]
buffer_load_dwordx4 a[208:211], v236, s[92:95], s96 offen sc1
v_mfma_f32_16x16x32_bf16 v[44:47], a[240:243], a[44:47], v[44:47]
s_barrier
s_waitcnt lgkmcnt(4)
v_mfma_f32_16x16x32_bf16 v[60:63], a[244:247], a[44:47], v[60:63]
ds_read_b64_tr_b16 a[240:241], v22 offset:9216
v_mfma_f32_16x16x32_bf16 v[56:59], a[244:247], a[40:43], v[56:59]
ds_read_b64_tr_b16 a[242:243], v22 offset:13312
v_mfma_f32_16x16x32_bf16 v[52:55], a[244:247], a[36:39], v[52:55]
buffer_load_dwordx4 a[212:215], v237, s[92:95], s96 offen sc1
v_mfma_f32_16x16x32_bf16 v[48:51], a[244:247], a[32:35], v[48:51]
s_waitcnt lgkmcnt(4)
v_mfma_f32_16x16x32_bf16 v[64:67], a[248:251], a[32:35], v[64:67]
ds_read_b64_tr_b16 a[244:245], v23 offset:9216
v_mfma_f32_16x16x32_bf16 v[68:71], a[248:251], a[36:39], v[68:71]
ds_read_b64_tr_b16 a[246:247], v23 offset:13312
v_mfma_f32_16x16x32_bf16 v[72:75], a[248:251], a[40:43], v[72:75]
buffer_load_dwordx4 a[216:219], v238, s[92:95], s96 offen sc1
v_mfma_f32_16x16x32_bf16 v[76:79], a[248:251], a[44:47], v[76:79]
s_waitcnt lgkmcnt(4)
v_mfma_f32_16x16x32_bf16 v[92:95], a[252:255], a[44:47], v[92:95]
ds_read_b64_tr_b16 a[248:249], v24 offset:9216
v_mfma_f32_16x16x32_bf16 v[88:91], a[252:255], a[40:43], v[88:91]
ds_read_b64_tr_b16 a[250:251], v24 offset:13312
v_mfma_f32_16x16x32_bf16 v[84:87], a[252:255], a[36:39], v[84:87]
buffer_load_dwordx4 a[220:223], v239, s[92:95], s96 offen sc1
v_mfma_f32_16x16x32_bf16 v[80:83], a[252:255], a[32:35], v[80:83]
s_waitcnt lgkmcnt(4)
v_mfma_f32_16x16x32_bf16 v[96:99], a[240:243], a[32:35], v[96:99]
ds_read_b64_tr_b16 a[252:253], v25 offset:9216
v_mfma_f32_16x16x32_bf16 v[100:103], a[240:243], a[36:39], v[100:103]
ds_read_b64_tr_b16 a[254:255], v25 offset:13312
v_mfma_f32_16x16x32_bf16 v[104:107], a[240:243], a[40:43], v[104:107]
buffer_load_dwordx4 v13, s[76:79], s54 offen lds
v_mfma_f32_16x16x32_bf16 v[108:111], a[240:243], a[44:47], v[108:111]
s_waitcnt lgkmcnt(4)
s_add_u32 m0, s70, 115584
v_mfma_f32_16x16x32_bf16 v[124:127], a[244:247], a[44:47], v[124:127]
ds_read_b64_tr_b16 a[240:241], v18 offset:0
v_mfma_f32_16x16x32_bf16 v[120:123], a[244:247], a[40:43], v[120:123]
ds_read_b64_tr_b16 a[242:243], v18 offset:4096
v_mfma_f32_16x16x32_bf16 v[116:119], a[244:247], a[36:39], v[116:119]
buffer_load_dwordx4 v13, s[76:79], s54 offen offset:128 lds
v_mfma_f32_16x16x32_bf16 v[112:115], a[244:247], a[32:35], v[112:115]
s_waitcnt lgkmcnt(4)
s_add_u32 s54, s54, 16384
s_add_u32 m0, s70, 116736
v_mfma_f32_16x16x32_bf16 v[128:131], a[248:251], a[32:35], v[128:131]
ds_read_b64_tr_b16 a[244:245], v19 offset:0
v_mfma_f32_16x16x32_bf16 v[132:135], a[248:251], a[36:39], v[132:135]
ds_read_b64_tr_b16 a[246:247], v19 offset:4096
v_mfma_f32_16x16x32_bf16 v[136:139], a[248:251], a[40:43], v[136:139]
buffer_load_dwordx4 v13, s[76:79], s54 offen lds
v_mfma_f32_16x16x32_bf16 v[140:143], a[248:251], a[44:47], v[140:143]
s_waitcnt lgkmcnt(4)
s_add_u32 m0, s70, 117632
v_mfma_f32_16x16x32_bf16 v[156:159], a[252:255], a[44:47], v[156:159]
ds_read_b64_tr_b16 a[248:249], v20 offset:0
v_mfma_f32_16x16x32_bf16 v[152:155], a[252:255], a[40:43], v[152:155]
ds_read_b64_tr_b16 a[250:251], v20 offset:4096
v_mfma_f32_16x16x32_bf16 v[148:151], a[252:255], a[36:39], v[148:151]
buffer_load_dwordx4 v13, s[76:79], s54 offen offset:128 lds
v_mfma_f32_16x16x32_bf16 v[144:147], a[252:255], a[32:35], v[144:147]
s_waitcnt lgkmcnt(4)
s_waitcnt vmcnt(28)
KS:
v_mfma_f32_16x16x32_bf16 a[0:3], a[240:243], a[128:131], 0
ds_read_b64_tr_b16 a[252:253], v21 offset:0
s_add_u32 s96, s0, 3
v_mfma_f32_16x16x32_bf16 a[4:7], a[240:243], a[132:135], 0
ds_read_b64_tr_b16 a[254:255], v21 offset:4096
s_and_b32 s96, s96, 7
v_mfma_f32_16x16x32_bf16 a[8:11], a[240:243], a[136:139], 0
s_mul_i32 s96, s96, 8192
s_add_u32 s96, s96, s3
s_add_u32 s32, s22, 37748736
v_mfma_f32_16x16x32_bf16 a[12:15], a[240:243], a[140:143], 0
s_addc_u32 s33, s23, 0
s_waitcnt lgkmcnt(4)
s_and_b32 s33, s33, 0xffff
v_mfma_f32_16x16x32_bf16 a[28:31], a[244:247], a[140:143], 0
ds_read_b64_tr_b16 a[240:241], v18 offset:1024
s_mov_b32 s34, 12582912
v_mfma_f32_16x16x32_bf16 a[24:27], a[244:247], a[136:139], 0
ds_read_b64_tr_b16 a[242:243], v18 offset:5120
s_mov_b32 s35, 0x20000
v_mfma_f32_16x16x32_bf16 a[20:23], a[244:247], a[132:135], 0
buffer_load_dwordx4 a[224:227], v236, s[92:95], s96 offen sc1
v_mfma_f32_16x16x32_bf16 a[16:19], a[244:247], a[128:131], 0
s_waitcnt lgkmcnt(4)
s_mul_i32 s81, s40, 49152
s_lshl_b32 s97, s43, 12
v_mfma_f32_16x16x32_bf16 a[32:35], a[248:251], a[128:131], 0
ds_read_b64_tr_b16 a[244:245], v19 offset:1024
s_add_u32 s81, s81, s97
v_mfma_f32_16x16x32_bf16 a[36:39], a[248:251], a[132:135], 0
ds_read_b64_tr_b16 a[246:247], v19 offset:5120
s_add_u32 s86, s81, 0
v_mfma_f32_16x16x32_bf16 a[40:43], a[248:251], a[136:139], 0
v_cvt_pk_bf16_f32 v36, v36, v37
v_cvt_pk_bf16_f32 v37, v38, v39
v_mfma_f32_16x16x32_bf16 a[44:47], a[248:251], a[140:143], 0
s_waitcnt lgkmcnt(4)
v_cvt_pk_bf16_f32 v38, v52, v53
v_cvt_pk_bf16_f32 v39, v54, v55
v_mfma_f32_16x16x32_bf16 a[60:63], a[252:255], a[140:143], 0
ds_read_b64_tr_b16 a[248:249], v20 offset:1024
v_cvt_pk_bf16_f32 v68, v68, v69
v_mfma_f32_16x16x32_bf16 a[56:59], a[252:255], a[136:139], 0
ds_read_b64_tr_b16 a[250:251], v20 offset:5120
v_add_u32 v22, 65536, v14
v_mfma_f32_16x16x32_bf16 a[52:55], a[252:255], a[132:135], 0
buffer_load_dwordx4 a[228:231], v237, s[92:95], s96 offen sc1
v_mfma_f32_16x16x32_bf16 a[48:51], a[252:255], a[128:131], 0
s_waitcnt lgkmcnt(4)
v_cvt_pk_bf16_f32 v69, v70, v71
v_cvt_pk_bf16_f32 v70, v84, v85
v_mfma_f32_16x16x32_bf16 a[64:67], a[240:243], a[128:131], 0
ds_read_b64_tr_b16 a[252:253], v21 offset:1024
v_cvt_pk_bf16_f32 v71, v86, v87
v_mfma_f32_16x16x32_bf16 a[68:71], a[240:243], a[132:135], 0
ds_read_b64_tr_b16 a[254:255], v21 offset:5120
v_add_u32 v23, 65536, v15
v_mfma_f32_16x16x32_bf16 a[72:75], a[240:243], a[136:139], 0
v_add_u32 v24, 65536, v16
v_add_u32 v25, 65536, v17
v_mfma_f32_16x16x32_bf16 a[76:79], a[240:243], a[140:143], 0
s_waitcnt lgkmcnt(4)
s_add_u32 s54, s0, 5
s_and_b32 s54, s54, 7
v_mfma_f32_16x16x32_bf16 a[92:95], a[244:247], a[140:143], 0
ds_read_b64_tr_b16 a[240:241], v18 offset:8192
s_mul_i32 s54, s54, 131072
v_mfma_f32_16x16x32_bf16 a[88:91], a[244:247], a[136:139], 0
ds_read_b64_tr_b16 a[242:243], v18 offset:12288
s_add_u32 s54, s54, s80
v_mfma_f32_16x16x32_bf16 a[84:87], a[244:247], a[132:135], 0
buffer_load_dwordx4 a[232:235], v238, s[92:95], s96 offen sc1
v_mfma_f32_16x16x32_bf16 a[80:83], a[244:247], a[128:131], 0
s_waitcnt lgkmcnt(4)
s_add_u32 m0, s70, 131072
v_cvt_pk_bf16_f32 v100, v100, v101
v_mfma_f32_16x16x32_bf16 a[96:99], a[248:251], a[128:131], 0
ds_read_b64_tr_b16 a[244:245], v19 offset:8192
v_cvt_pk_bf16_f32 v101, v102, v103
v_mfma_f32_16x16x32_bf16 a[100:103], a[248:251], a[132:135], 0
ds_read_b64_tr_b16 a[246:247], v19 offset:12288
v_cvt_pk_bf16_f32 v102, v116, v117
v_mfma_f32_16x16x32_bf16 a[104:107], a[248:251], a[136:139], 0
v_cvt_pk_bf16_f32 v103, v118, v119
v_mfma_f32_16x16x32_bf16 a[108:111], a[248:251], a[140:143], 0
s_waitcnt lgkmcnt(4)
v_mfma_f32_16x16x32_bf16 a[124:127], a[252:255], a[140:143], 0
ds_read_b64_tr_b16 a[248:249], v20 offset:8192
v_mfma_f32_16x16x32_bf16 a[120:123], a[252:255], a[136:139], 0
ds_read_b64_tr_b16 a[250:251], v20 offset:12288
v_mfma_f32_16x16x32_bf16 a[116:119], a[252:255], a[132:135], 0
buffer_load_dwordx4 a[236:239], v239, s[92:95], s96 offen sc1
v_mfma_f32_16x16x32_bf16 a[112:115], a[252:255], a[128:131], 0
s_waitcnt lgkmcnt(4)
s_waitcnt vmcnt(28)
s_add_u32 s96, s96, 4096
v_mfma_f32_16x16x32_bf16 a[0:3], a[240:243], a[144:147], a[0:3]
ds_read_b64_tr_b16 a[252:253], v21 offset:8192
v_cvt_pk_bf16_f32 v132, v132, v133
v_mfma_f32_16x16x32_bf16 a[4:7], a[240:243], a[148:151], a[4:7]
ds_read_b64_tr_b16 a[254:255], v21 offset:12288
v_cvt_pk_bf16_f32 v133, v134, v135
v_mfma_f32_16x16x32_bf16 a[8:11], a[240:243], a[152:155], a[8:11]
buffer_load_dwordx4 a[128:131], v236, s[92:95], s96 offen sc1
v_mfma_f32_16x16x32_bf16 a[12:15], a[240:243], a[156:159], a[12:15]
s_barrier
s_waitcnt lgkmcnt(4)
v_mfma_f32_16x16x32_bf16 a[28:31], a[244:247], a[156:159], a[28:31]
ds_read_b64_tr_b16 a[240:241], v18 offset:9216
v_cvt_pk_bf16_f32 v134, v148, v149
v_mfma_f32_16x16x32_bf16 a[24:27], a[244:247], a[152:155], a[24:27]
ds_read_b64_tr_b16 a[242:243], v18 offset:13312
v_cvt_pk_bf16_f32 v135, v150, v151
v_mfma_f32_16x16x32_bf16 a[20:23], a[244:247], a[148:151], a[20:23]
buffer_load_dwordx4 a[132:135], v237, s[92:95], s96 offen sc1
v_mfma_f32_16x16x32_bf16 a[16:19], a[244:247], a[144:147], a[16:19]
s_waitcnt lgkmcnt(4)
v_cvt_pk_bf16_f32 v40, v40, v41
v_cvt_pk_bf16_f32 v41, v42, v43
v_mfma_f32_16x16x32_bf16 a[32:35], a[248:251], a[144:147], a[32:35]
ds_read_b64_tr_b16 a[244:245], v19 offset:9216
v_cvt_pk_bf16_f32 v42, v56, v57
v_mfma_f32_16x16x32_bf16 a[36:39], a[248:251], a[148:151], a[36:39]
ds_read_b64_tr_b16 a[246:247], v19 offset:13312
v_cvt_pk_bf16_f32 v43, v58, v59
v_mfma_f32_16x16x32_bf16 a[40:43], a[248:251], a[152:155], a[40:43]
buffer_load_dwordx4 a[136:139], v238, s[92:95], s96 offen sc1
v_mfma_f32_16x16x32_bf16 a[44:47], a[248:251], a[156:159], a[44:47]
s_waitcnt lgkmcnt(4)
buffer_store_dwordx4 v[36:39], v2, s[32:35], s86 offen offset:0 sc1
v_mfma_f32_16x16x32_bf16 a[60:63], a[252:255], a[156:159], a[60:63]
ds_read_b64_tr_b16 a[248:249], v20 offset:9216
v_mfma_f32_16x16x32_bf16 a[56:59], a[252:255], a[152:155], a[56:59]
ds_read_b64_tr_b16 a[250:251], v20 offset:13312
v_mfma_f32_16x16x32_bf16 a[52:55], a[252:255], a[148:151], a[52:55]
buffer_load_dwordx4 a[140:143], v239, s[92:95], s96 offen sc1
v_mfma_f32_16x16x32_bf16 a[48:51], a[252:255], a[144:147], a[48:51]
s_waitcnt lgkmcnt(4)
s_add_u32 s96, s0, 4
s_and_b32 s96, s96, 7
v_mfma_f32_16x16x32_bf16 a[64:67], a[240:243], a[144:147], a[64:67]
ds_read_b64_tr_b16 a[252:253], v21 offset:9216
s_mul_i32 s96, s96, 8192
v_mfma_f32_16x16x32_bf16 a[68:71], a[240:243], a[148:151], a[68:71]
ds_read_b64_tr_b16 a[254:255], v21 offset:13312
s_add_u32 s96, s96, s3
v_mfma_f32_16x16x32_bf16 a[72:75], a[240:243], a[152:155], a[72:75]
buffer_load_dwordx4 v13, s[76:79], s54 offen lds
v_mfma_f32_16x16x32_bf16 a[76:79], a[240:243], a[156:159], a[76:79]
s_waitcnt lgkmcnt(4)
s_add_u32 m0, s70, 131968
v_cvt_pk_bf16_f32 v72, v72, v73
v_mfma_f32_16x16x32_bf16 a[92:95], a[244:247], a[156:159], a[92:95]
ds_read_b64_tr_b16 a[240:241], v22 offset:0
v_cvt_pk_bf16_f32 v73, v74, v75
v_mfma_f32_16x16x32_bf16 a[88:91], a[244:247], a[152:155], a[88:91]
ds_read_b64_tr_b16 a[242:243], v22 offset:4096
v_cvt_pk_bf16_f32 v74, v88, v89
v_mfma_f32_16x16x32_bf16 a[84:87], a[244:247], a[148:151], a[84:87]
buffer_load_dwordx4 v13, s[76:79], s54 offen offset:128 lds
v_mfma_f32_16x16x32_bf16 a[80:83], a[244:247], a[144:147], a[80:83]
s_waitcnt lgkmcnt(4)
s_add_u32 s54, s54, 16384
s_add_u32 m0, s70, 133120
buffer_store_dwordx4 v[68:71], v2, s[32:35], s86 offen offset:1024 sc1
v_mfma_f32_16x16x32_bf16 a[96:99], a[248:251], a[144:147], a[96:99]
ds_read_b64_tr_b16 a[244:245], v23 offset:0
v_cvt_pk_bf16_f32 v75, v90, v91
v_mfma_f32_16x16x32_bf16 a[100:103], a[248:251], a[148:151], a[100:103]
ds_read_b64_tr_b16 a[246:247], v23 offset:4096
v_mfma_f32_16x16x32_bf16 a[104:107], a[248:251], a[152:155], a[104:107]
buffer_load_dwordx4 v13, s[76:79], s54 offen lds
v_mfma_f32_16x16x32_bf16 a[108:111], a[248:251], a[156:159], a[108:111]
s_waitcnt lgkmcnt(4)
s_add_u32 m0, s70, 134016
v_mfma_f32_16x16x32_bf16 a[124:127], a[252:255], a[156:159], a[124:127]
ds_read_b64_tr_b16 a[248:249], v24 offset:0
v_mfma_f32_16x16x32_bf16 a[120:123], a[252:255], a[152:155], a[120:123]
ds_read_b64_tr_b16 a[250:251], v24 offset:4096
v_mfma_f32_16x16x32_bf16 a[116:119], a[252:255], a[148:151], a[116:119]
buffer_load_dwordx4 v13, s[76:79], s54 offen offset:128 lds
v_mfma_f32_16x16x32_bf16 a[112:115], a[252:255], a[144:147], a[112:115]
s_waitcnt lgkmcnt(4)
s_waitcnt vmcnt(34)
v_mfma_f32_16x16x32_bf16 a[0:3], a[240:243], a[160:163], a[0:3]
ds_read_b64_tr_b16 a[252:253], v25 offset:0
v_mov_b32 v216, 0
v_mfma_f32_16x16x32_bf16 a[4:7], a[240:243], a[164:167], a[4:7]
ds_read_b64_tr_b16 a[254:255], v25 offset:4096
v_mov_b32 v217, 0
v_mfma_f32_16x16x32_bf16 a[8:11], a[240:243], a[168:171], a[8:11]
buffer_store_dwordx4 v[100:103], v2, s[32:35], s86 offen offset:2048 sc1
v_mfma_f32_16x16x32_bf16 a[12:15], a[240:243], a[172:175], a[12:15]
s_waitcnt lgkmcnt(4)
v_mov_b32 v220, 0
v_mov_b32 v221, 0
v_mfma_f32_16x16x32_bf16 a[28:31], a[244:247], a[172:175], a[28:31]
ds_read_b64_tr_b16 a[240:241], v22 offset:1024
v_and_b32 v222, 1, v1
v_mfma_f32_16x16x32_bf16 a[24:27], a[244:247], a[168:171], a[24:27]
ds_read_b64_tr_b16 a[242:243], v22 offset:5120
v_lshlrev_b32 v222, 4, v222
v_mfma_f32_16x16x32_bf16 a[20:23], a[244:247], a[164:167], a[20:23]
buffer_load_dwordx4 a[144:147], v236, s[92:95], s96 offen sc1
v_mfma_f32_16x16x32_bf16 a[16:19], a[244:247], a[160:163], a[16:19]
s_waitcnt lgkmcnt(4)
v_mov_b32 v223, 0x3f80
v_lshlrev_b32 v222, v222, v223
v_mfma_f32_16x16x32_bf16 a[32:35], a[248:251], a[160:163], a[32:35]
ds_read_b64_tr_b16 a[244:245], v23 offset:1024
v_lshrrev_b32 v223, 4, v1
v_mfma_f32_16x16x32_bf16 a[36:39], a[248:251], a[164:167], a[36:39]
ds_read_b64_tr_b16 a[246:247], v23 offset:5120
v_bfe_u32 v218, v1, 1, 3
v_mfma_f32_16x16x32_bf16 a[40:43], a[248:251], a[168:171], a[40:43]
buffer_store_dwordx4 v[132:135], v2, s[32:35], s86 offen offset:3072 sc1
v_mfma_f32_16x16x32_bf16 a[44:47], a[248:251], a[172:175], a[44:47]
s_waitcnt lgkmcnt(4)
s_add_u32 s86, s81, 16384
v_lshlrev_b32 v223, 1, v223
v_mfma_f32_16x16x32_bf16 a[60:63], a[252:255], a[172:175], a[60:63]
ds_read_b64_tr_b16 a[248:249], v24 offset:1024
v_add_u32 v219, 1, v223
v_mfma_f32_16x16x32_bf16 a[56:59], a[252:255], a[168:171], a[56:59]
ds_read_b64_tr_b16 a[250:251], v24 offset:5120
v_add_u32 v18, 81920, v14
v_mfma_f32_16x16x32_bf16 a[52:55], a[252:255], a[164:167], a[52:55]
buffer_load_dwordx4 a[148:151], v237, s[92:95], s96 offen sc1
v_mfma_f32_16x16x32_bf16 a[48:51], a[252:255], a[160:163], a[48:51]
s_waitcnt lgkmcnt(4)
v_add_u32 v19, 81920, v15
v_add_u32 v20, 81920, v16
v_mfma_f32_16x16x32_bf16 a[64:67], a[240:243], a[160:163], a[64:67]
ds_read_b64_tr_b16 a[252:253], v25 offset:1024
v_add_u32 v21, 81920, v17
v_mfma_f32_16x16x32_bf16 a[68:71], a[240:243], a[164:167], a[68:71]
ds_read_b64_tr_b16 a[254:255], v25 offset:5120
v_cmp_eq_u32 vcc, v218, v219
v_mfma_f32_16x16x32_bf16 a[72:75], a[240:243], a[168:171], a[72:75]
buffer_store_dwordx4 v[40:43], v2, s[32:35], s86 offen offset:0 sc1
v_mfma_f32_16x16x32_bf16 a[76:79], a[240:243], a[172:175], a[76:79]
s_waitcnt lgkmcnt(4)
s_add_u32 s54, s0, 6
s_and_b32 s54, s54, 7
v_mfma_f32_16x16x32_bf16 a[92:95], a[244:247], a[172:175], a[92:95]
ds_read_b64_tr_b16 a[240:241], v22 offset:8192
s_mul_i32 s54, s54, 131072
v_mfma_f32_16x16x32_bf16 a[88:91], a[244:247], a[168:171], a[88:91]
ds_read_b64_tr_b16 a[242:243], v22 offset:12288
s_add_u32 s54, s54, s80
v_mfma_f32_16x16x32_bf16 a[84:87], a[244:247], a[164:167], a[84:87]
buffer_load_dwordx4 a[152:155], v238, s[92:95], s96 offen sc1
v_mfma_f32_16x16x32_bf16 a[80:83], a[244:247], a[160:163], a[80:83]
s_waitcnt lgkmcnt(4)
s_add_u32 m0, s70, 147456
v_cndmask_b32 v219, 0, v222, vcc
v_mfma_f32_16x16x32_bf16 a[96:99], a[248:251], a[160:163], a[96:99]
ds_read_b64_tr_b16 a[244:245], v23 offset:8192
v_cmp_eq_u32 vcc, v218, v223
v_mfma_f32_16x16x32_bf16 a[100:103], a[248:251], a[164:167], a[100:103]
ds_read_b64_tr_b16 a[246:247], v23 offset:12288
v_cndmask_b32 v218, 0, v222, vcc
v_mfma_f32_16x16x32_bf16 a[104:107], a[248:251], a[168:171], a[104:107]
buffer_store_dwordx4 v[72:75], v2, s[32:35], s86 offen offset:1024 sc1
v_mfma_f32_16x16x32_bf16 a[108:111], a[248:251], a[172:175], a[108:111]
s_waitcnt lgkmcnt(4)
v_mfma_f32_16x16x32_bf16 a[124:127], a[252:255], a[172:175], a[124:127]
ds_read_b64_tr_b16 a[248:249], v24 offset:8192
v_mfma_f32_16x16x32_bf16 a[120:123], a[252:255], a[168:171], a[120:123]
ds_read_b64_tr_b16 a[250:251], v24 offset:12288
v_mfma_f32_16x16x32_bf16 a[116:119], a[252:255], a[164:167], a[116:119]
buffer_load_dwordx4 a[156:159], v239, s[92:95], s96 offen sc1
v_mfma_f32_16x16x32_bf16 a[112:115], a[252:255], a[160:163], a[112:115]
s_waitcnt lgkmcnt(4)
s_waitcnt vmcnt(34)
s_add_u32 s96, s96, 4096
v_mfma_f32_16x16x32_bf16 a[0:3], a[240:243], a[176:179], a[0:3]
ds_read_b64_tr_b16 a[252:253], v25 offset:8192
v_mfma_f32_16x16x32_bf16 a[4:7], a[240:243], a[180:183], a[4:7]
ds_read_b64_tr_b16 a[254:255], v25 offset:12288
v_mfma_f32_16x16x32_bf16 a[8:11], a[240:243], a[184:187], a[8:11]
buffer_load_dwordx4 a[160:163], v236, s[92:95], s96 offen sc1
v_mfma_f32_16x16x32_bf16 a[12:15], a[240:243], a[188:191], a[12:15]
s_barrier
s_waitcnt lgkmcnt(4)
v_mfma_f32_16x16x32_bf16 a[28:31], a[244:247], a[188:191], a[28:31]
ds_read_b64_tr_b16 a[240:241], v22 offset:9216
v_mfma_f32_16x16x32_bf16 a[24:27], a[244:247], a[184:187], a[24:27]
ds_read_b64_tr_b16 a[242:243], v22 offset:13312
v_mfma_f32_16x16x32_bf16 a[20:23], a[244:247], a[180:183], a[20:23]
buffer_load_dwordx4 a[164:167], v237, s[92:95], s96 offen sc1
v_mfma_f32_16x16x32_bf16 a[16:19], a[244:247], a[176:179], a[16:19]
s_waitcnt lgkmcnt(4)
v_mfma_f32_16x16x32_bf16 a[32:35], a[248:251], a[176:179], a[32:35]
ds_read_b64_tr_b16 a[244:245], v23 offset:9216
v_mfma_f32_16x16x32_bf16 a[36:39], a[248:251], a[180:183], a[36:39]
ds_read_b64_tr_b16 a[246:247], v23 offset:13312
v_mfma_f32_16x16x32_bf16 a[40:43], a[248:251], a[184:187], a[40:43]
buffer_load_dwordx4 a[168:171], v238, s[92:95], s96 offen sc1
v_mfma_f32_16x16x32_bf16 a[44:47], a[248:251], a[188:191], a[44:47]
s_waitcnt lgkmcnt(4)
v_mfma_f32_16x16x32_bf16 a[60:63], a[252:255], a[188:191], a[60:63]
ds_read_b64_tr_b16 a[248:249], v24 offset:9216
v_mfma_f32_16x16x32_bf16 a[56:59], a[252:255], a[184:187], a[56:59]
ds_read_b64_tr_b16 a[250:251], v24 offset:13312
v_mfma_f32_16x16x32_bf16 a[52:55], a[252:255], a[180:183], a[52:55]
buffer_load_dwordx4 a[172:175], v239, s[92:95], s96 offen sc1
v_mfma_f32_16x16x32_bf16 a[48:51], a[252:255], a[176:179], a[48:51]
s_waitcnt lgkmcnt(4)
s_add_u32 s96, s0, 5
s_and_b32 s96, s96, 7
v_mfma_f32_16x16x32_bf16 a[64:67], a[240:243], a[176:179], a[64:67]
ds_read_b64_tr_b16 a[252:253], v25 offset:9216
s_mul_i32 s96, s96, 8192
v_mfma_f32_16x16x32_bf16 a[68:71], a[240:243], a[180:183], a[68:71]
ds_read_b64_tr_b16 a[254:255], v25 offset:13312
s_add_u32 s96, s96, s3
v_mfma_f32_16x16x32_bf16 a[72:75], a[240:243], a[184:187], a[72:75]
buffer_load_dwordx4 v13, s[76:79], s54 offen lds
v_mfma_f32_16x16x32_bf16 a[76:79], a[240:243], a[188:191], a[76:79]
s_waitcnt lgkmcnt(4)
s_add_u32 m0, s70, 148352
v_mfma_f32_16x16x32_bf16 a[92:95], a[244:247], a[188:191], a[92:95]
ds_read_b64_tr_b16 a[240:241], v18 offset:0
v_mfma_f32_16x16x32_bf16 a[88:91], a[244:247], a[184:187], a[88:91]
ds_read_b64_tr_b16 a[242:243], v18 offset:4096
v_mfma_f32_16x16x32_bf16 a[84:87], a[244:247], a[180:183], a[84:87]
buffer_load_dwordx4 v13, s[76:79], s54 offen offset:128 lds
v_mfma_f32_16x16x32_bf16 a[80:83], a[244:247], a[176:179], a[80:83]
s_waitcnt lgkmcnt(4)
s_add_u32 s54, s54, 16384
s_add_u32 m0, s70, 149504
v_mfma_f32_16x16x32_bf16 a[96:99], a[248:251], a[176:179], a[96:99]
ds_read_b64_tr_b16 a[244:245], v19 offset:0
v_mfma_f32_16x16x32_bf16 a[100:103], a[248:251], a[180:183], a[100:103]
ds_read_b64_tr_b16 a[246:247], v19 offset:4096
v_mfma_f32_16x16x32_bf16 a[104:107], a[248:251], a[184:187], a[104:107]
buffer_load_dwordx4 v13, s[76:79], s54 offen lds
v_mfma_f32_16x16x32_bf16 a[108:111], a[248:251], a[188:191], a[108:111]
s_waitcnt lgkmcnt(4)
s_add_u32 m0, s70, 150400
v_mfma_f32_16x16x32_bf16 a[124:127], a[252:255], a[188:191], a[124:127]
ds_read_b64_tr_b16 a[248:249], v20 offset:0
v_mfma_f32_16x16x32_bf16 a[120:123], a[252:255], a[184:187], a[120:123]
ds_read_b64_tr_b16 a[250:251], v20 offset:4096
v_mfma_f32_16x16x32_bf16 a[116:119], a[252:255], a[180:183], a[116:119]
buffer_load_dwordx4 v13, s[76:79], s54 offen offset:128 lds
v_mfma_f32_16x16x32_bf16 a[112:115], a[252:255], a[176:179], a[112:115]
s_waitcnt lgkmcnt(4)
s_waitcnt vmcnt(38)
v_mfma_f32_16x16x32_bf16 a[0:3], a[240:243], a[192:195], a[0:3]
ds_read_b64_tr_b16 a[252:253], v21 offset:0
v_mfma_f32_16x16x32_bf16 a[4:7], a[240:243], a[196:199], a[4:7]
ds_read_b64_tr_b16 a[254:255], v21 offset:4096
v_mfma_f32_16x16x32_bf16 a[8:11], a[240:243], a[200:203], a[8:11]
v_mfma_f32_16x16x32_bf16 a[12:15], a[240:243], a[204:207], a[12:15]
s_waitcnt lgkmcnt(4)
v_mfma_f32_16x16x32_bf16 a[28:31], a[244:247], a[204:207], a[28:31]
ds_read_b64_tr_b16 a[240:241], v18 offset:1024
v_mfma_f32_16x16x32_bf16 a[24:27], a[244:247], a[200:203], a[24:27]
ds_read_b64_tr_b16 a[242:243], v18 offset:5120
v_mfma_f32_16x16x32_bf16 a[20:23], a[244:247], a[196:199], a[20:23]
buffer_load_dwordx4 a[176:179], v236, s[92:95], s96 offen sc1
v_mfma_f32_16x16x32_bf16 a[16:19], a[244:247], a[192:195], a[16:19]
s_waitcnt lgkmcnt(4)
v_mfma_f32_16x16x32_bf16 a[32:35], a[248:251], a[192:195], a[32:35]
ds_read_b64_tr_b16 a[244:245], v19 offset:1024
v_mfma_f32_16x16x32_bf16 a[36:39], a[248:251], a[196:199], a[36:39]
ds_read_b64_tr_b16 a[246:247], v19 offset:5120
v_mfma_f32_16x16x32_bf16 a[40:43], a[248:251], a[200:203], a[40:43]
v_mfma_f32_16x16x32_bf16 a[44:47], a[248:251], a[204:207], a[44:47]
s_waitcnt lgkmcnt(4)
v_mfma_f32_16x16x32_bf16 a[60:63], a[252:255], a[204:207], a[60:63]
ds_read_b64_tr_b16 a[248:249], v20 offset:1024
v_mfma_f32_16x16x32_bf16 a[56:59], a[252:255], a[200:203], a[56:59]
ds_read_b64_tr_b16 a[250:251], v20 offset:5120
v_add_u32 v22, 98304, v14
v_mfma_f32_16x16x32_bf16 a[52:55], a[252:255], a[196:199], a[52:55]
buffer_load_dwordx4 a[180:183], v237, s[92:95], s96 offen sc1
v_mfma_f32_16x16x32_bf16 a[48:51], a[252:255], a[192:195], a[48:51]
s_waitcnt lgkmcnt(4)
v_add_u32 v23, 98304, v15
v_add_u32 v24, 98304, v16
v_mfma_f32_16x16x32_bf16 a[64:67], a[240:243], a[192:195], a[64:67]
ds_read_b64_tr_b16 a[252:253], v21 offset:1024
v_add_u32 v25, 98304, v17
v_mfma_f32_16x16x32_bf16 a[68:71], a[240:243], a[196:199], a[68:71]
ds_read_b64_tr_b16 a[254:255], v21 offset:5120
v_mfma_f32_16x16x32_bf16 a[72:75], a[240:243], a[200:203], a[72:75]
v_mfma_f32_16x16x32_bf16 a[76:79], a[240:243], a[204:207], a[76:79]
s_waitcnt lgkmcnt(4)
s_add_u32 s54, s0, 7
s_and_b32 s54, s54, 7
v_mfma_f32_16x16x32_bf16 a[92:95], a[244:247], a[204:207], a[92:95]
ds_read_b64_tr_b16 a[240:241], v18 offset:8192
s_mul_i32 s54, s54, 131072
v_mfma_f32_16x16x32_bf16 a[88:91], a[244:247], a[200:203], a[88:91]
ds_read_b64_tr_b16 a[242:243], v18 offset:12288
s_add_u32 s54, s54, s80
v_mfma_f32_16x16x32_bf16 a[84:87], a[244:247], a[196:199], a[84:87]
buffer_load_dwordx4 a[184:187], v238, s[92:95], s96 offen sc1
v_mfma_f32_16x16x32_bf16 a[80:83], a[244:247], a[192:195], a[80:83]
s_waitcnt lgkmcnt(4)
s_add_u32 m0, s70, 0
v_mfma_f32_16x16x32_bf16 a[96:99], a[248:251], a[192:195], a[96:99]
ds_read_b64_tr_b16 a[244:245], v19 offset:8192
v_mfma_f32_16x16x32_bf16 a[100:103], a[248:251], a[196:199], a[100:103]
ds_read_b64_tr_b16 a[246:247], v19 offset:12288
v_mfma_f32_16x16x32_bf16 a[104:107], a[248:251], a[200:203], a[104:107]
v_mfma_f32_16x16x32_bf16 a[108:111], a[248:251], a[204:207], a[108:111]
s_waitcnt lgkmcnt(4)
v_mfma_f32_16x16x32_bf16 a[124:127], a[252:255], a[204:207], a[124:127]
ds_read_b64_tr_b16 a[248:249], v20 offset:8192
v_mfma_f32_16x16x32_bf16 a[120:123], a[252:255], a[200:203], a[120:123]
ds_read_b64_tr_b16 a[250:251], v20 offset:12288
v_mfma_f32_16x16x32_bf16 a[116:119], a[252:255], a[196:199], a[116:119]
buffer_load_dwordx4 a[188:191], v239, s[92:95], s96 offen sc1
v_mfma_f32_16x16x32_bf16 a[112:115], a[252:255], a[192:195], a[112:115]
s_waitcnt lgkmcnt(4)
s_waitcnt vmcnt(38)
s_add_u32 s96, s96, 4096
v_mfma_f32_16x16x32_bf16 a[0:3], a[240:243], a[208:211], a[0:3]
ds_read_b64_tr_b16 a[252:253], v21 offset:8192
v_mfma_f32_16x16x32_bf16 a[4:7], a[240:243], a[212:215], a[4:7]
ds_read_b64_tr_b16 a[254:255], v21 offset:12288
v_mfma_f32_16x16x32_bf16 a[8:11], a[240:243], a[216:219], a[8:11]
buffer_load_dwordx4 v[52:55], v236, s[92:95], s96 offen sc1
v_mfma_f32_16x16x32_bf16 a[12:15], a[240:243], a[220:223], a[12:15]
s_barrier
s_waitcnt lgkmcnt(4)
v_mfma_f32_16x16x32_bf16 a[28:31], a[244:247], a[220:223], a[28:31]
ds_read_b64_tr_b16 a[240:241], v18 offset:9216
v_mfma_f32_16x16x32_bf16 a[24:27], a[244:247], a[216:219], a[24:27]
ds_read_b64_tr_b16 a[242:243], v18 offset:13312
v_mfma_f32_16x16x32_bf16 a[20:23], a[244:247], a[212:215], a[20:23]
buffer_load_dwordx4 v[84:87], v237, s[92:95], s96 offen sc1
v_mfma_f32_16x16x32_bf16 a[16:19], a[244:247], a[208:211], a[16:19]
s_waitcnt lgkmcnt(4)
v_mfma_f32_16x16x32_bf16 a[32:35], a[248:251], a[208:211], a[32:35]
ds_read_b64_tr_b16 a[244:245], v19 offset:9216
v_mfma_f32_16x16x32_bf16 a[36:39], a[248:251], a[212:215], a[36:39]
ds_read_b64_tr_b16 a[246:247], v19 offset:13312
v_mfma_f32_16x16x32_bf16 a[40:43], a[248:251], a[216:219], a[40:43]
buffer_load_dwordx4 v[116:119], v238, s[92:95], s96 offen sc1
v_mfma_f32_16x16x32_bf16 a[44:47], a[248:251], a[220:223], a[44:47]
s_waitcnt lgkmcnt(4)
v_mfma_f32_16x16x32_bf16 a[60:63], a[252:255], a[220:223], a[60:63]
ds_read_b64_tr_b16 a[248:249], v20 offset:9216
v_mfma_f32_16x16x32_bf16 a[56:59], a[252:255], a[216:219], a[56:59]
ds_read_b64_tr_b16 a[250:251], v20 offset:13312
v_mfma_f32_16x16x32_bf16 a[52:55], a[252:255], a[212:215], a[52:55]
buffer_load_dwordx4 v[148:151], v239, s[92:95], s96 offen sc1
v_mfma_f32_16x16x32_bf16 a[48:51], a[252:255], a[208:211], a[48:51]
s_waitcnt lgkmcnt(4)
s_add_u32 s96, s0, 6
s_and_b32 s96, s96, 7
v_mfma_f32_16x16x32_bf16 a[64:67], a[240:243], a[208:211], a[64:67]
ds_read_b64_tr_b16 a[252:253], v21 offset:9216
s_mul_i32 s96, s96, 8192
v_mfma_f32_16x16x32_bf16 a[68:71], a[240:243], a[212:215], a[68:71]
ds_read_b64_tr_b16 a[254:255], v21 offset:13312
s_add_u32 s96, s96, s3
v_mfma_f32_16x16x32_bf16 a[72:75], a[240:243], a[216:219], a[72:75]
buffer_load_dwordx4 v13, s[76:79], s54 offen lds
v_mfma_f32_16x16x32_bf16 a[76:79], a[240:243], a[220:223], a[76:79]
s_waitcnt lgkmcnt(4)
s_add_u32 m0, s70, 896
v_mfma_f32_16x16x32_bf16 a[92:95], a[244:247], a[220:223], a[92:95]
ds_read_b64_tr_b16 a[240:241], v22 offset:0
v_mfma_f32_16x16x32_bf16 a[88:91], a[244:247], a[216:219], a[88:91]
ds_read_b64_tr_b16 a[242:243], v22 offset:4096
v_mfma_f32_16x16x32_bf16 a[84:87], a[244:247], a[212:215], a[84:87]
buffer_load_dwordx4 v13, s[76:79], s54 offen offset:128 lds
v_mfma_f32_16x16x32_bf16 a[80:83], a[244:247], a[208:211], a[80:83]
s_waitcnt lgkmcnt(4)
s_add_u32 s54, s54, 16384
s_add_u32 m0, s70, 2048
v_mfma_f32_16x16x32_bf16 a[96:99], a[248:251], a[208:211], a[96:99]
ds_read_b64_tr_b16 a[244:245], v23 offset:0
v_mfma_f32_16x16x32_bf16 a[100:103], a[248:251], a[212:215], a[100:103]
ds_read_b64_tr_b16 a[246:247], v23 offset:4096
v_mfma_f32_16x16x32_bf16 a[104:107], a[248:251], a[216:219], a[104:107]
buffer_load_dwordx4 v13, s[76:79], s54 offen lds
v_mfma_f32_16x16x32_bf16 a[108:111], a[248:251], a[220:223], a[108:111]
s_waitcnt lgkmcnt(4)
s_add_u32 m0, s70, 2944
v_mfma_f32_16x16x32_bf16 a[124:127], a[252:255], a[220:223], a[124:127]
ds_read_b64_tr_b16 a[248:249], v24 offset:0
v_mfma_f32_16x16x32_bf16 a[120:123], a[252:255], a[216:219], a[120:123]
ds_read_b64_tr_b16 a[250:251], v24 offset:4096
v_mfma_f32_16x16x32_bf16 a[116:119], a[252:255], a[212:215], a[116:119]
buffer_load_dwordx4 v13, s[76:79], s54 offen offset:128 lds
v_mfma_f32_16x16x32_bf16 a[112:115], a[252:255], a[208:211], a[112:115]
s_waitcnt lgkmcnt(4)
s_waitcnt vmcnt(38)
v_mfma_f32_16x16x32_bf16 a[0:3], a[240:243], a[224:227], a[0:3]
ds_read_b64_tr_b16 a[252:253], v25 offset:0
v_mfma_f32_16x16x32_bf16 a[4:7], a[240:243], a[228:231], a[4:7]
ds_read_b64_tr_b16 a[254:255], v25 offset:4096
v_mfma_f32_16x16x32_bf16 a[8:11], a[240:243], a[232:235], a[8:11]
v_mfma_f32_16x16x32_bf16 a[12:15], a[240:243], a[236:239], a[12:15]
s_waitcnt lgkmcnt(4)
v_mfma_f32_16x16x32_bf16 a[28:31], a[244:247], a[236:239], a[28:31]
ds_read_b64_tr_b16 a[240:241], v22 offset:1024
v_mfma_f32_16x16x32_bf16 a[24:27], a[244:247], a[232:235], a[24:27]
ds_read_b64_tr_b16 a[242:243], v22 offset:5120
v_mfma_f32_16x16x32_bf16 a[20:23], a[244:247], a[228:231], a[20:23]
buffer_load_dwordx4 a[192:195], v236, s[92:95], s96 offen sc1
v_mfma_f32_16x16x32_bf16 a[16:19], a[244:247], a[224:227], a[16:19]
s_waitcnt lgkmcnt(4)
v_mfma_f32_16x16x32_bf16 a[32:35], a[248:251], a[224:227], a[32:35]
ds_read_b64_tr_b16 a[244:245], v23 offset:1024
v_mfma_f32_16x16x32_bf16 a[36:39], a[248:251], a[228:231], a[36:39]
ds_read_b64_tr_b16 a[246:247], v23 offset:5120
v_mfma_f32_16x16x32_bf16 a[40:43], a[248:251], a[232:235], a[40:43]
v_mfma_f32_16x16x32_bf16 a[44:47], a[248:251], a[236:239], a[44:47]
s_waitcnt lgkmcnt(4)
v_mfma_f32_16x16x32_bf16 a[60:63], a[252:255], a[236:239], a[60:63]
ds_read_b64_tr_b16 a[248:249], v24 offset:1024
v_mfma_f32_16x16x32_bf16 a[56:59], a[252:255], a[232:235], a[56:59]
ds_read_b64_tr_b16 a[250:251], v24 offset:5120
v_add_u32 v18, 114688, v14
v_mfma_f32_16x16x32_bf16 a[52:55], a[252:255], a[228:231], a[52:55]
buffer_load_dwordx4 a[196:199], v237, s[92:95], s96 offen sc1
v_mfma_f32_16x16x32_bf16 a[48:51], a[252:255], a[224:227], a[48:51]
s_waitcnt lgkmcnt(4)
v_add_u32 v19, 114688, v15
v_add_u32 v20, 114688, v16
v_mfma_f32_16x16x32_bf16 a[64:67], a[240:243], a[224:227], a[64:67]
ds_read_b64_tr_b16 a[252:253], v25 offset:1024
v_add_u32 v21, 114688, v17
v_mfma_f32_16x16x32_bf16 a[68:71], a[240:243], a[228:231], a[68:71]
ds_read_b64_tr_b16 a[254:255], v25 offset:5120
v_mfma_f32_16x16x32_bf16 a[72:75], a[240:243], a[232:235], a[72:75]
v_mfma_f32_16x16x32_bf16 a[76:79], a[240:243], a[236:239], a[76:79]
s_waitcnt lgkmcnt(4)
s_add_u32 s54, s0, 0
s_and_b32 s54, s54, 7
v_mfma_f32_16x16x32_bf16 a[92:95], a[244:247], a[236:239], a[92:95]
ds_read_b64_tr_b16 a[240:241], v22 offset:8192
s_mul_i32 s54, s54, 131072
v_mfma_f32_16x16x32_bf16 a[88:91], a[244:247], a[232:235], a[88:91]
ds_read_b64_tr_b16 a[242:243], v22 offset:12288
s_add_u32 s54, s54, s80
v_mfma_f32_16x16x32_bf16 a[84:87], a[244:247], a[228:231], a[84:87]
buffer_load_dwordx4 a[200:203], v238, s[92:95], s96 offen sc1
v_mfma_f32_16x16x32_bf16 a[80:83], a[244:247], a[224:227], a[80:83]
s_waitcnt lgkmcnt(4)
s_add_u32 s54, s54, 1048576
s_add_u32 m0, s70, 16384
v_mfma_f32_16x16x32_bf16 a[96:99], a[248:251], a[224:227], a[96:99]
ds_read_b64_tr_b16 a[244:245], v23 offset:8192
v_cvt_pk_bf16_f32 v104, v104, v105
v_mfma_f32_16x16x32_bf16 a[100:103], a[248:251], a[228:231], a[100:103]
ds_read_b64_tr_b16 a[246:247], v23 offset:12288
v_cvt_pk_bf16_f32 v105, v106, v107
v_mfma_f32_16x16x32_bf16 a[104:107], a[248:251], a[232:235], a[104:107]
v_cvt_pk_bf16_f32 v106, v120, v121
v_cvt_pk_bf16_f32 v107, v122, v123
v_mfma_f32_16x16x32_bf16 a[108:111], a[248:251], a[236:239], a[108:111]
s_waitcnt lgkmcnt(4)
v_mfma_f32_16x16x32_bf16 a[124:127], a[252:255], a[236:239], a[124:127]
ds_read_b64_tr_b16 a[248:249], v24 offset:8192
v_mfma_f32_16x16x32_bf16 a[120:123], a[252:255], a[232:235], a[120:123]
ds_read_b64_tr_b16 a[250:251], v24 offset:12288
v_mfma_f32_16x16x32_bf16 a[116:119], a[252:255], a[228:231], a[116:119]
buffer_load_dwordx4 a[204:207], v239, s[92:95], s96 offen sc1
v_mfma_f32_16x16x32_bf16 a[112:115], a[252:255], a[224:227], a[112:115]
s_waitcnt lgkmcnt(4)
s_waitcnt vmcnt(37)
s_add_u32 s96, s96, 4096
v_mfma_f32_16x16x32_bf16 a[0:3], a[240:243], a[128:131], a[0:3]
ds_read_b64_tr_b16 a[252:253], v25 offset:8192
v_cvt_pk_bf16_f32 v136, v136, v137
v_mfma_f32_16x16x32_bf16 a[4:7], a[240:243], a[132:135], a[4:7]
ds_read_b64_tr_b16 a[254:255], v25 offset:12288
v_cvt_pk_bf16_f32 v137, v138, v139
v_mfma_f32_16x16x32_bf16 a[8:11], a[240:243], a[136:139], a[8:11]
buffer_load_dwordx4 v[56:59], v236, s[92:95], s96 offen sc1
v_mfma_f32_16x16x32_bf16 a[12:15], a[240:243], a[140:143], a[12:15]
s_barrier
s_waitcnt lgkmcnt(4)
v_mfma_f32_16x16x32_bf16 a[28:31], a[244:247], a[140:143], a[28:31]
ds_read_b64_tr_b16 a[240:241], v22 offset:9216
v_cvt_pk_bf16_f32 v138, v152, v153
v_mfma_f32_16x16x32_bf16 a[24:27], a[244:247], a[136:139], a[24:27]
ds_read_b64_tr_b16 a[242:243], v22 offset:13312
v_cvt_pk_bf16_f32 v139, v154, v155
v_mfma_f32_16x16x32_bf16 a[20:23], a[244:247], a[132:135], a[20:23]
buffer_load_dwordx4 v[88:91], v237, s[92:95], s96 offen sc1
v_mfma_f32_16x16x32_bf16 a[16:19], a[244:247], a[128:131], a[16:19]
s_waitcnt lgkmcnt(4)
v_cvt_pk_bf16_f32 v44, v44, v45
v_cvt_pk_bf16_f32 v45, v46, v47
v_mfma_f32_16x16x32_bf16 a[32:35], a[248:251], a[128:131], a[32:35]
ds_read_b64_tr_b16 a[244:245], v23 offset:9216
v_cvt_pk_bf16_f32 v46, v60, v61
v_mfma_f32_16x16x32_bf16 a[36:39], a[248:251], a[132:135], a[36:39]
ds_read_b64_tr_b16 a[246:247], v23 offset:13312
v_cvt_pk_bf16_f32 v47, v62, v63
v_mfma_f32_16x16x32_bf16 a[40:43], a[248:251], a[136:139], a[40:43]
buffer_load_dwordx4 v[120:123], v238, s[92:95], s96 offen sc1
v_mfma_f32_16x16x32_bf16 a[44:47], a[248:251], a[140:143], a[44:47]
s_waitcnt lgkmcnt(4)
v_mfma_f32_16x16x32_bf16 a[60:63], a[252:255], a[140:143], a[60:63]
ds_read_b64_tr_b16 a[248:249], v24 offset:9216
v_mfma_f32_16x16x32_bf16 a[56:59], a[252:255], a[136:139], a[56:59]
ds_read_b64_tr_b16 a[250:251], v24 offset:13312
v_mfma_f32_16x16x32_bf16 a[52:55], a[252:255], a[132:135], a[52:55]
buffer_load_dwordx4 v[152:155], v239, s[92:95], s96 offen sc1
v_mfma_f32_16x16x32_bf16 a[48:51], a[252:255], a[128:131], a[48:51]
s_waitcnt lgkmcnt(4)
s_add_u32 s96, s0, 7
s_and_b32 s96, s96, 7
v_mfma_f32_16x16x32_bf16 a[64:67], a[240:243], a[128:131], a[64:67]
ds_read_b64_tr_b16 a[252:253], v25 offset:9216
s_mul_i32 s96, s96, 8192
v_mfma_f32_16x16x32_bf16 a[68:71], a[240:243], a[132:135], a[68:71]
ds_read_b64_tr_b16 a[254:255], v25 offset:13312
s_add_u32 s96, s96, s3
v_mfma_f32_16x16x32_bf16 a[72:75], a[240:243], a[136:139], a[72:75]
buffer_load_dwordx4 v13, s[76:79], s54 offen lds
v_mfma_f32_16x16x32_bf16 a[76:79], a[240:243], a[140:143], a[76:79]
s_waitcnt lgkmcnt(4)
s_add_u32 m0, s70, 17280
v_cvt_pk_bf16_f32 v76, v76, v77
v_mfma_f32_16x16x32_bf16 a[92:95], a[244:247], a[140:143], a[92:95]
ds_read_b64_tr_b16 a[240:241], v18 offset:0
v_cvt_pk_bf16_f32 v77, v78, v79
v_mfma_f32_16x16x32_bf16 a[88:91], a[244:247], a[136:139], a[88:91]
ds_read_b64_tr_b16 a[242:243], v18 offset:4096
v_cvt_pk_bf16_f32 v78, v92, v93
v_mfma_f32_16x16x32_bf16 a[84:87], a[244:247], a[132:135], a[84:87]
buffer_load_dwordx4 v13, s[76:79], s54 offen offset:128 lds
v_mfma_f32_16x16x32_bf16 a[80:83], a[244:247], a[128:131], a[80:83]
s_waitcnt lgkmcnt(4)
s_add_u32 s54, s54, 16384
s_add_u32 m0, s70, 18432
v_mfma_f32_16x16x32_bf16 a[96:99], a[248:251], a[128:131], a[96:99]
ds_read_b64_tr_b16 a[244:245], v19 offset:0
v_cvt_pk_bf16_f32 v79, v94, v95
v_mfma_f32_16x16x32_bf16 a[100:103], a[248:251], a[132:135], a[100:103]
ds_read_b64_tr_b16 a[246:247], v19 offset:4096
v_mfma_f32_16x16x32_bf16 a[104:107], a[248:251], a[136:139], a[104:107]
buffer_load_dwordx4 v13, s[76:79], s54 offen lds
v_mfma_f32_16x16x32_bf16 a[108:111], a[248:251], a[140:143], a[108:111]
s_waitcnt lgkmcnt(4)
s_add_u32 m0, s70, 19328
v_mfma_f32_16x16x32_bf16 a[124:127], a[252:255], a[140:143], a[124:127]
ds_read_b64_tr_b16 a[248:249], v20 offset:0
v_mfma_f32_16x16x32_bf16 a[120:123], a[252:255], a[136:139], a[120:123]
ds_read_b64_tr_b16 a[250:251], v20 offset:4096
v_mfma_f32_16x16x32_bf16 a[116:119], a[252:255], a[132:135], a[116:119]
buffer_load_dwordx4 v13, s[76:79], s54 offen offset:128 lds
v_mfma_f32_16x16x32_bf16 a[112:115], a[252:255], a[128:131], a[112:115]
s_waitcnt lgkmcnt(4)
s_waitcnt vmcnt(32)
v_cvt_pk_bf16_f32 v108, v108, v109
v_mfma_f32_16x16x32_bf16 a[0:3], a[240:243], a[144:147], a[0:3]
ds_read_b64_tr_b16 a[252:253], v21 offset:0
v_cvt_pk_bf16_f32 v109, v110, v111
v_mfma_f32_16x16x32_bf16 a[4:7], a[240:243], a[148:151], a[4:7]
ds_read_b64_tr_b16 a[254:255], v21 offset:4096
v_cvt_pk_bf16_f32 v110, v124, v125
v_mfma_f32_16x16x32_bf16 a[8:11], a[240:243], a[152:155], a[8:11]
buffer_store_dwordx4 v[104:107], v2, s[32:35], s86 offen offset:2048 sc1
v_mfma_f32_16x16x32_bf16 a[12:15], a[240:243], a[156:159], a[12:15]
s_waitcnt lgkmcnt(4)
v_cvt_pk_bf16_f32 v111, v126, v127
v_mfma_f32_16x16x32_bf16 a[28:31], a[244:247], a[156:159], a[28:31]
ds_read_b64_tr_b16 a[240:241], v18 offset:1024
v_mfma_f32_16x16x32_bf16 a[24:27], a[244:247], a[152:155], a[24:27]
ds_read_b64_tr_b16 a[242:243], v18 offset:5120
v_mfma_f32_16x16x32_bf16 a[20:23], a[244:247], a[148:151], a[20:23]
buffer_load_dwordx4 a[128:131], v236, s[92:95], s96 offen sc1
v_mfma_f32_16x16x32_bf16 a[16:19], a[244:247], a[144:147], a[16:19]
s_waitcnt lgkmcnt(4)
v_mfma_f32_16x16x32_bf16 a[32:35], a[248:251], a[144:147], a[32:35]
ds_read_b64_tr_b16 a[244:245], v19 offset:1024
v_mfma_f32_16x16x32_bf16 a[36:39], a[248:251], a[148:151], a[36:39]
ds_read_b64_tr_b16 a[246:247], v19 offset:5120
v_mfma_f32_16x16x32_bf16 a[40:43], a[248:251], a[152:155], a[40:43]
buffer_store_dwordx4 v[136:139], v2, s[32:35], s86 offen offset:3072 sc1
v_mfma_f32_16x16x32_bf16 a[44:47], a[248:251], a[156:159], a[44:47]
s_waitcnt lgkmcnt(4)
s_add_u32 s86, s81, 32768
v_cvt_pk_bf16_f32 v140, v140, v141
v_mfma_f32_16x16x32_bf16 a[60:63], a[252:255], a[156:159], a[60:63]
ds_read_b64_tr_b16 a[248:249], v20 offset:1024
v_cvt_pk_bf16_f32 v141, v142, v143
v_mfma_f32_16x16x32_bf16 a[56:59], a[252:255], a[152:155], a[56:59]
ds_read_b64_tr_b16 a[250:251], v20 offset:5120
v_add_u32 v22, 131072, v14
v_mfma_f32_16x16x32_bf16 a[52:55], a[252:255], a[148:151], a[52:55]
buffer_load_dwordx4 a[132:135], v237, s[92:95], s96 offen sc1
v_mfma_f32_16x16x32_bf16 a[48:51], a[252:255], a[144:147], a[48:51]
s_waitcnt lgkmcnt(4)
v_cvt_pk_bf16_f32 v142, v156, v157
v_cvt_pk_bf16_f32 v143, v158, v159
v_mfma_f32_16x16x32_bf16 a[64:67], a[240:243], a[144:147], a[64:67]
ds_read_b64_tr_b16 a[252:253], v21 offset:1024
v_add_u32 v23, 131072, v15
v_mfma_f32_16x16x32_bf16 a[68:71], a[240:243], a[148:151], a[68:71]
ds_read_b64_tr_b16 a[254:255], v21 offset:5120
v_add_u32 v24, 131072, v16
v_mfma_f32_16x16x32_bf16 a[72:75], a[240:243], a[152:155], a[72:75]
buffer_store_dwordx4 v[44:47], v2, s[32:35], s86 offen offset:0 sc1
v_mfma_f32_16x16x32_bf16 a[76:79], a[240:243], a[156:159], a[76:79]
s_waitcnt lgkmcnt(4)
s_add_u32 s54, s0, 1
s_and_b32 s54, s54, 7
v_mfma_f32_16x16x32_bf16 a[92:95], a[244:247], a[156:159], a[92:95]
ds_read_b64_tr_b16 a[240:241], v18 offset:8192
s_mul_i32 s54, s54, 131072
v_mfma_f32_16x16x32_bf16 a[88:91], a[244:247], a[152:155], a[88:91]
ds_read_b64_tr_b16 a[242:243], v18 offset:12288
s_add_u32 s54, s54, s80
v_mfma_f32_16x16x32_bf16 a[84:87], a[244:247], a[148:151], a[84:87]
buffer_load_dwordx4 a[136:139], v238, s[92:95], s96 offen sc1
v_mfma_f32_16x16x32_bf16 a[80:83], a[244:247], a[144:147], a[80:83]
s_waitcnt lgkmcnt(4)
s_add_u32 s54, s54, 1048576
s_add_u32 m0, s70, 32768
v_mfma_f32_16x16x32_bf16 a[96:99], a[248:251], a[144:147], a[96:99]
ds_read_b64_tr_b16 a[244:245], v19 offset:8192
v_add_u32 v25, 131072, v17
v_mfma_f32_16x16x32_bf16 a[100:103], a[248:251], a[148:151], a[100:103]
ds_read_b64_tr_b16 a[246:247], v19 offset:12288
v_mfma_f32_16x16x32_bf16 a[104:107], a[248:251], a[152:155], a[104:107]
buffer_store_dwordx4 v[76:79], v2, s[32:35], s86 offen offset:1024 sc1
v_mfma_f32_16x16x32_bf16 a[108:111], a[248:251], a[156:159], a[108:111]
s_waitcnt lgkmcnt(4)
v_mfma_f32_16x16x32_bf16 a[124:127], a[252:255], a[156:159], a[124:127]
ds_read_b64_tr_b16 a[248:249], v20 offset:8192
v_mfma_f32_16x16x32_bf16 a[120:123], a[252:255], a[152:155], a[120:123]
ds_read_b64_tr_b16 a[250:251], v20 offset:12288
v_mfma_f32_16x16x32_bf16 a[116:119], a[252:255], a[148:151], a[116:119]
buffer_load_dwordx4 a[140:143], v239, s[92:95], s96 offen sc1
v_mfma_f32_16x16x32_bf16 a[112:115], a[252:255], a[144:147], a[112:115]
s_waitcnt lgkmcnt(4)
s_waitcnt vmcnt(36)
s_add_u32 s96, s96, 4096
v_mfma_f32_16x16x32_bf16 a[0:3], a[240:243], a[160:163], a[0:3]
ds_read_b64_tr_b16 a[252:253], v21 offset:8192
v_mfma_f32_16x16x32_bf16 a[4:7], a[240:243], a[164:167], a[4:7]
ds_read_b64_tr_b16 a[254:255], v21 offset:12288
v_mfma_f32_16x16x32_bf16 a[8:11], a[240:243], a[168:171], a[8:11]
buffer_load_dwordx4 v[60:63], v236, s[92:95], s96 offen sc1
v_mfma_f32_16x16x32_bf16 a[12:15], a[240:243], a[172:175], a[12:15]
s_barrier
s_waitcnt lgkmcnt(4)
v_mfma_f32_16x16x32_bf16 a[28:31], a[244:247], a[172:175], a[28:31]
ds_read_b64_tr_b16 a[240:241], v18 offset:9216
v_mfma_f32_16x16x32_bf16 a[24:27], a[244:247], a[168:171], a[24:27]
ds_read_b64_tr_b16 a[242:243], v18 offset:13312
v_mfma_f32_16x16x32_bf16 a[20:23], a[244:247], a[164:167], a[20:23]
buffer_load_dwordx4 v[92:95], v237, s[92:95], s96 offen sc1
v_mfma_f32_16x16x32_bf16 a[16:19], a[244:247], a[160:163], a[16:19]
s_waitcnt lgkmcnt(4)
v_mfma_f32_16x16x32_bf16 a[32:35], a[248:251], a[160:163], a[32:35]
ds_read_b64_tr_b16 a[244:245], v19 offset:9216
v_mfma_f32_16x16x32_bf16 a[36:39], a[248:251], a[164:167], a[36:39]
ds_read_b64_tr_b16 a[246:247], v19 offset:13312
v_mfma_f32_16x16x32_bf16 a[40:43], a[248:251], a[168:171], a[40:43]
buffer_load_dwordx4 v[124:127], v238, s[92:95], s96 offen sc1
v_mfma_f32_16x16x32_bf16 a[44:47], a[248:251], a[172:175], a[44:47]
s_waitcnt lgkmcnt(4)
buffer_store_dwordx4 v[108:111], v2, s[32:35], s86 offen offset:2048 sc1
v_mfma_f32_16x16x32_bf16 a[60:63], a[252:255], a[172:175], a[60:63]
ds_read_b64_tr_b16 a[248:249], v20 offset:9216
v_mfma_f32_16x16x32_bf16 a[56:59], a[252:255], a[168:171], a[56:59]
ds_read_b64_tr_b16 a[250:251], v20 offset:13312
v_mfma_f32_16x16x32_bf16 a[52:55], a[252:255], a[164:167], a[52:55]
buffer_load_dwordx4 v[156:159], v239, s[92:95], s96 offen sc1
v_mfma_f32_16x16x32_bf16 a[48:51], a[252:255], a[160:163], a[48:51]
s_waitcnt lgkmcnt(4)
s_add_u32 s96, s0, 0
s_and_b32 s96, s96, 7
v_mfma_f32_16x16x32_bf16 a[64:67], a[240:243], a[160:163], a[64:67]
ds_read_b64_tr_b16 a[252:253], v21 offset:9216
s_mul_i32 s96, s96, 8192
v_mfma_f32_16x16x32_bf16 a[68:71], a[240:243], a[164:167], a[68:71]
ds_read_b64_tr_b16 a[254:255], v21 offset:13312
s_add_u32 s96, s96, s3
v_mfma_f32_16x16x32_bf16 a[72:75], a[240:243], a[168:171], a[72:75]
buffer_load_dwordx4 v13, s[76:79], s54 offen lds
v_mfma_f32_16x16x32_bf16 a[76:79], a[240:243], a[172:175], a[76:79]
s_waitcnt lgkmcnt(4)
s_add_u32 m0, s70, 33664
s_add_u32 s96, s96, 65536
v_mfma_f32_16x16x32_bf16 a[92:95], a[244:247], a[172:175], a[92:95]
ds_read_b64_tr_b16 a[240:241], v22 offset:0
v_mfma_f32_16x16x32_bf16 a[88:91], a[244:247], a[168:171], a[88:91]
ds_read_b64_tr_b16 a[242:243], v22 offset:4096
v_mfma_f32_16x16x32_bf16 a[84:87], a[244:247], a[164:167], a[84:87]
buffer_load_dwordx4 v13, s[76:79], s54 offen offset:128 lds
v_mfma_f32_16x16x32_bf16 a[80:83], a[244:247], a[160:163], a[80:83]
s_waitcnt lgkmcnt(4)
s_add_u32 s54, s54, 16384
s_add_u32 m0, s70, 34816
buffer_store_dwordx4 v[140:143], v2, s[32:35], s86 offen offset:3072 sc1
v_mfma_f32_16x16x32_bf16 a[96:99], a[248:251], a[160:163], a[96:99]
ds_read_b64_tr_b16 a[244:245], v23 offset:0
v_mfma_f32_16x16x32_bf16 a[100:103], a[248:251], a[164:167], a[100:103]
ds_read_b64_tr_b16 a[246:247], v23 offset:4096
v_mfma_f32_16x16x32_bf16 a[104:107], a[248:251], a[168:171], a[104:107]
buffer_load_dwordx4 v13, s[76:79], s54 offen lds
v_mfma_f32_16x16x32_bf16 a[108:111], a[248:251], a[172:175], a[108:111]
s_waitcnt lgkmcnt(4)
s_add_u32 m0, s70, 35712
v_mfma_f32_16x16x32_bf16 a[124:127], a[252:255], a[172:175], a[124:127]
ds_read_b64_tr_b16 a[248:249], v24 offset:0
v_mfma_f32_16x16x32_bf16 a[120:123], a[252:255], a[168:171], a[120:123]
ds_read_b64_tr_b16 a[250:251], v24 offset:4096
v_mfma_f32_16x16x32_bf16 a[116:119], a[252:255], a[164:167], a[116:119]
buffer_load_dwordx4 v13, s[76:79], s54 offen offset:128 lds
v_mfma_f32_16x16x32_bf16 a[112:115], a[252:255], a[160:163], a[112:115]
s_waitcnt lgkmcnt(4)
s_waitcnt vmcnt(38)
v_mfma_f32_16x16x32_bf16 a[0:3], a[240:243], a[176:179], a[0:3]
ds_read_b64_tr_b16 a[252:253], v25 offset:0
v_mov_b32 v214, 0xbfb8aa3b
v_mfma_f32_16x16x32_bf16 a[4:7], a[240:243], a[180:183], a[4:7]
ds_read_b64_tr_b16 a[254:255], v25 offset:4096
v_mfma_f32_16x16x32_bf16 a[8:11], a[240:243], a[184:187], a[8:11]
v_mfma_f32_16x16x32_bf16 a[12:15], a[240:243], a[188:191], a[12:15]
s_waitcnt lgkmcnt(4)
v_mfma_f32_16x16x32_bf16 a[28:31], a[244:247], a[188:191], a[28:31]
ds_read_b64_tr_b16 a[240:241], v22 offset:1024
v_mfma_f32_16x16x32_bf16 a[24:27], a[244:247], a[184:187], a[24:27]
ds_read_b64_tr_b16 a[242:243], v22 offset:5120
v_mfma_f32_16x16x32_bf16 a[20:23], a[244:247], a[180:183], a[20:23]
buffer_load_dwordx4 a[144:147], v236, s[92:95], s96 offen sc1
v_mfma_f32_16x16x32_bf16 a[16:19], a[244:247], a[176:179], a[16:19]
s_waitcnt lgkmcnt(4)
v_mfma_f32_16x16x32_bf16 a[32:35], a[248:251], a[176:179], a[32:35]
ds_read_b64_tr_b16 a[244:245], v23 offset:1024
v_mfma_f32_16x16x32_bf16 a[36:39], a[248:251], a[180:183], a[36:39]
ds_read_b64_tr_b16 a[246:247], v23 offset:5120
v_mfma_f32_16x16x32_bf16 a[40:43], a[248:251], a[184:187], a[40:43]
v_mfma_f32_16x16x32_bf16 a[44:47], a[248:251], a[188:191], a[44:47]
s_waitcnt lgkmcnt(4)
v_mfma_f32_16x16x32_bf16 a[60:63], a[252:255], a[188:191], a[60:63]
ds_read_b64_tr_b16 a[248:249], v24 offset:1024
v_mfma_f32_16x16x32_bf16 a[56:59], a[252:255], a[184:187], a[56:59]
ds_read_b64_tr_b16 a[250:251], v24 offset:5120
v_add_u32 v18, 147456, v14
v_mfma_f32_16x16x32_bf16 a[52:55], a[252:255], a[180:183], a[52:55]
buffer_load_dwordx4 a[148:151], v237, s[92:95], s96 offen sc1
v_mfma_f32_16x16x32_bf16 a[48:51], a[252:255], a[176:179], a[48:51]
s_waitcnt lgkmcnt(4)
v_add_u32 v19, 147456, v15
v_add_u32 v20, 147456, v16
v_mfma_f32_16x16x32_bf16 a[64:67], a[240:243], a[176:179], a[64:67]
ds_read_b64_tr_b16 a[252:253], v25 offset:1024
v_add_u32 v21, 147456, v17
v_mfma_f32_16x16x32_bf16 a[68:71], a[240:243], a[180:183], a[68:71]
ds_read_b64_tr_b16 a[254:255], v25 offset:5120
v_mfma_f32_16x16x32_bf16 a[72:75], a[240:243], a[184:187], a[72:75]
v_mfma_f32_16x16x32_bf16 a[76:79], a[240:243], a[188:191], a[76:79]
s_waitcnt lgkmcnt(4)
s_add_u32 s54, s0, 2
s_and_b32 s54, s54, 7
v_mfma_f32_16x16x32_bf16 a[92:95], a[244:247], a[188:191], a[92:95]
ds_read_b64_tr_b16 a[240:241], v22 offset:8192
s_mul_i32 s54, s54, 131072
v_mfma_f32_16x16x32_bf16 a[88:91], a[244:247], a[184:187], a[88:91]
ds_read_b64_tr_b16 a[242:243], v22 offset:12288
s_add_u32 s54, s54, s80
v_mfma_f32_16x16x32_bf16 a[84:87], a[244:247], a[180:183], a[84:87]
buffer_load_dwordx4 a[152:155], v238, s[92:95], s96 offen sc1
v_mfma_f32_16x16x32_bf16 a[80:83], a[244:247], a[176:179], a[80:83]
s_waitcnt lgkmcnt(4)
s_add_u32 s54, s54, 1048576
s_add_u32 m0, s70, 49152
v_mfma_f32_16x16x32_bf16 a[96:99], a[248:251], a[176:179], a[96:99]
ds_read_b64_tr_b16 a[244:245], v23 offset:8192
v_mfma_f32_16x16x32_bf16 a[100:103], a[248:251], a[180:183], a[100:103]
ds_read_b64_tr_b16 a[246:247], v23 offset:12288
v_mfma_f32_16x16x32_bf16 a[104:107], a[248:251], a[184:187], a[104:107]
s_mov_b32 s36, s49
s_mov_b32 s37, s50
s_mov_b32 s38, KTEXT1-KTEXT0
v_mfma_f32_16x16x32_bf16 a[108:111], a[248:251], a[188:191], a[108:111]
s_waitcnt lgkmcnt(4)
s_mov_b32 s39, 0x20000
s_lshr_b32 s69, s98, 3
v_mfma_f32_16x16x32_bf16 a[124:127], a[252:255], a[188:191], a[124:127]
ds_read_b64_tr_b16 a[248:249], v24 offset:8192
s_lshl_b32 s69, s69, 2
v_mfma_f32_16x16x32_bf16 a[120:123], a[252:255], a[184:187], a[120:123]
ds_read_b64_tr_b16 a[250:251], v24 offset:12288
s_add_u32 s69, s69, s43
v_mfma_f32_16x16x32_bf16 a[116:119], a[252:255], a[180:183], a[116:119]
buffer_load_dwordx4 a[156:159], v239, s[92:95], s96 offen sc1
v_mfma_f32_16x16x32_bf16 a[112:115], a[252:255], a[176:179], a[112:115]
s_waitcnt lgkmcnt(4)
s_waitcnt vmcnt(38)
s_add_u32 s96, s96, 4096
v_mfma_f32_16x16x32_bf16 a[0:3], a[240:243], v[52:55], a[0:3]
ds_read_b64_tr_b16 a[252:253], v25 offset:8192
s_and_b32 s69, s69, 63
v_mfma_f32_16x16x32_bf16 a[4:7], a[240:243], v[84:87], a[4:7]
ds_read_b64_tr_b16 a[254:255], v25 offset:12288
s_lshl_b32 s69, s69, 10
v_mfma_f32_16x16x32_bf16 a[8:11], a[240:243], v[116:119], a[8:11]
buffer_load_dwordx4 a[160:163], v236, s[92:95], s96 offen sc1
v_mfma_f32_16x16x32_bf16 a[12:15], a[240:243], v[148:151], a[12:15]
s_barrier
s_waitcnt lgkmcnt(4)
v_mfma_f32_16x16x32_bf16 a[28:31], a[244:247], v[148:151], a[28:31]
ds_read_b64_tr_b16 a[240:241], v22 offset:9216
s_add_u32 s69, s69, KF-KTEXT0
v_mfma_f32_16x16x32_bf16 a[24:27], a[244:247], v[116:119], a[24:27]
ds_read_b64_tr_b16 a[242:243], v22 offset:13312
v_mfma_f32_16x16x32_bf16 a[20:23], a[244:247], v[84:87], a[20:23]
buffer_load_dwordx4 a[164:167], v237, s[92:95], s96 offen sc1
v_mfma_f32_16x16x32_bf16 a[16:19], a[244:247], v[52:55], a[16:19]
s_waitcnt lgkmcnt(4)
v_mfma_f32_16x16x32_bf16 a[32:35], a[248:251], v[52:55], a[32:35]
ds_read_b64_tr_b16 a[244:245], v23 offset:9216
v_mfma_f32_16x16x32_bf16 a[36:39], a[248:251], v[84:87], a[36:39]
ds_read_b64_tr_b16 a[246:247], v23 offset:13312
v_mfma_f32_16x16x32_bf16 a[40:43], a[248:251], v[116:119], a[40:43]
buffer_load_dwordx4 a[168:171], v238, s[92:95], s96 offen sc1
v_mfma_f32_16x16x32_bf16 a[44:47], a[248:251], v[148:151], a[44:47]
s_waitcnt lgkmcnt(4)
v_mfma_f32_16x16x32_bf16 a[60:63], a[252:255], v[148:151], a[60:63]
ds_read_b64_tr_b16 a[248:249], v24 offset:9216
v_mfma_f32_16x16x32_bf16 a[56:59], a[252:255], v[116:119], a[56:59]
ds_read_b64_tr_b16 a[250:251], v24 offset:13312
v_mfma_f32_16x16x32_bf16 a[52:55], a[252:255], v[84:87], a[52:55]
buffer_load_dwordx4 a[172:175], v239, s[92:95], s96 offen sc1
v_mfma_f32_16x16x32_bf16 a[48:51], a[252:255], v[52:55], a[48:51]
s_waitcnt lgkmcnt(4)
s_add_u32 s96, s0, 1
s_and_b32 s96, s96, 7
v_mfma_f32_16x16x32_bf16 a[64:67], a[240:243], v[52:55], a[64:67]
ds_read_b64_tr_b16 a[252:253], v25 offset:9216
s_mul_i32 s96, s96, 8192
v_mfma_f32_16x16x32_bf16 a[68:71], a[240:243], v[84:87], a[68:71]
ds_read_b64_tr_b16 a[254:255], v25 offset:13312
s_add_u32 s96, s96, s3
v_mfma_f32_16x16x32_bf16 a[72:75], a[240:243], v[116:119], a[72:75]
buffer_load_dwordx4 v13, s[76:79], s54 offen lds
v_mfma_f32_16x16x32_bf16 a[76:79], a[240:243], v[148:151], a[76:79]
s_waitcnt lgkmcnt(4)
s_add_u32 m0, s70, 50048
s_add_u32 s96, s96, 65536
v_mfma_f32_16x16x32_bf16 a[92:95], a[244:247], v[148:151], a[92:95]
ds_read_b64_tr_b16 a[240:241], v18 offset:0
v_mfma_f32_16x16x32_bf16 a[88:91], a[244:247], v[116:119], a[88:91]
ds_read_b64_tr_b16 a[242:243], v18 offset:4096
v_mfma_f32_16x16x32_bf16 a[84:87], a[244:247], v[84:87], a[84:87]
buffer_load_dwordx4 v13, s[76:79], s54 offen offset:128 lds
v_mfma_f32_16x16x32_bf16 a[80:83], a[244:247], v[52:55], a[80:83]
s_waitcnt lgkmcnt(4)
s_add_u32 s54, s54, 16384
s_add_u32 m0, s70, 51200
v_mfma_f32_16x16x32_bf16 a[96:99], a[248:251], v[52:55], a[96:99]
ds_read_b64_tr_b16 a[244:245], v19 offset:0
v_mfma_f32_16x16x32_bf16 a[100:103], a[248:251], v[84:87], a[100:103]
ds_read_b64_tr_b16 a[246:247], v19 offset:4096
v_mfma_f32_16x16x32_bf16 a[104:107], a[248:251], v[116:119], a[104:107]
buffer_load_dwordx4 v13, s[76:79], s54 offen lds
v_mfma_f32_16x16x32_bf16 a[108:111], a[248:251], v[148:151], a[108:111]
s_waitcnt lgkmcnt(4)
s_add_u32 m0, s70, 52096
v_mfma_f32_16x16x32_bf16 a[124:127], a[252:255], v[148:151], a[124:127]
ds_read_b64_tr_b16 a[248:249], v20 offset:0
v_mfma_f32_16x16x32_bf16 a[120:123], a[252:255], v[116:119], a[120:123]
ds_read_b64_tr_b16 a[250:251], v20 offset:4096
v_mfma_f32_16x16x32_bf16 a[116:119], a[252:255], v[84:87], a[116:119]
buffer_load_dwordx4 v13, s[76:79], s54 offen offset:128 lds
v_mfma_f32_16x16x32_bf16 a[112:115], a[252:255], v[52:55], a[112:115]
s_waitcnt lgkmcnt(4)
s_waitcnt vmcnt(38)
v_mfma_f32_16x16x32_bf16 a[0:3], a[240:243], a[192:195], a[0:3]
ds_read_b64_tr_b16 a[252:253], v21 offset:0
s_add_u32 s24, s22, 37748736
v_mfma_f32_16x16x32_bf16 a[4:7], a[240:243], a[196:199], a[4:7]
s_addc_u32 s25, s23, 0
ds_read_b64_tr_b16 a[254:255], v21 offset:4096
v_mfma_f32_16x16x32_bf16 a[8:11], a[240:243], a[200:203], a[8:11]
buffer_load_dwordx4 v[244:247], v2, s[36:39], s69 offen
v_mfma_f32_16x16x32_bf16 a[12:15], a[240:243], a[204:207], a[12:15]
s_waitcnt lgkmcnt(4)
s_and_b32 s25, s25, 0xffff
s_mov_b32 s26, 12582912
v_mfma_f32_16x16x32_bf16 a[28:31], a[244:247], a[204:207], a[28:31]
ds_read_b64_tr_b16 a[240:241], v18 offset:1024
s_mov_b32 s27, 0x20000
v_mfma_f32_16x16x32_bf16 a[24:27], a[244:247], a[200:203], a[24:27]
ds_read_b64_tr_b16 a[242:243], v18 offset:5120
s_sub_u32 s87, s59, 1
v_mfma_f32_16x16x32_bf16 a[20:23], a[244:247], a[196:199], a[20:23]
buffer_load_dwordx4 v[52:55], v236, s[92:95], s96 offen sc1
v_mfma_f32_16x16x32_bf16 a[16:19], a[244:247], a[192:195], a[16:19]
s_waitcnt lgkmcnt(4)
s_and_b32 s87, s87, 3
s_lshl_b32 s87, s87, 3
v_mfma_f32_16x16x32_bf16 a[32:35], a[248:251], a[192:195], a[32:35]
ds_read_b64_tr_b16 a[244:245], v19 offset:1024
v_mfma_f32_16x16x32_bf16 a[36:39], a[248:251], a[196:199], a[36:39]
ds_read_b64_tr_b16 a[246:247], v19 offset:5120
v_mfma_f32_16x16x32_bf16 a[40:43], a[248:251], a[200:203], a[40:43]
v_mfma_f32_16x16x32_bf16 a[44:47], a[248:251], a[204:207], a[44:47]
s_waitcnt lgkmcnt(4)
v_mfma_f32_16x16x32_bf16 a[60:63], a[252:255], a[204:207], a[60:63]
ds_read_b64_tr_b16 a[248:249], v20 offset:1024
v_mfma_f32_16x16x32_bf16 a[56:59], a[252:255], a[200:203], a[56:59]
ds_read_b64_tr_b16 a[250:251], v20 offset:5120
v_add_u32 v22, 0, v14
v_mfma_f32_16x16x32_bf16 a[52:55], a[252:255], a[196:199], a[52:55]
buffer_load_dwordx4 v[84:87], v237, s[92:95], s96 offen sc1
v_mfma_f32_16x16x32_bf16 a[48:51], a[252:255], a[192:195], a[48:51]
s_waitcnt lgkmcnt(4)
v_add_u32 v23, 0, v15
v_add_u32 v24, 0, v16
v_mfma_f32_16x16x32_bf16 a[64:67], a[240:243], a[192:195], a[64:67]
ds_read_b64_tr_b16 a[252:253], v21 offset:1024
v_add_u32 v25, 0, v17
v_mfma_f32_16x16x32_bf16 a[68:71], a[240:243], a[196:199], a[68:71]
ds_read_b64_tr_b16 a[254:255], v21 offset:5120
v_mfma_f32_16x16x32_bf16 a[72:75], a[240:243], a[200:203], a[72:75]
v_mfma_f32_16x16x32_bf16 a[76:79], a[240:243], a[204:207], a[76:79]
s_waitcnt lgkmcnt(4)
s_add_u32 s54, s0, 3
s_and_b32 s54, s54, 7
v_mfma_f32_16x16x32_bf16 a[92:95], a[244:247], a[204:207], a[92:95]
ds_read_b64_tr_b16 a[240:241], v18 offset:8192
s_mul_i32 s54, s54, 131072
v_mfma_f32_16x16x32_bf16 a[88:91], a[244:247], a[200:203], a[88:91]
ds_read_b64_tr_b16 a[242:243], v18 offset:12288
s_add_u32 s54, s54, s80
v_mfma_f32_16x16x32_bf16 a[84:87], a[244:247], a[196:199], a[84:87]
buffer_load_dwordx4 v[116:119], v238, s[92:95], s96 offen sc1
v_mfma_f32_16x16x32_bf16 a[80:83], a[244:247], a[192:195], a[80:83]
s_waitcnt lgkmcnt(4)
s_add_u32 s54, s54, 1048576
s_add_u32 m0, s70, 65536
v_mfma_f32_16x16x32_bf16 a[96:99], a[248:251], a[192:195], a[96:99]
ds_read_b64_tr_b16 a[244:245], v19 offset:8192
v_mfma_f32_16x16x32_bf16 a[100:103], a[248:251], a[196:199], a[100:103]
ds_read_b64_tr_b16 a[246:247], v19 offset:12288
v_mfma_f32_16x16x32_bf16 a[104:107], a[248:251], a[200:203], a[104:107]
v_mfma_f32_16x16x32_bf16 a[108:111], a[248:251], a[204:207], a[108:111]
s_waitcnt lgkmcnt(4)
v_mfma_f32_16x16x32_bf16 a[124:127], a[252:255], a[204:207], a[124:127]
ds_read_b64_tr_b16 a[248:249], v20 offset:8192
v_mfma_f32_16x16x32_bf16 a[120:123], a[252:255], a[200:203], a[120:123]
ds_read_b64_tr_b16 a[250:251], v20 offset:12288
v_mfma_f32_16x16x32_bf16 a[116:119], a[252:255], a[196:199], a[116:119]
buffer_load_dwordx4 v[148:151], v239, s[92:95], s96 offen sc1
v_mfma_f32_16x16x32_bf16 a[112:115], a[252:255], a[192:195], a[112:115]
s_waitcnt lgkmcnt(4)
s_waitcnt vmcnt(39)
s_add_u32 s96, s96, 4096
v_mfma_f32_16x16x32_bf16 a[0:3], a[240:243], v[56:59], a[0:3]
ds_read_b64_tr_b16 a[252:253], v21 offset:8192
v_mfma_f32_16x16x32_bf16 a[4:7], a[240:243], v[88:91], a[4:7]
ds_read_b64_tr_b16 a[254:255], v21 offset:12288
v_mfma_f32_16x16x32_bf16 a[8:11], a[240:243], v[120:123], a[8:11]
buffer_load_dwordx4 a[176:179], v236, s[92:95], s96 offen sc1
v_mfma_f32_16x16x32_bf16 a[12:15], a[240:243], v[152:155], a[12:15]
s_barrier
s_waitcnt lgkmcnt(4)
v_mfma_f32_16x16x32_bf16 a[28:31], a[244:247], v[152:155], a[28:31]
ds_read_b64_tr_b16 a[240:241], v18 offset:9216
v_mfma_f32_16x16x32_bf16 a[24:27], a[244:247], v[120:123], a[24:27]
ds_read_b64_tr_b16 a[242:243], v18 offset:13312
v_mfma_f32_16x16x32_bf16 a[20:23], a[244:247], v[88:91], a[20:23]
buffer_load_dwordx4 a[180:183], v237, s[92:95], s96 offen sc1
v_mfma_f32_16x16x32_bf16 a[16:19], a[244:247], v[56:59], a[16:19]
s_waitcnt lgkmcnt(4)
s_lshl_b32 s97, s40, 2
s_add_u32 s97, s97, s43
v_mfma_f32_16x16x32_bf16 a[32:35], a[248:251], v[56:59], a[32:35]
ds_read_b64_tr_b16 a[244:245], v19 offset:9216
s_lshl_b32 s97, s97, 3
v_mfma_f32_16x16x32_bf16 a[36:39], a[248:251], v[88:91], a[36:39]
ds_read_b64_tr_b16 a[246:247], v19 offset:13312
s_add_u32 s97, s97, 24576
v_mfma_f32_16x16x32_bf16 a[40:43], a[248:251], v[120:123], a[40:43]
buffer_load_dwordx4 a[184:187], v238, s[92:95], s96 offen sc1
v_mfma_f32_16x16x32_bf16 a[44:47], a[248:251], v[152:155], a[44:47]
s_waitcnt lgkmcnt(4)
v_mov_b32 v26, s97
v_mov_b32 v28, s44
v_mfma_f32_16x16x32_bf16 a[60:63], a[252:255], v[152:155], a[60:63]
ds_read_b64_tr_b16 a[248:249], v20 offset:9216
v_mov_b32 v29, s45
v_mfma_f32_16x16x32_bf16 a[56:59], a[252:255], v[120:123], a[56:59]
ds_read_b64_tr_b16 a[250:251], v20 offset:13312
v_mfma_f32_16x16x32_bf16 a[52:55], a[252:255], v[88:91], a[52:55]
buffer_load_dwordx4 a[188:191], v239, s[92:95], s96 offen sc1
v_mfma_f32_16x16x32_bf16 a[48:51], a[252:255], v[56:59], a[48:51]
s_waitcnt lgkmcnt(4)
s_add_u32 s96, s0, 2
s_and_b32 s96, s96, 7
v_mfma_f32_16x16x32_bf16 a[64:67], a[240:243], v[56:59], a[64:67]
ds_read_b64_tr_b16 a[252:253], v21 offset:9216
s_mul_i32 s96, s96, 8192
v_mfma_f32_16x16x32_bf16 a[68:71], a[240:243], v[88:91], a[68:71]
ds_read_b64_tr_b16 a[254:255], v21 offset:13312
s_add_u32 s96, s96, s3
v_mfma_f32_16x16x32_bf16 a[72:75], a[240:243], v[120:123], a[72:75]
buffer_load_dwordx4 v13, s[76:79], s54 offen lds
v_mfma_f32_16x16x32_bf16 a[76:79], a[240:243], v[152:155], a[76:79]
s_waitcnt lgkmcnt(4)
s_add_u32 m0, s70, 66432
s_add_u32 s96, s96, 65536
v_mfma_f32_16x16x32_bf16 a[92:95], a[244:247], v[152:155], a[92:95]
ds_read_b64_tr_b16 a[240:241], v22 offset:0
v_mfma_f32_16x16x32_bf16 a[88:91], a[244:247], v[120:123], a[88:91]
ds_read_b64_tr_b16 a[242:243], v22 offset:4096
v_mfma_f32_16x16x32_bf16 a[84:87], a[244:247], v[88:91], a[84:87]
buffer_load_dwordx4 v13, s[76:79], s54 offen offset:128 lds
v_mfma_f32_16x16x32_bf16 a[80:83], a[244:247], v[56:59], a[80:83]
s_waitcnt lgkmcnt(4)
s_add_u32 s54, s54, 16384
s_add_u32 m0, s70, 67584
v_mfma_f32_16x16x32_bf16 a[96:99], a[248:251], v[56:59], a[96:99]
ds_read_b64_tr_b16 a[244:245], v23 offset:0
v_mfma_f32_16x16x32_bf16 a[100:103], a[248:251], v[88:91], a[100:103]
ds_read_b64_tr_b16 a[246:247], v23 offset:4096
v_mfma_f32_16x16x32_bf16 a[104:107], a[248:251], v[120:123], a[104:107]
buffer_load_dwordx4 v13, s[76:79], s54 offen lds
v_mfma_f32_16x16x32_bf16 a[108:111], a[248:251], v[152:155], a[108:111]
s_waitcnt lgkmcnt(4)
s_add_u32 m0, s70, 68480
v_mfma_f32_16x16x32_bf16 a[124:127], a[252:255], v[152:155], a[124:127]
ds_read_b64_tr_b16 a[248:249], v24 offset:0
v_mfma_f32_16x16x32_bf16 a[120:123], a[252:255], v[120:123], a[120:123]
ds_read_b64_tr_b16 a[250:251], v24 offset:4096
v_mfma_f32_16x16x32_bf16 a[116:119], a[252:255], v[88:91], a[116:119]
buffer_load_dwordx4 v13, s[76:79], s54 offen offset:128 lds
v_mfma_f32_16x16x32_bf16 a[112:115], a[252:255], v[56:59], a[112:115]
s_waitcnt lgkmcnt(4)
s_waitcnt vmcnt(35)
v_mfma_f32_16x16x32_bf16 a[0:3], a[240:243], a[128:131], a[0:3]
ds_read_b64_tr_b16 a[252:253], v25 offset:0
v_mfma_f32_16x16x32_bf16 a[4:7], a[240:243], a[132:135], a[4:7]
ds_read_b64_tr_b16 a[254:255], v25 offset:4096
v_mfma_f32_16x16x32_bf16 a[8:11], a[240:243], a[136:139], a[8:11]
v_mfma_f32_16x16x32_bf16 a[12:15], a[240:243], a[140:143], a[12:15]
s_waitcnt lgkmcnt(4)
v_mfma_f32_16x16x32_bf16 a[28:31], a[244:247], a[140:143], a[28:31]
ds_read_b64_tr_b16 a[240:241], v22 offset:1024
s_waitcnt vmcnt(27)
v_mfma_f32_16x16x32_bf16 a[24:27], a[244:247], a[136:139], a[24:27]
ds_read_b64_tr_b16 a[242:243], v22 offset:5120
v_mfma_f32_16x16x32_bf16 a[20:23], a[244:247], a[132:135], a[20:23]
buffer_load_dwordx4 v[56:59], v236, s[92:95], s96 offen sc1
v_mfma_f32_16x16x32_bf16 a[16:19], a[244:247], a[128:131], a[16:19]
s_waitcnt lgkmcnt(4)
v_mfma_f32_16x16x32_bf16 a[32:35], a[248:251], a[128:131], a[32:35]
ds_read_b64_tr_b16 a[244:245], v23 offset:1024
v_mfma_f32_16x16x32_bf16 a[36:39], a[248:251], a[132:135], a[36:39]
ds_read_b64_tr_b16 a[246:247], v23 offset:5120
v_mfma_f32_16x16x32_bf16 a[40:43], a[248:251], a[136:139], a[40:43]
global_store_dwordx2 v26, v[28:29], s[22:23] sc1
v_mfma_f32_16x16x32_bf16 a[44:47], a[248:251], a[140:143], a[44:47]
s_waitcnt lgkmcnt(4)
v_mfma_f32_16x16x32_bf16 a[60:63], a[252:255], a[140:143], a[60:63]
ds_read_b64_tr_b16 a[248:249], v24 offset:1024
v_mfma_f32_16x16x32_bf16 a[56:59], a[252:255], a[136:139], a[56:59]
ds_read_b64_tr_b16 a[250:251], v24 offset:5120
v_add_u32 v18, 16384, v14
v_mfma_f32_16x16x32_bf16 a[52:55], a[252:255], a[132:135], a[52:55]
buffer_load_dwordx4 v[88:91], v237, s[92:95], s96 offen sc1
v_mfma_f32_16x16x32_bf16 a[48:51], a[252:255], a[128:131], a[48:51]
s_waitcnt lgkmcnt(4)
v_add_u32 v19, 16384, v15
v_add_u32 v20, 16384, v16
v_mfma_f32_16x16x32_bf16 a[64:67], a[240:243], a[128:131], a[64:67]
ds_read_b64_tr_b16 a[252:253], v25 offset:1024
v_add_u32 v21, 16384, v17
v_mfma_f32_16x16x32_bf16 a[68:71], a[240:243], a[132:135], a[68:71]
ds_read_b64_tr_b16 a[254:255], v25 offset:5120
v_mfma_f32_16x16x32_bf16 a[72:75], a[240:243], a[136:139], a[72:75]
v_mfma_f32_16x16x32_bf16 a[76:79], a[240:243], a[140:143], a[76:79]
s_waitcnt lgkmcnt(4)
s_add_u32 s54, s0, 4
s_and_b32 s54, s54, 7
v_mfma_f32_16x16x32_bf16 a[92:95], a[244:247], a[140:143], a[92:95]
ds_read_b64_tr_b16 a[240:241], v22 offset:8192
s_mul_i32 s54, s54, 131072
v_mfma_f32_16x16x32_bf16 a[88:91], a[244:247], a[136:139], a[88:91]
ds_read_b64_tr_b16 a[242:243], v22 offset:12288
s_add_u32 s54, s54, s80
v_mfma_f32_16x16x32_bf16 a[84:87], a[244:247], a[132:135], a[84:87]
buffer_load_dwordx4 v[120:123], v238, s[92:95], s96 offen sc1
v_mfma_f32_16x16x32_bf16 a[80:83], a[244:247], a[128:131], a[80:83]
s_waitcnt lgkmcnt(4)
s_add_u32 s54, s54, 1048576
s_add_u32 m0, s70, 81920
v_mfma_f32_16x16x32_bf16 a[96:99], a[248:251], a[128:131], a[96:99]
ds_read_b64_tr_b16 a[244:245], v23 offset:8192
v_mfma_f32_16x16x32_bf16 a[100:103], a[248:251], a[132:135], a[100:103]
ds_read_b64_tr_b16 a[246:247], v23 offset:12288
v_mfma_f32_16x16x32_bf16 a[104:107], a[248:251], a[136:139], a[104:107]
v_mfma_f32_16x16x32_bf16 a[108:111], a[248:251], a[140:143], a[108:111]
s_waitcnt lgkmcnt(4)
v_mfma_f32_16x16x32_bf16 a[124:127], a[252:255], a[140:143], a[124:127]
ds_read_b64_tr_b16 a[248:249], v24 offset:8192
v_mfma_f32_16x16x32_bf16 a[120:123], a[252:255], a[136:139], a[120:123]
ds_read_b64_tr_b16 a[250:251], v24 offset:12288
v_mfma_f32_16x16x32_bf16 a[116:119], a[252:255], a[132:135], a[116:119]
buffer_load_dwordx4 v[152:155], v239, s[92:95], s96 offen sc1
v_mfma_f32_16x16x32_bf16 a[112:115], a[252:255], a[128:131], a[112:115]
s_waitcnt lgkmcnt(4)
s_add_u32 s96, s96, 4096
v_mfma_f32_16x16x32_bf16 a[0:3], a[240:243], v[60:63], a[0:3]
ds_read_b64_tr_b16 a[252:253], v25 offset:8192
v_mfma_f32_16x16x32_bf16 a[4:7], a[240:243], v[92:95], a[4:7]
ds_read_b64_tr_b16 a[254:255], v25 offset:12288
v_mfma_f32_16x16x32_bf16 a[8:11], a[240:243], v[124:127], a[8:11]
buffer_load_dwordx4 a[128:131], v236, s[92:95], s96 offen sc1
v_mfma_f32_16x16x32_bf16 a[12:15], a[240:243], v[156:159], a[12:15]
s_barrier
s_waitcnt lgkmcnt(4)
v_mfma_f32_16x16x32_bf16 a[28:31], a[244:247], v[156:159], a[28:31]
ds_read_b64_tr_b16 a[240:241], v22 offset:9216
v_mfma_f32_16x16x32_bf16 a[24:27], a[244:247], v[124:127], a[24:27]
ds_read_b64_tr_b16 a[242:243], v22 offset:13312
v_mfma_f32_16x16x32_bf16 a[20:23], a[244:247], v[92:95], a[20:23]
buffer_load_dwordx4 a[132:135], v237, s[92:95], s96 offen sc1
v_mfma_f32_16x16x32_bf16 a[16:19], a[244:247], v[60:63], a[16:19]
s_waitcnt lgkmcnt(4)
v_mfma_f32_16x16x32_bf16 a[32:35], a[248:251], v[60:63], a[32:35]
ds_read_b64_tr_b16 a[244:245], v23 offset:9216
v_mfma_f32_16x16x32_bf16 a[36:39], a[248:251], v[92:95], a[36:39]
ds_read_b64_tr_b16 a[246:247], v23 offset:13312
v_mfma_f32_16x16x32_bf16 a[40:43], a[248:251], v[124:127], a[40:43]
buffer_load_dwordx4 a[136:139], v238, s[92:95], s96 offen sc1
v_mfma_f32_16x16x32_bf16 a[44:47], a[248:251], v[156:159], a[44:47]
s_waitcnt lgkmcnt(4)
v_mfma_f32_16x16x32_bf16 a[60:63], a[252:255], v[156:159], a[60:63]
ds_read_b64_tr_b16 a[248:249], v24 offset:9216
v_mfma_f32_16x16x32_bf16 a[56:59], a[252:255], v[124:127], a[56:59]
ds_read_b64_tr_b16 a[250:251], v24 offset:13312
v_mfma_f32_16x16x32_bf16 a[52:55], a[252:255], v[92:95], a[52:55]
buffer_load_dwordx4 a[140:143], v239, s[92:95], s96 offen sc1
v_mfma_f32_16x16x32_bf16 a[48:51], a[252:255], v[60:63], a[48:51]
s_waitcnt lgkmcnt(4)
s_add_u32 s96, s0, 3
s_and_b32 s96, s96, 7
v_mfma_f32_16x16x32_bf16 a[64:67], a[240:243], v[60:63], a[64:67]
ds_read_b64_tr_b16 a[252:253], v25 offset:9216
s_mul_i32 s96, s96, 8192
v_mfma_f32_16x16x32_bf16 a[68:71], a[240:243], v[92:95], a[68:71]
ds_read_b64_tr_b16 a[254:255], v25 offset:13312
s_add_u32 s96, s96, s3
v_mfma_f32_16x16x32_bf16 a[72:75], a[240:243], v[124:127], a[72:75]
buffer_load_dwordx4 v13, s[76:79], s54 offen lds
v_mfma_f32_16x16x32_bf16 a[76:79], a[240:243], v[156:159], a[76:79]
s_waitcnt lgkmcnt(4)
s_add_u32 m0, s70, 82816
s_add_u32 s96, s96, 65536
v_mfma_f32_16x16x32_bf16 a[92:95], a[244:247], v[156:159], a[92:95]
ds_read_b64_tr_b16 a[240:241], v18 offset:0
v_mfma_f32_16x16x32_bf16 a[88:91], a[244:247], v[124:127], a[88:91]
ds_read_b64_tr_b16 a[242:243], v18 offset:4096
v_mfma_f32_16x16x32_bf16 a[84:87], a[244:247], v[92:95], a[84:87]
buffer_load_dwordx4 v13, s[76:79], s54 offen offset:128 lds
v_mfma_f32_16x16x32_bf16 a[80:83], a[244:247], v[60:63], a[80:83]
s_waitcnt lgkmcnt(4)
s_add_u32 s54, s54, 16384
s_add_u32 m0, s70, 83968
v_mfma_f32_16x16x32_bf16 a[96:99], a[248:251], v[60:63], a[96:99]
ds_read_b64_tr_b16 a[244:245], v19 offset:0
v_mfma_f32_16x16x32_bf16 a[100:103], a[248:251], v[92:95], a[100:103]
ds_read_b64_tr_b16 a[246:247], v19 offset:4096
v_mfma_f32_16x16x32_bf16 a[104:107], a[248:251], v[124:127], a[104:107]
buffer_load_dwordx4 v13, s[76:79], s54 offen lds
v_mfma_f32_16x16x32_bf16 a[108:111], a[248:251], v[156:159], a[108:111]
s_waitcnt lgkmcnt(4)
s_add_u32 m0, s70, 84864
v_mfma_f32_16x16x32_bf16 a[124:127], a[252:255], v[156:159], a[124:127]
ds_read_b64_tr_b16 a[248:249], v20 offset:0
v_mfma_f32_16x16x32_bf16 a[120:123], a[252:255], v[124:127], a[120:123]
ds_read_b64_tr_b16 a[250:251], v20 offset:4096
v_mfma_f32_16x16x32_bf16 a[116:119], a[252:255], v[92:95], a[116:119]
buffer_load_dwordx4 v13, s[76:79], s54 offen offset:128 lds
v_mfma_f32_16x16x32_bf16 a[112:115], a[252:255], v[60:63], a[112:115]
s_waitcnt lgkmcnt(4)
s_waitcnt vmcnt(34)
v_mfma_f32_16x16x32_bf16 a[0:3], a[240:243], a[144:147], a[0:3]
ds_read_b64_tr_b16 a[252:253], v21 offset:0
v_mfma_f32_16x16x32_bf16 a[4:7], a[240:243], a[148:151], a[4:7]
ds_read_b64_tr_b16 a[254:255], v21 offset:4096
v_mfma_f32_16x16x32_bf16 a[8:11], a[240:243], a[152:155], a[8:11]
v_mfma_f32_16x16x32_bf16 a[12:15], a[240:243], a[156:159], a[12:15]
s_waitcnt lgkmcnt(4)
v_mfma_f32_16x16x32_bf16 a[28:31], a[244:247], a[156:159], a[28:31]
ds_read_b64_tr_b16 a[240:241], v18 offset:1024
v_mfma_f32_16x16x32_bf16 a[24:27], a[244:247], a[152:155], a[24:27]
ds_read_b64_tr_b16 a[242:243], v18 offset:5120
v_mfma_f32_16x16x32_bf16 a[20:23], a[244:247], a[148:151], a[20:23]
buffer_load_dwordx4 v[60:63], v236, s[92:95], s96 offen sc1
v_mfma_f32_16x16x32_bf16 a[16:19], a[244:247], a[144:147], a[16:19]
s_waitcnt lgkmcnt(4)
v_mfma_f32_16x16x32_bf16 a[32:35], a[248:251], a[144:147], a[32:35]
ds_read_b64_tr_b16 a[244:245], v19 offset:1024
v_and_b32 v26, 3, v1
v_mfma_f32_16x16x32_bf16 a[36:39], a[248:251], a[148:151], a[36:39]
ds_read_b64_tr_b16 a[246:247], v19 offset:5120
v_min_u32 v26, 2, v26
v_mfma_f32_16x16x32_bf16 a[40:43], a[248:251], a[152:155], a[40:43]
v_sub_u32 v26, s59, v26
v_add_u32 v26, 3, v26
v_mfma_f32_16x16x32_bf16 a[44:47], a[248:251], a[156:159], a[44:47]
s_waitcnt lgkmcnt(4)
v_and_b32 v26, 3, v26
v_lshlrev_b32 v26, 8, v26
v_mfma_f32_16x16x32_bf16 a[60:63], a[252:255], a[156:159], a[60:63]
ds_read_b64_tr_b16 a[248:249], v20 offset:1024
s_lshl_b32 s97, s41, 5
v_mfma_f32_16x16x32_bf16 a[56:59], a[252:255], a[152:155], a[56:59]
ds_read_b64_tr_b16 a[250:251], v20 offset:5120
v_add_u32 v22, 32768, v14
v_mfma_f32_16x16x32_bf16 a[52:55], a[252:255], a[148:151], a[52:55]
buffer_load_dwordx4 v[92:95], v237, s[92:95], s96 offen sc1
v_mfma_f32_16x16x32_bf16 a[48:51], a[252:255], a[144:147], a[48:51]
s_waitcnt lgkmcnt(4)
v_add_u32 v23, 32768, v15
v_add_u32 v24, 32768, v16
v_mfma_f32_16x16x32_bf16 a[64:67], a[240:243], a[144:147], a[64:67]
ds_read_b64_tr_b16 a[252:253], v21 offset:1024
s_add_u32 s97, s97, s60
v_mfma_f32_16x16x32_bf16 a[68:71], a[240:243], a[148:151], a[68:71]
ds_read_b64_tr_b16 a[254:255], v21 offset:5120
s_lshl_b32 s97, s97, 2
v_mfma_f32_16x16x32_bf16 a[72:75], a[240:243], a[152:155], a[72:75]
s_add_u32 s97, s97, s43
s_lshl_b32 s97, s97, 3
s_add_u32 s97, s97, 24576
v_mfma_f32_16x16x32_bf16 a[76:79], a[240:243], a[156:159], a[76:79]
s_waitcnt lgkmcnt(4)
s_add_u32 s54, s0, 5
s_and_b32 s54, s54, 7
v_mfma_f32_16x16x32_bf16 a[92:95], a[244:247], a[156:159], a[92:95]
ds_read_b64_tr_b16 a[240:241], v18 offset:8192
s_mul_i32 s54, s54, 131072
v_mfma_f32_16x16x32_bf16 a[88:91], a[244:247], a[152:155], a[88:91]
ds_read_b64_tr_b16 a[242:243], v18 offset:12288
s_add_u32 s54, s54, s80
v_mfma_f32_16x16x32_bf16 a[84:87], a[244:247], a[148:151], a[84:87]
buffer_load_dwordx4 v[124:127], v238, s[92:95], s96 offen sc1
v_mfma_f32_16x16x32_bf16 a[80:83], a[244:247], a[144:147], a[80:83]
s_waitcnt lgkmcnt(4)
s_add_u32 s54, s54, 1048576
s_add_u32 m0, s70, 98304
v_mfma_f32_16x16x32_bf16 a[96:99], a[248:251], a[144:147], a[96:99]
ds_read_b64_tr_b16 a[244:245], v19 offset:8192
v_add_u32 v26, s97, v26
v_mfma_f32_16x16x32_bf16 a[100:103], a[248:251], a[148:151], a[100:103]
ds_read_b64_tr_b16 a[246:247], v19 offset:12288
v_add_u32 v25, 32768, v17
v_mfma_f32_16x16x32_bf16 a[104:107], a[248:251], a[152:155], a[104:107]
s_lshl_b32 s97, s41, 5
s_add_u32 s87, s87, s97
s_add_u32 s87, s87, s60
v_mfma_f32_16x16x32_bf16 a[108:111], a[248:251], a[156:159], a[108:111]
s_waitcnt lgkmcnt(4)
s_mul_i32 s87, s87, 49152
s_lshl_b32 s97, s43, 12
v_mfma_f32_16x16x32_bf16 a[124:127], a[252:255], a[156:159], a[124:127]
ds_read_b64_tr_b16 a[248:249], v20 offset:8192
s_add_u32 s87, s87, s97
v_mfma_f32_16x16x32_bf16 a[120:123], a[252:255], a[152:155], a[120:123]
ds_read_b64_tr_b16 a[250:251], v20 offset:12288
s_add_u32 s87, s87, 0
v_mfma_f32_16x16x32_bf16 a[116:119], a[252:255], a[148:151], a[116:119]
buffer_load_dwordx4 v[156:159], v239, s[92:95], s96 offen sc1
v_mfma_f32_16x16x32_bf16 a[112:115], a[252:255], a[144:147], a[112:115]
s_waitcnt lgkmcnt(4)
s_waitcnt vmcnt(34)
s_add_u32 s96, s96, 4096
v_mfma_f32_16x16x32_bf16 a[0:3], a[240:243], a[160:163], a[0:3]
ds_read_b64_tr_b16 a[252:253], v21 offset:8192
v_mfma_f32_16x16x32_bf16 a[4:7], a[240:243], a[164:167], a[4:7]
ds_read_b64_tr_b16 a[254:255], v21 offset:12288
v_mfma_f32_16x16x32_bf16 a[8:11], a[240:243], a[168:171], a[8:11]
buffer_load_dwordx4 a[144:147], v236, s[92:95], s96 offen sc1
v_mfma_f32_16x16x32_bf16 a[12:15], a[240:243], a[172:175], a[12:15]
s_barrier
s_waitcnt lgkmcnt(4)
v_mfma_f32_16x16x32_bf16 a[28:31], a[244:247], a[172:175], a[28:31]
ds_read_b64_tr_b16 a[240:241], v18 offset:9216
v_mfma_f32_16x16x32_bf16 a[24:27], a[244:247], a[168:171], a[24:27]
ds_read_b64_tr_b16 a[242:243], v18 offset:13312
v_mfma_f32_16x16x32_bf16 a[20:23], a[244:247], a[164:167], a[20:23]
buffer_load_dwordx4 a[148:151], v237, s[92:95], s96 offen sc1
v_mfma_f32_16x16x32_bf16 a[16:19], a[244:247], a[160:163], a[16:19]
s_waitcnt lgkmcnt(4)
v_mfma_f32_16x16x32_bf16 a[32:35], a[248:251], a[160:163], a[32:35]
ds_read_b64_tr_b16 a[244:245], v19 offset:9216
v_mfma_f32_16x16x32_bf16 a[36:39], a[248:251], a[164:167], a[36:39]
ds_read_b64_tr_b16 a[246:247], v19 offset:13312
v_mfma_f32_16x16x32_bf16 a[40:43], a[248:251], a[168:171], a[40:43]
buffer_load_dwordx4 a[152:155], v238, s[92:95], s96 offen sc1
v_mfma_f32_16x16x32_bf16 a[44:47], a[248:251], a[172:175], a[44:47]
s_waitcnt lgkmcnt(4)
v_mfma_f32_16x16x32_bf16 a[60:63], a[252:255], a[172:175], a[60:63]
ds_read_b64_tr_b16 a[248:249], v20 offset:9216
v_mfma_f32_16x16x32_bf16 a[56:59], a[252:255], a[168:171], a[56:59]
ds_read_b64_tr_b16 a[250:251], v20 offset:13312
v_mfma_f32_16x16x32_bf16 a[52:55], a[252:255], a[164:167], a[52:55]
buffer_load_dwordx4 a[156:159], v239, s[92:95], s96 offen sc1
v_mfma_f32_16x16x32_bf16 a[48:51], a[252:255], a[160:163], a[48:51]
s_waitcnt lgkmcnt(4)
s_add_u32 s96, s0, 4
s_and_b32 s96, s96, 7
v_mfma_f32_16x16x32_bf16 a[64:67], a[240:243], a[160:163], a[64:67]
ds_read_b64_tr_b16 a[252:253], v21 offset:9216
s_mul_i32 s96, s96, 8192
v_mfma_f32_16x16x32_bf16 a[68:71], a[240:243], a[164:167], a[68:71]
ds_read_b64_tr_b16 a[254:255], v21 offset:13312
s_add_u32 s96, s96, s3
v_mfma_f32_16x16x32_bf16 a[72:75], a[240:243], a[168:171], a[72:75]
buffer_load_dwordx4 v13, s[76:79], s54 offen lds
v_mfma_f32_16x16x32_bf16 a[76:79], a[240:243], a[172:175], a[76:79]
s_waitcnt lgkmcnt(4)
s_add_u32 m0, s70, 99200
s_add_u32 s96, s96, 65536
v_mfma_f32_16x16x32_bf16 a[92:95], a[244:247], a[172:175], a[92:95]
ds_read_b64_tr_b16 a[240:241], v22 offset:0
v_mfma_f32_16x16x32_bf16 a[88:91], a[244:247], a[168:171], a[88:91]
ds_read_b64_tr_b16 a[242:243], v22 offset:4096
v_mfma_f32_16x16x32_bf16 a[84:87], a[244:247], a[164:167], a[84:87]
buffer_load_dwordx4 v13, s[76:79], s54 offen offset:128 lds
v_mfma_f32_16x16x32_bf16 a[80:83], a[244:247], a[160:163], a[80:83]
s_waitcnt lgkmcnt(4)
s_add_u32 s54, s54, 16384
s_add_u32 m0, s70, 100352
v_mfma_f32_16x16x32_bf16 a[96:99], a[248:251], a[160:163], a[96:99]
ds_read_b64_tr_b16 a[244:245], v23 offset:0
v_mfma_f32_16x16x32_bf16 a[100:103], a[248:251], a[164:167], a[100:103]
ds_read_b64_tr_b16 a[246:247], v23 offset:4096
v_mfma_f32_16x16x32_bf16 a[104:107], a[248:251], a[168:171], a[104:107]
buffer_load_dwordx4 v13, s[76:79], s54 offen lds
v_mfma_f32_16x16x32_bf16 a[108:111], a[248:251], a[172:175], a[108:111]
s_waitcnt lgkmcnt(4)
s_add_u32 m0, s70, 101248
v_mfma_f32_16x16x32_bf16 a[124:127], a[252:255], a[172:175], a[124:127]
ds_read_b64_tr_b16 a[248:249], v24 offset:0
v_mfma_f32_16x16x32_bf16 a[120:123], a[252:255], a[168:171], a[120:123]
ds_read_b64_tr_b16 a[250:251], v24 offset:4096
v_mfma_f32_16x16x32_bf16 a[116:119], a[252:255], a[164:167], a[116:119]
buffer_load_dwordx4 v13, s[76:79], s54 offen offset:128 lds
global_load_dwordx2 v[28:29], v26, s[22:23] sc1
v_mfma_f32_16x16x32_bf16 a[112:115], a[252:255], a[160:163], a[112:115]
s_waitcnt lgkmcnt(4)
s_waitcnt vmcnt(34)
v_mfma_f32_16x16x32_bf16 a[0:3], a[240:243], v[52:55], a[0:3]
ds_read_b64_tr_b16 a[252:253], v25 offset:0
v_mfma_f32_16x16x32_bf16 a[4:7], a[240:243], v[84:87], a[4:7]
ds_read_b64_tr_b16 a[254:255], v25 offset:4096
v_mfma_f32_16x16x32_bf16 a[8:11], a[240:243], v[116:119], a[8:11]
v_mfma_f32_16x16x32_bf16 a[12:15], a[240:243], v[148:151], a[12:15]
s_waitcnt lgkmcnt(4)
v_mfma_f32_16x16x32_bf16 a[28:31], a[244:247], v[148:151], a[28:31]
ds_read_b64_tr_b16 a[240:241], v22 offset:1024
v_mfma_f32_16x16x32_bf16 a[24:27], a[244:247], v[116:119], a[24:27]
ds_read_b64_tr_b16 a[242:243], v22 offset:5120
v_mfma_f32_16x16x32_bf16 a[20:23], a[244:247], v[84:87], a[20:23]
buffer_load_dwordx4 a[160:163], v236, s[92:95], s96 offen sc1
v_mfma_f32_16x16x32_bf16 a[16:19], a[244:247], v[52:55], a[16:19]
s_waitcnt lgkmcnt(4)
v_mfma_f32_16x16x32_bf16 a[32:35], a[248:251], v[52:55], a[32:35]
ds_read_b64_tr_b16 a[244:245], v23 offset:1024
v_mfma_f32_16x16x32_bf16 a[36:39], a[248:251], v[84:87], a[36:39]
ds_read_b64_tr_b16 a[246:247], v23 offset:5120
v_mfma_f32_16x16x32_bf16 a[40:43], a[248:251], v[116:119], a[40:43]
v_mfma_f32_16x16x32_bf16 a[44:47], a[248:251], v[148:151], a[44:47]
s_waitcnt lgkmcnt(4)
v_mfma_f32_16x16x32_bf16 a[60:63], a[252:255], v[148:151], a[60:63]
ds_read_b64_tr_b16 a[248:249], v24 offset:1024
v_mfma_f32_16x16x32_bf16 a[56:59], a[252:255], v[116:119], a[56:59]
ds_read_b64_tr_b16 a[250:251], v24 offset:5120
v_add_u32 v18, 49152, v14
v_mfma_f32_16x16x32_bf16 a[52:55], a[252:255], v[84:87], a[52:55]
buffer_load_dwordx4 a[164:167], v237, s[92:95], s96 offen sc1
v_mfma_f32_16x16x32_bf16 a[48:51], a[252:255], v[52:55], a[48:51]
s_waitcnt lgkmcnt(4)
v_add_u32 v19, 49152, v15
v_add_u32 v20, 49152, v16
v_mfma_f32_16x16x32_bf16 a[64:67], a[240:243], v[52:55], a[64:67]
ds_read_b64_tr_b16 a[252:253], v25 offset:1024
v_add_u32 v21, 49152, v17
v_mfma_f32_16x16x32_bf16 a[68:71], a[240:243], v[84:87], a[68:71]
ds_read_b64_tr_b16 a[254:255], v25 offset:5120
v_mfma_f32_16x16x32_bf16 a[72:75], a[240:243], v[116:119], a[72:75]
v_mfma_f32_16x16x32_bf16 a[76:79], a[240:243], v[148:151], a[76:79]
s_waitcnt lgkmcnt(4)
s_add_u32 s54, s0, 6
s_and_b32 s54, s54, 7
v_mfma_f32_16x16x32_bf16 a[92:95], a[244:247], v[148:151], a[92:95]
ds_read_b64_tr_b16 a[240:241], v22 offset:8192
s_mul_i32 s54, s54, 131072
v_mfma_f32_16x16x32_bf16 a[88:91], a[244:247], v[116:119], a[88:91]
ds_read_b64_tr_b16 a[242:243], v22 offset:12288
s_add_u32 s54, s54, s80
v_mfma_f32_16x16x32_bf16 a[84:87], a[244:247], v[84:87], a[84:87]
buffer_load_dwordx4 a[168:171], v238, s[92:95], s96 offen sc1
v_mfma_f32_16x16x32_bf16 a[80:83], a[244:247], v[52:55], a[80:83]
s_waitcnt lgkmcnt(4)
s_add_u32 s54, s54, 1048576
s_add_u32 m0, s70, 114688
v_mfma_f32_16x16x32_bf16 a[96:99], a[248:251], v[52:55], a[96:99]
ds_read_b64_tr_b16 a[244:245], v23 offset:8192
v_mfma_f32_16x16x32_bf16 a[100:103], a[248:251], v[84:87], a[100:103]
ds_read_b64_tr_b16 a[246:247], v23 offset:12288
v_mfma_f32_16x16x32_bf16 a[104:107], a[248:251], v[116:119], a[104:107]
v_mfma_f32_16x16x32_bf16 a[108:111], a[248:251], v[148:151], a[108:111]
s_waitcnt lgkmcnt(4)
v_mfma_f32_16x16x32_bf16 a[124:127], a[252:255], v[148:151], a[124:127]
ds_read_b64_tr_b16 a[248:249], v24 offset:8192
v_mfma_f32_16x16x32_bf16 a[120:123], a[252:255], v[116:119], a[120:123]
ds_read_b64_tr_b16 a[250:251], v24 offset:12288
v_mfma_f32_16x16x32_bf16 a[116:119], a[252:255], v[84:87], a[116:119]
buffer_load_dwordx4 a[172:175], v239, s[92:95], s96 offen sc1
v_mfma_f32_16x16x32_bf16 a[112:115], a[252:255], v[52:55], a[112:115]
s_waitcnt lgkmcnt(4)
s_waitcnt vmcnt(34)
s_add_u32 s96, s96, 4096
v_mfma_f32_16x16x32_bf16 a[0:3], a[240:243], a[176:179], a[0:3]
ds_read_b64_tr_b16 a[252:253], v25 offset:8192
v_mfma_f32_16x16x32_bf16 a[4:7], a[240:243], a[180:183], a[4:7]
ds_read_b64_tr_b16 a[254:255], v25 offset:12288
v_mfma_f32_16x16x32_bf16 a[8:11], a[240:243], a[184:187], a[8:11]
buffer_load_dwordx4 a[192:195], v236, s[92:95], s96 offen sc1
v_mfma_f32_16x16x32_bf16 a[12:15], a[240:243], a[188:191], a[12:15]
s_barrier
s_waitcnt lgkmcnt(4)
v_mfma_f32_16x16x32_bf16 a[28:31], a[244:247], a[188:191], a[28:31]
ds_read_b64_tr_b16 a[240:241], v22 offset:9216
v_mfma_f32_16x16x32_bf16 a[24:27], a[244:247], a[184:187], a[24:27]
ds_read_b64_tr_b16 a[242:243], v22 offset:13312
v_mfma_f32_16x16x32_bf16 a[20:23], a[244:247], a[180:183], a[20:23]
buffer_load_dwordx4 a[196:199], v237, s[92:95], s96 offen sc1
v_mfma_f32_16x16x32_bf16 a[16:19], a[244:247], a[176:179], a[16:19]
s_waitcnt lgkmcnt(4)
v_mfma_f32_16x16x32_bf16 a[32:35], a[248:251], a[176:179], a[32:35]
ds_read_b64_tr_b16 a[244:245], v23 offset:9216
v_mfma_f32_16x16x32_bf16 a[36:39], a[248:251], a[180:183], a[36:39]
ds_read_b64_tr_b16 a[246:247], v23 offset:13312
v_mfma_f32_16x16x32_bf16 a[40:43], a[248:251], a[184:187], a[40:43]
buffer_load_dwordx4 a[200:203], v238, s[92:95], s96 offen sc1
v_mfma_f32_16x16x32_bf16 a[44:47], a[248:251], a[188:191], a[44:47]
s_waitcnt lgkmcnt(4)
v_mfma_f32_16x16x32_bf16 a[60:63], a[252:255], a[188:191], a[60:63]
ds_read_b64_tr_b16 a[248:249], v24 offset:9216
v_mfma_f32_16x16x32_bf16 a[56:59], a[252:255], a[184:187], a[56:59]
ds_read_b64_tr_b16 a[250:251], v24 offset:13312
v_mfma_f32_16x16x32_bf16 a[52:55], a[252:255], a[180:183], a[52:55]
buffer_load_dwordx4 a[204:207], v239, s[92:95], s96 offen sc1
v_mfma_f32_16x16x32_bf16 a[48:51], a[252:255], a[176:179], a[48:51]
s_waitcnt lgkmcnt(4)
s_add_u32 s96, s0, 5
s_and_b32 s96, s96, 7
v_mfma_f32_16x16x32_bf16 a[64:67], a[240:243], a[176:179], a[64:67]
ds_read_b64_tr_b16 a[252:253], v25 offset:9216
s_mul_i32 s96, s96, 8192
v_mfma_f32_16x16x32_bf16 a[68:71], a[240:243], a[180:183], a[68:71]
ds_read_b64_tr_b16 a[254:255], v25 offset:13312
s_add_u32 s96, s96, s3
v_mfma_f32_16x16x32_bf16 a[72:75], a[240:243], a[184:187], a[72:75]
buffer_load_dwordx4 v13, s[76:79], s54 offen lds
v_mfma_f32_16x16x32_bf16 a[76:79], a[240:243], a[188:191], a[76:79]
s_waitcnt lgkmcnt(4)
s_add_u32 m0, s70, 115584
s_add_u32 s96, s96, 65536
v_mfma_f32_16x16x32_bf16 a[92:95], a[244:247], a[188:191], a[92:95]
ds_read_b64_tr_b16 a[240:241], v18 offset:0
s_waitcnt vmcnt(9)
v_mfma_f32_16x16x32_bf16 a[88:91], a[244:247], a[184:187], a[88:91]
ds_read_b64_tr_b16 a[242:243], v18 offset:4096
v_mfma_f32_16x16x32_bf16 a[84:87], a[244:247], a[180:183], a[84:87]
buffer_load_dwordx4 v13, s[76:79], s54 offen offset:128 lds
v_mfma_f32_16x16x32_bf16 a[80:83], a[244:247], a[176:179], a[80:83]
s_waitcnt lgkmcnt(4)
s_add_u32 s54, s54, 16384
s_add_u32 m0, s70, 116736
v_mfma_f32_16x16x32_bf16 a[96:99], a[248:251], a[176:179], a[96:99]
ds_read_b64_tr_b16 a[244:245], v19 offset:0
v_mfma_f32_16x16x32_bf16 a[100:103], a[248:251], a[180:183], a[100:103]
ds_read_b64_tr_b16 a[246:247], v19 offset:4096
v_mfma_f32_16x16x32_bf16 a[104:107], a[248:251], a[184:187], a[104:107]
buffer_load_dwordx4 v13, s[76:79], s54 offen lds
v_mfma_f32_16x16x32_bf16 a[108:111], a[248:251], a[188:191], a[108:111]
s_waitcnt lgkmcnt(4)
s_add_u32 m0, s70, 117632
v_mfma_f32_16x16x32_bf16 a[124:127], a[252:255], a[188:191], a[124:127]
v_cmp_eq_u64 vcc, s[44:45], v[28:29]
s_nop 4
s_cmp_eq_u64 vcc, -1
s_cbranch_scc0 DP_20_s
DP_20_b:
v_mfma_f32_16x16x32_bf16 a[120:123], a[252:255], a[184:187], a[120:123]
ds_read_b64_tr_b16 a[248:249], v20 offset:0
v_mfma_f32_16x16x32_bf16 a[116:119], a[252:255], a[180:183], a[116:119]
buffer_load_dwordx4 v13, s[76:79], s54 offen offset:128 lds
ds_read_b64_tr_b16 a[250:251], v20 offset:4096
v_mfma_f32_16x16x32_bf16 a[112:115], a[252:255], a[176:179], a[112:115]
s_waitcnt lgkmcnt(4)
v_mfma_f32_16x16x32_bf16 a[0:3], a[240:243], v[56:59], a[0:3]
ds_read_b64_tr_b16 a[252:253], v21 offset:0
s_mov_b32 s36, s4
v_mfma_f32_16x16x32_bf16 a[4:7], a[240:243], v[88:91], a[4:7]
ds_read_b64_tr_b16 a[254:255], v21 offset:4096
s_mov_b32 s37, s5
v_mfma_f32_16x16x32_bf16 a[8:11], a[240:243], v[120:123], a[8:11]
buffer_load_dwordx4 v[36:39], v2, s[24:27], s87 offen offset:0 sc1
v_mfma_f32_16x16x32_bf16 a[12:15], a[240:243], v[152:155], a[12:15]
s_waitcnt lgkmcnt(4)
s_and_b32 s37, s37, 0xffff
s_mov_b32 s38, 4194304
v_mfma_f32_16x16x32_bf16 a[28:31], a[244:247], v[152:155], a[28:31]
ds_read_b64_tr_b16 a[240:241], v18 offset:1024
s_mov_b32 s39, 0x20000
v_mfma_f32_16x16x32_bf16 a[24:27], a[244:247], v[120:123], a[24:27]
ds_read_b64_tr_b16 a[242:243], v18 offset:5120
s_lshl_b32 s82, s41, 2
v_mfma_f32_16x16x32_bf16 a[20:23], a[244:247], v[88:91], a[20:23]
buffer_load_dwordx4 a[176:179], v236, s[92:95], s96 offen sc1
v_mfma_f32_16x16x32_bf16 a[16:19], a[244:247], v[56:59], a[16:19]
s_waitcnt lgkmcnt(4)
s_add_u32 s82, s82, s43
s_lshl_b32 s82, s82, 2
v_mfma_f32_16x16x32_bf16 a[32:35], a[248:251], v[56:59], a[32:35]
ds_read_b64_tr_b16 a[244:245], v19 offset:1024
s_add_u32 s82, s82, s59
v_mfma_f32_16x16x32_bf16 a[36:39], a[248:251], v[88:91], a[36:39]
ds_read_b64_tr_b16 a[246:247], v19 offset:5120
s_lshl_b32 s82, s82, 15
v_mfma_f32_16x16x32_bf16 a[40:43], a[248:251], v[120:123], a[40:43]
buffer_load_dwordx4 v[68:71], v2, s[24:27], s87 offen offset:1024 sc1
v_mfma_f32_16x16x32_bf16 a[44:47], a[248:251], v[152:155], a[44:47]
s_waitcnt lgkmcnt(4)
v_mfma_f32_16x16x32_bf16 a[60:63], a[252:255], v[152:155], a[60:63]
ds_read_b64_tr_b16 a[248:249], v20 offset:1024
v_mfma_f32_16x16x32_bf16 a[56:59], a[252:255], v[120:123], a[56:59]
ds_read_b64_tr_b16 a[250:251], v20 offset:5120
v_add_u32 v22, 65536, v14
v_mfma_f32_16x16x32_bf16 a[52:55], a[252:255], v[88:91], a[52:55]
buffer_load_dwordx4 a[180:183], v237, s[92:95], s96 offen sc1
v_mfma_f32_16x16x32_bf16 a[48:51], a[252:255], v[56:59], a[48:51]
s_waitcnt lgkmcnt(4)
v_add_u32 v23, 65536, v15
v_add_u32 v24, 65536, v16
v_mfma_f32_16x16x32_bf16 a[64:67], a[240:243], v[56:59], a[64:67]
ds_read_b64_tr_b16 a[252:253], v21 offset:1024
v_add_u32 v25, 65536, v17
v_mfma_f32_16x16x32_bf16 a[68:71], a[240:243], v[88:91], a[68:71]
ds_read_b64_tr_b16 a[254:255], v21 offset:5120
v_mfma_f32_16x16x32_bf16 a[72:75], a[240:243], v[120:123], a[72:75]
buffer_load_dwordx4 v[100:103], v2, s[24:27], s87 offen offset:2048 sc1
v_mfma_f32_16x16x32_bf16 a[76:79], a[240:243], v[152:155], a[76:79]
s_waitcnt lgkmcnt(4)
s_add_u32 s54, s0, 7
s_and_b32 s54, s54, 7
v_mfma_f32_16x16x32_bf16 a[92:95], a[244:247], v[152:155], a[92:95]
ds_read_b64_tr_b16 a[240:241], v18 offset:8192
s_mul_i32 s54, s54, 131072
v_mfma_f32_16x16x32_bf16 a[88:91], a[244:247], v[120:123], a[88:91]
ds_read_b64_tr_b16 a[242:243], v18 offset:12288
s_add_u32 s54, s54, s80
v_mfma_f32_16x16x32_bf16 a[84:87], a[244:247], v[88:91], a[84:87]
buffer_load_dwordx4 a[184:187], v238, s[92:95], s96 offen sc1
v_mfma_f32_16x16x32_bf16 a[80:83], a[244:247], v[56:59], a[80:83]
s_waitcnt lgkmcnt(4)
s_add_u32 s54, s54, 1048576
s_add_u32 m0, s70, 131072
v_mfma_f32_16x16x32_bf16 a[96:99], a[248:251], v[56:59], a[96:99]
ds_read_b64_tr_b16 a[244:245], v19 offset:8192
v_mfma_f32_16x16x32_bf16 a[100:103], a[248:251], v[88:91], a[100:103]
ds_read_b64_tr_b16 a[246:247], v19 offset:12288
v_mfma_f32_16x16x32_bf16 a[104:107], a[248:251], v[120:123], a[104:107]
buffer_load_dwordx4 v[132:135], v2, s[24:27], s87 offen offset:3072 sc1
v_mfma_f32_16x16x32_bf16 a[108:111], a[248:251], v[152:155], a[108:111]
s_waitcnt lgkmcnt(4)
s_sub_u32 s87, s59, 2
s_and_b32 s87, s87, 3
v_mfma_f32_16x16x32_bf16 a[124:127], a[252:255], v[152:155], a[124:127]
ds_read_b64_tr_b16 a[248:249], v20 offset:8192
s_lshl_b32 s87, s87, 3
v_mfma_f32_16x16x32_bf16 a[120:123], a[252:255], v[120:123], a[120:123]
ds_read_b64_tr_b16 a[250:251], v20 offset:12288
s_lshl_b32 s97, s41, 5
v_mfma_f32_16x16x32_bf16 a[116:119], a[252:255], v[88:91], a[116:119]
buffer_load_dwordx4 a[188:191], v239, s[92:95], s96 offen sc1
v_mfma_f32_16x16x32_bf16 a[112:115], a[252:255], v[56:59], a[112:115]
s_waitcnt lgkmcnt(4)
s_add_u32 s96, s96, 4096
v_mfma_f32_16x16x32_bf16 a[0:3], a[240:243], a[128:131], a[0:3]
ds_read_b64_tr_b16 a[252:253], v21 offset:8192
s_add_u32 s87, s87, s97
v_mfma_f32_16x16x32_bf16 a[4:7], a[240:243], a[132:135], a[4:7]
ds_read_b64_tr_b16 a[254:255], v21 offset:12288
s_add_u32 s87, s87, s60
v_mfma_f32_16x16x32_bf16 a[8:11], a[240:243], a[136:139], a[8:11]
buffer_load_dwordx4 a[208:211], v236, s[92:95], s96 offen sc1
v_mfma_f32_16x16x32_bf16 a[12:15], a[240:243], a[140:143], a[12:15]
s_barrier
s_waitcnt lgkmcnt(4)
v_mfma_f32_16x16x32_bf16 a[28:31], a[244:247], a[140:143], a[28:31]
ds_read_b64_tr_b16 a[240:241], v18 offset:9216
s_mul_i32 s87, s87, 49152
v_mfma_f32_16x16x32_bf16 a[24:27], a[244:247], a[136:139], a[24:27]
ds_read_b64_tr_b16 a[242:243], v18 offset:13312
s_lshl_b32 s97, s43, 12
v_mfma_f32_16x16x32_bf16 a[20:23], a[244:247], a[132:135], a[20:23]
buffer_load_dwordx4 a[212:215], v237, s[92:95], s96 offen sc1
v_mfma_f32_16x16x32_bf16 a[16:19], a[244:247], a[128:131], a[16:19]
s_waitcnt lgkmcnt(4)
s_add_u32 s87, s87, s97
s_add_u32 s87, s87, 16384
v_mfma_f32_16x16x32_bf16 a[32:35], a[248:251], a[128:131], a[32:35]
ds_read_b64_tr_b16 a[244:245], v19 offset:9216
v_mfma_f32_16x16x32_bf16 a[36:39], a[248:251], a[132:135], a[36:39]
ds_read_b64_tr_b16 a[246:247], v19 offset:13312
v_mfma_f32_16x16x32_bf16 a[40:43], a[248:251], a[136:139], a[40:43]
buffer_load_dwordx4 a[216:219], v238, s[92:95], s96 offen sc1
v_mfma_f32_16x16x32_bf16 a[44:47], a[248:251], a[140:143], a[44:47]
s_waitcnt lgkmcnt(4)
buffer_load_dwordx4 v[40:43], v2, s[24:27], s87 offen offset:0 sc1
v_mfma_f32_16x16x32_bf16 a[60:63], a[252:255], a[140:143], a[60:63]
ds_read_b64_tr_b16 a[248:249], v20 offset:9216
v_mfma_f32_16x16x32_bf16 a[56:59], a[252:255], a[136:139], a[56:59]
ds_read_b64_tr_b16 a[250:251], v20 offset:13312
v_mfma_f32_16x16x32_bf16 a[52:55], a[252:255], a[132:135], a[52:55]
buffer_load_dwordx4 a[220:223], v239, s[92:95], s96 offen sc1
v_mfma_f32_16x16x32_bf16 a[48:51], a[252:255], a[128:131], a[48:51]
s_waitcnt lgkmcnt(4)
s_add_u32 s96, s0, 6
s_and_b32 s96, s96, 7
v_mfma_f32_16x16x32_bf16 a[64:67], a[240:243], a[128:131], a[64:67]
ds_read_b64_tr_b16 a[252:253], v21 offset:9216
s_mul_i32 s96, s96, 8192
v_mfma_f32_16x16x32_bf16 a[68:71], a[240:243], a[132:135], a[68:71]
ds_read_b64_tr_b16 a[254:255], v21 offset:13312
s_add_u32 s96, s96, s3
v_mfma_f32_16x16x32_bf16 a[72:75], a[240:243], a[136:139], a[72:75]
buffer_load_dwordx4 v13, s[76:79], s54 offen lds
v_mfma_f32_16x16x32_bf16 a[76:79], a[240:243], a[140:143], a[76:79]
s_waitcnt lgkmcnt(4)
s_add_u32 m0, s70, 131968
s_add_u32 s96, s96, 65536
v_mfma_f32_16x16x32_bf16 a[92:95], a[244:247], a[140:143], a[92:95]
ds_read_b64_tr_b16 a[240:241], v22 offset:0
v_mfma_f32_16x16x32_bf16 a[88:91], a[244:247], a[136:139], a[88:91]
ds_read_b64_tr_b16 a[242:243], v22 offset:4096
v_mfma_f32_16x16x32_bf16 a[84:87], a[244:247], a[132:135], a[84:87]
buffer_load_dwordx4 v13, s[76:79], s54 offen offset:128 lds
v_mfma_f32_16x16x32_bf16 a[80:83], a[244:247], a[128:131], a[80:83]
s_waitcnt lgkmcnt(4)
s_add_u32 s54, s54, 16384
s_add_u32 m0, s70, 133120
buffer_load_dwordx4 v[72:75], v2, s[24:27], s87 offen offset:1024 sc1
v_mfma_f32_16x16x32_bf16 a[96:99], a[248:251], a[128:131], a[96:99]
ds_read_b64_tr_b16 a[244:245], v23 offset:0
v_mfma_f32_16x16x32_bf16 a[100:103], a[248:251], a[132:135], a[100:103]
ds_read_b64_tr_b16 a[246:247], v23 offset:4096
v_mfma_f32_16x16x32_bf16 a[104:107], a[248:251], a[136:139], a[104:107]
buffer_load_dwordx4 v13, s[76:79], s54 offen lds
v_mfma_f32_16x16x32_bf16 a[108:111], a[248:251], a[140:143], a[108:111]
s_waitcnt lgkmcnt(4)
s_add_u32 m0, s70, 134016
v_mfma_f32_16x16x32_bf16 a[124:127], a[252:255], a[140:143], a[124:127]
ds_read_b64_tr_b16 a[248:249], v24 offset:0
v_mfma_f32_16x16x32_bf16 a[120:123], a[252:255], a[136:139], a[120:123]
ds_read_b64_tr_b16 a[250:251], v24 offset:4096
v_mfma_f32_16x16x32_bf16 a[116:119], a[252:255], a[132:135], a[116:119]
buffer_load_dwordx4 v13, s[76:79], s54 offen offset:128 lds
v_mfma_f32_16x16x32_bf16 a[112:115], a[252:255], a[128:131], a[112:115]
s_waitcnt lgkmcnt(4)
v_mfma_f32_16x16x32_bf16 a[0:3], a[240:243], v[60:63], a[0:3]
ds_read_b64_tr_b16 a[252:253], v25 offset:0
v_mfma_f32_16x16x32_bf16 a[4:7], a[240:243], v[92:95], a[4:7]
ds_read_b64_tr_b16 a[254:255], v25 offset:4096
v_mfma_f32_16x16x32_bf16 a[8:11], a[240:243], v[124:127], a[8:11]
buffer_load_dwordx4 v[104:107], v2, s[24:27], s87 offen offset:2048 sc1
v_mfma_f32_16x16x32_bf16 a[12:15], a[240:243], v[156:159], a[12:15]
s_waitcnt lgkmcnt(4)
v_mfma_f32_16x16x32_bf16 a[28:31], a[244:247], v[156:159], a[28:31]
ds_read_b64_tr_b16 a[240:241], v22 offset:1024
v_mfma_f32_16x16x32_bf16 a[24:27], a[244:247], v[124:127], a[24:27]
ds_read_b64_tr_b16 a[242:243], v22 offset:5120
v_mfma_f32_16x16x32_bf16 a[20:23], a[244:247], v[92:95], a[20:23]
buffer_load_dwordx4 a[128:131], v236, s[92:95], s96 offen sc1
v_mfma_f32_16x16x32_bf16 a[16:19], a[244:247], v[60:63], a[16:19]
s_waitcnt lgkmcnt(4)
v_mfma_f32_16x16x32_bf16 a[32:35], a[248:251], v[60:63], a[32:35]
ds_read_b64_tr_b16 a[244:245], v23 offset:1024
v_mfma_f32_16x16x32_bf16 a[36:39], a[248:251], v[92:95], a[36:39]
ds_read_b64_tr_b16 a[246:247], v23 offset:5120
v_mfma_f32_16x16x32_bf16 a[40:43], a[248:251], v[124:127], a[40:43]
buffer_load_dwordx4 v[136:139], v2, s[24:27], s87 offen offset:3072 sc1
v_mfma_f32_16x16x32_bf16 a[44:47], a[248:251], v[156:159], a[44:47]
s_waitcnt lgkmcnt(4)
s_sub_u32 s87, s59, 3
s_and_b32 s87, s87, 3
v_mfma_f32_16x16x32_bf16 a[60:63], a[252:255], v[156:159], a[60:63]
ds_read_b64_tr_b16 a[248:249], v24 offset:1024
s_lshl_b32 s87, s87, 3
v_mfma_f32_16x16x32_bf16 a[56:59], a[252:255], v[124:127], a[56:59]
ds_read_b64_tr_b16 a[250:251], v24 offset:5120
s_lshl_b32 s97, s41, 5
v_mfma_f32_16x16x32_bf16 a[52:55], a[252:255], v[92:95], a[52:55]
buffer_load_dwordx4 a[132:135], v237, s[92:95], s96 offen sc1
v_mfma_f32_16x16x32_bf16 a[48:51], a[252:255], v[60:63], a[48:51]
s_waitcnt lgkmcnt(4)
s_add_u32 s87, s87, s97
s_add_u32 s87, s87, s60
v_mfma_f32_16x16x32_bf16 a[64:67], a[240:243], v[60:63], a[64:67]
s_mul_i32 s87, s87, 49152
s_lshl_b32 s97, s43, 12
s_add_u32 s87, s87, s97
v_mfma_f32_16x16x32_bf16 a[68:71], a[240:243], v[92:95], a[68:71]
s_add_u32 s87, s87, 32768
ds_read_b64_tr_b16 a[252:253], v25 offset:1024
v_mfma_f32_16x16x32_bf16 a[72:75], a[240:243], v[124:127], a[72:75]
ds_read_b64_tr_b16 a[254:255], v25 offset:5120
buffer_load_dwordx4 v[44:47], v2, s[24:27], s87 offen offset:0 sc1
v_add_u32 v18, 81920, v14
v_mfma_f32_16x16x32_bf16 a[76:79], a[240:243], v[156:159], a[76:79]
s_waitcnt lgkmcnt(4)
v_add_u32 v19, 81920, v15
v_add_u32 v20, 81920, v16
v_mfma_f32_16x16x32_bf16 a[92:95], a[244:247], v[156:159], a[92:95]
ds_read_b64_tr_b16 a[240:241], v22 offset:8192
v_add_u32 v21, 81920, v17
v_mfma_f32_16x16x32_bf16 a[88:91], a[244:247], v[124:127], a[88:91]
ds_read_b64_tr_b16 a[242:243], v22 offset:12288
s_lshl_b32 s97, s60, 8
v_mfma_f32_16x16x32_bf16 a[84:87], a[244:247], v[92:95], a[84:87]
buffer_load_dwordx4 a[136:139], v238, s[92:95], s96 offen sc1
v_mfma_f32_16x16x32_bf16 a[80:83], a[244:247], v[60:63], a[80:83]
s_waitcnt lgkmcnt(4)
s_add_u32 s82, s82, s97
v_and_b32 v208, 15, v1
v_mfma_f32_16x16x32_bf16 a[96:99], a[248:251], v[60:63], a[96:99]
ds_read_b64_tr_b16 a[244:245], v23 offset:8192
v_lshrrev_b32 v209, 4, v1
v_mfma_f32_16x16x32_bf16 a[100:103], a[248:251], v[92:95], a[100:103]
ds_read_b64_tr_b16 a[246:247], v23 offset:12288
v_lshlrev_b32 v3, 11, v208
v_mfma_f32_16x16x32_bf16 a[104:107], a[248:251], v[124:127], a[104:107]
buffer_load_dwordx4 v[76:79], v2, s[24:27], s87 offen offset:1024 sc1
v_mfma_f32_16x16x32_bf16 a[108:111], a[248:251], v[156:159], a[108:111]
s_waitcnt lgkmcnt(4)
v_lshl_add_u32 v3, v209, 3, v3
v_xor_b32 v210, v208, v209
v_mfma_f32_16x16x32_bf16 a[124:127], a[252:255], v[156:159], a[124:127]
ds_read_b64_tr_b16 a[248:249], v24 offset:8192
v_lshlrev_b32 v210, 4, v210
v_mfma_f32_16x16x32_bf16 a[120:123], a[252:255], v[124:127], a[120:123]
ds_read_b64_tr_b16 a[250:251], v24 offset:12288
v_lshl_add_u32 v210, v209, 11, v210
v_mfma_f32_16x16x32_bf16 a[116:119], a[252:255], v[92:95], a[116:119]
buffer_load_dwordx4 a[140:143], v239, s[92:95], s96 offen sc1
v_mfma_f32_16x16x32_bf16 a[112:115], a[252:255], v[60:63], a[112:115]
s_waitcnt lgkmcnt(4)
s_add_u32 s96, s96, 4096
v_mfma_f32_16x16x32_bf16 a[0:3], a[240:243], a[144:147], a[0:3]
ds_read_b64_tr_b16 a[252:253], v25 offset:8192
v_lshrrev_b32 v211, 1, v209
v_mfma_f32_16x16x32_bf16 a[4:7], a[240:243], a[148:151], a[4:7]
ds_read_b64_tr_b16 a[254:255], v25 offset:12288
v_xor_b32 v211, v211, v208
v_mfma_f32_16x16x32_bf16 a[8:11], a[240:243], a[152:155], a[8:11]
buffer_load_dwordx4 v[60:63], v236, s[92:95], s96 offen sc1
v_mfma_f32_16x16x32_bf16 a[12:15], a[240:243], a[156:159], a[12:15]
s_barrier
s_waitcnt lgkmcnt(4)
v_mfma_f32_16x16x32_bf16 a[28:31], a[244:247], a[156:159], a[28:31]
ds_read_b64_tr_b16 a[240:241], v22 offset:9216
v_lshlrev_b32 v211, 4, v211
v_mfma_f32_16x16x32_bf16 a[24:27], a[244:247], a[152:155], a[24:27]
ds_read_b64_tr_b16 a[242:243], v22 offset:13312
v_lshl_add_u32 v211, v208, 8, v211
v_mfma_f32_16x16x32_bf16 a[20:23], a[244:247], a[148:151], a[20:23]
buffer_load_dwordx4 v[92:95], v237, s[92:95], s96 offen sc1
v_mfma_f32_16x16x32_bf16 a[16:19], a[244:247], a[144:147], a[16:19]
s_waitcnt lgkmcnt(4)
v_and_b32 v209, 1, v209
v_lshl_add_u32 v211, v209, 3, v211
v_mfma_f32_16x16x32_bf16 a[32:35], a[248:251], a[144:147], a[32:35]
ds_read_b64_tr_b16 a[244:245], v23 offset:9216
s_add_u32 s97, s70, 147456
v_mfma_f32_16x16x32_bf16 a[36:39], a[248:251], a[148:151], a[36:39]
ds_read_b64_tr_b16 a[246:247], v23 offset:13312
v_add_u32 v211, s97, v211
v_mfma_f32_16x16x32_bf16 a[40:43], a[248:251], a[152:155], a[40:43]
buffer_load_dwordx4 v[124:127], v238, s[92:95], s96 offen sc1
v_mfma_f32_16x16x32_bf16 a[44:47], a[248:251], a[156:159], a[44:47]
s_waitcnt lgkmcnt(4)
v_mfma_f32_16x16x32_bf16 a[60:63], a[252:255], a[156:159], a[60:63]
ds_read_b64_tr_b16 a[248:249], v24 offset:9216
v_mfma_f32_16x16x32_bf16 a[56:59], a[252:255], a[152:155], a[56:59]
ds_read_b64_tr_b16 a[250:251], v24 offset:13312
v_mfma_f32_16x16x32_bf16 a[52:55], a[252:255], a[148:151], a[52:55]
buffer_load_dwordx4 v[156:159], v239, s[92:95], s96 offen sc1
v_mfma_f32_16x16x32_bf16 a[48:51], a[252:255], a[144:147], a[48:51]
s_waitcnt lgkmcnt(4)
s_add_u32 s96, s0, 7
s_and_b32 s96, s96, 7
v_mfma_f32_16x16x32_bf16 a[64:67], a[240:243], a[144:147], a[64:67]
ds_read_b64_tr_b16 a[252:253], v25 offset:9216
s_mul_i32 s96, s96, 8192
v_mfma_f32_16x16x32_bf16 a[68:71], a[240:243], a[148:151], a[68:71]
ds_read_b64_tr_b16 a[254:255], v25 offset:13312
s_add_u32 s96, s96, s3
v_mfma_f32_16x16x32_bf16 a[72:75], a[240:243], a[152:155], a[72:75]
buffer_load_dwordx4 v[108:111], v2, s[24:27], s87 offen offset:2048 sc1
v_mfma_f32_16x16x32_bf16 a[76:79], a[240:243], a[156:159], a[76:79]
s_waitcnt lgkmcnt(4)
s_add_u32 s96, s96, 65536
v_mfma_f32_16x16x32_bf16 a[92:95], a[244:247], a[156:159], a[92:95]
ds_read_b64_tr_b16 a[240:241], v18 offset:0
v_mfma_f32_16x16x32_bf16 a[88:91], a[244:247], a[152:155], a[88:91]
ds_read_b64_tr_b16 a[242:243], v18 offset:4096
v_mfma_f32_16x16x32_bf16 a[84:87], a[244:247], a[148:151], a[84:87]
buffer_load_dwordx4 v[140:143], v2, s[24:27], s87 offen offset:3072 sc1
v_mfma_f32_16x16x32_bf16 a[80:83], a[244:247], a[144:147], a[80:83]
s_waitcnt lgkmcnt(4)
v_mfma_f32_16x16x32_bf16 a[96:99], a[248:251], a[144:147], a[96:99]
ds_read_b64_tr_b16 a[244:245], v19 offset:0
v_mfma_f32_16x16x32_bf16 a[100:103], a[248:251], a[148:151], a[100:103]
ds_read_b64_tr_b16 a[246:247], v19 offset:4096
v_mfma_f32_16x16x32_bf16 a[104:107], a[248:251], a[152:155], a[104:107]
v_mfma_f32_16x16x32_bf16 a[108:111], a[248:251], a[156:159], a[108:111]
s_waitcnt lgkmcnt(4)
v_mfma_f32_16x16x32_bf16 a[124:127], a[252:255], a[156:159], a[124:127]
ds_read_b64_tr_b16 a[248:249], v20 offset:0
v_mfma_f32_16x16x32_bf16 a[120:123], a[252:255], a[152:155], a[120:123]
ds_read_b64_tr_b16 a[250:251], v20 offset:4096
v_mfma_f32_16x16x32_bf16 a[116:119], a[252:255], a[148:151], a[116:119]
v_mfma_f32_16x16x32_bf16 a[112:115], a[252:255], a[144:147], a[112:115]
s_waitcnt lgkmcnt(4)
s_waitcnt vmcnt(40)
v_mfma_f32_16x16x32_bf16 a[0:3], a[240:243], a[160:163], a[0:3]
ds_read_b64_tr_b16 a[252:253], v21 offset:0
s_add_u32 s32, s22, 62914560
v_mfma_f32_16x16x32_bf16 a[4:7], a[240:243], a[164:167], a[4:7]
s_addc_u32 s33, s23, 0
ds_read_b64_tr_b16 a[254:255], v21 offset:4096
s_waitcnt vmcnt(31)
v_mfma_f32_16x16x32_bf16 v[32:35], v[218:221], v[36:39], v[32:35]
v_mfma_f32_16x16x32_bf16 v[48:51], v[216:219], v[36:39], v[48:51]
v_mfma_f32_16x16x32_bf16 a[8:11], a[240:243], a[168:171], a[8:11]
s_and_b32 s33, s33, 0xffff
s_mov_b32 s34, 12582912
s_mov_b32 s35, 0x20000
v_mfma_f32_16x16x32_bf16 a[12:15], a[240:243], a[172:175], a[12:15]
s_waitcnt lgkmcnt(4)
v_mfma_f32_16x16x32_bf16 a[28:31], a[244:247], a[172:175], a[28:31]
ds_read_b64_tr_b16 a[240:241], v18 offset:1024
v_mfma_f32_16x16x32_bf16 a[24:27], a[244:247], a[168:171], a[24:27]
ds_read_b64_tr_b16 a[242:243], v18 offset:5120
v_mfma_f32_16x16x32_bf16 a[20:23], a[244:247], a[164:167], a[20:23]
buffer_load_dwordx4 a[144:147], v236, s[92:95], s96 offen sc1
v_mfma_f32_16x16x32_bf16 a[16:19], a[244:247], a[160:163], a[16:19]
s_waitcnt lgkmcnt(4)
v_mfma_f32_16x16x32_bf16 a[32:35], a[248:251], a[160:163], a[32:35]
ds_read_b64_tr_b16 a[244:245], v19 offset:1024
v_mfma_f32_16x16x32_bf16 a[36:39], a[248:251], a[164:167], a[36:39]
ds_read_b64_tr_b16 a[246:247], v19 offset:5120
s_waitcnt vmcnt(30)
v_mfma_f32_16x16x32_bf16 v[64:67], v[218:221], v[68:71], v[64:67]
v_mfma_f32_16x16x32_bf16 v[80:83], v[216:219], v[68:71], v[80:83]
v_mfma_f32_16x16x32_bf16 a[40:43], a[248:251], a[168:171], a[40:43]
v_mfma_f32_16x16x32_bf16 a[44:47], a[248:251], a[172:175], a[44:47]
s_waitcnt lgkmcnt(4)
v_mfma_f32_16x16x32_bf16 a[60:63], a[252:255], a[172:175], a[60:63]
ds_read_b64_tr_b16 a[248:249], v20 offset:1024
v_mfma_f32_16x16x32_bf16 a[56:59], a[252:255], a[168:171], a[56:59]
ds_read_b64_tr_b16 a[250:251], v20 offset:5120
v_add_u32 v22, 98304, v14
v_mfma_f32_16x16x32_bf16 a[52:55], a[252:255], a[164:167], a[52:55]
buffer_load_dwordx4 a[148:151], v237, s[92:95], s96 offen sc1
v_mfma_f32_16x16x32_bf16 a[48:51], a[252:255], a[160:163], a[48:51]
s_waitcnt lgkmcnt(4)
v_add_u32 v23, 98304, v15
v_add_u32 v24, 98304, v16
v_mfma_f32_16x16x32_bf16 a[64:67], a[240:243], a[160:163], a[64:67]
ds_read_b64_tr_b16 a[252:253], v21 offset:1024
v_add_u32 v25, 98304, v17
v_mfma_f32_16x16x32_bf16 a[68:71], a[240:243], a[164:167], a[68:71]
ds_read_b64_tr_b16 a[254:255], v21 offset:5120
s_waitcnt vmcnt(29)
v_mfma_f32_16x16x32_bf16 v[96:99], v[218:221], v[100:103], v[96:99]
v_mfma_f32_16x16x32_bf16 v[112:115], v[216:219], v[100:103], v[112:115]
v_mfma_f32_16x16x32_bf16 a[72:75], a[240:243], a[168:171], a[72:75]
v_mfma_f32_16x16x32_bf16 a[76:79], a[240:243], a[172:175], a[76:79]
s_waitcnt lgkmcnt(4)
v_mfma_f32_16x16x32_bf16 a[92:95], a[244:247], a[172:175], a[92:95]
ds_read_b64_tr_b16 a[240:241], v18 offset:8192
v_mfma_f32_16x16x32_bf16 a[88:91], a[244:247], a[168:171], a[88:91]
ds_read_b64_tr_b16 a[242:243], v18 offset:12288
v_mfma_f32_16x16x32_bf16 a[84:87], a[244:247], a[164:167], a[84:87]
buffer_load_dwordx4 a[152:155], v238, s[92:95], s96 offen sc1
v_mfma_f32_16x16x32_bf16 a[80:83], a[244:247], a[160:163], a[80:83]
s_waitcnt lgkmcnt(4)
v_mfma_f32_16x16x32_bf16 a[96:99], a[248:251], a[160:163], a[96:99]
ds_read_b64_tr_b16 a[244:245], v19 offset:8192
v_mfma_f32_16x16x32_bf16 a[100:103], a[248:251], a[164:167], a[100:103]
ds_read_b64_tr_b16 a[246:247], v19 offset:12288
s_waitcnt vmcnt(28)
v_mfma_f32_16x16x32_bf16 v[128:131], v[218:221], v[132:135], v[128:131]
v_mfma_f32_16x16x32_bf16 v[144:147], v[216:219], v[132:135], v[144:147]
v_mfma_f32_16x16x32_bf16 a[104:107], a[248:251], a[168:171], a[104:107]
v_mfma_f32_16x16x32_bf16 a[108:111], a[248:251], a[172:175], a[108:111]
s_waitcnt lgkmcnt(4)
v_mfma_f32_16x16x32_bf16 a[124:127], a[252:255], a[172:175], a[124:127]
ds_read_b64_tr_b16 a[248:249], v20 offset:8192
v_mfma_f32_16x16x32_bf16 a[120:123], a[252:255], a[168:171], a[120:123]
ds_read_b64_tr_b16 a[250:251], v20 offset:12288
v_mfma_f32_16x16x32_bf16 a[116:119], a[252:255], a[164:167], a[116:119]
buffer_load_dwordx4 a[156:159], v239, s[92:95], s96 offen sc1
v_mfma_f32_16x16x32_bf16 a[112:115], a[252:255], a[160:163], a[112:115]
s_waitcnt lgkmcnt(4)
s_add_u32 s96, s96, 4096
v_mfma_f32_16x16x32_bf16 a[0:3], a[240:243], a[192:195], a[0:3]
ds_read_b64_tr_b16 a[252:253], v21 offset:8192
s_add_u32 s56, s82, 0
v_mfma_f32_16x16x32_bf16 a[4:7], a[240:243], a[196:199], a[4:7]
ds_read_b64_tr_b16 a[254:255], v21 offset:12288
v_xor_b32 v212, 0, v210
v_mfma_f32_16x16x32_bf16 a[8:11], a[240:243], a[200:203], a[8:11]
buffer_load_dwordx4 a[160:163], v236, s[92:95], s96 offen sc1
v_mfma_f32_16x16x32_bf16 a[12:15], a[240:243], a[204:207], a[12:15]
s_barrier
s_waitcnt lgkmcnt(4)
v_mfma_f32_16x16x32_bf16 a[28:31], a[244:247], a[204:207], a[28:31]
ds_read_b64_tr_b16 a[240:241], v18 offset:9216
v_mfma_f32_16x16x32_bf16 a[24:27], a[244:247], a[200:203], a[24:27]
ds_read_b64_tr_b16 a[242:243], v18 offset:13312
v_mfma_f32_16x16x32_bf16 a[20:23], a[244:247], a[196:199], a[20:23]
buffer_load_dwordx4 a[164:167], v237, s[92:95], s96 offen sc1
v_mfma_f32_16x16x32_bf16 a[16:19], a[244:247], a[192:195], a[16:19]
s_waitcnt lgkmcnt(4)
v_mfma_f32_16x16x32_bf16 a[32:35], a[248:251], a[192:195], a[32:35]
ds_read_b64_tr_b16 a[244:245], v19 offset:9216
v_mfma_f32_16x16x32_bf16 a[36:39], a[248:251], a[196:199], a[36:39]
ds_read_b64_tr_b16 a[246:247], v19 offset:13312
v_mfma_f32_16x16x32_bf16 a[40:43], a[248:251], a[200:203], a[40:43]
buffer_load_dwordx4 a[168:171], v238, s[92:95], s96 offen sc1
s_waitcnt vmcnt(27)
v_mfma_f32_16x16x32_bf16 v[32:35], v[218:221], v[40:43], v[32:35]
v_mfma_f32_16x16x32_bf16 v[48:51], v[216:219], v[40:43], v[48:51]
v_mfma_f32_16x16x32_bf16 a[44:47], a[248:251], a[204:207], a[44:47]
s_waitcnt lgkmcnt(4)
v_mfma_f32_16x16x32_bf16 a[60:63], a[252:255], a[204:207], a[60:63]
ds_read_b64_tr_b16 a[248:249], v20 offset:9216
v_mfma_f32_16x16x32_bf16 a[56:59], a[252:255], a[200:203], a[56:59]
ds_read_b64_tr_b16 a[250:251], v20 offset:13312
v_mfma_f32_16x16x32_bf16 a[52:55], a[252:255], a[196:199], a[52:55]
buffer_load_dwordx4 a[172:175], v239, s[92:95], s96 offen sc1
v_mfma_f32_16x16x32_bf16 a[48:51], a[252:255], a[192:195], a[48:51]
s_waitcnt lgkmcnt(4)
v_mfma_f32_16x16x32_bf16 a[64:67], a[240:243], a[192:195], a[64:67]
ds_read_b64_tr_b16 a[252:253], v21 offset:9216
v_mfma_f32_16x16x32_bf16 a[68:71], a[240:243], a[196:199], a[68:71]
ds_read_b64_tr_b16 a[254:255], v21 offset:13312
v_mfma_f32_16x16x32_bf16 a[72:75], a[240:243], a[200:203], a[72:75]
v_mfma_f32_16x16x32_bf16 a[76:79], a[240:243], a[204:207], a[76:79]
s_waitcnt lgkmcnt(4)
v_mfma_f32_16x16x32_bf16 a[92:95], a[244:247], a[204:207], a[92:95]
ds_read_b64_tr_b16 a[240:241], v22 offset:0
v_mfma_f32_16x16x32_bf16 a[88:91], a[244:247], a[200:203], a[88:91]
ds_read_b64_tr_b16 a[242:243], v22 offset:4096
v_mfma_f32_16x16x32_bf16 a[84:87], a[244:247], a[196:199], a[84:87]
s_waitcnt vmcnt(24)
v_mfma_f32_16x16x32_bf16 v[64:67], v[218:221], v[72:75], v[64:67]
v_mfma_f32_16x16x32_bf16 v[80:83], v[216:219], v[72:75], v[80:83]
v_mfma_f32_16x16x32_bf16 a[80:83], a[244:247], a[192:195], a[80:83]
s_waitcnt lgkmcnt(4)
v_mfma_f32_16x16x32_bf16 a[96:99], a[248:251], a[192:195], a[96:99]
ds_read_b64_tr_b16 a[244:245], v23 offset:0
v_mfma_f32_16x16x32_bf16 a[100:103], a[248:251], a[196:199], a[100:103]
ds_read_b64_tr_b16 a[246:247], v23 offset:4096
v_mfma_f32_16x16x32_bf16 a[104:107], a[248:251], a[200:203], a[104:107]
v_mfma_f32_16x16x32_bf16 a[108:111], a[248:251], a[204:207], a[108:111]
s_waitcnt lgkmcnt(4)
v_mfma_f32_16x16x32_bf16 a[124:127], a[252:255], a[204:207], a[124:127]
ds_read_b64_tr_b16 a[248:249], v24 offset:0
s_add_u32 m0, s70, 147456
v_mfma_f32_16x16x32_bf16 a[120:123], a[252:255], a[200:203], a[120:123]
ds_read_b64_tr_b16 a[250:251], v24 offset:4096
v_mfma_f32_16x16x32_bf16 a[116:119], a[252:255], a[196:199], a[116:119]
v_mfma_f32_16x16x32_bf16 a[112:115], a[252:255], a[192:195], a[112:115]
s_waitcnt lgkmcnt(4)
v_mfma_f32_16x16x32_bf16 a[0:3], a[240:243], a[176:179], a[0:3]
ds_read_b64_tr_b16 a[252:253], v25 offset:0
v_mfma_f32_16x16x32_bf16 a[4:7], a[240:243], a[180:183], a[4:7]
ds_read_b64_tr_b16 a[254:255], v25 offset:4096
s_waitcnt vmcnt(21)
v_mfma_f32_16x16x32_bf16 v[96:99], v[218:221], v[104:107], v[96:99]
v_mfma_f32_16x16x32_bf16 v[112:115], v[216:219], v[104:107], v[112:115]
v_mfma_f32_16x16x32_bf16 a[8:11], a[240:243], a[184:187], a[8:11]
buffer_load_dwordx4 v212, s[36:39], s56 offen lds
v_mfma_f32_16x16x32_bf16 a[12:15], a[240:243], a[188:191], a[12:15]
s_waitcnt lgkmcnt(4)
s_add_u32 s56, s82, 8192
v_xor_b32 v212, 64, v210
v_mfma_f32_16x16x32_bf16 a[28:31], a[244:247], a[188:191], a[28:31]
ds_read_b64_tr_b16 a[240:241], v22 offset:1024
s_add_u32 m0, s70, 148480
v_mfma_f32_16x16x32_bf16 a[24:27], a[244:247], a[184:187], a[24:27]
ds_read_b64_tr_b16 a[242:243], v22 offset:5120
v_mfma_f32_16x16x32_bf16 a[20:23], a[244:247], a[180:183], a[20:23]
v_mfma_f32_16x16x32_bf16 a[16:19], a[244:247], a[176:179], a[16:19]
s_waitcnt lgkmcnt(4)
v_mfma_f32_16x16x32_bf16 a[32:35], a[248:251], a[176:179], a[32:35]
ds_read_b64_tr_b16 a[244:245], v23 offset:1024
v_mfma_f32_16x16x32_bf16 a[36:39], a[248:251], a[180:183], a[36:39]
ds_read_b64_tr_b16 a[246:247], v23 offset:5120
s_waitcnt vmcnt(20)
v_mfma_f32_16x16x32_bf16 v[128:131], v[218:221], v[136:139], v[128:131]
v_mfma_f32_16x16x32_bf16 v[144:147], v[216:219], v[136:139], v[144:147]
v_mfma_f32_16x16x32_bf16 a[40:43], a[248:251], a[184:187], a[40:43]
buffer_load_dwordx4 v212, s[36:39], s56 offen lds
v_mfma_f32_16x16x32_bf16 a[44:47], a[248:251], a[188:191], a[44:47]
s_waitcnt lgkmcnt(4)
s_add_u32 s56, s82, 16384
v_xor_b32 v212, 128, v210
v_mfma_f32_16x16x32_bf16 a[60:63], a[252:255], a[188:191], a[60:63]
ds_read_b64_tr_b16 a[248:249], v24 offset:1024
s_add_u32 m0, s70, 149504
v_mfma_f32_16x16x32_bf16 a[56:59], a[252:255], a[184:187], a[56:59]
ds_read_b64_tr_b16 a[250:251], v24 offset:5120
v_add_u32 v18, 114688, v14
v_mfma_f32_16x16x32_bf16 a[52:55], a[252:255], a[180:183], a[52:55]
v_add_u32 v19, 114688, v15
v_add_u32 v20, 114688, v16
v_mfma_f32_16x16x32_bf16 a[48:51], a[252:255], a[176:179], a[48:51]
s_waitcnt lgkmcnt(4)
v_add_u32 v21, 114688, v17
v_mfma_f32_16x16x32_bf16 a[64:67], a[240:243], a[176:179], a[64:67]
ds_read_b64_tr_b16 a[252:253], v25 offset:1024
v_mfma_f32_16x16x32_bf16 a[68:71], a[240:243], a[180:183], a[68:71]
ds_read_b64_tr_b16 a[254:255], v25 offset:5120
s_waitcnt vmcnt(19)
v_mfma_f32_16x16x32_bf16 v[32:35], v[218:221], v[44:47], v[32:35]
v_mfma_f32_16x16x32_bf16 v[48:51], v[216:219], v[44:47], v[48:51]
v_mfma_f32_16x16x32_bf16 a[72:75], a[240:243], a[184:187], a[72:75]
buffer_load_dwordx4 v212, s[36:39], s56 offen lds
v_mfma_f32_16x16x32_bf16 a[76:79], a[240:243], a[188:191], a[76:79]
s_waitcnt lgkmcnt(4)
s_add_u32 s56, s82, 24576
v_xor_b32 v212, 192, v210
v_mfma_f32_16x16x32_bf16 a[92:95], a[244:247], a[188:191], a[92:95]
ds_read_b64_tr_b16 a[240:241], v22 offset:8192
s_add_u32 m0, s70, 150528
v_mfma_f32_16x16x32_bf16 a[88:91], a[244:247], a[184:187], a[88:91]
ds_read_b64_tr_b16 a[242:243], v22 offset:12288
v_mfma_f32_16x16x32_bf16 a[84:87], a[244:247], a[180:183], a[84:87]
v_mul_f32 v56, v214, v32
v_mul_f32 v57, v214, v33
v_mfma_f32_16x16x32_bf16 a[80:83], a[244:247], a[176:179], a[80:83]
s_waitcnt lgkmcnt(4)
v_mul_f32 v58, v214, v34
v_mul_f32 v59, v214, v35
v_mfma_f32_16x16x32_bf16 a[96:99], a[248:251], a[176:179], a[96:99]
ds_read_b64_tr_b16 a[244:245], v23 offset:8192
v_mul_f32 v52, v214, v48
v_mfma_f32_16x16x32_bf16 a[100:103], a[248:251], a[180:183], a[100:103]
ds_read_b64_tr_b16 a[246:247], v23 offset:12288
v_mul_f32 v53, v214, v49
s_waitcnt vmcnt(18)
v_mfma_f32_16x16x32_bf16 v[64:67], v[218:221], v[76:79], v[64:67]
v_mfma_f32_16x16x32_bf16 v[80:83], v[216:219], v[76:79], v[80:83]
v_mfma_f32_16x16x32_bf16 a[104:107], a[248:251], a[184:187], a[104:107]
buffer_load_dwordx4 v212, s[36:39], s56 offen lds
v_mfma_f32_16x16x32_bf16 a[108:111], a[248:251], a[188:191], a[108:111]
s_waitcnt lgkmcnt(4)
v_mul_f32 v54, v214, v50
v_mul_f32 v55, v214, v51
v_mfma_f32_16x16x32_bf16 a[124:127], a[252:255], a[188:191], a[124:127]
ds_read_b64_tr_b16 a[248:249], v24 offset:8192
v_mfma_f32_16x16x32_bf16 a[120:123], a[252:255], a[184:187], a[120:123]
ds_read_b64_tr_b16 a[250:251], v24 offset:12288
v_mfma_f32_16x16x32_bf16 a[116:119], a[252:255], a[180:183], a[116:119]
v_mul_f32 v88, v214, v64
v_mul_f32 v89, v214, v65
v_mfma_f32_16x16x32_bf16 a[112:115], a[252:255], a[176:179], a[112:115]
s_waitcnt lgkmcnt(4)
v_mul_f32 v90, v214, v66
v_mfma_f32_16x16x32_bf16 a[0:3], a[240:243], a[208:211], a[0:3]
ds_read_b64_tr_b16 a[252:253], v25 offset:8192
v_mul_f32 v91, v214, v67
v_mfma_f32_16x16x32_bf16 a[4:7], a[240:243], a[212:215], a[4:7]
ds_read_b64_tr_b16 a[254:255], v25 offset:12288
v_mul_f32 v84, v214, v80
v_mfma_f32_16x16x32_bf16 a[8:11], a[240:243], a[216:219], a[8:11]
v_mul_f32 v85, v214, v81
v_mul_f32 v86, v214, v82
v_mfma_f32_16x16x32_bf16 a[12:15], a[240:243], a[220:223], a[12:15]
s_barrier
s_waitcnt lgkmcnt(4)
v_mfma_f32_16x16x32_bf16 a[28:31], a[244:247], a[220:223], a[28:31]
ds_read_b64_tr_b16 a[240:241], v22 offset:9216
v_mul_f32 v87, v214, v83
v_mfma_f32_16x16x32_bf16 a[24:27], a[244:247], a[216:219], a[24:27]
ds_read_b64_tr_b16 a[242:243], v22 offset:13312
v_mfma_f32_16x16x32_bf16 a[20:23], a[244:247], a[212:215], a[20:23]
v_exp_f32 v56, v56
v_mfma_f32_16x16x32_bf16 a[16:19], a[244:247], a[208:211], a[16:19]
s_waitcnt lgkmcnt(4)
v_exp_f32 v57, v57
v_mfma_f32_16x16x32_bf16 a[32:35], a[248:251], a[208:211], a[32:35]
ds_read_b64_tr_b16 a[244:245], v23 offset:9216
v_add_f32 v56, 1.0, v56
v_mfma_f32_16x16x32_bf16 a[36:39], a[248:251], a[212:215], a[36:39]
ds_read_b64_tr_b16 a[246:247], v23 offset:13312
v_add_f32 v57, 1.0, v57
v_mfma_f32_16x16x32_bf16 a[40:43], a[248:251], a[216:219], a[40:43]
v_exp_f32 v58, v58
v_mfma_f32_16x16x32_bf16 a[44:47], a[248:251], a[220:223], a[44:47]
s_waitcnt lgkmcnt(4)
v_exp_f32 v59, v59
v_mfma_f32_16x16x32_bf16 a[60:63], a[252:255], a[220:223], a[60:63]
ds_read_b64_tr_b16 a[248:249], v24 offset:9216
v_add_f32 v58, 1.0, v58
v_mfma_f32_16x16x32_bf16 a[56:59], a[252:255], a[216:219], a[56:59]
ds_read_b64_tr_b16 a[250:251], v24 offset:13312
v_add_f32 v59, 1.0, v59
v_mfma_f32_16x16x32_bf16 a[52:55], a[252:255], a[212:215], a[52:55]
v_exp_f32 v52, v52
v_mfma_f32_16x16x32_bf16 a[48:51], a[252:255], a[208:211], a[48:51]
s_waitcnt lgkmcnt(4)
v_exp_f32 v53, v53
v_mfma_f32_16x16x32_bf16 a[64:67], a[240:243], a[208:211], a[64:67]
ds_read_b64_tr_b16 a[252:253], v25 offset:9216
v_add_f32 v52, 1.0, v52
v_mfma_f32_16x16x32_bf16 a[68:71], a[240:243], a[212:215], a[68:71]
ds_read_b64_tr_b16 a[254:255], v25 offset:13312
v_add_f32 v53, 1.0, v53
s_waitcnt vmcnt(13)
v_mfma_f32_16x16x32_bf16 v[96:99], v[218:221], v[108:111], v[96:99]
v_mfma_f32_16x16x32_bf16 v[112:115], v[216:219], v[108:111], v[112:115]
v_mfma_f32_16x16x32_bf16 a[72:75], a[240:243], a[216:219], a[72:75]
v_exp_f32 v54, v54
v_mfma_f32_16x16x32_bf16 a[76:79], a[240:243], a[220:223], a[76:79]
s_waitcnt lgkmcnt(4)
v_add_u32 v22, 131072, v14
v_add_u32 v23, 131072, v15
v_mfma_f32_16x16x32_bf16 a[92:95], a[244:247], a[220:223], a[92:95]
ds_read_b64_tr_b16 a[240:241], v18 offset:0
v_add_u32 v24, 131072, v16
v_mfma_f32_16x16x32_bf16 a[88:91], a[244:247], a[216:219], a[88:91]
ds_read_b64_tr_b16 a[242:243], v18 offset:4096
v_add_u32 v25, 131072, v17
s_waitcnt vmcnt(12)
v_mfma_f32_16x16x32_bf16 v[128:131], v[218:221], v[140:143], v[128:131]
v_mfma_f32_16x16x32_bf16 v[144:147], v[216:219], v[140:143], v[144:147]
v_mfma_f32_16x16x32_bf16 a[84:87], a[244:247], a[212:215], a[84:87]
v_mul_f32 v120, v214, v96
v_mul_f32 v121, v214, v97
v_mfma_f32_16x16x32_bf16 a[80:83], a[244:247], a[208:211], a[80:83]
s_waitcnt lgkmcnt(4)
v_mul_f32 v122, v214, v98
v_mul_f32 v123, v214, v99
v_mfma_f32_16x16x32_bf16 a[96:99], a[248:251], a[208:211], a[96:99]
ds_read_b64_tr_b16 a[244:245], v18 offset:8192
v_mul_f32 v116, v214, v112
v_mfma_f32_16x16x32_bf16 a[100:103], a[248:251], a[212:215], a[100:103]
ds_read_b64_tr_b16 a[246:247], v18 offset:12288
v_mul_f32 v117, v214, v113
v_mfma_f32_16x16x32_bf16 a[104:107], a[248:251], a[216:219], a[104:107]
v_mul_f32 v118, v214, v114
v_mul_f32 v119, v214, v115
v_mfma_f32_16x16x32_bf16 a[108:111], a[248:251], a[220:223], a[108:111]
s_waitcnt lgkmcnt(4)
v_mul_f32 v152, v214, v128
v_mul_f32 v153, v214, v129
v_mfma_f32_16x16x32_bf16 a[124:127], a[252:255], a[220:223], a[124:127]
ds_read_b64_tr_b16 a[248:249], v22 offset:0
v_mul_f32 v154, v214, v130
v_mfma_f32_16x16x32_bf16 a[120:123], a[252:255], a[216:219], a[120:123]
ds_read_b64_tr_b16 a[250:251], v22 offset:4096
v_mul_f32 v155, v214, v131
v_mfma_f32_16x16x32_bf16 a[116:119], a[252:255], a[212:215], a[116:119]
v_mul_f32 v148, v214, v144
v_mul_f32 v149, v214, v145
v_mfma_f32_16x16x32_bf16 a[112:115], a[252:255], a[208:211], a[112:115]
s_waitcnt lgkmcnt(4)
v_mul_f32 v150, v214, v146
v_mfma_f32_16x16x32_bf16 a[0:3], a[240:243], a[128:131], a[0:3]
ds_read_b64_tr_b16 a[252:253], v22 offset:8192
v_mul_f32 v151, v214, v147
v_mfma_f32_16x16x32_bf16 a[4:7], a[240:243], a[132:135], a[4:7]
ds_read_b64_tr_b16 a[254:255], v22 offset:12288
v_add_f32 v54, 1.0, v54
v_mfma_f32_16x16x32_bf16 a[8:11], a[240:243], a[136:139], a[8:11]
v_exp_f32 v55, v55
v_mfma_f32_16x16x32_bf16 a[12:15], a[240:243], a[140:143], a[12:15]
s_waitcnt lgkmcnt(4)
v_add_f32 v55, 1.0, v55
v_mfma_f32_16x16x32_bf16 a[12:15], a[244:247], v[156:159], a[12:15]
ds_read_b64_tr_b16 a[240:241], v19 offset:0
v_mfma_f32_16x16x32_bf16 a[8:11], a[244:247], v[124:127], a[8:11]
ds_read_b64_tr_b16 a[242:243], v19 offset:4096
v_mfma_f32_16x16x32_bf16 a[4:7], a[244:247], v[92:95], a[4:7]
v_exp_f32 v88, v88
v_mfma_f32_16x16x32_bf16 a[0:3], a[244:247], v[60:63], a[0:3]
s_waitcnt lgkmcnt(4)
s_waitcnt vmcnt(8)
v_add_f32 v88, 1.0, v88
v_mfma_f32_16x16x32_bf16 a[0:3], a[248:251], a[144:147], a[0:3]
ds_read_b64_tr_b16 a[244:245], v19 offset:8192
v_mfma_f32_16x16x32_bf16 a[4:7], a[248:251], a[148:151], a[4:7]
ds_read_b64_tr_b16 a[246:247], v19 offset:12288
v_mfma_f32_16x16x32_bf16 a[8:11], a[248:251], a[152:155], a[8:11]
v_exp_f32 v89, v89
v_mfma_f32_16x16x32_bf16 a[12:15], a[248:251], a[156:159], a[12:15]
s_waitcnt lgkmcnt(4)
s_waitcnt vmcnt(4)
v_add_f32 v89, 1.0, v89
v_mfma_f32_16x16x32_bf16 a[12:15], a[252:255], a[172:175], a[12:15]
ds_read_b64_tr_b16 a[248:249], v23 offset:0
v_mfma_f32_16x16x32_bf16 a[8:11], a[252:255], a[168:171], a[8:11]
ds_read_b64_tr_b16 a[250:251], v23 offset:4096
v_mfma_f32_16x16x32_bf16 a[4:7], a[252:255], a[164:167], a[4:7]
v_exp_f32 v90, v90
v_mfma_f32_16x16x32_bf16 a[0:3], a[252:255], a[160:163], a[0:3]
s_waitcnt lgkmcnt(4)
v_exp_f32 v91, v91
v_mfma_f32_16x16x32_bf16 a[16:19], a[240:243], a[128:131], a[16:19]
ds_read_b64_tr_b16 a[252:253], v23 offset:8192
v_add_f32 v90, 1.0, v90
v_mfma_f32_16x16x32_bf16 a[20:23], a[240:243], a[132:135], a[20:23]
ds_read_b64_tr_b16 a[254:255], v23 offset:12288
v_add_f32 v91, 1.0, v91
v_mfma_f32_16x16x32_bf16 a[24:27], a[240:243], a[136:139], a[24:27]
v_exp_f32 v84, v84
v_mfma_f32_16x16x32_bf16 a[28:31], a[240:243], a[140:143], a[28:31]
s_waitcnt lgkmcnt(4)
v_exp_f32 v85, v85
v_mfma_f32_16x16x32_bf16 a[28:31], a[244:247], v[156:159], a[28:31]
ds_read_b64_tr_b16 a[240:241], v20 offset:0
v_add_f32 v84, 1.0, v84
v_mfma_f32_16x16x32_bf16 a[24:27], a[244:247], v[124:127], a[24:27]
ds_read_b64_tr_b16 a[242:243], v20 offset:4096
v_add_f32 v85, 1.0, v85
v_mfma_f32_16x16x32_bf16 a[20:23], a[244:247], v[92:95], a[20:23]
v_exp_f32 v86, v86
v_mfma_f32_16x16x32_bf16 a[16:19], a[244:247], v[60:63], a[16:19]
s_waitcnt lgkmcnt(4)
v_exp_f32 v87, v87
v_mfma_f32_16x16x32_bf16 a[16:19], a[248:251], a[144:147], a[16:19]
ds_read_b64_tr_b16 a[244:245], v20 offset:8192
v_add_f32 v86, 1.0, v86
v_mfma_f32_16x16x32_bf16 a[20:23], a[248:251], a[148:151], a[20:23]
ds_read_b64_tr_b16 a[246:247], v20 offset:12288
v_add_f32 v87, 1.0, v87
v_mfma_f32_16x16x32_bf16 a[24:27], a[248:251], a[152:155], a[24:27]
v_exp_f32 v120, v120
v_mfma_f32_16x16x32_bf16 a[28:31], a[248:251], a[156:159], a[28:31]
s_waitcnt lgkmcnt(4)
v_exp_f32 v121, v121
v_mfma_f32_16x16x32_bf16 a[28:31], a[252:255], a[172:175], a[28:31]
ds_read_b64_tr_b16 a[248:249], v24 offset:0
v_add_f32 v120, 1.0, v120
v_mfma_f32_16x16x32_bf16 a[24:27], a[252:255], a[168:171], a[24:27]
ds_read_b64_tr_b16 a[250:251], v24 offset:4096
v_add_f32 v121, 1.0, v121
v_mfma_f32_16x16x32_bf16 a[20:23], a[252:255], a[164:167], a[20:23]
v_exp_f32 v122, v122
v_mfma_f32_16x16x32_bf16 a[16:19], a[252:255], a[160:163], a[16:19]
s_waitcnt lgkmcnt(4)
v_exp_f32 v123, v123
v_mfma_f32_16x16x32_bf16 a[32:35], a[240:243], a[128:131], a[32:35]
ds_read_b64_tr_b16 a[252:253], v24 offset:8192
v_xor_b32 v212, 0, v211
v_mfma_f32_16x16x32_bf16 a[36:39], a[240:243], a[132:135], a[36:39]
ds_read_b64_tr_b16 a[254:255], v24 offset:12288
v_xor_b32 v213, 32, v211
v_mfma_f32_16x16x32_bf16 a[40:43], a[240:243], a[136:139], a[40:43]
v_accvgpr_read_b32 v232, a4
v_accvgpr_read_b32 v233, a5
v_mfma_f32_16x16x32_bf16 a[44:47], a[240:243], a[140:143], a[44:47]
s_waitcnt lgkmcnt(4)
v_accvgpr_read_b32 v234, a6
v_accvgpr_read_b32 v235, a7
v_mfma_f32_16x16x32_bf16 a[44:47], a[244:247], v[156:159], a[44:47]
ds_read_b64_tr_b16 a[240:241], v21 offset:0
v_accvgpr_read_b32 v236, a20
v_mfma_f32_16x16x32_bf16 a[40:43], a[244:247], v[124:127], a[40:43]
ds_read_b64_tr_b16 a[242:243], v21 offset:4096
v_accvgpr_read_b32 v237, a21
v_mfma_f32_16x16x32_bf16 a[36:39], a[244:247], v[92:95], a[36:39]
v_accvgpr_read_b32 v238, a22
v_accvgpr_read_b32 v239, a23
v_mfma_f32_16x16x32_bf16 a[32:35], a[244:247], v[60:63], a[32:35]
s_waitcnt lgkmcnt(4)
v_cvt_pk_bf16_f32 v36, v232, v233
v_cvt_pk_bf16_f32 v37, v234, v235
v_mfma_f32_16x16x32_bf16 a[32:35], a[248:251], a[144:147], a[32:35]
ds_read_b64_tr_b16 a[244:245], v21 offset:8192
v_cvt_pk_bf16_f32 v38, v236, v237
v_mfma_f32_16x16x32_bf16 a[36:39], a[248:251], a[148:151], a[36:39]
ds_read_b64_tr_b16 a[246:247], v21 offset:12288
v_cvt_pk_bf16_f32 v39, v238, v239
v_mfma_f32_16x16x32_bf16 a[40:43], a[248:251], a[152:155], a[40:43]
v_accvgpr_read_b32 v224, a8
v_accvgpr_read_b32 v225, a9
v_mfma_f32_16x16x32_bf16 a[44:47], a[248:251], a[156:159], a[44:47]
s_waitcnt lgkmcnt(4)
v_accvgpr_read_b32 v226, a10
v_accvgpr_read_b32 v227, a11
v_mfma_f32_16x16x32_bf16 a[44:47], a[252:255], a[172:175], a[44:47]
ds_read_b64_tr_b16 a[248:249], v25 offset:0
v_accvgpr_read_b32 v228, a24
v_mfma_f32_16x16x32_bf16 a[40:43], a[252:255], a[168:171], a[40:43]
ds_read_b64_tr_b16 a[250:251], v25 offset:4096
v_accvgpr_read_b32 v229, a25
v_mfma_f32_16x16x32_bf16 a[36:39], a[252:255], a[164:167], a[36:39]
s_add_u32 s55, s81, 0
v_accvgpr_read_b32 v230, a26
v_accvgpr_read_b32 v231, a27
v_mfma_f32_16x16x32_bf16 a[32:35], a[252:255], a[160:163], a[32:35]
s_waitcnt lgkmcnt(4)
v_cvt_pk_bf16_f32 v40, v224, v225
v_cvt_pk_bf16_f32 v41, v226, v227
v_mfma_f32_16x16x32_bf16 a[48:51], a[240:243], a[128:131], a[48:51]
ds_read_b64_tr_b16 a[252:253], v25 offset:8192
v_cvt_pk_bf16_f32 v42, v228, v229
v_mfma_f32_16x16x32_bf16 a[52:55], a[240:243], a[132:135], a[52:55]
ds_read_b64_tr_b16 a[254:255], v25 offset:12288
v_cvt_pk_bf16_f32 v43, v230, v231
v_mfma_f32_16x16x32_bf16 a[56:59], a[240:243], a[136:139], a[56:59]
v_accvgpr_read_b32 v232, a12
v_accvgpr_read_b32 v233, a13
v_mfma_f32_16x16x32_bf16 a[60:63], a[240:243], a[140:143], a[60:63]
s_waitcnt lgkmcnt(4)
v_accvgpr_read_b32 v234, a14
v_accvgpr_read_b32 v235, a15
v_mfma_f32_16x16x32_bf16 a[60:63], a[244:247], v[156:159], a[60:63]
ds_read_b64_tr_b16 a[240:241], v18 offset:1024
v_accvgpr_read_b32 v236, a28
v_mfma_f32_16x16x32_bf16 a[56:59], a[244:247], v[124:127], a[56:59]
ds_read_b64_tr_b16 a[242:243], v18 offset:5120
v_accvgpr_read_b32 v237, a29
v_mfma_f32_16x16x32_bf16 a[52:55], a[244:247], v[92:95], a[52:55]
buffer_store_dwordx4 v[36:39], v2, s[32:35], s55 offen offset:0 sc1
v_mfma_f32_16x16x32_bf16 a[48:51], a[244:247], v[60:63], a[48:51]
s_waitcnt lgkmcnt(4)
v_accvgpr_read_b32 v238, a30
v_accvgpr_read_b32 v239, a31
v_mfma_f32_16x16x32_bf16 a[48:51], a[248:251], a[144:147], a[48:51]
ds_read_b64_tr_b16 a[244:245], v18 offset:9216
v_cvt_pk_bf16_f32 v44, v232, v233
v_mfma_f32_16x16x32_bf16 a[52:55], a[248:251], a[148:151], a[52:55]
ds_read_b64_tr_b16 a[246:247], v18 offset:13312
v_cvt_pk_bf16_f32 v45, v234, v235
v_mfma_f32_16x16x32_bf16 a[56:59], a[248:251], a[152:155], a[56:59]
s_add_u32 s55, s81, 16384
v_cvt_pk_bf16_f32 v46, v236, v237
v_cvt_pk_bf16_f32 v47, v238, v239
v_mfma_f32_16x16x32_bf16 a[60:63], a[248:251], a[156:159], a[60:63]
s_waitcnt lgkmcnt(4)
v_exp_f32 v116, v116
v_mfma_f32_16x16x32_bf16 a[60:63], a[252:255], a[172:175], a[60:63]
ds_read_b64_tr_b16 a[248:249], v22 offset:1024
v_add_f32 v122, 1.0, v122
v_mfma_f32_16x16x32_bf16 a[56:59], a[252:255], a[168:171], a[56:59]
ds_read_b64_tr_b16 a[250:251], v22 offset:5120
v_add_f32 v123, 1.0, v123
v_mfma_f32_16x16x32_bf16 a[52:55], a[252:255], a[164:167], a[52:55]
v_exp_f32 v117, v117
v_mfma_f32_16x16x32_bf16 a[48:51], a[252:255], a[160:163], a[48:51]
s_waitcnt lgkmcnt(4)
v_exp_f32 v118, v118
v_mfma_f32_16x16x32_bf16 a[64:67], a[240:243], a[128:131], a[64:67]
ds_read_b64_tr_b16 a[252:253], v22 offset:9216
v_add_f32 v116, 1.0, v116
v_mfma_f32_16x16x32_bf16 a[68:71], a[240:243], a[132:135], a[68:71]
ds_read_b64_tr_b16 a[254:255], v22 offset:13312
v_add_f32 v117, 1.0, v117
v_mfma_f32_16x16x32_bf16 a[72:75], a[240:243], a[136:139], a[72:75]
buffer_store_dwordx4 v[40:43], v2, s[32:35], s55 offen offset:0 sc1
v_mfma_f32_16x16x32_bf16 a[76:79], a[240:243], a[140:143], a[76:79]
s_waitcnt lgkmcnt(4)
s_waitcnt vmcnt(2)
s_add_u32 s55, s81, 32768
v_mfma_f32_16x16x32_bf16 a[76:79], a[244:247], v[156:159], a[76:79]
ds_read_b64_tr_b16 a[240:241], v19 offset:1024
v_accvgpr_read_b32 v224, a36
v_mfma_f32_16x16x32_bf16 a[72:75], a[244:247], v[124:127], a[72:75]
ds_read_b64_tr_b16 a[242:243], v19 offset:5120
v_accvgpr_read_b32 v225, a37
v_mfma_f32_16x16x32_bf16 a[68:71], a[244:247], v[92:95], a[68:71]
v_accvgpr_read_b32 v226, a38
v_accvgpr_read_b32 v227, a39
v_mfma_f32_16x16x32_bf16 a[64:67], a[244:247], v[60:63], a[64:67]
s_waitcnt lgkmcnt(4)
v_accvgpr_read_b32 v228, a52
v_accvgpr_read_b32 v229, a53
v_mfma_f32_16x16x32_bf16 a[64:67], a[248:251], a[144:147], a[64:67]
ds_read_b64_tr_b16 a[244:245], v19 offset:9216
v_accvgpr_read_b32 v230, a54
v_mfma_f32_16x16x32_bf16 a[68:71], a[248:251], a[148:151], a[68:71]
ds_read_b64_tr_b16 a[246:247], v19 offset:13312
v_accvgpr_read_b32 v231, a55
v_mfma_f32_16x16x32_bf16 a[72:75], a[248:251], a[152:155], a[72:75]
buffer_store_dwordx4 v[44:47], v2, s[32:35], s55 offen offset:0 sc1
v_mfma_f32_16x16x32_bf16 a[76:79], a[248:251], a[156:159], a[76:79]
s_waitcnt lgkmcnt(4)
v_cvt_pk_bf16_f32 v68, v224, v225
v_cvt_pk_bf16_f32 v69, v226, v227
v_mfma_f32_16x16x32_bf16 a[76:79], a[252:255], a[172:175], a[76:79]
ds_read_b64_tr_b16 a[248:249], v23 offset:1024
v_cvt_pk_bf16_f32 v70, v228, v229
v_mfma_f32_16x16x32_bf16 a[72:75], a[252:255], a[168:171], a[72:75]
ds_read_b64_tr_b16 a[250:251], v23 offset:5120
v_cvt_pk_bf16_f32 v71, v230, v231
v_mfma_f32_16x16x32_bf16 a[68:71], a[252:255], a[164:167], a[68:71]
s_add_u32 s55, s81, 0
v_accvgpr_read_b32 v232, a40
v_accvgpr_read_b32 v233, a41
v_mfma_f32_16x16x32_bf16 a[64:67], a[252:255], a[160:163], a[64:67]
s_waitcnt lgkmcnt(4)
v_accvgpr_read_b32 v234, a42
v_accvgpr_read_b32 v235, a43
v_mfma_f32_16x16x32_bf16 a[80:83], a[240:243], a[128:131], a[80:83]
ds_read_b64_tr_b16 a[252:253], v23 offset:9216
v_accvgpr_read_b32 v236, a56
v_mfma_f32_16x16x32_bf16 a[84:87], a[240:243], a[132:135], a[84:87]
ds_read_b64_tr_b16 a[254:255], v23 offset:13312
v_accvgpr_read_b32 v237, a57
v_mfma_f32_16x16x32_bf16 a[88:91], a[240:243], a[136:139], a[88:91]
v_accvgpr_read_b32 v238, a58
v_accvgpr_read_b32 v239, a59
v_mfma_f32_16x16x32_bf16 a[92:95], a[240:243], a[140:143], a[92:95]
s_waitcnt lgkmcnt(4)
v_cvt_pk_bf16_f32 v72, v232, v233
v_cvt_pk_bf16_f32 v73, v234, v235
v_mfma_f32_16x16x32_bf16 a[92:95], a[244:247], v[156:159], a[92:95]
ds_read_b64_tr_b16 a[240:241], v20 offset:1024
v_cvt_pk_bf16_f32 v74, v236, v237
v_mfma_f32_16x16x32_bf16 a[88:91], a[244:247], v[124:127], a[88:91]
ds_read_b64_tr_b16 a[242:243], v20 offset:5120
v_cvt_pk_bf16_f32 v75, v238, v239
v_mfma_f32_16x16x32_bf16 a[84:87], a[244:247], v[92:95], a[84:87]
buffer_store_dwordx4 v[68:71], v2, s[32:35], s55 offen offset:1024 sc1
v_mfma_f32_16x16x32_bf16 a[80:83], a[244:247], v[60:63], a[80:83]
s_waitcnt lgkmcnt(4)
ds_read_b64 v[160:161], v212
v_mfma_f32_16x16x32_bf16 a[80:83], a[248:251], a[144:147], a[80:83]
ds_read_b64_tr_b16 a[244:245], v20 offset:9216
v_xor_b32 v212, 64, v211
v_mfma_f32_16x16x32_bf16 a[84:87], a[248:251], a[148:151], a[84:87]
ds_read_b64_tr_b16 a[246:247], v20 offset:13312
v_accvgpr_read_b32 v224, a44
v_mfma_f32_16x16x32_bf16 a[88:91], a[248:251], a[152:155], a[88:91]
s_add_u32 s55, s81, 16384
ds_read_b64 v[162:163], v213
v_mfma_f32_16x16x32_bf16 a[92:95], a[248:251], a[156:159], a[92:95]
s_waitcnt lgkmcnt(6)
v_accvgpr_read_b32 v225, a45
v_accvgpr_read_b32 v226, a46
v_mfma_f32_16x16x32_bf16 a[92:95], a[252:255], a[172:175], a[92:95]
ds_read_b64_tr_b16 a[248:249], v24 offset:1024
v_accvgpr_read_b32 v227, a47
v_mfma_f32_16x16x32_bf16 a[88:91], a[252:255], a[168:171], a[88:91]
ds_read_b64_tr_b16 a[250:251], v24 offset:5120
v_accvgpr_read_b32 v228, a60
v_mfma_f32_16x16x32_bf16 a[84:87], a[252:255], a[164:167], a[84:87]
v_accvgpr_read_b32 v229, a61
v_accvgpr_read_b32 v230, a62
v_mfma_f32_16x16x32_bf16 a[80:83], a[252:255], a[160:163], a[80:83]
s_waitcnt lgkmcnt(6)
v_accvgpr_read_b32 v231, a63
v_xor_b32 v213, 96, v211
v_mfma_f32_16x16x32_bf16 a[96:99], a[240:243], a[128:131], a[96:99]
ds_read_b64_tr_b16 a[252:253], v24 offset:9216
v_cvt_pk_bf16_f32 v76, v224, v225
v_mfma_f32_16x16x32_bf16 a[100:103], a[240:243], a[132:135], a[100:103]
ds_read_b64_tr_b16 a[254:255], v24 offset:13312
v_cvt_pk_bf16_f32 v77, v226, v227
v_mfma_f32_16x16x32_bf16 a[104:107], a[240:243], a[136:139], a[104:107]
buffer_store_dwordx4 v[72:75], v2, s[32:35], s55 offen offset:1024 sc1
v_mfma_f32_16x16x32_bf16 a[108:111], a[240:243], a[140:143], a[108:111]
s_waitcnt lgkmcnt(5)
ds_read_b64 v[164:165], v212
v_mfma_f32_16x16x32_bf16 a[108:111], a[244:247], v[156:159], a[108:111]
ds_read_b64_tr_b16 a[240:241], v21 offset:1024
v_cvt_pk_bf16_f32 v78, v228, v229
v_mfma_f32_16x16x32_bf16 a[104:107], a[244:247], v[124:127], a[104:107]
ds_read_b64_tr_b16 a[242:243], v21 offset:5120
v_cvt_pk_bf16_f32 v79, v230, v231
v_mfma_f32_16x16x32_bf16 a[100:103], a[244:247], v[92:95], a[100:103]
s_add_u32 s55, s81, 32768
v_accvgpr_read_b32 v232, a68
v_accvgpr_read_b32 v233, a69
v_mfma_f32_16x16x32_bf16 a[96:99], a[244:247], v[60:63], a[96:99]
s_waitcnt lgkmcnt(5)
v_accvgpr_read_b32 v234, a70
v_accvgpr_read_b32 v235, a71
v_accvgpr_read_b32 v236, a84
v_accvgpr_read_b32 v237, a85
v_accvgpr_read_b32 v238, a86
v_accvgpr_read_b32 v239, a87
v_mfma_f32_16x16x32_bf16 a[96:99], a[248:251], a[144:147], a[96:99]
ds_read_b64 v[166:167], v213
v_xor_b32 v212, 128, v211
v_mfma_f32_16x16x32_bf16 a[100:103], a[248:251], a[148:151], a[100:103]
v_cvt_pk_bf16_f32 v100, v232, v233
v_cvt_pk_bf16_f32 v101, v234, v235
v_cvt_pk_bf16_f32 v102, v236, v237
v_cvt_pk_bf16_f32 v103, v238, v239
v_mfma_f32_16x16x32_bf16 a[104:107], a[248:251], a[152:155], a[104:107]
ds_read_b64_tr_b16 a[244:245], v21 offset:9216
ds_read_b64_tr_b16 a[246:247], v21 offset:13312
buffer_store_dwordx4 v[76:79], v2, s[32:35], s55 offen offset:1024 sc1
ds_read_b64 v[168:169], v212
v_xor_b32 v213, 160, v211
v_mfma_f32_16x16x32_bf16 a[108:111], a[248:251], a[156:159], a[108:111]
s_waitcnt lgkmcnt(7)
ds_read_b64 v[170:171], v213
v_mfma_f32_16x16x32_bf16 a[108:111], a[252:255], a[172:175], a[108:111]
s_add_u32 s55, s81, 0
v_xor_b32 v212, 192, v211
v_xor_b32 v213, 224, v211
v_mfma_f32_16x16x32_bf16 a[104:107], a[252:255], a[168:171], a[104:107]
ds_read_b64_tr_b16 a[248:249], v25 offset:1024
v_accvgpr_read_b32 v224, a72
v_mfma_f32_16x16x32_bf16 a[100:103], a[252:255], a[164:167], a[100:103]
ds_read_b64_tr_b16 a[250:251], v25 offset:5120
buffer_store_dwordx4 v[100:103], v2, s[32:35], s55 offen offset:2048 sc1
ds_read_b64 v[172:173], v212
v_accvgpr_read_b32 v225, a73
v_mfma_f32_16x16x32_bf16 a[96:99], a[252:255], a[160:163], a[96:99]
s_waitcnt lgkmcnt(8)
v_accvgpr_read_b32 v226, a74
v_accvgpr_read_b32 v227, a75
v_accvgpr_read_b32 v228, a88
v_accvgpr_read_b32 v229, a89
v_accvgpr_read_b32 v230, a90
v_accvgpr_read_b32 v231, a91
v_mfma_f32_16x16x32_bf16 a[112:115], a[240:243], a[128:131], a[112:115]
ds_read_b64 v[174:175], v213
v_cvt_pk_bf16_f32 v104, v224, v225
v_mfma_f32_16x16x32_bf16 a[116:119], a[240:243], a[132:135], a[116:119]
s_add_u32 s55, s81, 16384
v_cvt_pk_bf16_f32 v105, v226, v227
v_cvt_pk_bf16_f32 v106, v228, v229
v_cvt_pk_bf16_f32 v107, v230, v231
v_mfma_f32_16x16x32_bf16 a[120:123], a[240:243], a[136:139], a[120:123]
ds_read_b64_tr_b16 a[252:253], v25 offset:9216
ds_read_b64_tr_b16 a[254:255], v25 offset:13312
v_accvgpr_read_b32 v232, a76
v_mfma_f32_16x16x32_bf16 a[124:127], a[240:243], a[140:143], a[124:127]
s_waitcnt lgkmcnt(8)
v_accvgpr_read_b32 v233, a77
v_accvgpr_read_b32 v234, a78
v_mfma_f32_16x16x32_bf16 a[124:127], a[244:247], v[156:159], a[124:127]
v_accvgpr_read_b32 v235, a79
v_accvgpr_read_b32 v236, a92
v_mfma_f32_16x16x32_bf16 a[120:123], a[244:247], v[124:127], a[120:123]
v_accvgpr_read_b32 v237, a93
v_accvgpr_read_b32 v238, a94
v_mfma_f32_16x16x32_bf16 a[116:119], a[244:247], v[92:95], a[116:119]
buffer_store_dwordx4 v[104:107], v2, s[32:35], s55 offen offset:2048 sc1
v_mfma_f32_16x16x32_bf16 a[112:115], a[244:247], v[60:63], a[112:115]
s_waitcnt lgkmcnt(4)
v_accvgpr_read_b32 v239, a95
v_cvt_pk_bf16_f32 v108, v232, v233
v_mfma_f32_16x16x32_bf16 a[112:115], a[248:251], a[144:147], a[112:115]
s_add_u32 s55, s81, 32768
v_exp_f32 v119, v119
v_exp_f32 v152, v152
v_exp_f32 v153, v153
v_exp_f32 v154, v154
v_exp_f32 v155, v155
v_exp_f32 v148, v148
v_exp_f32 v149, v149
v_exp_f32 v150, v150
v_exp_f32 v151, v151
v_mfma_f32_16x16x32_bf16 a[116:119], a[248:251], a[148:151], a[116:119]
v_cvt_pk_bf16_f32 v109, v234, v235
v_cvt_pk_bf16_f32 v110, v236, v237
v_cvt_pk_bf16_f32 v111, v238, v239
v_mfma_f32_16x16x32_bf16 a[120:123], a[248:251], a[152:155], a[120:123]
v_add_f32 v118, 1.0, v118
v_add_f32 v119, 1.0, v119
v_add_f32 v152, 1.0, v152
v_add_f32 v153, 1.0, v153
v_add_f32 v154, 1.0, v154
v_add_f32 v155, 1.0, v155
v_add_f32 v148, 1.0, v148
v_add_f32 v149, 1.0, v149
v_add_f32 v150, 1.0, v150
v_add_f32 v151, 1.0, v151
v_mfma_f32_16x16x32_bf16 a[124:127], a[248:251], a[156:159], a[124:127]
s_waitcnt lgkmcnt(0)
v_rcp_f32 v56, v56
v_mfma_f32_16x16x32_bf16 a[124:127], a[252:255], a[172:175], a[124:127]
v_rcp_f32 v57, v57
v_rcp_f32 v58, v58
v_rcp_f32 v59, v59
v_rcp_f32 v52, v52
v_rcp_f32 v53, v53
v_rcp_f32 v54, v54
v_rcp_f32 v55, v55
v_rcp_f32 v88, v88
v_rcp_f32 v89, v89
v_rcp_f32 v90, v90
v_rcp_f32 v91, v91
v_rcp_f32 v84, v84
v_rcp_f32 v85, v85
v_rcp_f32 v86, v86
v_rcp_f32 v87, v87
v_rcp_f32 v120, v120
v_rcp_f32 v121, v121
v_rcp_f32 v122, v122
v_rcp_f32 v123, v123
v_rcp_f32 v116, v116
v_rcp_f32 v117, v117
v_rcp_f32 v118, v118
v_rcp_f32 v119, v119
v_rcp_f32 v152, v152
v_rcp_f32 v153, v153
v_rcp_f32 v154, v154
v_rcp_f32 v155, v155
v_rcp_f32 v148, v148
v_rcp_f32 v149, v149
v_rcp_f32 v150, v150
v_rcp_f32 v151, v151
v_mfma_f32_16x16x32_bf16 a[120:123], a[252:255], a[168:171], a[120:123]
v_mul_f32 v32, v32, v56
v_mul_f32 v33, v33, v57
v_mfma_f32_16x16x32_bf16 a[116:119], a[252:255], a[164:167], a[116:119]
buffer_store_dwordx4 v[108:111], v2, s[32:35], s55 offen offset:2048 sc1
v_mfma_f32_16x16x32_bf16 a[112:115], a[252:255], a[160:163], a[112:115]
v_mul_f32 v34, v34, v58
v_mul_f32 v35, v35, v59
v_mul_f32 v48, v48, v52
v_mul_f32 v49, v49, v53
v_mul_f32 v50, v50, v54
v_mul_f32 v51, v51, v55
v_mul_f32 v64, v64, v88
v_mul_f32 v65, v65, v89
v_mul_f32 v66, v66, v90
v_mul_f32 v67, v67, v91
v_mul_f32 v80, v80, v84
v_mul_f32 v81, v81, v85
v_mul_f32 v82, v82, v86
v_mul_f32 v83, v83, v87
v_mul_f32 v96, v96, v120
v_mul_f32 v97, v97, v121
v_mul_f32 v98, v98, v122
v_mul_f32 v99, v99, v123
v_mul_f32 v112, v112, v116
v_mul_f32 v113, v113, v117
v_mul_f32 v114, v114, v118
v_mul_f32 v115, v115, v119
v_mul_f32 v128, v128, v152
v_mul_f32 v129, v129, v153
v_mul_f32 v130, v130, v154
v_mul_f32 v131, v131, v155
v_mul_f32 v144, v144, v148
v_mul_f32 v145, v145, v149
v_mul_f32 v146, v146, v150
v_mul_f32 v147, v147, v151
KF:
s_add_u32 s86, s81, 0
v_accvgpr_read_b32 v240, a100
v_accvgpr_read_b32 v241, a101
v_accvgpr_read_b32 v242, a102
v_accvgpr_read_b32 v243, a103
v_accvgpr_read_b32 v244, a116
v_accvgpr_read_b32 v245, a117
v_accvgpr_read_b32 v246, a118
v_accvgpr_read_b32 v247, a119
v_cvt_pk_bf16_f32 v132, v240, v241
v_cvt_pk_bf16_f32 v133, v242, v243
v_cvt_pk_bf16_f32 v134, v244, v245
v_cvt_pk_bf16_f32 v135, v246, v247
buffer_store_dwordx4 v[132:135], v2, s[32:35], s86 offen offset:3072 sc1
s_add_u32 s86, s81, 16384
v_accvgpr_read_b32 v232, a104
v_accvgpr_read_b32 v233, a105
v_accvgpr_read_b32 v234, a106
v_accvgpr_read_b32 v235, a107
v_accvgpr_read_b32 v236, a120
v_accvgpr_read_b32 v237, a121
v_accvgpr_read_b32 v238, a122
v_accvgpr_read_b32 v239, a123
v_cvt_pk_bf16_f32 v136, v232, v233
v_cvt_pk_bf16_f32 v137, v234, v235
v_cvt_pk_bf16_f32 v138, v236, v237
v_cvt_pk_bf16_f32 v139, v238, v239
buffer_store_dwordx4 v[136:139], v2, s[32:35], s86 offen offset:3072 sc1
s_add_u32 s86, s81, 32768
v_accvgpr_read_b32 v240, a108
v_accvgpr_read_b32 v241, a109
v_accvgpr_read_b32 v242, a110
v_accvgpr_read_b32 v243, a111
v_accvgpr_read_b32 v244, a124
v_accvgpr_read_b32 v245, a125
v_accvgpr_read_b32 v246, a126
v_accvgpr_read_b32 v247, a127
v_cvt_pk_bf16_f32 v140, v240, v241
v_cvt_pk_bf16_f32 v141, v242, v243
v_cvt_pk_bf16_f32 v142, v244, v245
v_cvt_pk_bf16_f32 v143, v246, v247
buffer_store_dwordx4 v[140:143], v2, s[32:35], s86 offen offset:3072 sc1
s_sub_u32 s51, s59, 1
s_and_b32 s51, s51, 3
s_lshl_b32 s51, s51, 3
s_lshl_b32 s97, s41, 5
s_add_u32 s51, s51, s97
s_add_u32 s51, s51, s60
s_mul_i32 s51, s51, 49152
s_lshl_b32 s97, s43, 12
s_add_u32 s51, s51, s97
s_add_u32 s51, s51, 0
s_sub_u32 s52, s59, 2
s_and_b32 s52, s52, 3
s_lshl_b32 s52, s52, 3
s_lshl_b32 s97, s41, 5
s_add_u32 s52, s52, s97
s_add_u32 s52, s52, s60
s_mul_i32 s52, s52, 49152
s_lshl_b32 s97, s43, 12
s_add_u32 s52, s52, s97
s_add_u32 s52, s52, 16384
s_sub_u32 s53, s59, 3
s_and_b32 s53, s53, 3
s_lshl_b32 s53, s53, 3
s_lshl_b32 s97, s41, 5
s_add_u32 s53, s53, s97
s_add_u32 s53, s53, s60
s_mul_i32 s53, s53, 49152
s_lshl_b32 s97, s43, 12
s_add_u32 s53, s53, s97
s_add_u32 s53, s53, 32768
s_lshl_b32 s97, s40, 2
s_add_u32 s97, s97, s43
s_lshl_b32 s97, s97, 3
s_add_u32 s97, s97, 32768
v_mov_b32 v30, s97
v_mov_b32 v212, s44
v_mov_b32 v213, s45
v_and_b32 v26, 3, v1
v_min_u32 v26, 2, v26
v_sub_u32 v26, s59, v26
v_add_u32 v26, 3, v26
v_and_b32 v26, 3, v26
v_lshlrev_b32 v26, 8, v26
s_lshl_b32 s97, s41, 5
s_add_u32 s97, s97, s60
s_lshl_b32 s97, s97, 2
s_add_u32 s97, s97, s43
s_lshl_b32 s97, s97, 3
s_add_u32 s97, s97, 32768
v_add_u32 v26, s97, v26
v_lshlrev_b32 v176, 16, v160
v_and_b32 v177, 0xffff0000, v160
v_lshlrev_b32 v178, 16, v161
v_and_b32 v179, 0xffff0000, v161
v_lshlrev_b32 v180, 16, v162
v_and_b32 v181, 0xffff0000, v162
v_lshlrev_b32 v182, 16, v163
v_and_b32 v183, 0xffff0000, v163
v_lshlrev_b32 v184, 16, v164
v_and_b32 v185, 0xffff0000, v164
v_lshlrev_b32 v186, 16, v165
v_and_b32 v187, 0xffff0000, v165
v_lshlrev_b32 v188, 16, v166
v_and_b32 v189, 0xffff0000, v166
v_lshlrev_b32 v190, 16, v167
v_and_b32 v191, 0xffff0000, v167
v_lshlrev_b32 v192, 16, v168
v_and_b32 v193, 0xffff0000, v168
v_lshlrev_b32 v194, 16, v169
v_and_b32 v195, 0xffff0000, v169
v_lshlrev_b32 v196, 16, v170
v_and_b32 v197, 0xffff0000, v170
v_lshlrev_b32 v198, 16, v171
v_and_b32 v199, 0xffff0000, v171
v_lshlrev_b32 v200, 16, v172
v_and_b32 v201, 0xffff0000, v172
v_lshlrev_b32 v202, 16, v173
v_and_b32 v203, 0xffff0000, v173
v_lshlrev_b32 v204, 16, v174
v_and_b32 v205, 0xffff0000, v174
v_lshlrev_b32 v206, 16, v175
v_and_b32 v207, 0xffff0000, v175
s_waitcnt vmcnt(0)
global_store_dwordx2 v30, v[212:213], s[22:23] sc1
s_mov_b32 s61, 0
PX_21:
global_load_dwordx2 v[28:29], v26, s[22:23] sc1
s_waitcnt vmcnt(0)
v_cmp_eq_u64 vcc, s[44:45], v[28:29]
s_nop 4
s_cmp_eq_u64 vcc, -1
s_cbranch_scc1 PX_21_x
s_add_u32 s61, s61, 1
s_cmp_lt_u32 s61, 0x40000
s_cbranch_scc1 PX_21
PX_21_x:
s_nop 0
s_add_u32 s24, s22, 62914560
s_addc_u32 s25, s23, 0
s_and_b32 s25, s25, 0xffff
s_mov_b32 s26, 12582912
s_mov_b32 s27, 0x20000
buffer_load_dwordx4 v[36:39], v2, s[24:27], s51 offen offset:0 sc1
buffer_load_dwordx4 v[68:71], v2, s[24:27], s51 offen offset:1024 sc1
buffer_load_dwordx4 v[100:103], v2, s[24:27], s51 offen offset:2048 sc1
buffer_load_dwordx4 v[132:135], v2, s[24:27], s51 offen offset:3072 sc1
buffer_load_dwordx4 v[40:43], v2, s[24:27], s52 offen offset:0 sc1
buffer_load_dwordx4 v[72:75], v2, s[24:27], s52 offen offset:1024 sc1
buffer_load_dwordx4 v[104:107], v2, s[24:27], s52 offen offset:2048 sc1
buffer_load_dwordx4 v[136:139], v2, s[24:27], s52 offen offset:3072 sc1
buffer_load_dwordx4 v[44:47], v2, s[24:27], s53 offen offset:0 sc1
buffer_load_dwordx4 v[76:79], v2, s[24:27], s53 offen offset:1024 sc1
buffer_load_dwordx4 v[108:111], v2, s[24:27], s53 offen offset:2048 sc1
buffer_load_dwordx4 v[140:143], v2, s[24:27], s53 offen offset:3072 sc1
s_mov_b32 s32, s20
s_mov_b32 s33, s21
s_and_b32 s33, s33, 0xffff
s_mov_b32 s34, 4194304
s_mov_b32 s35, 0x20000
s_waitcnt vmcnt(11)
v_mfma_f32_16x16x32_bf16 a[0:3], v[218:221], v[36:39], a[0:3]
v_mfma_f32_16x16x32_bf16 a[16:19], v[216:219], v[36:39], a[16:19]
s_waitcnt vmcnt(10)
v_mfma_f32_16x16x32_bf16 a[32:35], v[218:221], v[68:71], a[32:35]
v_mfma_f32_16x16x32_bf16 a[48:51], v[216:219], v[68:71], a[48:51]
s_waitcnt vmcnt(9)
v_mfma_f32_16x16x32_bf16 a[64:67], v[218:221], v[100:103], a[64:67]
v_mfma_f32_16x16x32_bf16 a[80:83], v[216:219], v[100:103], a[80:83]
s_waitcnt vmcnt(8)
v_mfma_f32_16x16x32_bf16 a[96:99], v[218:221], v[132:135], a[96:99]
v_mfma_f32_16x16x32_bf16 a[112:115], v[216:219], v[132:135], a[112:115]
s_waitcnt vmcnt(7)
v_mfma_f32_16x16x32_bf16 a[0:3], v[218:221], v[40:43], a[0:3]
v_mfma_f32_16x16x32_bf16 a[16:19], v[216:219], v[40:43], a[16:19]
s_waitcnt vmcnt(6)
v_mfma_f32_16x16x32_bf16 a[32:35], v[218:221], v[72:75], a[32:35]
v_mfma_f32_16x16x32_bf16 a[48:51], v[216:219], v[72:75], a[48:51]
s_waitcnt vmcnt(5)
v_mfma_f32_16x16x32_bf16 a[64:67], v[218:221], v[104:107], a[64:67]
v_mfma_f32_16x16x32_bf16 a[80:83], v[216:219], v[104:107], a[80:83]
s_waitcnt vmcnt(4)
v_mfma_f32_16x16x32_bf16 a[96:99], v[218:221], v[136:139], a[96:99]
v_mfma_f32_16x16x32_bf16 a[112:115], v[216:219], v[136:139], a[112:115]
s_waitcnt vmcnt(3)
v_mfma_f32_16x16x32_bf16 a[0:3], v[218:221], v[44:47], a[0:3]
v_mfma_f32_16x16x32_bf16 a[16:19], v[216:219], v[44:47], a[16:19]
s_waitcnt vmcnt(2)
v_mfma_f32_16x16x32_bf16 a[32:35], v[218:221], v[76:79], a[32:35]
v_mfma_f32_16x16x32_bf16 a[48:51], v[216:219], v[76:79], a[48:51]
s_waitcnt vmcnt(1)
v_mfma_f32_16x16x32_bf16 a[64:67], v[218:221], v[108:111], a[64:67]
v_mfma_f32_16x16x32_bf16 a[80:83], v[216:219], v[108:111], a[80:83]
s_waitcnt vmcnt(0)
v_mfma_f32_16x16x32_bf16 a[96:99], v[218:221], v[140:143], a[96:99]
v_mfma_f32_16x16x32_bf16 a[112:115], v[216:219], v[140:143], a[112:115]
v_accvgpr_read_b32 v224, a0
v_accvgpr_read_b32 v225, a1
v_accvgpr_read_b32 v226, a2
v_accvgpr_read_b32 v227, a3
v_pk_fma_f32 v[176:177], v[224:225], v[32:33], v[176:177]
v_pk_fma_f32 v[178:179], v[226:227], v[34:35], v[178:179]
v_cvt_pk_bf16_f32 v208, v176, v177
v_cvt_pk_bf16_f32 v209, v178, v179
buffer_store_dwordx2 v[208:209], v3, s[32:35], s82 offen offset:0
v_accvgpr_read_b32 v228, a16
v_accvgpr_read_b32 v229, a17
v_accvgpr_read_b32 v230, a18
v_accvgpr_read_b32 v231, a19
v_pk_fma_f32 v[180:181], v[228:229], v[48:49], v[180:181]
v_pk_fma_f32 v[182:183], v[230:231], v[50:51], v[182:183]
v_cvt_pk_bf16_f32 v210, v180, v181
v_cvt_pk_bf16_f32 v211, v182, v183
buffer_store_dwordx2 v[210:211], v3, s[32:35], s82 offen offset:32
v_accvgpr_read_b32 v224, a32
v_accvgpr_read_b32 v225, a33
v_accvgpr_read_b32 v226, a34
v_accvgpr_read_b32 v227, a35
v_pk_fma_f32 v[184:185], v[224:225], v[64:65], v[184:185]
v_pk_fma_f32 v[186:187], v[226:227], v[66:67], v[186:187]
v_cvt_pk_bf16_f32 v208, v184, v185
v_cvt_pk_bf16_f32 v209, v186, v187
buffer_store_dwordx2 v[208:209], v3, s[32:35], s82 offen offset:64
v_accvgpr_read_b32 v228, a48
v_accvgpr_read_b32 v229, a49
v_accvgpr_read_b32 v230, a50
v_accvgpr_read_b32 v231, a51
v_pk_fma_f32 v[188:189], v[228:229], v[80:81], v[188:189]
v_pk_fma_f32 v[190:191], v[230:231], v[82:83], v[190:191]
v_cvt_pk_bf16_f32 v210, v188, v189
v_cvt_pk_bf16_f32 v211, v190, v191
buffer_store_dwordx2 v[210:211], v3, s[32:35], s82 offen offset:96
v_accvgpr_read_b32 v224, a64
v_accvgpr_read_b32 v225, a65
v_accvgpr_read_b32 v226, a66
v_accvgpr_read_b32 v227, a67
v_pk_fma_f32 v[192:193], v[224:225], v[96:97], v[192:193]
v_pk_fma_f32 v[194:195], v[226:227], v[98:99], v[194:195]
v_cvt_pk_bf16_f32 v208, v192, v193
v_cvt_pk_bf16_f32 v209, v194, v195
buffer_store_dwordx2 v[208:209], v3, s[32:35], s82 offen offset:128
v_accvgpr_read_b32 v228, a80
v_accvgpr_read_b32 v229, a81
v_accvgpr_read_b32 v230, a82
v_accvgpr_read_b32 v231, a83
v_pk_fma_f32 v[196:197], v[228:229], v[112:113], v[196:197]
v_pk_fma_f32 v[198:199], v[230:231], v[114:115], v[198:199]
v_cvt_pk_bf16_f32 v210, v196, v197
v_cvt_pk_bf16_f32 v211, v198, v199
buffer_store_dwordx2 v[210:211], v3, s[32:35], s82 offen offset:160
v_accvgpr_read_b32 v224, a96
v_accvgpr_read_b32 v225, a97
v_accvgpr_read_b32 v226, a98
v_accvgpr_read_b32 v227, a99
v_pk_fma_f32 v[200:201], v[224:225], v[128:129], v[200:201]
v_pk_fma_f32 v[202:203], v[226:227], v[130:131], v[202:203]
v_cvt_pk_bf16_f32 v208, v200, v201
v_cvt_pk_bf16_f32 v209, v202, v203
buffer_store_dwordx2 v[208:209], v3, s[32:35], s82 offen offset:192
v_accvgpr_read_b32 v228, a112
v_accvgpr_read_b32 v229, a113
v_accvgpr_read_b32 v230, a114
v_accvgpr_read_b32 v231, a115
v_pk_fma_f32 v[204:205], v[228:229], v[144:145], v[204:205]
v_pk_fma_f32 v[206:207], v[230:231], v[146:147], v[206:207]
v_cvt_pk_bf16_f32 v210, v204, v205
v_cvt_pk_bf16_f32 v211, v206, v207
buffer_store_dwordx2 v[210:211], v3, s[32:35], s82 offen offset:224
s_nop 7
s_nop 7
s_waitcnt vmcnt(0)
s_or_b32 s51, s40, s43
s_cmp_lg_u32 s51, 0
s_cbranch_scc1 EP_22
v_mov_b32 v29, 0
v_mov_b32 v30, s44
v_mov_b32 v31, s45
s_mov_b64 exec, 1
s_nop 1
global_store_dwordx2 v29, v[30:31], s[22:23] sc1
s_waitcnt vmcnt(0)
EP_22:
s_endpgm
DP_16_s:
s_mov_b32 s61, 0
DP_16_s_l:
s_sleep 1
global_load_dwordx2 v[28:29], v26, s[22:23] sc1
s_waitcnt vmcnt(0)
v_cmp_eq_u64 vcc, s[44:45], v[28:29]
s_nop 4
s_cmp_eq_u64 vcc, -1
s_cbranch_scc1 DP_16_b
s_add_u32 s61, s61, 1
s_cmp_lt_u32 s61, 0x20000
s_cbranch_scc1 DP_16_s_l
s_branch DP_16_b
SMF_18:
s_waitcnt vmcnt(0)
s_nop 7
s_nop 7
s_lshr_b32 s51, s42, 3
s_lshl_b32 s51, s51, 18
s_mov_b32 s46, 8
v_accvgpr_write_b32 a0, 0
v_accvgpr_write_b32 a1, 0
v_accvgpr_write_b32 a2, 0
v_accvgpr_write_b32 a3, 0
v_accvgpr_write_b32 a4, 0
v_accvgpr_write_b32 a5, 0
v_accvgpr_write_b32 a6, 0
v_accvgpr_write_b32 a7, 0
v_accvgpr_write_b32 a8, 0
v_accvgpr_write_b32 a9, 0
v_accvgpr_write_b32 a10, 0
v_accvgpr_write_b32 a11, 0
v_accvgpr_write_b32 a12, 0
v_accvgpr_write_b32 a13, 0
v_accvgpr_write_b32 a14, 0
v_accvgpr_write_b32 a15, 0
v_accvgpr_write_b32 a16, 0
v_accvgpr_write_b32 a17, 0
v_accvgpr_write_b32 a18, 0
v_accvgpr_write_b32 a19, 0
v_accvgpr_write_b32 a20, 0
v_accvgpr_write_b32 a21, 0
v_accvgpr_write_b32 a22, 0
v_accvgpr_write_b32 a23, 0
v_accvgpr_write_b32 a24, 0
v_accvgpr_write_b32 a25, 0
v_accvgpr_write_b32 a26, 0
v_accvgpr_write_b32 a27, 0
v_accvgpr_write_b32 a28, 0
v_accvgpr_write_b32 a29, 0
v_accvgpr_write_b32 a30, 0
v_accvgpr_write_b32 a31, 0
v_accvgpr_write_b32 a32, 0
v_accvgpr_write_b32 a33, 0
v_accvgpr_write_b32 a34, 0
v_accvgpr_write_b32 a35, 0
v_accvgpr_write_b32 a36, 0
v_accvgpr_write_b32 a37, 0
v_accvgpr_write_b32 a38, 0
v_accvgpr_write_b32 a39, 0
v_accvgpr_write_b32 a40, 0
v_accvgpr_write_b32 a41, 0
v_accvgpr_write_b32 a42, 0
v_accvgpr_write_b32 a43, 0
v_accvgpr_write_b32 a44, 0
v_accvgpr_write_b32 a45, 0
v_accvgpr_write_b32 a46, 0
v_accvgpr_write_b32 a47, 0
v_accvgpr_write_b32 a48, 0
v_accvgpr_write_b32 a49, 0
v_accvgpr_write_b32 a50, 0
v_accvgpr_write_b32 a51, 0
v_accvgpr_write_b32 a52, 0
v_accvgpr_write_b32 a53, 0
v_accvgpr_write_b32 a54, 0
v_accvgpr_write_b32 a55, 0
v_accvgpr_write_b32 a56, 0
v_accvgpr_write_b32 a57, 0
v_accvgpr_write_b32 a58, 0
v_accvgpr_write_b32 a59, 0
v_accvgpr_write_b32 a60, 0
v_accvgpr_write_b32 a61, 0
v_accvgpr_write_b32 a62, 0
v_accvgpr_write_b32 a63, 0
v_accvgpr_write_b32 a64, 0
v_accvgpr_write_b32 a65, 0
v_accvgpr_write_b32 a66, 0
v_accvgpr_write_b32 a67, 0
v_accvgpr_write_b32 a68, 0
v_accvgpr_write_b32 a69, 0
v_accvgpr_write_b32 a70, 0
v_accvgpr_write_b32 a71, 0
v_accvgpr_write_b32 a72, 0
v_accvgpr_write_b32 a73, 0
v_accvgpr_write_b32 a74, 0
v_accvgpr_write_b32 a75, 0
v_accvgpr_write_b32 a76, 0
v_accvgpr_write_b32 a77, 0
v_accvgpr_write_b32 a78, 0
v_accvgpr_write_b32 a79, 0
v_accvgpr_write_b32 a80, 0
v_accvgpr_write_b32 a81, 0
v_accvgpr_write_b32 a82, 0
v_accvgpr_write_b32 a83, 0
v_accvgpr_write_b32 a84, 0
v_accvgpr_write_b32 a85, 0
v_accvgpr_write_b32 a86, 0
v_accvgpr_write_b32 a87, 0
v_accvgpr_write_b32 a88, 0
v_accvgpr_write_b32 a89, 0
v_accvgpr_write_b32 a90, 0
v_accvgpr_write_b32 a91, 0
v_accvgpr_write_b32 a92, 0
v_accvgpr_write_b32 a93, 0
v_accvgpr_write_b32 a94, 0
v_accvgpr_write_b32 a95, 0
v_accvgpr_write_b32 a96, 0
v_accvgpr_write_b32 a97, 0
v_accvgpr_write_b32 a98, 0
v_accvgpr_write_b32 a99, 0
v_accvgpr_write_b32 a100, 0
v_accvgpr_write_b32 a101, 0
v_accvgpr_write_b32 a102, 0
v_accvgpr_write_b32 a103, 0
v_accvgpr_write_b32 a104, 0
v_accvgpr_write_b32 a105, 0
v_accvgpr_write_b32 a106, 0
v_accvgpr_write_b32 a107, 0
v_accvgpr_write_b32 a108, 0
v_accvgpr_write_b32 a109, 0
v_accvgpr_write_b32 a110, 0
v_accvgpr_write_b32 a111, 0
v_accvgpr_write_b32 a112, 0
v_accvgpr_write_b32 a113, 0
v_accvgpr_write_b32 a114, 0
v_accvgpr_write_b32 a115, 0
v_accvgpr_write_b32 a116, 0
v_accvgpr_write_b32 a117, 0
v_accvgpr_write_b32 a118, 0
v_accvgpr_write_b32 a119, 0
v_accvgpr_write_b32 a120, 0
v_accvgpr_write_b32 a121, 0
v_accvgpr_write_b32 a122, 0
v_accvgpr_write_b32 a123, 0
v_accvgpr_write_b32 a124, 0
v_accvgpr_write_b32 a125, 0
v_accvgpr_write_b32 a126, 0
v_accvgpr_write_b32 a127, 0
v_mov_b32 v192, 0xff800000
v_mov_b32 v208, 0
v_mov_b32 v209, 0
v_mov_b32 v193, 0xff800000
v_mov_b32 v210, 0
v_mov_b32 v211, 0
v_mov_b32 v194, 0xff800000
v_mov_b32 v212, 0
v_mov_b32 v213, 0
v_mov_b32 v195, 0xff800000
v_mov_b32 v214, 0
v_mov_b32 v215, 0
SMF_18_l:
s_add_u32 s52, s51, 0
s_add_u32 s53, s52, 4096
buffer_load_dwordx4 a[128:131], v2, s[72:75], s52 offen offset:0
buffer_load_dwordx4 a[132:135], v2, s[72:75], s52 offen offset:1024
buffer_load_dwordx4 a[136:139], v2, s[72:75], s52 offen offset:2048
buffer_load_dwordx4 a[140:143], v2, s[72:75], s52 offen offset:3072
buffer_load_dwordx4 a[144:147], v2, s[72:75], s53 offen offset:0
buffer_load_dwordx4 a[148:151], v2, s[72:75], s53 offen offset:1024
buffer_load_dwordx4 a[152:155], v2, s[72:75], s53 offen offset:2048
buffer_load_dwordx4 a[156:159], v2, s[72:75], s53 offen offset:3072
s_waitcnt vmcnt(0)
v_mfma_f32_16x16x32_bf16 v[96:99], a[128:131], v[32:35], 0
v_mfma_f32_16x16x32_bf16 v[100:103], a[128:131], v[36:39], 0
v_mfma_f32_16x16x32_bf16 v[104:107], a[128:131], v[40:43], 0
v_mfma_f32_16x16x32_bf16 v[108:111], a[128:131], v[44:47], 0
v_mfma_f32_16x16x32_bf16 v[96:99], a[132:135], v[48:51], v[96:99]
v_mfma_f32_16x16x32_bf16 v[100:103], a[132:135], v[52:55], v[100:103]
v_mfma_f32_16x16x32_bf16 v[104:107], a[132:135], v[56:59], v[104:107]
v_mfma_f32_16x16x32_bf16 v[108:111], a[132:135], v[60:63], v[108:111]
v_mfma_f32_16x16x32_bf16 v[96:99], a[136:139], v[64:67], v[96:99]
v_mfma_f32_16x16x32_bf16 v[100:103], a[136:139], v[68:71], v[100:103]
v_mfma_f32_16x16x32_bf16 v[104:107], a[136:139], v[72:75], v[104:107]
v_mfma_f32_16x16x32_bf16 v[108:111], a[136:139], v[76:79], v[108:111]
v_mfma_f32_16x16x32_bf16 v[96:99], a[140:143], v[80:83], v[96:99]
v_mfma_f32_16x16x32_bf16 v[100:103], a[140:143], v[84:87], v[100:103]
v_mfma_f32_16x16x32_bf16 v[104:107], a[140:143], v[88:91], v[104:107]
v_mfma_f32_16x16x32_bf16 v[108:111], a[140:143], v[92:95], v[108:111]
v_mfma_f32_16x16x32_bf16 v[112:115], a[144:147], v[32:35], 0
v_mfma_f32_16x16x32_bf16 v[116:119], a[144:147], v[36:39], 0
v_mfma_f32_16x16x32_bf16 v[120:123], a[144:147], v[40:43], 0
v_mfma_f32_16x16x32_bf16 v[124:127], a[144:147], v[44:47], 0
v_mfma_f32_16x16x32_bf16 v[112:115], a[148:151], v[48:51], v[112:115]
v_mfma_f32_16x16x32_bf16 v[116:119], a[148:151], v[52:55], v[116:119]
v_mfma_f32_16x16x32_bf16 v[120:123], a[148:151], v[56:59], v[120:123]
v_mfma_f32_16x16x32_bf16 v[124:127], a[148:151], v[60:63], v[124:127]
v_mfma_f32_16x16x32_bf16 v[112:115], a[152:155], v[64:67], v[112:115]
v_mfma_f32_16x16x32_bf16 v[116:119], a[152:155], v[68:71], v[116:119]
v_mfma_f32_16x16x32_bf16 v[120:123], a[152:155], v[72:75], v[120:123]
v_mfma_f32_16x16x32_bf16 v[124:127], a[152:155], v[76:79], v[124:127]
v_mfma_f32_16x16x32_bf16 v[112:115], a[156:159], v[80:83], v[112:115]
v_mfma_f32_16x16x32_bf16 v[116:119], a[156:159], v[84:87], v[116:119]
v_mfma_f32_16x16x32_bf16 v[120:123], a[156:159], v[88:91], v[120:123]
v_mfma_f32_16x16x32_bf16 v[124:127], a[156:159], v[92:95], v[124:127]
s_add_u32 s52, s51, 8192
s_add_u32 s53, s52, 4096
buffer_load_dwordx4 a[128:131], v2, s[72:75], s52 offen offset:0
buffer_load_dwordx4 a[132:135], v2, s[72:75], s52 offen offset:1024
buffer_load_dwordx4 a[136:139], v2, s[72:75], s52 offen offset:2048
buffer_load_dwordx4 a[140:143], v2, s[72:75], s52 offen offset:3072
buffer_load_dwordx4 a[144:147], v2, s[72:75], s53 offen offset:0
buffer_load_dwordx4 a[148:151], v2, s[72:75], s53 offen offset:1024
buffer_load_dwordx4 a[152:155], v2, s[72:75], s53 offen offset:2048
buffer_load_dwordx4 a[156:159], v2, s[72:75], s53 offen offset:3072
s_waitcnt vmcnt(0)
v_mfma_f32_16x16x32_bf16 v[128:131], a[128:131], v[32:35], 0
v_mfma_f32_16x16x32_bf16 v[132:135], a[128:131], v[36:39], 0
v_mfma_f32_16x16x32_bf16 v[136:139], a[128:131], v[40:43], 0
v_mfma_f32_16x16x32_bf16 v[140:143], a[128:131], v[44:47], 0
v_mfma_f32_16x16x32_bf16 v[128:131], a[132:135], v[48:51], v[128:131]
v_mfma_f32_16x16x32_bf16 v[132:135], a[132:135], v[52:55], v[132:135]
v_mfma_f32_16x16x32_bf16 v[136:139], a[132:135], v[56:59], v[136:139]
v_mfma_f32_16x16x32_bf16 v[140:143], a[132:135], v[60:63], v[140:143]
v_mfma_f32_16x16x32_bf16 v[128:131], a[136:139], v[64:67], v[128:131]
v_mfma_f32_16x16x32_bf16 v[132:135], a[136:139], v[68:71], v[132:135]
v_mfma_f32_16x16x32_bf16 v[136:139], a[136:139], v[72:75], v[136:139]
v_mfma_f32_16x16x32_bf16 v[140:143], a[136:139], v[76:79], v[140:143]
v_mfma_f32_16x16x32_bf16 v[128:131], a[140:143], v[80:83], v[128:131]
v_mfma_f32_16x16x32_bf16 v[132:135], a[140:143], v[84:87], v[132:135]
v_mfma_f32_16x16x32_bf16 v[136:139], a[140:143], v[88:91], v[136:139]
v_mfma_f32_16x16x32_bf16 v[140:143], a[140:143], v[92:95], v[140:143]
v_mfma_f32_16x16x32_bf16 v[144:147], a[144:147], v[32:35], 0
v_mfma_f32_16x16x32_bf16 v[148:151], a[144:147], v[36:39], 0
v_mfma_f32_16x16x32_bf16 v[152:155], a[144:147], v[40:43], 0
v_mfma_f32_16x16x32_bf16 v[156:159], a[144:147], v[44:47], 0
v_mfma_f32_16x16x32_bf16 v[144:147], a[148:151], v[48:51], v[144:147]
v_mfma_f32_16x16x32_bf16 v[148:151], a[148:151], v[52:55], v[148:151]
v_mfma_f32_16x16x32_bf16 v[152:155], a[148:151], v[56:59], v[152:155]
v_mfma_f32_16x16x32_bf16 v[156:159], a[148:151], v[60:63], v[156:159]
v_mfma_f32_16x16x32_bf16 v[144:147], a[152:155], v[64:67], v[144:147]
v_mfma_f32_16x16x32_bf16 v[148:151], a[152:155], v[68:71], v[148:151]
v_mfma_f32_16x16x32_bf16 v[152:155], a[152:155], v[72:75], v[152:155]
v_mfma_f32_16x16x32_bf16 v[156:159], a[152:155], v[76:79], v[156:159]
v_mfma_f32_16x16x32_bf16 v[144:147], a[156:159], v[80:83], v[144:147]
v_mfma_f32_16x16x32_bf16 v[148:151], a[156:159], v[84:87], v[148:151]
v_mfma_f32_16x16x32_bf16 v[152:155], a[156:159], v[88:91], v[152:155]
v_mfma_f32_16x16x32_bf16 v[156:159], a[156:159], v[92:95], v[156:159]
s_nop 7
s_nop 7
v_max3_f32 v200, v96, v97, v192
v_max3_f32 v200, v98, v99, v200
v_max3_f32 v200, v112, v113, v200
v_max3_f32 v200, v114, v115, v200
v_max3_f32 v200, v128, v129, v200
v_max3_f32 v200, v130, v131, v200
v_max3_f32 v200, v144, v145, v200
v_max3_f32 v200, v146, v147, v200
v_mov_b32 v204, v200
s_nop 1
v_permlane16_swap_b32 v200, v204
s_nop 1
v_max_f32 v200, v200, v204
v_mov_b32 v204, v200
s_nop 1
v_permlane32_swap_b32 v200, v204
s_nop 1
v_max_f32 v200, v200, v204
v_sub_f32 v196, v192, v200
v_exp_f32 v196, v196
v_mov_b32 v192, v200
s_nop 0
v_mul_f32 v208, v208, v196
v_sub_f32 v96, v96, v200
v_sub_f32 v97, v97, v200
v_sub_f32 v98, v98, v200
v_sub_f32 v99, v99, v200
v_sub_f32 v112, v112, v200
v_sub_f32 v113, v113, v200
v_sub_f32 v114, v114, v200
v_sub_f32 v115, v115, v200
v_sub_f32 v128, v128, v200
v_sub_f32 v129, v129, v200
v_sub_f32 v130, v130, v200
v_sub_f32 v131, v131, v200
v_sub_f32 v144, v144, v200
v_sub_f32 v145, v145, v200
v_sub_f32 v146, v146, v200
v_sub_f32 v147, v147, v200
v_exp_f32 v96, v96
v_exp_f32 v97, v97
v_exp_f32 v98, v98
v_exp_f32 v99, v99
v_exp_f32 v112, v112
v_exp_f32 v113, v113
v_exp_f32 v114, v114
v_exp_f32 v115, v115
v_exp_f32 v128, v128
v_exp_f32 v129, v129
v_exp_f32 v130, v130
v_exp_f32 v131, v131
v_exp_f32 v144, v144
v_exp_f32 v145, v145
v_exp_f32 v146, v146
v_exp_f32 v147, v147
s_nop 0
v_add_f32 v208, v208, v96
v_add_f32 v208, v208, v97
v_add_f32 v208, v208, v98
v_add_f32 v208, v208, v99
v_add_f32 v208, v208, v112
v_add_f32 v208, v208, v113
v_add_f32 v208, v208, v114
v_add_f32 v208, v208, v115
v_add_f32 v208, v208, v128
v_add_f32 v208, v208, v129
v_add_f32 v208, v208, v130
v_add_f32 v208, v208, v131
v_add_f32 v208, v208, v144
v_add_f32 v208, v208, v145
v_add_f32 v208, v208, v146
v_add_f32 v208, v208, v147
v_cvt_pk_bf16_f32 v160, v96, v97
v_cvt_pk_bf16_f32 v161, v98, v99
v_cvt_pk_bf16_f32 v162, v112, v113
v_cvt_pk_bf16_f32 v163, v114, v115
v_cvt_pk_bf16_f32 v176, v128, v129
v_cvt_pk_bf16_f32 v177, v130, v131
v_cvt_pk_bf16_f32 v178, v144, v145
v_cvt_pk_bf16_f32 v179, v146, v147
v_accvgpr_read_b32 v204, a0
v_mul_f32 v204, v204, v196
v_accvgpr_write_b32 a0, v204
v_accvgpr_read_b32 v204, a1
v_mul_f32 v204, v204, v196
v_accvgpr_write_b32 a1, v204
v_accvgpr_read_b32 v204, a2
v_mul_f32 v204, v204, v196
v_accvgpr_write_b32 a2, v204
v_accvgpr_read_b32 v204, a3
v_mul_f32 v204, v204, v196
v_accvgpr_write_b32 a3, v204
v_accvgpr_read_b32 v204, a16
v_mul_f32 v204, v204, v196
v_accvgpr_write_b32 a16, v204
v_accvgpr_read_b32 v204, a17
v_mul_f32 v204, v204, v196
v_accvgpr_write_b32 a17, v204
v_accvgpr_read_b32 v204, a18
v_mul_f32 v204, v204, v196
v_accvgpr_write_b32 a18, v204
v_accvgpr_read_b32 v204, a19
v_mul_f32 v204, v204, v196
v_accvgpr_write_b32 a19, v204
v_accvgpr_read_b32 v204, a32
v_mul_f32 v204, v204, v196
v_accvgpr_write_b32 a32, v204
v_accvgpr_read_b32 v204, a33
v_mul_f32 v204, v204, v196
v_accvgpr_write_b32 a33, v204
v_accvgpr_read_b32 v204, a34
v_mul_f32 v204, v204, v196
v_accvgpr_write_b32 a34, v204
v_accvgpr_read_b32 v204, a35
v_mul_f32 v204, v204, v196
v_accvgpr_write_b32 a35, v204
v_accvgpr_read_b32 v204, a48
v_mul_f32 v204, v204, v196
v_accvgpr_write_b32 a48, v204
v_accvgpr_read_b32 v204, a49
v_mul_f32 v204, v204, v196
v_accvgpr_write_b32 a49, v204
v_accvgpr_read_b32 v204, a50
v_mul_f32 v204, v204, v196
v_accvgpr_write_b32 a50, v204
v_accvgpr_read_b32 v204, a51
v_mul_f32 v204, v204, v196
v_accvgpr_write_b32 a51, v204
v_accvgpr_read_b32 v204, a64
v_mul_f32 v204, v204, v196
v_accvgpr_write_b32 a64, v204
v_accvgpr_read_b32 v204, a65
v_mul_f32 v204, v204, v196
v_accvgpr_write_b32 a65, v204
v_accvgpr_read_b32 v204, a66
v_mul_f32 v204, v204, v196
v_accvgpr_write_b32 a66, v204
v_accvgpr_read_b32 v204, a67
v_mul_f32 v204, v204, v196
v_accvgpr_write_b32 a67, v204
v_accvgpr_read_b32 v204, a80
v_mul_f32 v204, v204, v196
v_accvgpr_write_b32 a80, v204
v_accvgpr_read_b32 v204, a81
v_mul_f32 v204, v204, v196
v_accvgpr_write_b32 a81, v204
v_accvgpr_read_b32 v204, a82
v_mul_f32 v204, v204, v196
v_accvgpr_write_b32 a82, v204
v_accvgpr_read_b32 v204, a83
v_mul_f32 v204, v204, v196
v_accvgpr_write_b32 a83, v204
v_accvgpr_read_b32 v204, a96
v_mul_f32 v204, v204, v196
v_accvgpr_write_b32 a96, v204
v_accvgpr_read_b32 v204, a97
v_mul_f32 v204, v204, v196
v_accvgpr_write_b32 a97, v204
v_accvgpr_read_b32 v204, a98
v_mul_f32 v204, v204, v196
v_accvgpr_write_b32 a98, v204
v_accvgpr_read_b32 v204, a99
v_mul_f32 v204, v204, v196
v_accvgpr_write_b32 a99, v204
v_accvgpr_read_b32 v204, a112
v_mul_f32 v204, v204, v196
v_accvgpr_write_b32 a112, v204
v_accvgpr_read_b32 v204, a113
v_mul_f32 v204, v204, v196
v_accvgpr_write_b32 a113, v204
v_accvgpr_read_b32 v204, a114
v_mul_f32 v204, v204, v196
v_accvgpr_write_b32 a114, v204
v_accvgpr_read_b32 v204, a115
v_mul_f32 v204, v204, v196
v_accvgpr_write_b32 a115, v204
v_max3_f32 v201, v100, v101, v193
v_max3_f32 v201, v102, v103, v201
v_max3_f32 v201, v116, v117, v201
v_max3_f32 v201, v118, v119, v201
v_max3_f32 v201, v132, v133, v201
v_max3_f32 v201, v134, v135, v201
v_max3_f32 v201, v148, v149, v201
v_max3_f32 v201, v150, v151, v201
v_mov_b32 v205, v201
s_nop 1
v_permlane16_swap_b32 v201, v205
s_nop 1
v_max_f32 v201, v201, v205
v_mov_b32 v205, v201
s_nop 1
v_permlane32_swap_b32 v201, v205
s_nop 1
v_max_f32 v201, v201, v205
v_sub_f32 v197, v193, v201
v_exp_f32 v197, v197
v_mov_b32 v193, v201
s_nop 0
v_mul_f32 v210, v210, v197
v_sub_f32 v100, v100, v201
v_sub_f32 v101, v101, v201
v_sub_f32 v102, v102, v201
v_sub_f32 v103, v103, v201
v_sub_f32 v116, v116, v201
v_sub_f32 v117, v117, v201
v_sub_f32 v118, v118, v201
v_sub_f32 v119, v119, v201
v_sub_f32 v132, v132, v201
v_sub_f32 v133, v133, v201
v_sub_f32 v134, v134, v201
v_sub_f32 v135, v135, v201
v_sub_f32 v148, v148, v201
v_sub_f32 v149, v149, v201
v_sub_f32 v150, v150, v201
v_sub_f32 v151, v151, v201
v_exp_f32 v100, v100
v_exp_f32 v101, v101
v_exp_f32 v102, v102
v_exp_f32 v103, v103
v_exp_f32 v116, v116
v_exp_f32 v117, v117
v_exp_f32 v118, v118
v_exp_f32 v119, v119
v_exp_f32 v132, v132
v_exp_f32 v133, v133
v_exp_f32 v134, v134
v_exp_f32 v135, v135
v_exp_f32 v148, v148
v_exp_f32 v149, v149
v_exp_f32 v150, v150
v_exp_f32 v151, v151
s_nop 0
v_add_f32 v210, v210, v100
v_add_f32 v210, v210, v101
v_add_f32 v210, v210, v102
v_add_f32 v210, v210, v103
v_add_f32 v210, v210, v116
v_add_f32 v210, v210, v117
v_add_f32 v210, v210, v118
v_add_f32 v210, v210, v119
v_add_f32 v210, v210, v132
v_add_f32 v210, v210, v133
v_add_f32 v210, v210, v134
v_add_f32 v210, v210, v135
v_add_f32 v210, v210, v148
v_add_f32 v210, v210, v149
v_add_f32 v210, v210, v150
v_add_f32 v210, v210, v151
v_cvt_pk_bf16_f32 v164, v100, v101
v_cvt_pk_bf16_f32 v165, v102, v103
v_cvt_pk_bf16_f32 v166, v116, v117
v_cvt_pk_bf16_f32 v167, v118, v119
v_cvt_pk_bf16_f32 v180, v132, v133
v_cvt_pk_bf16_f32 v181, v134, v135
v_cvt_pk_bf16_f32 v182, v148, v149
v_cvt_pk_bf16_f32 v183, v150, v151
v_accvgpr_read_b32 v205, a4
v_mul_f32 v205, v205, v197
v_accvgpr_write_b32 a4, v205
v_accvgpr_read_b32 v205, a5
v_mul_f32 v205, v205, v197
v_accvgpr_write_b32 a5, v205
v_accvgpr_read_b32 v205, a6
v_mul_f32 v205, v205, v197
v_accvgpr_write_b32 a6, v205
v_accvgpr_read_b32 v205, a7
v_mul_f32 v205, v205, v197
v_accvgpr_write_b32 a7, v205
v_accvgpr_read_b32 v205, a20
v_mul_f32 v205, v205, v197
v_accvgpr_write_b32 a20, v205
v_accvgpr_read_b32 v205, a21
v_mul_f32 v205, v205, v197
v_accvgpr_write_b32 a21, v205
v_accvgpr_read_b32 v205, a22
v_mul_f32 v205, v205, v197
v_accvgpr_write_b32 a22, v205
v_accvgpr_read_b32 v205, a23
v_mul_f32 v205, v205, v197
v_accvgpr_write_b32 a23, v205
v_accvgpr_read_b32 v205, a36
v_mul_f32 v205, v205, v197
v_accvgpr_write_b32 a36, v205
v_accvgpr_read_b32 v205, a37
v_mul_f32 v205, v205, v197
v_accvgpr_write_b32 a37, v205
v_accvgpr_read_b32 v205, a38
v_mul_f32 v205, v205, v197
v_accvgpr_write_b32 a38, v205
v_accvgpr_read_b32 v205, a39
v_mul_f32 v205, v205, v197
v_accvgpr_write_b32 a39, v205
v_accvgpr_read_b32 v205, a52
v_mul_f32 v205, v205, v197
v_accvgpr_write_b32 a52, v205
v_accvgpr_read_b32 v205, a53
v_mul_f32 v205, v205, v197
v_accvgpr_write_b32 a53, v205
v_accvgpr_read_b32 v205, a54
v_mul_f32 v205, v205, v197
v_accvgpr_write_b32 a54, v205
v_accvgpr_read_b32 v205, a55
v_mul_f32 v205, v205, v197
v_accvgpr_write_b32 a55, v205
v_accvgpr_read_b32 v205, a68
v_mul_f32 v205, v205, v197
v_accvgpr_write_b32 a68, v205
v_accvgpr_read_b32 v205, a69
v_mul_f32 v205, v205, v197
v_accvgpr_write_b32 a69, v205
v_accvgpr_read_b32 v205, a70
v_mul_f32 v205, v205, v197
v_accvgpr_write_b32 a70, v205
v_accvgpr_read_b32 v205, a71
v_mul_f32 v205, v205, v197
v_accvgpr_write_b32 a71, v205
v_accvgpr_read_b32 v205, a84
v_mul_f32 v205, v205, v197
v_accvgpr_write_b32 a84, v205
v_accvgpr_read_b32 v205, a85
v_mul_f32 v205, v205, v197
v_accvgpr_write_b32 a85, v205
v_accvgpr_read_b32 v205, a86
v_mul_f32 v205, v205, v197
v_accvgpr_write_b32 a86, v205
v_accvgpr_read_b32 v205, a87
v_mul_f32 v205, v205, v197
v_accvgpr_write_b32 a87, v205
v_accvgpr_read_b32 v205, a100
v_mul_f32 v205, v205, v197
v_accvgpr_write_b32 a100, v205
v_accvgpr_read_b32 v205, a101
v_mul_f32 v205, v205, v197
v_accvgpr_write_b32 a101, v205
v_accvgpr_read_b32 v205, a102
v_mul_f32 v205, v205, v197
v_accvgpr_write_b32 a102, v205
v_accvgpr_read_b32 v205, a103
v_mul_f32 v205, v205, v197
v_accvgpr_write_b32 a103, v205
v_accvgpr_read_b32 v205, a116
v_mul_f32 v205, v205, v197
v_accvgpr_write_b32 a116, v205
v_accvgpr_read_b32 v205, a117
v_mul_f32 v205, v205, v197
v_accvgpr_write_b32 a117, v205
v_accvgpr_read_b32 v205, a118
v_mul_f32 v205, v205, v197
v_accvgpr_write_b32 a118, v205
v_accvgpr_read_b32 v205, a119
v_mul_f32 v205, v205, v197
v_accvgpr_write_b32 a119, v205
v_max3_f32 v202, v104, v105, v194
v_max3_f32 v202, v106, v107, v202
v_max3_f32 v202, v120, v121, v202
v_max3_f32 v202, v122, v123, v202
v_max3_f32 v202, v136, v137, v202
v_max3_f32 v202, v138, v139, v202
v_max3_f32 v202, v152, v153, v202
v_max3_f32 v202, v154, v155, v202
v_mov_b32 v206, v202
s_nop 1
v_permlane16_swap_b32 v202, v206
s_nop 1
v_max_f32 v202, v202, v206
v_mov_b32 v206, v202
s_nop 1
v_permlane32_swap_b32 v202, v206
s_nop 1
v_max_f32 v202, v202, v206
v_sub_f32 v198, v194, v202
v_exp_f32 v198, v198
v_mov_b32 v194, v202
s_nop 0
v_mul_f32 v212, v212, v198
v_sub_f32 v104, v104, v202
v_sub_f32 v105, v105, v202
v_sub_f32 v106, v106, v202
v_sub_f32 v107, v107, v202
v_sub_f32 v120, v120, v202
v_sub_f32 v121, v121, v202
v_sub_f32 v122, v122, v202
v_sub_f32 v123, v123, v202
v_sub_f32 v136, v136, v202
v_sub_f32 v137, v137, v202
v_sub_f32 v138, v138, v202
v_sub_f32 v139, v139, v202
v_sub_f32 v152, v152, v202
v_sub_f32 v153, v153, v202
v_sub_f32 v154, v154, v202
v_sub_f32 v155, v155, v202
v_exp_f32 v104, v104
v_exp_f32 v105, v105
v_exp_f32 v106, v106
v_exp_f32 v107, v107
v_exp_f32 v120, v120
v_exp_f32 v121, v121
v_exp_f32 v122, v122
v_exp_f32 v123, v123
v_exp_f32 v136, v136
v_exp_f32 v137, v137
v_exp_f32 v138, v138
v_exp_f32 v139, v139
v_exp_f32 v152, v152
v_exp_f32 v153, v153
v_exp_f32 v154, v154
v_exp_f32 v155, v155
s_nop 0
v_add_f32 v212, v212, v104
v_add_f32 v212, v212, v105
v_add_f32 v212, v212, v106
v_add_f32 v212, v212, v107
v_add_f32 v212, v212, v120
v_add_f32 v212, v212, v121
v_add_f32 v212, v212, v122
v_add_f32 v212, v212, v123
v_add_f32 v212, v212, v136
v_add_f32 v212, v212, v137
v_add_f32 v212, v212, v138
v_add_f32 v212, v212, v139
v_add_f32 v212, v212, v152
v_add_f32 v212, v212, v153
v_add_f32 v212, v212, v154
v_add_f32 v212, v212, v155
v_cvt_pk_bf16_f32 v168, v104, v105
v_cvt_pk_bf16_f32 v169, v106, v107
v_cvt_pk_bf16_f32 v170, v120, v121
v_cvt_pk_bf16_f32 v171, v122, v123
v_cvt_pk_bf16_f32 v184, v136, v137
v_cvt_pk_bf16_f32 v185, v138, v139
v_cvt_pk_bf16_f32 v186, v152, v153
v_cvt_pk_bf16_f32 v187, v154, v155
v_accvgpr_read_b32 v206, a8
v_mul_f32 v206, v206, v198
v_accvgpr_write_b32 a8, v206
v_accvgpr_read_b32 v206, a9
v_mul_f32 v206, v206, v198
v_accvgpr_write_b32 a9, v206
v_accvgpr_read_b32 v206, a10
v_mul_f32 v206, v206, v198
v_accvgpr_write_b32 a10, v206
v_accvgpr_read_b32 v206, a11
v_mul_f32 v206, v206, v198
v_accvgpr_write_b32 a11, v206
v_accvgpr_read_b32 v206, a24
v_mul_f32 v206, v206, v198
v_accvgpr_write_b32 a24, v206
v_accvgpr_read_b32 v206, a25
v_mul_f32 v206, v206, v198
v_accvgpr_write_b32 a25, v206
v_accvgpr_read_b32 v206, a26
v_mul_f32 v206, v206, v198
v_accvgpr_write_b32 a26, v206
v_accvgpr_read_b32 v206, a27
v_mul_f32 v206, v206, v198
v_accvgpr_write_b32 a27, v206
v_accvgpr_read_b32 v206, a40
v_mul_f32 v206, v206, v198
v_accvgpr_write_b32 a40, v206
v_accvgpr_read_b32 v206, a41
v_mul_f32 v206, v206, v198
v_accvgpr_write_b32 a41, v206
v_accvgpr_read_b32 v206, a42
v_mul_f32 v206, v206, v198
v_accvgpr_write_b32 a42, v206
v_accvgpr_read_b32 v206, a43
v_mul_f32 v206, v206, v198
v_accvgpr_write_b32 a43, v206
v_accvgpr_read_b32 v206, a56
v_mul_f32 v206, v206, v198
v_accvgpr_write_b32 a56, v206
v_accvgpr_read_b32 v206, a57
v_mul_f32 v206, v206, v198
v_accvgpr_write_b32 a57, v206
v_accvgpr_read_b32 v206, a58
v_mul_f32 v206, v206, v198
v_accvgpr_write_b32 a58, v206
v_accvgpr_read_b32 v206, a59
v_mul_f32 v206, v206, v198
v_accvgpr_write_b32 a59, v206
v_accvgpr_read_b32 v206, a72
v_mul_f32 v206, v206, v198
v_accvgpr_write_b32 a72, v206
v_accvgpr_read_b32 v206, a73
v_mul_f32 v206, v206, v198
v_accvgpr_write_b32 a73, v206
v_accvgpr_read_b32 v206, a74
v_mul_f32 v206, v206, v198
v_accvgpr_write_b32 a74, v206
v_accvgpr_read_b32 v206, a75
v_mul_f32 v206, v206, v198
v_accvgpr_write_b32 a75, v206
v_accvgpr_read_b32 v206, a88
v_mul_f32 v206, v206, v198
v_accvgpr_write_b32 a88, v206
v_accvgpr_read_b32 v206, a89
v_mul_f32 v206, v206, v198
v_accvgpr_write_b32 a89, v206
v_accvgpr_read_b32 v206, a90
v_mul_f32 v206, v206, v198
v_accvgpr_write_b32 a90, v206
v_accvgpr_read_b32 v206, a91
v_mul_f32 v206, v206, v198
v_accvgpr_write_b32 a91, v206
v_accvgpr_read_b32 v206, a104
v_mul_f32 v206, v206, v198
v_accvgpr_write_b32 a104, v206
v_accvgpr_read_b32 v206, a105
v_mul_f32 v206, v206, v198
v_accvgpr_write_b32 a105, v206
v_accvgpr_read_b32 v206, a106
v_mul_f32 v206, v206, v198
v_accvgpr_write_b32 a106, v206
v_accvgpr_read_b32 v206, a107
v_mul_f32 v206, v206, v198
v_accvgpr_write_b32 a107, v206
v_accvgpr_read_b32 v206, a120
v_mul_f32 v206, v206, v198
v_accvgpr_write_b32 a120, v206
v_accvgpr_read_b32 v206, a121
v_mul_f32 v206, v206, v198
v_accvgpr_write_b32 a121, v206
v_accvgpr_read_b32 v206, a122
v_mul_f32 v206, v206, v198
v_accvgpr_write_b32 a122, v206
v_accvgpr_read_b32 v206, a123
v_mul_f32 v206, v206, v198
v_accvgpr_write_b32 a123, v206
v_max3_f32 v203, v108, v109, v195
v_max3_f32 v203, v110, v111, v203
v_max3_f32 v203, v124, v125, v203
v_max3_f32 v203, v126, v127, v203
v_max3_f32 v203, v140, v141, v203
v_max3_f32 v203, v142, v143, v203
v_max3_f32 v203, v156, v157, v203
v_max3_f32 v203, v158, v159, v203
v_mov_b32 v207, v203
s_nop 1
v_permlane16_swap_b32 v203, v207
s_nop 1
v_max_f32 v203, v203, v207
v_mov_b32 v207, v203
s_nop 1
v_permlane32_swap_b32 v203, v207
s_nop 1
v_max_f32 v203, v203, v207
v_sub_f32 v199, v195, v203
v_exp_f32 v199, v199
v_mov_b32 v195, v203
s_nop 0
v_mul_f32 v214, v214, v199
v_sub_f32 v108, v108, v203
v_sub_f32 v109, v109, v203
v_sub_f32 v110, v110, v203
v_sub_f32 v111, v111, v203
v_sub_f32 v124, v124, v203
v_sub_f32 v125, v125, v203
v_sub_f32 v126, v126, v203
v_sub_f32 v127, v127, v203
v_sub_f32 v140, v140, v203
v_sub_f32 v141, v141, v203
v_sub_f32 v142, v142, v203
v_sub_f32 v143, v143, v203
v_sub_f32 v156, v156, v203
v_sub_f32 v157, v157, v203
v_sub_f32 v158, v158, v203
v_sub_f32 v159, v159, v203
v_exp_f32 v108, v108
v_exp_f32 v109, v109
v_exp_f32 v110, v110
v_exp_f32 v111, v111
v_exp_f32 v124, v124
v_exp_f32 v125, v125
v_exp_f32 v126, v126
v_exp_f32 v127, v127
v_exp_f32 v140, v140
v_exp_f32 v141, v141
v_exp_f32 v142, v142
v_exp_f32 v143, v143
v_exp_f32 v156, v156
v_exp_f32 v157, v157
v_exp_f32 v158, v158
v_exp_f32 v159, v159
s_nop 0
v_add_f32 v214, v214, v108
v_add_f32 v214, v214, v109
v_add_f32 v214, v214, v110
v_add_f32 v214, v214, v111
v_add_f32 v214, v214, v124
v_add_f32 v214, v214, v125
v_add_f32 v214, v214, v126
v_add_f32 v214, v214, v127
v_add_f32 v214, v214, v140
v_add_f32 v214, v214, v141
v_add_f32 v214, v214, v142
v_add_f32 v214, v214, v143
v_add_f32 v214, v214, v156
v_add_f32 v214, v214, v157
v_add_f32 v214, v214, v158
v_add_f32 v214, v214, v159
v_cvt_pk_bf16_f32 v172, v108, v109
v_cvt_pk_bf16_f32 v173, v110, v111
v_cvt_pk_bf16_f32 v174, v124, v125
v_cvt_pk_bf16_f32 v175, v126, v127
v_cvt_pk_bf16_f32 v188, v140, v141
v_cvt_pk_bf16_f32 v189, v142, v143
v_cvt_pk_bf16_f32 v190, v156, v157
v_cvt_pk_bf16_f32 v191, v158, v159
v_accvgpr_read_b32 v207, a12
v_mul_f32 v207, v207, v199
v_accvgpr_write_b32 a12, v207
v_accvgpr_read_b32 v207, a13
v_mul_f32 v207, v207, v199
v_accvgpr_write_b32 a13, v207
v_accvgpr_read_b32 v207, a14
v_mul_f32 v207, v207, v199
v_accvgpr_write_b32 a14, v207
v_accvgpr_read_b32 v207, a15
v_mul_f32 v207, v207, v199
v_accvgpr_write_b32 a15, v207
v_accvgpr_read_b32 v207, a28
v_mul_f32 v207, v207, v199
v_accvgpr_write_b32 a28, v207
v_accvgpr_read_b32 v207, a29
v_mul_f32 v207, v207, v199
v_accvgpr_write_b32 a29, v207
v_accvgpr_read_b32 v207, a30
v_mul_f32 v207, v207, v199
v_accvgpr_write_b32 a30, v207
v_accvgpr_read_b32 v207, a31
v_mul_f32 v207, v207, v199
v_accvgpr_write_b32 a31, v207
v_accvgpr_read_b32 v207, a44
v_mul_f32 v207, v207, v199
v_accvgpr_write_b32 a44, v207
v_accvgpr_read_b32 v207, a45
v_mul_f32 v207, v207, v199
v_accvgpr_write_b32 a45, v207
v_accvgpr_read_b32 v207, a46
v_mul_f32 v207, v207, v199
v_accvgpr_write_b32 a46, v207
v_accvgpr_read_b32 v207, a47
v_mul_f32 v207, v207, v199
v_accvgpr_write_b32 a47, v207
v_accvgpr_read_b32 v207, a60
v_mul_f32 v207, v207, v199
v_accvgpr_write_b32 a60, v207
v_accvgpr_read_b32 v207, a61
v_mul_f32 v207, v207, v199
v_accvgpr_write_b32 a61, v207
v_accvgpr_read_b32 v207, a62
v_mul_f32 v207, v207, v199
v_accvgpr_write_b32 a62, v207
v_accvgpr_read_b32 v207, a63
v_mul_f32 v207, v207, v199
v_accvgpr_write_b32 a63, v207
v_accvgpr_read_b32 v207, a76
v_mul_f32 v207, v207, v199
v_accvgpr_write_b32 a76, v207
v_accvgpr_read_b32 v207, a77
v_mul_f32 v207, v207, v199
v_accvgpr_write_b32 a77, v207
v_accvgpr_read_b32 v207, a78
v_mul_f32 v207, v207, v199
v_accvgpr_write_b32 a78, v207
v_accvgpr_read_b32 v207, a79
v_mul_f32 v207, v207, v199
v_accvgpr_write_b32 a79, v207
v_accvgpr_read_b32 v207, a92
v_mul_f32 v207, v207, v199
v_accvgpr_write_b32 a92, v207
v_accvgpr_read_b32 v207, a93
v_mul_f32 v207, v207, v199
v_accvgpr_write_b32 a93, v207
v_accvgpr_read_b32 v207, a94
v_mul_f32 v207, v207, v199
v_accvgpr_write_b32 a94, v207
v_accvgpr_read_b32 v207, a95
v_mul_f32 v207, v207, v199
v_accvgpr_write_b32 a95, v207
v_accvgpr_read_b32 v207, a108
v_mul_f32 v207, v207, v199
v_accvgpr_write_b32 a108, v207
v_accvgpr_read_b32 v207, a109
v_mul_f32 v207, v207, v199
v_accvgpr_write_b32 a109, v207
v_accvgpr_read_b32 v207, a110
v_mul_f32 v207, v207, v199
v_accvgpr_write_b32 a110, v207
v_accvgpr_read_b32 v207, a111
v_mul_f32 v207, v207, v199
v_accvgpr_write_b32 a111, v207
v_accvgpr_read_b32 v207, a124
v_mul_f32 v207, v207, v199
v_accvgpr_write_b32 a124, v207
v_accvgpr_read_b32 v207, a125
v_mul_f32 v207, v207, v199
v_accvgpr_write_b32 a125, v207
v_accvgpr_read_b32 v207, a126
v_mul_f32 v207, v207, v199
v_accvgpr_write_b32 a126, v207
v_accvgpr_read_b32 v207, a127
v_mul_f32 v207, v207, v199
v_accvgpr_write_b32 a127, v207
s_add_u32 s52, s51, 16384
s_add_u32 s53, s52, 4096
buffer_load_dwordx4 a[128:131], v2, s[72:75], s52 offen offset:0
buffer_load_dwordx4 a[132:135], v2, s[72:75], s52 offen offset:1024
buffer_load_dwordx4 a[136:139], v2, s[72:75], s52 offen offset:2048
buffer_load_dwordx4 a[140:143], v2, s[72:75], s52 offen offset:3072
buffer_load_dwordx4 a[144:147], v2, s[72:75], s53 offen offset:0
buffer_load_dwordx4 a[148:151], v2, s[72:75], s53 offen offset:1024
buffer_load_dwordx4 a[152:155], v2, s[72:75], s53 offen offset:2048
buffer_load_dwordx4 a[156:159], v2, s[72:75], s53 offen offset:3072
s_waitcnt vmcnt(0)
s_nop 1
v_mfma_f32_16x16x32_bf16 a[0:3], a[128:131], v[160:163], a[0:3]
v_mfma_f32_16x16x32_bf16 a[4:7], a[128:131], v[164:167], a[4:7]
v_mfma_f32_16x16x32_bf16 a[8:11], a[128:131], v[168:171], a[8:11]
v_mfma_f32_16x16x32_bf16 a[12:15], a[128:131], v[172:175], a[12:15]
v_mfma_f32_16x16x32_bf16 a[0:3], a[132:135], v[176:179], a[0:3]
v_mfma_f32_16x16x32_bf16 a[4:7], a[132:135], v[180:183], a[4:7]
v_mfma_f32_16x16x32_bf16 a[8:11], a[132:135], v[184:187], a[8:11]
v_mfma_f32_16x16x32_bf16 a[12:15], a[132:135], v[188:191], a[12:15]
v_mfma_f32_16x16x32_bf16 a[16:19], a[136:139], v[160:163], a[16:19]
v_mfma_f32_16x16x32_bf16 a[20:23], a[136:139], v[164:167], a[20:23]
v_mfma_f32_16x16x32_bf16 a[24:27], a[136:139], v[168:171], a[24:27]
v_mfma_f32_16x16x32_bf16 a[28:31], a[136:139], v[172:175], a[28:31]
v_mfma_f32_16x16x32_bf16 a[16:19], a[140:143], v[176:179], a[16:19]
v_mfma_f32_16x16x32_bf16 a[20:23], a[140:143], v[180:183], a[20:23]
v_mfma_f32_16x16x32_bf16 a[24:27], a[140:143], v[184:187], a[24:27]
v_mfma_f32_16x16x32_bf16 a[28:31], a[140:143], v[188:191], a[28:31]
v_mfma_f32_16x16x32_bf16 a[32:35], a[144:147], v[160:163], a[32:35]
v_mfma_f32_16x16x32_bf16 a[36:39], a[144:147], v[164:167], a[36:39]
v_mfma_f32_16x16x32_bf16 a[40:43], a[144:147], v[168:171], a[40:43]
v_mfma_f32_16x16x32_bf16 a[44:47], a[144:147], v[172:175], a[44:47]
v_mfma_f32_16x16x32_bf16 a[32:35], a[148:151], v[176:179], a[32:35]
v_mfma_f32_16x16x32_bf16 a[36:39], a[148:151], v[180:183], a[36:39]
v_mfma_f32_16x16x32_bf16 a[40:43], a[148:151], v[184:187], a[40:43]
v_mfma_f32_16x16x32_bf16 a[44:47], a[148:151], v[188:191], a[44:47]
v_mfma_f32_16x16x32_bf16 a[48:51], a[152:155], v[160:163], a[48:51]
v_mfma_f32_16x16x32_bf16 a[52:55], a[152:155], v[164:167], a[52:55]
v_mfma_f32_16x16x32_bf16 a[56:59], a[152:155], v[168:171], a[56:59]
v_mfma_f32_16x16x32_bf16 a[60:63], a[152:155], v[172:175], a[60:63]
v_mfma_f32_16x16x32_bf16 a[48:51], a[156:159], v[176:179], a[48:51]
v_mfma_f32_16x16x32_bf16 a[52:55], a[156:159], v[180:183], a[52:55]
v_mfma_f32_16x16x32_bf16 a[56:59], a[156:159], v[184:187], a[56:59]
v_mfma_f32_16x16x32_bf16 a[60:63], a[156:159], v[188:191], a[60:63]
s_add_u32 s52, s51, 24576
s_add_u32 s53, s52, 4096
buffer_load_dwordx4 a[128:131], v2, s[72:75], s52 offen offset:0
buffer_load_dwordx4 a[132:135], v2, s[72:75], s52 offen offset:1024
buffer_load_dwordx4 a[136:139], v2, s[72:75], s52 offen offset:2048
buffer_load_dwordx4 a[140:143], v2, s[72:75], s52 offen offset:3072
buffer_load_dwordx4 a[144:147], v2, s[72:75], s53 offen offset:0
buffer_load_dwordx4 a[148:151], v2, s[72:75], s53 offen offset:1024
buffer_load_dwordx4 a[152:155], v2, s[72:75], s53 offen offset:2048
buffer_load_dwordx4 a[156:159], v2, s[72:75], s53 offen offset:3072
s_waitcnt vmcnt(0)
s_nop 1
v_mfma_f32_16x16x32_bf16 a[64:67], a[128:131], v[160:163], a[64:67]
v_mfma_f32_16x16x32_bf16 a[68:71], a[128:131], v[164:167], a[68:71]
v_mfma_f32_16x16x32_bf16 a[72:75], a[128:131], v[168:171], a[72:75]
v_mfma_f32_16x16x32_bf16 a[76:79], a[128:131], v[172:175], a[76:79]
v_mfma_f32_16x16x32_bf16 a[64:67], a[132:135], v[176:179], a[64:67]
v_mfma_f32_16x16x32_bf16 a[68:71], a[132:135], v[180:183], a[68:71]
v_mfma_f32_16x16x32_bf16 a[72:75], a[132:135], v[184:187], a[72:75]
v_mfma_f32_16x16x32_bf16 a[76:79], a[132:135], v[188:191], a[76:79]
v_mfma_f32_16x16x32_bf16 a[80:83], a[136:139], v[160:163], a[80:83]
v_mfma_f32_16x16x32_bf16 a[84:87], a[136:139], v[164:167], a[84:87]
v_mfma_f32_16x16x32_bf16 a[88:91], a[136:139], v[168:171], a[88:91]
v_mfma_f32_16x16x32_bf16 a[92:95], a[136:139], v[172:175], a[92:95]
v_mfma_f32_16x16x32_bf16 a[80:83], a[140:143], v[176:179], a[80:83]
v_mfma_f32_16x16x32_bf16 a[84:87], a[140:143], v[180:183], a[84:87]
v_mfma_f32_16x16x32_bf16 a[88:91], a[140:143], v[184:187], a[88:91]
v_mfma_f32_16x16x32_bf16 a[92:95], a[140:143], v[188:191], a[92:95]
v_mfma_f32_16x16x32_bf16 a[96:99], a[144:147], v[160:163], a[96:99]
v_mfma_f32_16x16x32_bf16 a[100:103], a[144:147], v[164:167], a[100:103]
v_mfma_f32_16x16x32_bf16 a[104:107], a[144:147], v[168:171], a[104:107]
v_mfma_f32_16x16x32_bf16 a[108:111], a[144:147], v[172:175], a[108:111]
v_mfma_f32_16x16x32_bf16 a[96:99], a[148:151], v[176:179], a[96:99]
v_mfma_f32_16x16x32_bf16 a[100:103], a[148:151], v[180:183], a[100:103]
v_mfma_f32_16x16x32_bf16 a[104:107], a[148:151], v[184:187], a[104:107]
v_mfma_f32_16x16x32_bf16 a[108:111], a[148:151], v[188:191], a[108:111]
v_mfma_f32_16x16x32_bf16 a[112:115], a[152:155], v[160:163], a[112:115]
v_mfma_f32_16x16x32_bf16 a[116:119], a[152:155], v[164:167], a[116:119]
v_mfma_f32_16x16x32_bf16 a[120:123], a[152:155], v[168:171], a[120:123]
v_mfma_f32_16x16x32_bf16 a[124:127], a[152:155], v[172:175], a[124:127]
v_mfma_f32_16x16x32_bf16 a[112:115], a[156:159], v[176:179], a[112:115]
v_mfma_f32_16x16x32_bf16 a[116:119], a[156:159], v[180:183], a[116:119]
v_mfma_f32_16x16x32_bf16 a[120:123], a[156:159], v[184:187], a[120:123]
v_mfma_f32_16x16x32_bf16 a[124:127], a[156:159], v[188:191], a[124:127]
s_nop 7
s_nop 7
s_add_u32 s51, s51, 32768
s_sub_u32 s46, s46, 1
s_cmp_lg_u32 s46, 0
s_cbranch_scc1 SMF_18_l
v_add_f32 v216, v208, v209
v_add_f32 v217, v210, v211
v_add_f32 v218, v212, v213
v_add_f32 v219, v214, v215
s_nop 1
s_branch SMB_17
DP_19_s:
s_mov_b32 s61, 0
DP_19_s_l:
s_sleep 1
global_load_dwordx2 v[28:29], v26, s[22:23] sc1
s_waitcnt vmcnt(0)
v_cmp_eq_u64 vcc, s[44:45], v[28:29]
s_nop 4
s_cmp_eq_u64 vcc, -1
s_cbranch_scc1 DP_19_b
s_add_u32 s61, s61, 1
s_cmp_lt_u32 s61, 0x20000
s_cbranch_scc1 DP_19_s_l
s_branch DP_19_b
DP_20_s:
s_mov_b32 s61, 0
DP_20_s_l:
s_sleep 1
global_load_dwordx2 v[28:29], v26, s[22:23] sc1
s_waitcnt vmcnt(0)
v_cmp_eq_u64 vcc, s[44:45], v[28:29]
s_nop 4
s_cmp_eq_u64 vcc, -1
s_cbranch_scc1 DP_20_b
s_add_u32 s61, s61, 1
s_cmp_lt_u32 s61, 0x20000
s_cbranch_scc1 DP_20_s_l
s_branch DP_20_b
KTEXT1:
)ASM" :: "s"(__builtin_amdgcn_kernarg_segment_ptr()), "s"(__builtin_amdgcn_workgroup_id_x()), "v"(threadIdx.x) : "v1", "v2", "v3", "v4", "v5", "v6", "v7", "v8", "v9", "v10", "v11", "v12", "v13", "v14", "v15", "v16", "v17", "v18", "v19", "v20", "v21", "v22", "v23", "v24", "v25", "v26", "v27", "v28", "v29", "v30", "v31", "v32", "v33", "v34", "v35", "v36", "v37", "v38", "v39", "v40", "v41", "v42", "v43", "v44", "v45", "v46", "v47", "v48", "v49", "v50", "v51", "v52", "v53", "v54", "v55", "v56", "v57", "v58", "v59", "v60", "v61", "v62", "v63", "v64", "v65", "v66", "v67", "v68", "v69", "v70", "v71", "v72", "v73", "v74", "v75", "v76", "v77", "v78", "v79", "v80", "v81", "v82", "v83", "v84", "v85", "v86", "v87", "v88", "v89", "v90", "v91", "v92", "v93", "v94", "v95", "v96", "v97", "v98", "v99", "v100", "v101", "v102", "v103", "v104", "v105", "v106", "v107", "v108", "v109", "v110", "v111", "v112", "v113", "v114", "v115", "v116", "v117", "v118", "v119", "v120", "v121", "v122", "v123", "v124", "v125", "v126", "v127", "v128", "v129", "v130", "v131", "v132", "v133", "v134", "v135", "v136", "v137", "v138", "v139", "v140", "v141", "v142", "v143", "v144", "v145", "v146", "v147", "v148", "v149", "v150", "v151", "v152", "v153", "v154", "v155", "v156", "v157", "v158", "v159", "v160", "v161", "v162", "v163", "v164", "v165", "v166", "v167", "v168", "v169", "v170", "v171", "v172", "v173", "v174", "v175", "v176", "v177", "v178", "v179", "v180", "v181", "v182", "v183", "v184", "v185", "v186", "v187", "v188", "v189", "v190", "v191", "v192", "v193", "v194", "v195", "v196", "v197", "v198", "v199", "v200", "v201", "v202", "v203", "v204", "v205", "v206", "v207", "v208", "v209", "v210", "v211", "v212", "v213", "v214", "v215", "v216", "v217", "v218", "v219", "v220", "v221", "v222", "v223", "v224", "v225", "v226", "v227", "v228", "v229", "v230", "v231", "v232", "v233", "v234", "v235", "v236", "v237", "v238", "v239", "v240", "v241", "v242", "v243", "v244", "v245", "v246", "v247", "v248", "v249", "v250", "v251", "v252", "v253", "v254", "v255", "a0", "a1", "a2", "a3", "a4", "a5", "a6", "a7", "a8", "a9", "a10", "a11", "a12", "a13", "a14", "a15", "a16", "a17", "a18", "a19", "a20", "a21", "a22", "a23", "a24", "a25", "a26", "a27", "a28", "a29", "a30", "a31", "a32", "a33", "a34", "a35", "a36", "a37", "a38", "a39", "a40", "a41", "a42", "a43", "a44", "a45", "a46", "a47", "a48", "a49", "a50", "a51", "a52", "a53", "a54", "a55", "a56", "a57", "a58", "a59", "a60", "a61", "a62", "a63", "a64", "a65", "a66", "a67", "a68", "a69", "a70", "a71", "a72", "a73", "a74", "a75", "a76", "a77", "a78", "a79", "a80", "a81", "a82", "a83", "a84", "a85", "a86", "a87", "a88", "a89", "a90", "a91", "a92", "a93", "a94", "a95", "a96", "a97", "a98", "a99", "a100", "a101", "a102", "a103", "a104", "a105", "a106", "a107", "a108", "a109", "a110", "a111", "a112", "a113", "a114", "a115", "a116", "a117", "a118", "a119", "a120", "a121", "a122", "a123", "a124", "a125", "a126", "a127", "a128", "a129", "a130", "a131", "a132", "a133", "a134", "a135", "a136", "a137", "a138", "a139", "a140", "a141", "a142", "a143", "a144", "a145", "a146", "a147", "a148", "a149", "a150", "a151", "a152", "a153", "a154", "a155", "a156", "a157", "a158", "a159", "a160", "a161", "a162", "a163", "a164", "a165", "a166", "a167", "a168", "a169", "a170", "a171", "a172", "a173", "a174", "a175", "a176", "a177", "a178", "a179", "a180", "a181", "a182", "a183", "a184", "a185", "a186", "a187", "a188", "a189", "a190", "a191", "a192", "a193", "a194", "a195", "a196", "a197", "a198", "a199", "a200", "a201", "a202", "a203", "a204", "a205", "a206", "a207", "a208", "a209", "a210", "a211", "a212", "a213", "a214", "a215", "a216", "a217", "a218", "a219", "a220", "a221", "a222", "a223", "a224", "a225", "a226", "a227", "a228", "a229", "a230", "a231", "a232", "a233", "a234", "a235", "a236", "a237", "a238", "a239", "a240", "a241", "a242", "a243", "a244", "a245", "a246", "a247", "a248", "a249", "a250", "a251", "a252", "a253", "a254", "a255", "s36", "s37", "s38", "s39", "s40", "s41", "s42", "s43", "s44", "s45", "s46", "s47", "s48", "s49", "s50", "s51", "s52", "s53", "s54", "s55", "s56", "s57", "s58", "s59", "s60", "s61", "s62", "s63", "s64", "s65", "s66", "s67", "s68", "s69", "s70", "s71", "s72", "s73", "s74", "s75", "s76", "s77", "s78", "s79", "s80", "s81", "s82", "s83", "s84", "s85", "s86", "s87", "s88", "s89", "s90", "s91", "s92", "s93", "s94", "s95", "s96", "s97", "s98", "s99", "s100", "s101", "vcc", "memory");
}
extern "C" void kernel_launch(void* const* d_in, const int* in_sizes, int n_in, void* d_out, int out_size, void* d_ws, size_t ws_size, hipStream_t stream) {
  KArgs a;
  a.x = d_in[0]; a.kc = d_in[1]; a.vc = d_in[2]; a.wq = d_in[3]; a.wo = d_in[4]; a.wg = d_in[6]; a.wu = d_in[7]; a.wd = d_in[8]; a.out = d_out; a.ws = d_ws;
  fused_block<<<256, 256, 0, stream>>>(a);
}
```
